```python
import torch

def _softmax(x, axis=-1):
    return torch.softmax(x, dim=axis)
def _gelu(x):
    return torch.nn.functional.gelu(x, approximate="tanh")
def _silu(x):
    return torch.nn.functional.silu(x)
def _sigmoid(x):
    return torch.sigmoid(x)
def _layernorm(x, axis=-1, eps=1e-5):
    m = x.mean(dim=axis, keepdim=True)
    v = x.var(dim=axis, keepdim=True, unbiased=False)
    return (x - m) / torch.sqrt(v + eps)
def _scatter_add(tgt, idx, src, axis=0):
    return tgt.clone().index_add_(axis, idx, src)
def _segment_sum(src, seg, n):
    out = torch.zeros((n,) + src.shape[1:], dtype=src.dtype)
    return out.index_add_(0, seg, src)
def _pad(x, pw):
    flat = tuple(v for pair in reversed(pw) for v in pair)
    return torch.nn.functional.pad(x, flat)

kernel_name = 'recurrent_scan__gqa_block_cached__moe_arch_mod__prompt'

def setup_inputs():

    _w = (lambda a: (lambda t: t.to(torch.bfloat16) if t.is_floating_point() else t)(torch.as_tensor(a)))
    def _A_setup():
        B, Sq, D = 1, 2048, 576
        Hq, Hkv, Sk, Dh = 32, 16, 2048, 128
        assert Hq % Hkv == 0, (Hq, Hkv)
        return {
            "x": _w((lambda *s: torch.randn(*s, dtype=torch.bfloat16) * 0.05)(B, Sq, D)),
            "k_cache": _w((lambda *s: torch.randn(*s, dtype=torch.bfloat16) * 0.8)(B, Hkv, Sk, Dh)),
            "v_cache": _w((lambda *s: torch.randn(*s, dtype=torch.bfloat16) * 0.2)(B, Hkv, Sk, Dh)),
            "wq": _w((lambda *s: torch.randn(*s, dtype=torch.bfloat16) * 0.8)(D, Hq * Dh) * (D ** -0.5)),
            "wo": _w((lambda *s: torch.randn(*s, dtype=torch.bfloat16) * 0.2)(Hq * Dh, D) * ((Hq * Dh) ** -0.5)),
        }
    _A_in = _A_setup()

    def _B_setup():
        B, S, D, F, C = 1, 2048, 576, 4608, 8
        return {
            "x": _w((lambda *s: torch.randn(*s, dtype=torch.bfloat16) * 0.05)(B, S, D)),
            "w_r": _w((lambda *s: torch.randn(*s, dtype=torch.bfloat16) * 0.2)(D)),
            "w_gate": _w((lambda *s: torch.randn(*s, dtype=torch.bfloat16) * 0.1)(D, F)),
            "w_up": _w((lambda *s: torch.randn(*s, dtype=torch.bfloat16) * 0.1)(D, F)),
            "w_down": _w((lambda *s: torch.randn(*s, dtype=torch.bfloat16) * 0.1)(F, D)),
        }
    _B_in = _B_setup()

    return {
        "A_x": _A_in["x"],
        "A_k_cache": _A_in["k_cache"],
        "A_v_cache": _A_in["v_cache"],
        "A_wq": _A_in["wq"],
        "A_wo": _A_in["wo"],
        "B_x": _B_in["x"],
        "B_w_r": _B_in["w_r"],
        "B_w_gate": _B_in["w_gate"],
        "B_w_up": _B_in["w_up"],
        "B_w_down": _B_in["w_down"],
    }

def reference(A_x, A_k_cache, A_v_cache, A_wq, A_wo, B_x, B_w_r, B_w_gate, B_w_up, B_w_down):
    def _A_ref(x, k_cache, v_cache, wq, wo):
        B, Sq, D = x.shape
        Hq, Dh = 32, 128
        Hkv = k_cache.shape[1]
        rep = Hq // Hkv
        q = (lambda x, *d: x.permute(*d))((x @ wq).reshape(B, Sq, Hq, Dh), 0, 2, 1, 3)
        k = (lambda x, n, axis: torch.repeat_interleave(x, n, dim=axis))(k_cache, rep, axis=1)
        v = (lambda x, n, axis: torch.repeat_interleave(x, n, dim=axis))(v_cache, rep, axis=1)
        scores = torch.einsum("bhqd,bhkd->bhqk", q, k) * (Dh ** -0.5)
        p = _softmax(scores, axis=-1)
        o = torch.einsum("bhqk,bhkd->bhqd", p, v)
        return (lambda x, *d: x.permute(*d))(o, 0, 2, 1, 3).reshape(B, Sq, Hq * Dh) @ wo

    def _B_ref(x, w_r, w_gate, w_up, w_down):
        B, S, D = x.shape
        C = 8
        r = x @ w_r
        idx = torch.argsort(-r, axis=-1)[:, :C]
        idx_e = (lambda x, n, axis: torch.repeat_interleave(x, n, dim=axis))(idx.reshape(B, C, 1), D, axis=2)
        x_sel = (lambda x, i, axis: torch.take_along_dim(x, i, dim=axis))(x, idx_e, axis=1)
        g = _silu(x_sel @ w_gate)
        u = x_sel @ w_up
        y = (g * u) @ w_down
        r_sel = _sigmoid((lambda x, i, axis: torch.take_along_dim(x, i, dim=axis))(r, idx, axis=1))
        y = y * r_sel.reshape(B, C, 1)
        b_off = (torch.arange(B) * S).reshape(B, 1)
        flat_idx = (idx + b_off).reshape(B * C)
        zero = (x * 0.0).reshape(B * S, D)
        upd = _scatter_add(zero, flat_idx, y.reshape(B * C, D), axis=0)
        return x + upd.reshape(B, S, D)

    x = A_x
    xn = _layernorm(x, axis=-1)
    s = _A_ref(xn, A_k_cache, A_v_cache, A_wq, A_wo)
    g = _silu(_B_ref(xn, B_w_r, B_w_gate, B_w_up, B_w_down))
    return x + s * g
```

```cpp
#include <hip/hip_runtime.h>
#include <hip/hip_bf16.h>
#include <utility>
#include <type_traits>

constexpr int SEQ = 2048, DM = 576, HQ = 32, HKV = 16, DH = 128, FF = 4608, NSEL = 8;
constexpr int NT = SEQ / 64;
constexpr float LIM = 1073741824.0f;

typedef float    f32x16 __attribute__((ext_vector_type(16)));
typedef float    f32x2  __attribute__((ext_vector_type(2)));
typedef float    f32x4  __attribute__((ext_vector_type(4)));
typedef __bf16   bf16x8 __attribute__((ext_vector_type(8)));
typedef __bf16   bf16x4 __attribute__((ext_vector_type(4)));
typedef __bf16   bf16x2 __attribute__((ext_vector_type(2)));
typedef unsigned u32x8  __attribute__((ext_vector_type(8)));
typedef unsigned u32x4  __attribute__((ext_vector_type(4)));
typedef unsigned u32x2  __attribute__((ext_vector_type(2)));
typedef unsigned short u16;
template <int... Is> using iseq = std::integer_sequence<int, Is...>;
template <int N> using make_iseq = std::make_integer_sequence<int, N>;

#define DI __device__ __forceinline__
#define GAP() __builtin_amdgcn_sched_barrier(0)
#define IC(x) std::integral_constant<int, (x)>{}
#define SRD_WORD3 0x00020000

DI unsigned rfl(unsigned x) { return __builtin_amdgcn_readfirstlane(x); }
DI unsigned cvtpk(float lo, float hi) { f32x2 v = {lo, hi}; bf16x2 b = __builtin_convertvector(v, bf16x2); return __builtin_bit_cast(unsigned, b); }
DI __amdgpu_buffer_rsrc_t mk_srd(const void* p, const unsigned bytes) {
  asm volatile("" : "+s"(p));
  return __builtin_amdgcn_make_buffer_rsrc((void*)p, (short)0, (int)bytes, SRD_WORD3);
}
DI unsigned vsel(const bool c, unsigned a, const unsigned b) { asm volatile("" : "+v"(a)); return c ? a : b; }
DI float bflo(unsigned w) { return __uint_as_float(w << 16); }
DI float bfhi(unsigned w) { return __uint_as_float(w & 0xffff0000u); }
DI float rbf(float x) { return bflo(cvtpk(x, 0.f)); }
DI u16 f2bf(float x) { return (u16)(cvtpk(x, 0.f) & 0xffffu); }

constexpr size_t WS_XN   = 0;
constexpr size_t WS_KEY  = WS_XN + (size_t)(SEQ + NSEL) * DM * 2;
constexpr size_t WS_ST   = WS_KEY + (size_t)SEQ * 8;
constexpr size_t WS_SEL  = WS_ST + (size_t)SEQ * 8;
constexpr unsigned H_G = 32u, H_W = 64u, H_Y = 128u, H_O = 4096u, H_X = 1536u, H_E = 8192u;
constexpr size_t WS_O    = WS_SEL + 65536;
constexpr size_t WS_WOT  = WS_O + (size_t)SEQ * HQ * DH * 2;
constexpr int    MOE_WG  = 144;
constexpr size_t WS_YP   = WS_WOT + (size_t)HQ * DH * DM * 2;
constexpr size_t WS_END  = WS_YP + (size_t)MOE_WG * NSEL * DM * 4 + 256u * 18432u;

DI void ce(unsigned& a, unsigned& b) { const unsigned hi_ = a > b ? a : b, lo_ = a > b ? b : a; a = hi_; b = lo_; }
DI int key_row(unsigned k) { return 0xffff - (int)(k & 0xffffu); }
DI float key_val(unsigned k) { const unsigned mono = k >> 16; const unsigned rb = (mono & 0x8000u) ? (mono & 0x7fffu) : (~mono & 0xffffu); return __uint_as_float(rb << 16); }

struct LNJ { u32x4 a[3], w[3]; float x0, s[3], q[3], mb, den, rden, t0, t1, t2, t3; unsigned stat, key; bool three, up; };
DI float rcp_nr(float d) { const float r = __builtin_amdgcn_rcpf(d); return __builtin_fmaf(__builtin_fmaf(-d, r, 1.f), r, r); }
template <int CTRL> DI float dpp_add(float v) { return v + __int_as_float(__builtin_amdgcn_update_dpp(0, __float_as_int(v), CTRL, 0xf, 0xf, false)); }
template <int K> DI float red_step(float v) {
  if constexpr (K == 0) return dpp_add<0xB1>(v);
  else if constexpr (K == 1) return dpp_add<0x4E>(v);
  else if constexpr (K == 2) return dpp_add<0x141>(v);
  else if constexpr (K == 3) return dpp_add<0x140>(v);
  else { auto pr = __builtin_amdgcn_permlane16_swap(__float_as_uint(v), __float_as_uint(v), false, false); return __uint_as_float(pr[0]) + __uint_as_float(pr[1]); }
}
#define PIN2(a, b) asm volatile("" : "+v"(a), "+v"(b))
constexpr int LN_STAT = 34, LN_N0 = 40, LN_XN = LN_N0 + 96, LN_KEY = LN_XN + 7;
template <int I> DI void ln_step(LNJ& j, const unsigned row) {
  if constexpr (I == 0) {
    const float xa = bflo((unsigned)__builtin_amdgcn_readlane((int)j.a[0][0], 0)), xb = bflo((unsigned)__builtin_amdgcn_readlane((int)j.a[0][0], 32));
    j.x0 = j.up ? xb : xa;
    asm volatile("" : "+v"(j.x0));
  } else if constexpr (I <= 24) {
    constexpr int e = I - 1, c = e >> 3, i = (e >> 1) & 3;
    const float d = ((e & 1) ? bfhi(j.a[c][i]) : bflo(j.a[c][i])) - j.x0;
    if constexpr ((e & 7) == 0) { j.s[c] = d; j.q[c] = d * d; } else { j.s[c] += d; j.q[c] = __builtin_fmaf(d, d, j.q[c]); }
    PIN2(j.s[c], j.q[c]);
  } else if constexpr (I == 25) {
    j.t0 = (j.s[0] + j.s[1]) + (j.three ? j.s[2] : 0.f); j.t1 = (j.q[0] + j.q[1]) + (j.three ? j.q[2] : 0.f);
    PIN2(j.t0, j.t1);
  } else if constexpr (I <= 30) {
    j.t0 = red_step<I - 26>(j.t0); j.t1 = red_step<I - 26>(j.t1);
    PIN2(j.t0, j.t1);
  } else if constexpr (I == 31) {
    j.t0 *= 1.0f / DM; j.t1 = __builtin_fmaxf(__builtin_fmaf(-j.t0, j.t0, j.t1 * (1.0f / DM)), 0.f);
    PIN2(j.t0, j.t1);
  } else if constexpr (I == 32) {
    j.mb = rbf(j.x0 + j.t0); j.t1 = rbf(j.t1) + 1e-5f;
    PIN2(j.mb, j.t1);
  } else if constexpr (I == 33) {
    j.den = rbf(__builtin_amdgcn_sqrtf(rbf(j.t1)));
    j.stat = (__float_as_uint(j.mb) & 0xffff0000u) | (__float_as_uint(j.den) >> 16);
    PIN2(j.den, j.stat);
  } else if constexpr (I < LN_N0) {
    if constexpr (I == 34) { j.rden = rcp_nr(j.den); j.s[0] = 0.f; j.s[1] = 0.f; asm volatile("" : "+v"(j.rden)); }
  } else if constexpr (I < LN_XN) {
    constexpr int e = (I - LN_N0) >> 3, st = (I - LN_N0) & 7, c = e >> 2, i = e & 3;
    if constexpr (st == 0)      { j.t0 = bflo(j.a[c][i]); j.t1 = bfhi(j.a[c][i]); }
    else if constexpr (st == 1) { j.t0 -= j.mb; j.t1 -= j.mb; }
    else if constexpr (st == 2) { const unsigned pk = cvtpk(j.t0, j.t1); j.t0 = bflo(pk); j.t1 = __uint_as_float(pk); }
    else if constexpr (st == 3) { j.t0 *= j.rden; j.t1 = bfhi(__float_as_uint(j.t1)); }
    else if constexpr (st == 4) { unsigned pk = cvtpk(j.t0, j.t1 * j.rden); asm volatile("" : "+v"(pk)); j.a[c][i] = pk; }
    else if constexpr (st == 5) { j.t0 = bflo(j.a[c][i]); j.t1 = bflo(j.w[c][i]); }
    else if constexpr (st == 6) { j.s[0] = __builtin_fmaf(j.t0, j.t1, j.s[0]); j.t0 = bfhi(j.a[c][i]); asm volatile("" : "+v"(j.s[0])); }
    else                        { j.s[1] = __builtin_fmaf(j.t0, bfhi(j.w[c][i]), j.s[1]); asm volatile("" : "+v"(j.s[1])); }
    PIN2(j.t0, j.t1);
  } else if constexpr (I == LN_XN) {
    j.t0 = j.s[0] + j.s[1]; asm volatile("" : "+v"(j.t0));
  } else if constexpr (I < LN_XN + 6) {
    j.t0 = red_step<I - LN_XN - 1>(j.t0); asm volatile("" : "+v"(j.t0));
  } else if constexpr (I == LN_XN + 6) {
    const unsigned rb = f2bf(j.t0);
    const unsigned mono = (rb & 0x8000u) ? (~rb & 0xffffu) : (rb | 0x8000u);
    j.key = (mono << 16) | (0xffffu - row);
    asm volatile("" : "+v"(j.key));
  }
}

struct RTS { unsigned k[8], bad, m, cnd, lq, va, cv, rank; u32x4 g[4]; float rs; unsigned long long pm[2]; };
DI void cnd_s(unsigned& d, const unsigned a, const unsigned long long m) { asm volatile("v_cndmask_b32_e64 %0, %0, %1, %2" : "+v"(d) : "v"(a), "s"(m)); }
DI void cnt_s(unsigned& d, const unsigned long long m) { unsigned long long co; asm volatile("v_addc_co_u32_e64 %0, %1, 0, %0, %2" : "+v"(d), "=s"(co) : "s"(m)); }
constexpr int RT_SORT = 3, RT_POP = RT_SORT + 19, RT_END = RT_POP + 64;
template <int CTRL> DI unsigned dpp_umax(unsigned v) { const unsigned t = (unsigned)__builtin_amdgcn_update_dpp(0, (int)v, CTRL, 0xf, 0xf, false); return t > v ? t : v; }
template <int I> DI void rt_step(RTS& r, const unsigned tag) {
  constexpr int net[19][2] = {{0, 1}, {2, 3}, {4, 5}, {6, 7}, {0, 2}, {1, 3}, {4, 6}, {5, 7}, {1, 2}, {5, 6}, {0, 4}, {3, 7}, {1, 5}, {2, 6}, {1, 4}, {3, 6}, {2, 4}, {3, 5}, {3, 4}};
  if constexpr (I == 0) { r.bad = (r.g[0][1] ^ tag) | (r.g[0][3] ^ tag) | (r.g[1][1] ^ tag); asm volatile("" : "+v"(r.bad)); }
  else if constexpr (I == 1) { r.bad |= (r.g[1][3] ^ tag) | (r.g[2][1] ^ tag) | (r.g[2][3] ^ tag); asm volatile("" : "+v"(r.bad)); }
  else if constexpr (I == 2) {
    r.bad |= (r.g[3][1] ^ tag) | (r.g[3][3] ^ tag); r.cnd = 0u; r.cv = r.lq & 15u;
#pragma unroll
    for (int j = 0; j < 8; ++j) r.k[j] = r.g[j >> 1][2 * (j & 1)];
    asm volatile("" : "+v"(r.bad));
  } else if constexpr (I < RT_POP) { ce(r.k[net[I - RT_SORT][0]], r.k[net[I - RT_SORT][1]]); PIN2(r.k[net[I - RT_SORT][0]], r.k[net[I - RT_SORT][1]]); }
  else if constexpr (I < RT_END) {
    constexpr int n = (I - RT_POP) >> 3, p = (I - RT_POP) & 7;
    if constexpr (p == 0) r.m = dpp_umax<0xB1>(r.k[0]);
    else if constexpr (p == 1) { r.m = dpp_umax<0x4E>(r.m); asm volatile("v_cmp_eq_u32_e64 %0, %1, %2" : "=s"(r.pm[1]) : "i"(n), "v"(r.cv)); }
    else if constexpr (p == 2) r.m = dpp_umax<0x141>(r.m);
    else if constexpr (p == 3) r.m = dpp_umax<0x140>(r.m);
    else if constexpr (p == 4) { asm volatile("v_cmp_eq_u32_e64 %0, %1, %2" : "=s"(r.pm[0]) : "v"(r.m), "v"(r.k[0])); cnd_s(r.cnd, r.m, r.pm[1]); }
    else {
#pragma unroll
      for (int i = p == 5 ? 0 : 2 * p - 9; i < 2 * p - 7 && i < 7 - n; ++i) cnd_s(r.k[i], r.k[i + 1], r.pm[0]);
    }
    asm volatile("" : "+v"(r.m));
  }
}

constexpr unsigned PF_DUM = 0x27C00u;
#define KRING 0x0000u
#define VRING 0x8000u
constexpr unsigned SD_BASE = 0x10000u, MO_XR = 0x22000u, MO_H = 0x25200u, MO_C = 0x26400u;
constexpr unsigned RED0 = 208u, RED1 = 244u;
constexpr unsigned MO_HH = 0x26C00u;
constexpr unsigned WF_FL = 0x27000u;
constexpr unsigned MO_RS = 0x26800u, MO_KEY = 0x20000u;
constexpr unsigned BP_W = 23552u;
constexpr size_t SHM_ATTN = 0x28000;
constexpr unsigned KRS = 256u, KTILEB = 64u * KRS;
constexpr unsigned LAH = 3u;
constexpr unsigned KMASK = SEQ * KRS - 1u;
constexpr unsigned WRS = HQ * DH * 2u;
constexpr unsigned XRS = DM * 2u;

#define W4_ACLOB_HI "a128","a129","a130","a131","a132","a133","a134","a135","a136","a137","a138","a139","a140","a141","a142","a143","a144","a145","a146","a147","a148","a149","a150","a151","a152","a153","a154","a155","a156","a157","a158","a159","a160","a161","a162","a163","a164","a165","a166","a167","a168","a169","a170","a171","a172","a173","a174","a175","a176","a177","a178","a179","a180","a181","a182","a183","a184","a185","a186","a187","a188","a189","a190","a191","a192","a193","a194","a195","a196","a197","a198","a199","a200","a201","a202","a203","a204","a205","a206","a207","a208","a209","a210","a211","a212","a213","a214","a215","a216","a217","a218","a219","a220","a221","a222","a223","a224","a225","a226","a227","a228","a229","a230","a231","a232","a233","a234","a235","a236","a237","a238","a239","a240","a241","a242","a243","a244","a245","a246","a247","a248","a249","a250","a251","a252","a253","a254","a255"
#define W4_ACLOB "a0","a1","a2","a3","a4","a5","a6","a7","a8","a9","a10","a11","a12","a13","a14","a15","a16","a17","a18","a19","a20","a21","a22","a23","a24","a25","a26","a27","a28","a29","a30","a31","a32","a33","a34","a35","a36","a37","a38","a39","a40","a41","a42","a43","a44","a45","a46","a47","a48","a49","a50","a51","a52","a53","a54","a55","a56","a57","a58","a59","a60","a61","a62","a63","a64","a65","a66","a67","a68","a69","a70","a71","a72","a73","a74","a75","a76","a77","a78","a79","a80","a81","a82","a83","a84","a85","a86","a87","a88","a89","a90","a91","a92","a93","a94","a95","a96","a97","a98","a99","a100","a101","a102","a103","a104","a105","a106","a107","a108","a109","a110","a111","a112","a113","a114","a115","a116","a117","a118","a119","a120","a121","a122","a123","a124","a125","a126","a127"

DI float max2f(float a, float b) { float r; asm("v_max_f32_e32 %0, %1, %2" : "=v"(r) : "v"(a), "v"(b)); return r; }
DI void lds_st32(unsigned addr, unsigned v) { asm volatile("ds_write_b32 %0, %1" :: "v"(addr), "v"(v) : "memory"); }

struct W4C {
  unsigned ksx[2];
  unsigned vkb[4], vvb[2], vksrc[2], vvsrc, vvsrc0, m0k, m0v, sofk, ldsb;
  __amdgpu_buffer_rsrc_t srdK, srdV, srdF; unsigned sfo;
  f32x4 S[2][4][4];
  f32x4 NEGM[4], L[4];
  u32x4 ONE; unsigned IDX;
  bf16x8 Vf[8][2];
  u32x4 Pk[4][2];
  bf16x4 vlo[8][2], vup[8][2];
  unsigned pm0[2], psf[2];
  __amdgpu_buffer_rsrc_t srdP; unsigned pfo, pfm0, pfv, pfst, pfend, pdum;
};

constexpr unsigned FCT = 0x27400u;
constexpr int AO_(int db, int qb) { return 32 * qb + 4 * db; }
constexpr int AQ_(int qb, int j) { return 128 + 16 * qb + 4 * j; }
constexpr int AK_(int kb, int j) { return 192 + 16 * kb + 4 * j; }

template <int I> DI float acc_read() { float r; asm volatile("v_accvgpr_read_b32 %0, a[%c1]" : "=v"(r) : "i"(I)); return r; }
template <int I> DI void acc_write(const unsigned v) { asm volatile("v_accvgpr_write_b32 a[%c1], %0" :: "v"(v), "i"(I)); }

template <int I, int VSLOT> DI void tr_rd(const W4C& c, bf16x4 (&lo)[8][2], bf16x4 (&up)[8][2]) {
  constexpr int db = I >> 2, k2 = (I >> 1) & 1, off = VSLOT + 8192 * k2 + 4096 * (I & 1) + 512 * (db >> 1);
  if constexpr ((I & 1) == 0) asm volatile("ds_read_b64_tr_b16 %0, %1 offset:%2" : "=v"(lo[db][k2]) : "v"(c.vvb[db & 1]), "i"(off) : "memory");
  else                        asm volatile("ds_read_b64_tr_b16 %0, %1 offset:%2" : "=v"(up[db][k2]) : "v"(c.vvb[db & 1]), "i"(off) : "memory");
}
template <int I, int VSLOT> DI void tr_read_(W4C& c) { tr_rd<I, VSLOT>(c, c.vlo, c.vup); }
template <int I, int SLOT> DI void k_read(W4C& c) {
  asm volatile("ds_read_b128 a[%c1:%c2], %0 offset:%c3" :: "v"(c.vkb[I & 3]), "i"(192 + 4 * I), "i"(195 + 4 * I), "i"(SLOT + 4096 * (I >> 2)) : "memory");
}
template <int KB, int QB, int J, int CM> DI void qk16(f32x4& s, const f32x4& negm) {
  constexpr int ak = AK_(KB, J), aq = AQ_(QB, J);
  if constexpr (CM == 2)      asm volatile("v_mfma_f32_16x16x32_bf16 %0, a[%c1:%c2], a[%c3:%c4], 0" : "=&v"(s) : "i"(ak), "i"(ak + 3), "i"(aq), "i"(aq + 3));
  else if constexpr (CM == 1) asm volatile("v_mfma_f32_16x16x32_bf16 %0, a[%c2:%c3], a[%c4:%c5], %1" : "=&v"(s) : "v"(negm), "i"(ak), "i"(ak + 3), "i"(aq), "i"(aq + 3));
  else                        asm volatile("v_mfma_f32_16x16x32_bf16 %0, a[%c1:%c2], a[%c3:%c4], %0" : "+v"(s) : "i"(ak), "i"(ak + 3), "i"(aq), "i"(aq + 3));
}
template <int X, int M> DI void qk_mfma(W4C& c) { constexpr int kb = M >> 4, qb = (M >> 2) & 3, j = (qb & 1) ? 3 - (M & 3) : (M & 3); qk16<kb, qb, j, ((M & 3) == 0)>(c.S[X][kb][qb], c.NEGM[qb]); }
template <int AO, bool ZERO> DI void pv16(const bf16x8& A, const u32x4& B) {
  if constexpr (ZERO) asm volatile("v_mfma_f32_16x16x32_bf16 a[%c2:%c3], %0, %1, 0" :: "v"(A), "v"(B), "i"(AO), "i"(AO + 3));
  else                asm volatile("v_mfma_f32_16x16x32_bf16 a[%c2:%c3], %0, %1, a[%c2:%c3]" :: "v"(A), "v"(B), "i"(AO), "i"(AO + 3));
}
template <bool ZERO> DI void sum16(f32x4& l, const u32x4& one, const u32x4& B) {
  if constexpr (ZERO) asm volatile("v_mfma_f32_16x16x32_bf16 %0, %1, %2, 0" : "=&v"(l) : "v"(one), "v"(B));
  else                asm volatile("v_mfma_f32_16x16x32_bf16 %0, %1, %2, %0" : "+v"(l) : "v"(one), "v"(B));
}
DI void sum64(W4C& c, const int qb, const u32x4 (&P)[2]) {
  const u32x8 B = __builtin_shufflevector(P[0], P[1], 0, 1, 2, 3, 4, 5, 6, 7);
  asm volatile("v_smfmac_f32_16x16x64_bf16 %0, %1, %2, %3" : "+v"(c.L[qb]) : "v"(c.ONE), "v"(B), "v"(c.IDX));
}
constexpr int NGB = 68;
template <int G, bool ZERO> DI void pv_mfma(W4C& c) {
  constexpr int qb = G / 17, r = G % 17, db = (r >> 1) & 7, k2 = (r ^ db) & 1;
  if constexpr (r < 16) pv16<AO_(db, qb), (ZERO && (r & 1) == 0)>(c.Vf[db][k2], c.Pk[qb][k2]);
  else sum64(c, qb, c.Pk[qb]);
}
DI void real_op() { GAP(); __builtin_amdgcn_s_setprio(0); GAP(); }
DI void add_v(float& acc, float x) { asm volatile("v_add_f32_e32 %0, %0, %1" : "+v"(acc) : "v"(x)); }
template <int X, int E> DI void exp_e(W4C& c) {
  GAP();
  const float e = __builtin_amdgcn_exp2f(c.S[X][E >> 4][(E >> 2) & 3][E & 3]);
  asm volatile("" :: "v"(e));
  c.S[X][E >> 4][(E >> 2) & 3][E & 3] = e;
  GAP();
}
template <int X, int U> DI void pack_to(W4C& c, u32x4 (&P)[4][2]) {
  constexpr int kb = U >> 3, qb = (U >> 1) & 3, h = U & 1;
  GAP();
  const unsigned w = cvtpk(c.S[X][kb][qb][2 * h], c.S[X][kb][qb][2 * h + 1]);
  asm volatile("" :: "v"(w));
  P[qb][kb >> 1][2 * (kb & 1) + h] = w;
  GAP();
}
template <int X, int U> DI void pack_e(W4C& c) { pack_to<X, U>(c, c.Pk); }

constexpr int NBE = 40;
constexpr char PA0[] = "EXETEXFTEXETEXFT" "EXETEXFTEXETEXFT" "EXXEXFEXXEXEXEXF" "ECCECFECCECECECF";
constexpr char PB2[] = "EFEKEEEEKEEFEKEES" "EEKEEDEKEEDEKEEDS" "EKEDEKEEEFEEEEEES" "EEEEEEEEEEEEEEEES";
constexpr char PB0[] = "EDEKEEDEKEEDEKFES" "DEKEEDEKFEDEKEEDS" "EKEDEKEEFDEEEFDES" "EEEEEFEEEEEFEEEES";
constexpr int pcnt(const char* p, char ch, int g) { int n = 0; for (int i = 0; i < g; ++i) n += p[i] == ch; return n; }
constexpr int ppos(const char* p, char ch, int n) { int i = 0; while (p[i] != ch || n-- > 0) ++i; return i; }
constexpr int FA(int n) { constexpr int g[12] = {6, 10, 14, 18, 22, 30, 37, 41, 47, 53, 57, 63}; return g[n]; }
constexpr int FB(int n) { constexpr int g[10] = {14, 25, 33, 42, 47, 50, 56, 59, 62, 67}; return g[n]; }
constexpr int fa_of(int m) { for (int n = 0; n < 12; ++n) if (FA(n) == m) return n; return -1; }
constexpr int fb_of(int m) { for (int n = 0; n < 10; ++n) if (FB(n) == m) return n; return -1; }
constexpr int DB(int n) { return ppos(PB0, 'D', n); }
constexpr int kyo(int g0, int g1) { const int n = 2 * (pcnt(PB0, 'K', g1) - pcnt(PB0, 'K', g0)); return n < 15 ? n : 15; }
static_assert(pcnt(PA0, 'E', 64) == 64 - NBE && pcnt(PB0, 'E', NGB) == NBE && pcnt(PB0, 'D', NGB) == 10 && pcnt(PB0, 'K', NGB) == 8 && FB(3) == DB(8) - 1 && FB(4) == DB(9) - 1 && PA0[FA(6)] == 'F' && PA0[FA(8)] == 'F', "");
template <int X, int M, typename H> DI void gapA(W4C& c, const int t, H& hook) {
  constexpr int Xp = X ^ 1;
  constexpr const char* pa = PA0;
  constexpr char ty = pa[M];
  constexpr int nt = 2 * pcnt(pa, 'T', M) + pcnt(pa, 'X', M), np = pcnt(pa, 'X', M) + 2 * pcnt(pa, 'C', M);
  if constexpr (M == 0) asm volatile("" ::: W4_ACLOB, W4_ACLOB_HI);
  qk_mfma<X, M>(c);
  hook(std::integral_constant<int, M>{});
  if constexpr (ty == 'T' || ty == 'X') tr_read_<nt, Xp * 0x4000>(c);
  if constexpr (ty == 'T') tr_read_<nt + 1, Xp * 0x4000>(c);
  if constexpr (ty == 'X' || ty == 'C') pack_e<Xp, np>(c);
  if constexpr (ty == 'C') pack_e<Xp, np + 1>(c);
  if constexpr (ty == 'E') exp_e<Xp, NBE + pcnt(pa, 'E', M)>(c);
  {
    if constexpr (M == FA(6))      { c.pm0[0] = c.m0k + (unsigned)X * 0x4000u; c.pm0[1] = c.m0v + (unsigned)(X ^ 1) * 0x4000u; asm volatile("" : "+s"(c.pm0[0]), "+s"(c.pm0[1])); }
    else if constexpr (M == FA(8)) asm volatile("s_mov_b32 %1, %0\n\ts_add_u32 %0, %0, %2\n\ts_and_b32 %0, %0, %3" : "+s"(c.psf[0]), "=&s"(c.psf[1]) : "i"(KTILEB), "i"(KMASK) : "scc");
  }
  hook(std::integral_constant<int, 100 + M>{});
  GAP();
}
#define W4_V8(a, k) "+v"(a[0][k]), "+v"(a[1][k]), "+v"(a[2][k]), "+v"(a[3][k]), "+v"(a[4][k]), "+v"(a[5][k]), "+v"(a[6][k]), "+v"(a[7][k])
template <int PFW> DI void phaseA_end(W4C& c) {
  asm volatile("s_waitcnt vmcnt(%16) lgkmcnt(0)" : W4_V8(c.vlo, 0), W4_V8(c.vlo, 1) : "i"(PFW) : "memory");
  asm volatile("" : W4_V8(c.vup, 0), W4_V8(c.vup, 1) : "v"(c.NEGM[0]), "v"(c.NEGM[1]), "v"(c.NEGM[2]), "v"(c.NEGM[3]) : "memory");
  GAP();
  __builtin_amdgcn_s_barrier();
  asm volatile("" ::: "memory");
  GAP();
#pragma unroll
  for (int k2 = 0; k2 < 2; ++k2)
#pragma unroll
    for (int db = 0; db < 8; ++db) c.Vf[db][k2] = __builtin_shufflevector(c.vlo[db][k2], c.vup[db][k2], 0, 1, 2, 3, 4, 5, 6, 7);
}
template <int X, int PFW, typename H, int... Ms> DI void phaseA_(W4C& c, const int t, H& hook, iseq<Ms...>) {
  (gapA<X, Ms>(c, t, hook), ...);
  phaseA_end<PFW>(c);
}
struct NoHook { template <typename T> DI void operator()(T) const {} };
template <int X, int PFW = 0> DI void phaseA(W4C& c, const int t) { NoHook h; phaseA_<X, PFW>(c, t, h, make_iseq<64>{}); }
template <int X, int PFW, typename H> DI void phaseAh(W4C& c, const int t, H& hook) { phaseA_<X, PFW>(c, t, hook, make_iseq<64>{}); }

template <int X, bool ZERO1, bool DK, int PF, int M, typename H> DI void gapB(W4C& c, H& hook) {
  constexpr const char* pb = DK ? PB0 : PB2;
  constexpr char ty = pb[M];
  constexpr int n = pcnt(pb, ty, M) + (!DK && ty == 'D' ? 4 : 0), d8 = DB(8), d9 = DB(9);
  pv_mfma<M, ZERO1>(c);
  if constexpr (DK && M == DB(0) - 1) asm volatile("s_mov_b32 m0, %0" :: "s"(c.pm0[0]) : "m0", "memory");
  if constexpr (M == DB(4) - 1) asm volatile("s_mov_b32 m0, %0" :: "s"(c.pm0[1]) : "m0", "memory");
  if constexpr (M == 57) asm volatile("s_waitcnt lgkmcnt(0)" ::: "memory");
  if constexpr (PF && (M == d8 - 1 || M == d9 - 1))
    asm volatile("s_cmp_lt_u32 %0, %1\n\ts_cselect_b32 m0, %0, %2\n\ts_add_u32 %0, %0, 0x400" : "+s"(c.pfm0) : "s"(c.pfend), "s"(c.pdum) : "m0", "memory", "scc");
  if constexpr (PF && (M == d8 - 2 || M == d9 - 2)) asm volatile("s_add_u32 %0, %0, %1" : "+s"(c.pfo) : "s"(c.pfst) : "scc");
  constexpr int lg = PF ? 56 : 40;
  if constexpr (X == 1 && M == lg - 5) asm volatile("s_add_u32 %0, %0, %1" : "+s"(c.sfo) : "i"(2 * KTILEB) : "scc");
  if constexpr (X == 1 && M == lg - 4) asm volatile("s_and_b32 %0, %0, %1" : "+s"(c.sfo) : "i"(KMASK) : "scc");
  if constexpr (X == 1 && M == lg - 2) asm volatile("s_mov_b32 m0, %0" :: "s"(c.pdum) : "m0", "memory");
  if constexpr (X == 1 && M == lg) asm volatile("buffer_load_dwordx4 %0, %1, %2 offen lds" :: "v"(c.vksrc[0]), "s"(c.srdF), "s"(c.sfo) : "memory");
  hook(std::integral_constant<int, M>{});
  if constexpr (ty == 'K') { k_read<2 * n, (X ^ 1) * 0x4000>(c); k_read<2 * n + 1, (X ^ 1) * 0x4000>(c); }
  if constexpr (ty == 'D') {
    if constexpr (n < 4) { asm volatile("buffer_load_dwordx4 %0, %1, %2 offen offset:%c3 lds" :: "v"((n & 2) ? c.ksx[n & 1] : c.vksrc[n & 1]), "s"(c.srdK),  "s"(c.psf[0]), "i"(1024 * (n & 3)) : "memory"); }
    else if constexpr (n < 8) asm volatile("buffer_load_dwordx4 %0, %1, %2 offen offset:%c3 lds" :: "v"((n & 1) ? c.vvsrc : c.vvsrc0), "s"(c.srdV),  "s"(c.psf[1]), "i"(1024 * (n & 3)) : "memory");
    else if constexpr (PF) asm volatile("buffer_load_dwordx4 %0, %1, %2 offen sc1 lds" :: "v"(c.pfv), "s"(c.srdP), "s"(c.pfo) : "memory");
  }
  if constexpr (ty == 'E') exp_e<X, n>(c);
  hook(std::integral_constant<int, 100 + M>{});
  GAP();
}
template <int X, bool ZERO1, bool DK, int PF, typename H, int... Ms> DI void phaseB_(W4C& c, H& hook, iseq<Ms...>) {
  GAP();
  (gapB<X, ZERO1, DK, PF, Ms>(c, hook), ...);
  hook(std::integral_constant<int, 72>{});
  GAP();
}
template <int X, int PF> DI void phaseB(W4C& c) { NoHook h; phaseB_<X, false, true, PF>(c, h, make_iseq<NGB>{}); }
template <int X, int PF, typename H> DI void phaseBh(W4C& c, H& hook) { phaseB_<X, false, true, PF>(c, hook, make_iseq<NGB>{}); }

constexpr unsigned QP_STG = 0xC000u, QP_W8 = 0x24000u, QP_CS = 0x18000u;
constexpr int PR0 = 1, PR1 = 8;
constexpr int PJ = 224;
constexpr int NJ = 200, JK2 = 79, JR0 = 103;
constexpr int JT[] = {1000,20004,20005,32000,1001,20006,20007,32000,31000,1002,22000,32000,1003,21000,21001,32000,1016,34000,21002,21003,32000,1017,34000,21004,21005,32000,1018,34000,21006,21007,30000,32000,16000,1019,
  34000,32000,16001,30001,1004,34000,20008,20009,32000,16002,1005,34000,20010,20011,32000,16003,1006,34000,20012,20013,32000,16004,1007,34000,20014,20015,32000,16005,1008,34000,32000,16006,1020,34000,32000,16007,1021,
  34000,32000,10000,10001,10002,10003,10004,10005,1022,34000,32000,10006,10007,10008,10009,10010,10011,1023,34000,32000,10012,10013,10014,10015,10016,10017,1024,34000,32000,10018,10019,10020,10021,10022,10023,1032,34000,
  32000,10024,10025,10026,1033,34000,32000,10027,10028,10029,10030,10031,1034,34000,32000,10032,10033,10034,10035,10036,10037,1035,34000,32000,10038,10039,10040,10041,10042,10043,30002,1036,34000,32000,10044,10045,10046,
  10047,10048,10049,1037,34000,32000,10050,10051,10052,10053,10054,10055,1038,34000,32000,10056,10057,10058,10059,10060,1039,34000,32000,10061,10062,10063,1040,34000,32000,10064,10065,10066,10067,10068,10069,1048,34000,
  20016,32000,10070,10071,1049,34000,20017,32000,10072,10073,10074,10075,10076,10077,1050,34000,20018,32000,10078,10079,10080,10081,10082,10083,1051,34000,20019,32000,10084,10085,10086,10087,10088,10089,1052,34000,20020,
  32000,10090,10091,10092,10093,10094,10095,1053,34000,20021,32000,10096,10097,10098,1054,34000,20022,32000,10099,10100,10101,10102,10103,1055,34000,20023,32000,10104,10105,10106,10107,1056,34000,32000,1064,34000,32000,
  1065,34000,32000,10108,10109,10110,10111,10112,10113,1066,34000,32000,10114,10115,10116,10117,10118,10119,1067,34000,32000,10120,10121,10122,10123,10124,10125,30003,1068,34000,32000,10126,10127,10128,10129,10130,10131,
  1069,34000,32000,10132,10133,10134,1070,34000,32000,10135,10136,10137,10138,10139,1071,34000,32000,10140,10141,10142,10143,1072,34000,20024,32000,1080,34000,20025,32000,1081,34000,20026,32000,10144,10145,10146,10147,
  10148,10149,1082,34000,20027,32000,10150,10151,10152,10153,10154,10155,1083,34000,20028,32000,10156,10157,10158,10159,10160,10161,1084,34000,20029,32000,10162,10163,10164,10165,10166,10167,1085,34000,20030,32000,10168,
  10169,10170,1086,34000,20031,32000,10171,10172,10173,10174,10175,1087,34000,32000,10176,10177,10178,10179,1088,34000,32000,1096,34000,32000,1097,34000,32000,10180,10181,10182,10183,10184,10185,31001,1098,34000,32000,
  10186,10187,10188,10189,10190,10191,1099,34000,32000,10192,10193,10194,10195,30004,1100,34000,23000,23001,32000,10196,10197,10198,10199,10200,1101,34000,23002,23003,32000,10201,10202,10203,10204,1102,34000,23004,23005,
  24000,32000,1103,34000,23006,23007,32000,10205,10206,1104,34000,23008,32000,10207,10208,10209,10210,10211,1112,34000,23009,32000,10212,10213,10214,10215,1113,34000,23010,32000,10216,10217,10218,10219,10220,10221,1114,
  34000,23011,32000,10222,10223,10224,10225,10226,10227,1115,34000,23012,32000,10228,10229,10230,10231,1116,34000,23013,32000,10232,10233,10234,10235,10236,10237,1117,34000,23014,32000,10238,10239,10240,10241,1118,34000,
  23015,32000,10242,10243,1119,34000,32000,10244,10245,10246,10247,10248,10249,1120,34000,32000,10250,10251,30005,2128,34000,24001,24002,32000,2132,34000,32000,10252,10253,2136,34000,32000,10254,10255,2140,34000,23100,
  32000,10256,2001,34000,32000,10257,10258,2005,34000,32000,10259,10260,31002,2009,34000,32000,10261,10262,2013,32000,2002,34000,23101,32000,10263,2003,33000,32000,2006,34000,32000,10264,10265,2007,33001,32000,2010,
  34000,32000,11000,11001,2014,34000,32000,11002,11016,11017,11003,11004,11005,11018,23102,11006,11007,11008,11009,11010,11011,11012,2080,34000,32000,11013,11014,2017,34000,32000,11019,2018,34000,32000,11020,11021,2019,
  34000,32000,11022,2096,34000,32000,11023,11024,2033,34000,32000,2034,34000,32000,11025,11026,2035,34000,32000,11027,11028,2112,34000,32000,11029,11030,2049,34000,32000,10266,10267,2050,34000,32000,10268,10269,2051,
  34000,32000,2087,34000,32000,10270,10271,2022,34000,32000,10272,10273,2021,34000,32000,10274,10275,2020,34000,32000,10276,2103,34000,32000,10277,2038,32000,2037,34000,32000,10278,2036,34000,32000,10279,2119,34000,
  32000,10280,10281,2054,34000,32000,10282,10283,2053,34000,32000,10284,10285,2052,32000,10286,10287,24003,24004,2011,32000,13000,2015,34000,23103,24005,32000,13001,13002,11032,11033,11034,13003,11048,11035,11036,11037,
  11049,11050,11038,11039,11040,11041,11042,11043,11044,2088,34000,32000,11045,11046,2025,34000,32000,11051,2026,34000,32000,11052,11053,2027,34000,32000,11054,2104,34000,32000,11055,11056,2041,34000,32000,11057,11058,
  2042,34000,32000,11059,11060,2043,34000,32000,11061,11062,2120,32000,13016,2057,34000,24006,32000,15000,2058,32000,13017,2059,32000,13018,2095,34000,24007,32000,15001,2030,32000,13019,2029,32000,13032,2028,34000,23104,
  32000,15008,2111,34000,23105,32000,15009,2046,34000,23106,23107,32000,2045,34000,24008,24009,32000,2044,34000,23108,23109,32000,2127,34000,23110,23111,32000,2062,34000,24010,24011,32000,2061,32000,13033,2060,34000,
  23112,32000,15016,30105,3064,34000,23113,23114,32000,3001,34000,23115,24012,32000,3002,32000,13034,3003,34000,24013,24014,32000,3071,32000,13035,3006,32000,13048,3005,34000,24015,32000,15017,3004,32000,13049,3072,
  32000,13050,3009,34000,24016,32000,15024,3010,32000,13051,3011,32000,13004,3079,34000,24017,32000,15025,3014,32000,13005,3013,32000,13006,3012,34000,24018,32000,15002,3080,32000,13007,3017,32000,13020,3018,34000,24019,
  32000,15003,3019,32000,13021,3087,32000,13022,3022,34000,24020,32000,15010,3021,32000,13023,3020,32000,13036,3088,34000,24021,32000,15011,3025,32000,13037,3026,32000,13038,3027,34000,24022,32000,15018,3095,32000,13039,
  3030,32000,13052,3029,34000,24023,32000,15019,3028,32000,13053,3096,32000,13054,3033,34000,24024,32000,15026,30102,3034,32000,13055,3035,32000,13008,3103,34000,24025,32000,15027,3038,32000,13009,3037,32000,13010,3036,
  34000,24026,32000,15004,3104,32000,13011,3041,32000,13024,3042,34000,24027,32000,15005,3043,32000,13025,3111,32000,13026,3046,34000,24028,32000,15012,3045,32000,13027,3044,32000,13040,3112,34000,24029,32000,15013,3049,
  32000,13041,3050,32000,13042,3051,34000,24030,32000,15020,3119,32000,13043,3054,34000,24031,32000,15021,3053,32000,13056,3052,32000,13057,3120,32000,13058,3057,32000,13059,3058,32000,13012,3059,32000,13013,3127,34000,
  32000,15028,15029,3062,32000,13014,3061,32000,13015,3060,34000,32000,15006,13028,13029,13030,15007,13031,13044,15014,13045,13046,15015,13047,13060,15022,13061,13062,15023,13063,15030,15031,};
constexpr short JS[] = {0,4,8,12,16,21,26,33,37,44,50,56,62,66,70,79,88,97,106,112,120,129,138,148,157,165,171,180,186,196,206,216,226,233,242,250,253,256,265,274,283,293,299,307,314,318,322,332,342,352,362,369,378,385,
  388,391,400,410,417,428,437,443,450,459,467,477,487,495,505,513,519,528,533,539,544,549,554,559,564,570,572,577,580,585,588,593,611,616,620,625,629,634,637,642,647,652,657,662,665,670,675,680,684,688,690,694,698,703,
  708,713,719,722,746,751,755,760,764,769,774,779,784,787,792,795,798,803,806,809,814,819,824,829,834,839,844,847,852,858,863,866,871,874,877,882,885,888,893,896,899,904,907,910,915,918,921,926,929,932,937,940,943,948,
  951,954,959,962,965,970,973,976,981,985,988,993,996,999,1004,1007,1010,1015,1018,1021,1026,1029,1032,1037,1040,1043,1048,1051,1056,1059,1062,1065,1068,1071,1074,1079,1082,1085,1108,};
enum : int { VK_CW, VK_GEN, VK_S, VK_W8, VK_A, VK_WR, VK_STS, VK_GAT, VK_XNS, VK_KV, VK_KEY, VK_V1, VK_K2, VK_PF };
struct VOp { int kind, st, q, pos; };
struct VTab { VOp v[192]; int n; };
constexpr VTab mk_vtab() {
  VTab t{}; int n = 0;
  auto add = [&](int kind, int st, int q, int pos) { t.v[n].kind = kind; t.v[n].st = st; t.v[n].q = q; t.v[n].pos = pos; ++n; };
  const int PS[12] = {7, 10, 12, 15, 18, 20, 23, 26, 28, 31, 33, 36}, P3[12] = {20, 22, 24, 26, 28, 30, 32, 34, 36, 38, 40, 42}, P4[12] = {12, 14, 16, 19, 21, 23, 26, 28, 30, 32, 34, 36};
  for (int q = 0; q < 12; ++q) add(VK_S, 0, q, -1);
  for (int q = 0; q < 3; ++q) add(VK_A, 0, q, -1);
  add(VK_GEN, 0, 0, -1); add(VK_CW, 0, 0, -1);
  for (int q = 0; q < 12; ++q) add(VK_S, 1, q, q < 8 ? -1 : q - 8);
  for (int q = 0; q < 12; ++q) add(VK_S, 2, q, 4 + q);
  add(VK_STS, 0, 0, 12);
  for (int q = 0; q < 3; ++q) add(VK_WR, 0, q, 16 + q);
  for (int s = 3; s < 9; ++s) for (int q = 0; q < 12; ++q) add(s == 8 && q < 4 ? VK_W8 : VK_S, s, q, 32 * (s - 3) + (s == 3 ? P3[q] : s == 4 ? P4[q] : PS[q]));
  add(VK_GAT, 0, 0, 128 + 9); add(VK_GAT, 0, 1, 128 + 14);
  add(VK_XNS, 0, 0, 160 + 22); add(VK_XNS, 0, 1, 160 + 25); add(VK_XNS, 0, 2, 160 + 30);
  for (int q = 0; q < 12; ++q) add(VK_KV, 0, q, 192 + PS[q]);
  add(VK_KEY, 0, 0, PJ + 57);
  for (int q = 0; q < 4; ++q) add(VK_V1, 0, q, PJ + 60 + 3 * q);
  for (int q = 0; q < 4; ++q) add(VK_K2, 0, q, PJ + JK2 + 6 * q);
  add(VK_PF, 0, 0, PJ + JR0); add(VK_PF, 0, 1, PJ + JR0 + 6);
  for (int i = 1; i < n; ++i) { const VOp x = t.v[i]; int j = i; while (j > 0 && t.v[j - 1].pos > x.pos) { t.v[j] = t.v[j - 1]; --j; } t.v[j] = x; }
  t.n = n;
  return t;
}
constexpr VTab VT = mk_vtab();
constexpr int vq_first(int pos) { int i = 0; while (i < VT.n && VT.v[i].pos < pos) ++i; return i; }
constexpr int vq_wait(int kind, int st, int pos) {
  int last = -1;
  for (int i = 0; i < VT.n; ++i) if (VT.v[i].kind == kind && VT.v[i].st == st) last = i;
  return vq_first(pos) - 1 - last;
}
constexpr int imin(int a, int b) { return a < b ? a : b; }
constexpr int vq_grp(int i) {
  const VOp o = VT.v[i];
  return o.kind == VK_S ? 3 * o.st + (o.q >> 2) : o.kind == VK_W8 ? 40 : o.kind == VK_KV ? 41 + (o.q >> 2) : o.kind == VK_V1 ? 44 : o.kind == VK_K2 ? 45 : o.kind == VK_PF ? 50 + o.q : -1;
}
constexpr bool vq_newm0(int i) { int j = i - 1; while (j >= 0 && vq_grp(j) < 0) --j; return j < 0 || vq_grp(j) != vq_grp(i); }
template <bool NEWM0, int OFF> DI void lds_dma(const unsigned m0, const unsigned vo, const __amdgpu_buffer_rsrc_t srd, const unsigned so) {
  if constexpr (NEWM0) asm volatile("s_mov_b32 m0, %0\n\ts_nop 0\n\tbuffer_load_dwordx4 %1, %2, %3 offen offset:%c4 lds" :: "s"(m0), "v"(vo), "s"(srd), "s"(so), "i"(OFF) : "m0", "memory");
  else                 asm volatile("buffer_load_dwordx4 %0, %1, %2 offen offset:%c3 lds" :: "v"(vo), "s"(srd), "s"(so), "i"(OFF) : "memory");
}
template <int I0, typename F, int... Is> DI void vq_run(F& vm, iseq<Is...>) { (vm(std::integral_constant<int, I0 + Is>{}), ...); }
template <int POS, typename F> DI void vq_issue(F& vm) { vq_run<vq_first(POS)>(vm, make_iseq<vq_first(POS + 1) - vq_first(POS)>{}); }

constexpr int AW_(int T, int db, int ks) { return ((T & 1) ? 192 : 0) + 16 * db + 4 * ks; }
constexpr int AX_(int T, int qb, int ks) { return ((T & 1) ? 96 : 64) + 16 * qb + 4 * ks; }
template <int T, int DB, int QB, int KS> DI void qp_mfma(f32x16& d) {
  constexpr int aw = AW_(T, DB, KS), ax = AX_(T, QB, KS);
  if constexpr (T == 0 && KS == 0) asm volatile("v_mfma_f32_32x32x16_bf16 %0, a[%c1:%c2], a[%c3:%c4], 0" : "=&v"(d) : "i"(aw), "i"(aw + 3), "i"(ax), "i"(ax + 3));
  else                             asm volatile("v_mfma_f32_32x32x16_bf16 %0, a[%c1:%c2], a[%c3:%c4], %0" : "+v"(d) : "i"(aw), "i"(aw + 3), "i"(ax), "i"(ax + 3));
}
struct QP {
  f32x16 A[4][2];
  bf16x4 cl[2][4], cu[2][4];
  f32x4 cs; u32x4 sel;
  unsigned vwb, vwc, vw[4], vx[4], vxb[4], m0w, m0x, sofw, sofx, kk, sw, sx;
  __amdgpu_buffer_rsrc_t srdW, srdX;
};
template <int S> DI void qp_next(QP& p) {
  constexpr int d = S < PR0 || S > PR1 ? 0 : ((S - PR0) & 1) ? -1 : 1;
  const unsigned k = (unsigned)S + (d == 0 ? 0u : d > 0 ? p.kk : 0u - p.kk);
  p.sw = p.sofw + (k << 19); p.sx = p.sofx + (k << 7);
  asm volatile("" : "+s"(p.sw), "+s"(p.sx));
}
constexpr unsigned qp_wbase(int T) { return T == 8 ? QP_W8 : QP_STG * (T % 3); }
template <int T, int DB, int KS, int U> DI void qp_tr(QP& p) {
  asm volatile("ds_read_b64_tr_b16 a[%c1:%c2], %0 offset:%c3" :: "v"(p.vwb + qp_wbase(T)), "i"(AW_(T, DB, KS) + 2 * U), "i"(AW_(T, DB, KS) + 2 * U + 1), "i"(4096 * KS + 2048 * U + 512 * DB) : "memory");
}
template <int T, int QB, int KS> DI void qp_xr(QP& p) {
  asm volatile("ds_read_b128 a[%c1:%c2], %0 offset:%c3" :: "v"(p.vxb[KS] + QP_STG * (T % 3)), "i"(AX_(T, QB, KS)), "i"(AX_(T, QB, KS) + 3), "i"(4096 * QB) : "memory");
}
template <int T, int I> DI void qp_csr(QP& p) {
  constexpr int ks = I >> 1;
  if constexpr ((I & 1) == 0) asm volatile("ds_read_b64_tr_b16 %0, %1 offset:%2" : "=v"(p.cl[T == 8][ks]) : "v"(p.vwc + qp_wbase(T)), "i"(4096 * ks) : "memory");
  else                        asm volatile("ds_read_b64_tr_b16 %0, %1 offset:%2" : "=v"(p.cu[T == 8][ks]) : "v"(p.vwc + qp_wbase(T)), "i"(4096 * ks + 2048) : "memory");
}
template <int KS, bool ZERO, int L = 0> DI void qp_csd(QP& p) {
  const bf16x8 w = __builtin_shufflevector(p.cl[L][KS], p.cu[L][KS], 0, 1, 2, 3, 4, 5, 6, 7);
  if constexpr (ZERO) asm volatile("v_mfma_f32_16x16x32_bf16 %0, %1, %2, 0" : "=&v"(p.cs) : "v"(w), "v"(p.sel));
  else                asm volatile("v_mfma_f32_16x16x32_bf16 %0, %1, %2, %0" : "+v"(p.cs) : "v"(w), "v"(p.sel));
}
template <int T, int R> DI void qp_rd(QP& p) {
  if constexpr (R < 8) qp_csr<T, R>(p);
  else {
    constexpr int ks = (R - 8) / 10, u = (R - 8) % 10;
    if constexpr (u == 0) qp_xr<T, 0, ks>(p);
    else if constexpr (u == 3) qp_xr<T, 1, ks>(p);
    else { constexpr int v = u < 3 ? u - 1 : u - 2; qp_tr<T, (v >> 1), ks, (v & 1)>(p); }
  }
}
template <int T, int R0, int... Rs> DI void qp_rds(QP& p, iseq<Rs...>) { (qp_rd<T, R0 + Rs>(p), ...); }
#define QP_CSV(L) "+v"(p.cl[L][0]), "+v"(p.cl[L][1]), "+v"(p.cl[L][2]), "+v"(p.cl[L][3]), "+v"(p.cu[L][0]), "+v"(p.cu[L][1]), "+v"(p.cu[L][2]), "+v"(p.cu[L][3])
constexpr int LN_P0 = 32 + 18, LN_P1 = 160 + 21;
constexpr int ln_slots(int pos) { int n = 0; for (int q = LN_P0; q < pos; ++q) if (vq_first(q + 1) == vq_first(q) && (q & 31) != 5) ++n; return n; }
constexpr int ln_lo(int pos) {
  if (pos <= 1) return 0;
  if (pos <= 9) return 3 * (pos - 1);
  if (pos <= 11) return 24 + 4 * (pos - 9);
  if (pos < LN_P0) return LN_N0;
  if (pos <= LN_P1) return LN_N0 + (ln_slots(pos) * 97) / ln_slots(LN_P1);
  const int s = LN_XN + 1 + (pos - LN_P1) / 3;
  return s < LN_KEY ? s : LN_KEY;
}
template <int I0, int... Is> DI void ln_steps(LNJ& j, const unsigned row, iseq<Is...>) { (ln_step<I0 + Is>(j, row), ...); }
template <int T, int M, typename FV, typename FH> DI void qp_gap(QP& p, FV& vm, FH& hook, LNJ& lj, const unsigned lrow) {
  constexpr int ks = M >> 3, db = (M >> 1) & 3, qb = M & 1, pos = 32 * T + M, BG = T == 0 ? 12 : 6;
  if constexpr (T == 0 && M == 0) asm volatile("s_waitcnt lgkmcnt(10)" ::: "memory", W4_ACLOB, W4_ACLOB_HI);
  if constexpr (T == 0 && M == 8) asm volatile("s_waitcnt lgkmcnt(0)" : QP_CSV(0) :: "memory");
  if constexpr (T > 0 && M == 0) asm volatile("s_waitcnt lgkmcnt(0)" ::: "memory");
  if constexpr (M == BG) {
    constexpr int w0 = vq_wait(VK_S, T + 1, pos);
    constexpr int w = T == 1 ? imin(w0, vq_wait(VK_WR, 0, pos)) : T == 6 ? imin(w0, vq_wait(VK_W8, 8, pos)) : w0;
    asm volatile("s_waitcnt vmcnt(%0)\n\ts_barrier" :: "i"(w) : "memory");
  }
  qp_mfma<T, db, qb, ks>(p.A[db][qb]);
  if constexpr (T == 0) {
    if constexpr (M < 5) qp_rds<0, 28 + 4 * M>(p, make_iseq<4>{});
    if constexpr (M == 5 || M == 6) qp_rds<0, 4 * (M - 5)>(p, make_iseq<4>{});
    if constexpr (M == 18) asm volatile("s_waitcnt lgkmcnt(10)" : QP_CSV(0) :: "memory");
    if constexpr (M >= 12 && M < 28) qp_rds<1, 3 * (M - 12)>(p, make_iseq<3>{});
  } else {
    if constexpr (M == 13) asm volatile("s_waitcnt lgkmcnt(%8)" : QP_CSV(0) : "i"(T == 6 ? 10 : 6) : "memory");
    if constexpr (T == 6 && M == 17) asm volatile("s_waitcnt lgkmcnt(6)" : QP_CSV(1) :: "memory");
    if constexpr (M >= 6 && M < 30) qp_rds<T + 1, 2 * (M - 6)>(p, make_iseq<2>{});
    if constexpr (T == 6 && M >= 6 && M < 14) qp_csr<8, M - 6>(p);
    if constexpr (T == 6 && M >= 30) { qp_tr<8, 0, M - 30, 0>(p); qp_tr<8, 0, M - 30, 1>(p); }
  }
  vq_issue<pos>(vm);
  if constexpr (pos == 3 || pos == 19 || pos == 32 + 11 || (M == 5 && T >= 2 && T <= 5)) qp_next<(pos == 3 ? 2 : pos == 19 ? 3 : T + 3)>(p);
  GAP();
  hook(std::integral_constant<int, pos>{});
  ln_steps<ln_lo(pos)>(lj, lrow, make_iseq<ln_lo(pos + 1) - ln_lo(pos)>{});
  if constexpr (T == 0 && M >= 8 && M < 12) qp_csd<M - 8, M == 8>(p);
  if constexpr (T == 0 && M >= 18 && M < 28 && M % 3 == 0) qp_csd<(M - 18) / 3, false>(p);
  if constexpr (T > 0 && M >= 13 && M < 23 && M % 3 == 1) qp_csd<(M - 13) / 3, false>(p);
  if constexpr (T == 6 && (M == 17 || M == 21 || M == 24 || M == 27)) qp_csd<(M == 17 ? 0 : (M - 18) / 3), false, 1>(p);
  GAP();
}
template <int T, typename FV, typename FH, int... Ms> DI void qp_gaps(QP& p, FV& vm, FH& hook, LNJ& lj, const unsigned lrow, iseq<Ms...>) { (qp_gap<T, Ms>(p, vm, hook, lj, lrow), ...); }
template <int T, typename FV, typename FH> DI void qp_iter(QP& p, FV& vm, FH& hook, LNJ& lj, const unsigned lrow) { qp_gaps<T>(p, vm, hook, lj, lrow, make_iseq<32>{}); }
template <typename FV, typename FH, int... Ts> DI void qp_iters(QP& p, FV& vm, FH& hook, LNJ& lj, const unsigned lrow, iseq<Ts...>) { (qp_iter<Ts>(p, vm, hook, lj, lrow), ...); }

struct QFT { float a[2], cv[4], r1, m[4], m2[4]; unsigned w[8]; u32x4 ca[4], nb[2]; };
template <int N> DI void q_fin(QP& p, QFT& t) {
  constexpr int tau = N / 36, n = N % 36, h = n / 12, u = n % 12, db = tau >> 1, qb = tau & 1;
  f32x16& d = p.A[db][qb];
  if constexpr (n >= 28) acc_write<AQ_(2 * qb + ((n - 28) >> 2), db) + (n & 3)>(t.w[n - 28]);
  else if constexpr (n >= 24) { GAP(); auto pr = __builtin_amdgcn_permlane16_swap(t.w[u], t.w[4 + u], false, false); t.w[u] = pr[0]; t.w[4 + u] = pr[1]; asm volatile("" :: "v"(t.w[u]), "v"(t.w[4 + u])); GAP(); }
  else if constexpr (u < 8) { float v = d[8 * h + u]; asm volatile("v_mul_f32_e32 %0, %0, %1" : "+v"(v) : "v"(t.a[qb])); d[8 * h + u] = v; }
  else { GAP(); t.w[4 * h + u - 8] = cvtpk(d[8 * h + 2 * (u - 8)], d[8 * h + 2 * (u - 8) + 1]); asm volatile("" :: "v"(t.w[4 * h + u - 8])); GAP(); }
}
template <int Q, int I> DI void ref_step(W4C& c, QFT& t) {
  f32x4& s = c.S[0][0][Q];
  if constexpr (I == 0) asm volatile("v_max3_f32 %0, %1, %2, %3" : "=v"(t.m[Q]) : "v"(s[0]), "v"(s[1]), "v"(s[2]));
  else if constexpr (I == 1) asm volatile("v_max_f32_e32 %0, %0, %1" : "+v"(t.m[Q]) : "v"(s[3]));
  else if constexpr (I == 2 || I == 5) asm volatile("v_mov_b32_e32 %0, %1" : "=v"(t.m2[Q]) : "v"(t.m[Q]));
  else if constexpr (I == 3) asm volatile("v_permlane16_swap_b32_e32 %0, %1" : "+v"(t.m[Q]), "+v"(t.m2[Q]));
  else if constexpr (I == 4) asm volatile("v_max_f32_e32 %0, %0, %1" : "+v"(t.m[Q]) : "v"(t.m2[Q]));
  else if constexpr (I == 6) asm volatile("v_permlane32_swap_b32_e32 %0, %1" : "+v"(t.m[Q]), "+v"(t.m2[Q]));
  else if constexpr (I < 11) { float n; asm volatile("v_min_f32_e64 %0, -%1, -%2" : "=v"(n) : "v"(t.m[Q]), "v"(t.m2[Q])); c.NEGM[Q][I - 7] = n; }
  else { float v = s[I - 11]; add_v(v, c.NEGM[Q][0]); s[I - 11] = v; }
}
template <int J, int CODE, typename FV> DI void qj_op(QP& p, W4C& c, QFT& t, FV& vm, const unsigned csa) {
  constexpr int kind = CODE / 1000, a = CODE % 1000;
  if constexpr (kind == 1) {
    constexpr int tau = a >> 4, u = a & 15, db = tau >> 1, qb = tau & 1;
    if constexpr (u < 8) qp_mfma<7 + (u >> 2), db, qb, (u & 3)>(p.A[db][qb]);
    else asm volatile("v_mfma_f32_32x32x16_bf16 %0, %1, %2, %0" : "+v"(p.A[db][qb]) : "v"(t.ca[db]), "v"(t.nb[qb]));
  } else if constexpr (kind == 2 || kind == 3) { constexpr int kb = (a >> 4) & 3, qb = (a >> 2) & 3; qk16<kb, qb, (a & 3), (a >> 6)>(c.S[kind - 2][kb][qb], c.NEGM[qb]); }
  else if constexpr (kind == 10) q_fin<a>(p, t);
  else if constexpr (kind == 11) ref_step<(a >> 4), (a & 15)>(c, t);
  else if constexpr (kind == 13) exp_e<0, a>(c);
  else if constexpr (kind == 15) pack_e<0, a>(c);
  else if constexpr (kind == 16) {
    constexpr int d = a >> 1;
    if constexpr ((a & 1) == 0) { t.r1 = t.cv[d] - rbf(t.cv[d]); t.ca[d][0] = cvtpk(t.cv[d], t.r1); asm volatile("" : "+v"(t.r1), "+v"(t.ca[d])); }
    else { t.ca[d][1] = cvtpk(t.r1 - bfhi(t.ca[d][0]), 0.f); asm volatile("" : "+v"(t.ca[d])); }
  } else if constexpr (kind == 20) qp_tr<8, (a >> 3), ((a >> 1) & 3), (a & 1)>(p);
  else if constexpr (kind == 21) qp_xr<8, (a >> 2), (a & 3)>(p);
  else if constexpr (kind == 22)
    asm volatile("ds_read_b32 %0, %4\n\tds_read_b32 %1, %4 offset:128\n\tds_read_b32 %2, %4 offset:256\n\tds_read_b32 %3, %4 offset:384"
                 : "=&v"(t.cv[0]), "=&v"(t.cv[1]), "=&v"(t.cv[2]), "=&v"(t.cv[3]) : "v"(csa) : "memory");
  else if constexpr (kind == 23) k_read<a % 100, (a >= 100 ? 0x4000 : 0)>(c);
  else if constexpr (kind == 24) tr_read_<a, 0>(c);
  else if constexpr (kind == 30) {
    if constexpr (a == 0) asm volatile("s_waitcnt lgkmcnt(8)" : "+v"(t.cv[0]), "+v"(t.cv[1]), "+v"(t.cv[2]), "+v"(t.cv[3]) :: "memory");
    else if constexpr (a == 200) asm volatile("s_nop 0");
    else asm volatile("s_waitcnt lgkmcnt(%0)" :: "i"(a >= 100 ? a - 100 : 0) : "memory");
  } else if constexpr (kind == 31) {
    if constexpr (a == 0) asm volatile("s_waitcnt vmcnt(%0) lgkmcnt(6)\n\ts_barrier" :: "i"(vq_wait(VK_S, 8, PJ + J)) : "memory");
    else if constexpr (a == 1) asm volatile("s_waitcnt vmcnt(%0)\n\ts_barrier" :: "i"(vq_wait(VK_KV, 0, PJ + J)) : "memory");
    else asm volatile("s_barrier" ::: "memory");
  } else if constexpr (kind == 32) { vq_issue<PJ + J>(vm); GAP(); }
  else if constexpr (kind == 34) real_op();
  else if constexpr (kind == 33) {
    if constexpr (a == 0) { c.pm0[0] = c.m0k + 0x4000u; c.psf[0] = (KTILEB * 3u + c.sofk) & KMASK; asm volatile("" : "+s"(c.pm0[0]), "+s"(c.psf[0])); }
    else { c.pm0[1] = c.m0v; c.psf[1] = (KTILEB * 2u + c.sofk) & KMASK; asm volatile("" : "+s"(c.pm0[1]), "+s"(c.psf[1])); }
  }
}
template <int J, typename FV, int... Is> DI void qj_gap(QP& p, W4C& c, QFT& t, FV& vm, const unsigned csa, iseq<Is...>) { (qj_op<J, JT[JS[J] + Is]>(p, c, t, vm, csa), ...); GAP(); }
template <typename FV, int... Js> DI void qj_gaps(QP& p, W4C& c, QFT& t, FV& vm, const unsigned csa, iseq<Js...>) { (qj_gap<Js>(p, c, t, vm, csa, make_iseq<JS[Js + 1] - JS[Js]>{}), ...); }

constexpr unsigned XRS_ = DM * 2u;
constexpr int RSTR = 84;
constexpr int op_vr(int f) { return f == 8 ? 0 : f >= 13 && f < 32 ? f - 12 : -1; }
constexpr int op_ar(int f) { return f < 64 ? (((f >> 2) & 1) ? 128 : 192) + 16 * ((f - 32) >> 3) + 4 * (f & 3) : 96 + 4 * (f - 64); }
constexpr int op_pair(int st) { constexpr int p[16] = {0, 2, 1, 3, 4, 5, 6, 7, 2, 3, 1, 4, 0, 5, 6, 7}; return p[st >> 1]; }
constexpr int OP_LA = 4, OP_SF = 24, OP_SP = 27, OP_SD = 30, OP_FEW = 3, OP_NR = 27;
constexpr short OPW[] = {13,15,17,18,17,21,20,20,17,15,13,12,12,12,12,12,12,12,12,14,16,18,16,14,12,13,13,13,13,12,8,10,};
constexpr short TT[] = {0,0,9059,0,0,8000,0,0,0,0,8001,0,0,0,0,0,8002,0,9060,0,0,0,8003,0,0,0,0,0,8000,0,0,9061,0,0,8001,0,0,0,0,0,8002,0,0,0,0,8003,0,9062,0,0,0,0,8000,0,0,0,0,8001,0,0,0,9063,0,0,8002,0,0,0,0,8003,0,0,
  0,13016,13017,0,9064,0,8000,0,0,13018,13019,0,0,8001,0,0,0,0,0,8002,0,9065,0,0,0,8003,0,0,0,0,0,8000,0,10000,0,0,9066,0,8001,0,0,0,10001,0,0,8002,0,0,10002,0,0,8003,0,9067,0,0,0,0,8000,0,0,0,0,8001,0,0,0,9068,0,0,8002,
  0,0,0,0,8003,0,0,0,0,0,8000,0,9069,0,0,0,8001,0,0,0,0,0,8002,0,0,9070,0,0,8003,0,0,0,0,0,8000,0,0,0,0,8001,0,9071,0,0,0,0,8002,0,0,0,0,8003,0,0,0,0,0,8000,0,0,0,0,8001,0,0,0,0,0,8002,0,0,0,0,8003,0,0,0,11000,0,11001,0,
  8000,0,11002,0,11003,0,11004,0,8001,0,11005,0,0,11006,0,11007,0,8002,0,11008,0,11009,0,11010,0,8003,0,11011,0,0,11012,0,11013,0,8000,0,11014,0,11015,0,11016,0,8001,0,11017,0,0,11018,0,11019,0,8002,0,11020,0,11021,0,
  11022,0,8003,0,11023,0,0,11024,0,11025,0,8000,0,11026,0,11027,0,11028,0,8001,0,11029,0,0,11030,0,11031,0,8002,0,11032,0,11033,0,11034,0,8003,0,11035,0,0,11036,0,11037,0,8000,0,11038,0,11039,0,11040,0,8001,0,11041,0,0,
  11042,0,11043,0,8002,0,11044,0,11045,0,11046,0,8003,0,11047,0,0,11048,0,11049,0,8000,0,11050,0,11051,0,11052,0,8001,0,11053,0,0,11054,0,11055,0,8002,0,11056,0,11057,0,11058,0,8003,0,11059,0,0,11060,0,11061,0,8000,0,
  15000,0,1000,15001,0,15002,0,8001,0,1005,15003,0,1001,15004,0,15005,0,1006,0,8002,0,15010,0,1002,15011,0,15012,0,8003,0,1007,15013,0,1003,15014,0,15015,0,1008,0,8000,0,15020,0,1010,15021,0,15022,0,8001,0,1015,15023,0,
  1011,15024,0,15025,0,1016,0,8002,0,15030,0,1012,15031,0,15032,0,8003,0,1017,15033,0,1013,15034,2009,0,15035,0,1018,0,8000,3000,0,4000,0,4001,0,4002,0,8001,0,4003,0,4004,0,4005,0,4006,0,8002,0,4007,0,4008,0,4009,0,8003,
  0,4010,0,4011,5004,0,6000,6001,0,6002,6003,0,8000,0,6004,6005,0,6006,6007,0,4012,6008,0,8001,0,4013,6009,0,6010,0,6011,6012,0,6013,6014,0,8002,0,6015,6016,0,4014,6017,0,4015,6018,0,8003,0,6019,6020,0,6021,0,6022,6023,
  0,7000,4016,0,8000,5101,0,7001,4017,6100,0,4018,6101,0,4019,6102,0,8001,0,4020,6103,0,4021,0,4022,6104,0,4023,6105,0,8002,0,6106,6107,0,6108,6109,0,6110,6111,0,8003,0,6112,6113,0,6114,0,13016,13017,0,6115,6116,0,8000,
  0,6117,6118,0,13018,13019,0,6119,6120,0,8001,0,6121,6122,0,6123,5204,0,7002,6200,6201,0,7003,6202,6203,0,8002,0,6204,6205,0,6206,6207,0,6208,6209,0,8003,0,6210,6211,14000,0,6212,0,6213,6214,0,6215,6216,0,8000,0,6217,
  6218,0,6219,6220,0,6221,6222,0,8001,0,6223,0,7004,0,7005,0,11062,0,8002,0,11063,0,11064,0,11065,0,8003,0,11066,0,0,11067,0,11068,0,8000,0,11069,0,11070,0,11071,0,8001,0,11072,0,0,11073,0,11074,0,8002,0,11075,0,11076,0,
  11077,0,8003,0,11078,0,0,13012,13013,0,11079,0,8000,0,11080,0,13014,13015,0,11081,0,8001,0,11082,0,0,11083,0,11084,0,8002,0,11085,0,11086,0,11087,0,8003,0,11088,0,11089,0,13000,13001,0,11090,0,8000,0,11091,0,13002,
  13003,0,11092,0,8001,0,11093,0,0,11094,0,11095,0,8002,0,11096,0,11097,0,11098,0,8003,0,11099,0,0,11100,0,11101,0,8000,0,11102,0,11103,0,11104,0,8001,0,11105,0,0,11106,0,11107,0,8002,0,0,0,0,8003,0,0,0,0,0,8000,0,0,0,0,
  8001,0,0,0,0,0,8002,0,0,0,0,8003,0,0,0,0,0,8000,0,0,0,0,8001,0,0,0,0,0,8002,0,0,0,0,8003,0,0,0,0,0,8000,0,0,0,0,8001,0,0,0,0,0,8002,0,0,0,0,8003,0,0,0,0,10003,0,0,0,0,10004,0,0,0,0,10005,0,0,0,0,0,0,0,0,0,0,0,0,0,0,0,
  0,0,0,0,0,0,0,0,0,0,0,0,0,0,15100,0,15101,0,12000,15102,0,15103,0,12005,15104,0,15105,0,12001,0,0,12006,0,15110,0,15111,0,12002,15112,0,15113,0,12007,15114,0,15115,0,12003,0,0,12008,0,15120,0,15121,0,12010,15122,0,
  15123,0,12015,15124,0,15125,0,12011,0,0,12016,0,15130,0,15131,0,12012,15132,0,15133,0,12017,15134,0,15135,12013,12018,};
constexpr short TS[] = {0,1,3,4,6,7,8,9,11,12,13,14,15,17,19,20,21,23,24,25,26,27,29,30,32,33,35,36,37,38,39,41,42,43,44,46,48,49,50,51,53,54,55,56,58,59,60,62,63,65,66,67,68,70,71,72,75,77,79,80,83,84,86,87,88,89,90,92,
  94,95,96,98,99,100,101,102,104,106,107,109,111,112,113,115,116,118,119,121,122,124,126,127,128,129,131,132,133,134,136,137,138,140,141,143,144,145,146,148,149,150,151,152,154,156,157,158,160,161,162,163,164,166,167,
  169,170,172,173,174,175,176,178,179,180,181,183,185,186,187,188,190,191,192,193,195,196,197,198,199,201,202,203,204,206,207,208,209,210,212,213,214,215,217,218,219,221,223,225,227,229,231,233,235,236,238,240,242,244,
  246,248,250,252,253,255,257,259,261,263,265,267,269,270,272,274,276,278,280,282,284,286,287,289,291,293,295,297,299,301,303,304,306,308,310,312,314,316,318,320,321,323,325,327,329,331,333,335,337,338,340,342,344,346,
  348,350,352,354,355,357,359,361,363,365,367,369,371,372,374,376,378,380,382,384,386,388,389,391,393,395,397,400,402,404,407,410,412,414,416,418,421,423,425,428,431,433,435,437,439,442,444,446,449,452,454,456,458,460,
  463,465,467,470,473,476,478,480,483,485,487,489,491,493,495,497,499,501,503,505,507,509,511,515,518,520,523,526,529,531,534,536,539,542,544,547,550,553,555,558,560,563,566,568,573,576,579,581,584,586,589,592,594,597,
  600,603,605,608,610,613,616,618,621,624,627,629,632,634,639,643,645,648,651,654,656,659,662,665,668,670,673,676,679,681,683,685,687,689,691,693,695,697,699,701,702,704,706,708,710,712,714,716,718,719,721,723,725,727,
  729,731,733,735,736,739,741,743,745,748,750,752,754,755,757,759,761,763,765,767,769,771,773,776,778,780,782,785,787,789,791,792,794,796,798,800,802,804,806,808,809,811,813,815,817,819,821,823,825,826,828,830,832,833,
  834,835,837,838,839,840,841,843,844,845,846,848,849,850,851,852,854,855,856,857,859,860,861,862,863,865,866,867,868,870,871,872,873,874,876,877,878,879,881,882,883,884,885,887,888,889,890,892,893,894,895,896,898,899,
  900,901,903,904,905,906,908,909,910,911,913,914,915,916,918,919,920,921,922,923,924,925,926,927,928,929,930,931,932,933,934,935,936,937,938,939,940,941,942,943,944,945,946,948,950,953,955,958,960,962,963,965,967,969,
  972,974,977,979,981,982,984,986,988,991,993,996,998,1000,1001,1003,1005,1007,1010,1012,1015,1017,1019,};
struct OPS {
  u32x4 fa[OP_LA + 1][4], fb[20], fs[2][4], gv[3]; float g0, g1, t0, t1, h[4]; f32x4 m[3][8];
  u32x4 xv[3]; unsigned ra, rb, ma[3], ms1, sw, an[3], ax[3], sn; __amdgpu_buffer_rsrc_t srdE, srdN, srdX;
  unsigned va, idx, hs, sa[4], lb5;
  __amdgpu_buffer_rsrc_t srdA, srdB;
};
template <int F> DI void op_reqB(OPS& o) {
  asm volatile("buffer_load_dwordx4 a[%c3:%c4], %0, %1, %2 offen offset:%c5 sc1" :: "v"(o.va), "s"(o.srdB), "s"(1024u * (F & ~3)), "i"(op_ar(F)), "i"(op_ar(F) + 3), "i"(1024 * (F & 3)) : "memory");
}
template <int ST, int I> DI void op_reqA1(OPS& o) {
  constexpr int k = 2 * op_pair(ST) + (I & 1), rb = 2 * (ST & 1) + (I >> 1);
  const unsigned so = o.sa[(k & 3) ^ (ST >> 4)];
  asm volatile("buffer_load_dwordx4 %0, %1, %2, %3 offen sc1" : "=v"(o.fa[ST % (OP_LA + 1)][I]) : "v"(o.va), "s"(o.srdA), "s"(so + (131072u * rb + 1024u * (k & ~3))) : "memory");
}
template <int... Qs> DI void op_reqA0(OPS& o, iseq<Qs...>) { (op_reqA1<Qs / 4, Qs % 4>(o), ...); }
template <int K, int SL, int J> DI void op_stage(OPS& o) { asm volatile("ds_read_b128 %0, %1 offset:%2" : "=v"(o.fs[SL][J]) : "v"(o.lb5), "i"(1024 * (9 * (K >> 1) + 4 * (K & 1) + J)) : "memory"); }
template <int F> DI void op_ldsB(OPS& o, const unsigned lb2) { asm volatile("ds_read_b128 %0, %1 offset:%2" : "=v"(o.fb[op_vr(F)]) : "v"(F < 23 ? o.lb5 : lb2), "i"(1024 * (F < 23 ? F : F - 23)) : "memory"); }
template <int F0, int... Is> DI void op_ldsBs(OPS& o, const unsigned lb2, iseq<Is...>) { (op_ldsB<F0 + Is>(o, lb2), ...); }
template <int ST, int Q> DI void op_mfma(OPS& o) {
  constexpr int A = ST % (OP_LA + 1), p = op_pair(ST), h = 2 * (ST & 1);
  if constexpr (Q == 0 || Q == 9) {
    constexpr int i = Q == 9, f = 9 * p + 8, ac = 64 + 16 * (ST >> 4) + 4 * (h + i);
    const u32x8 B = __builtin_shufflevector(o.fa[A][2 * i], o.fa[A][2 * i + 1], 0, 1, 2, 3, 4, 5, 6, 7);
    if constexpr (f < 32) asm volatile("v_smfmac_f32_16x16x64_bf16 a[%c3:%c4], %0, %1, %2" :: "v"(o.fb[op_vr(f)]), "v"(B), "v"(o.idx), "i"(ac), "i"(ac + 3));
    else                  asm volatile("v_smfmac_f32_16x16x64_bf16 a[%c2:%c3], a[%c4:%c5], %0, %1" :: "v"(B), "v"(o.idx), "i"(ac), "i"(ac + 3), "i"(op_ar(f)), "i"(op_ar(f) + 3));
  } else {
    constexpr int d = Q - 1 - (Q > 9), J = d >> 2, i = (d >> 1) & 1, kk = d & 1, f = 9 * p + 4 * kk + J, ac = 16 * (h + i) + 4 * J;
    const u32x4& B = o.fa[A][2 * i + kk];
    if constexpr (f >= 32) asm volatile("v_mfma_f32_16x16x32_bf16 a[%c1:%c2], a[%c3:%c4], %0, a[%c1:%c2]" :: "v"(B), "i"(ac), "i"(ac + 3), "i"(op_ar(f)), "i"(op_ar(f) + 3));
    else if constexpr ((ST & 15) < 2 && kk == 0) asm volatile("v_mfma_f32_16x16x32_bf16 a[%c2:%c3], %0, %1, 0" :: "v"(f < 13 ? o.fs[0][J] : o.fb[op_vr(f) & 31]), "v"(B), "i"(ac), "i"(ac + 3));
    else asm volatile("v_mfma_f32_16x16x32_bf16 a[%c2:%c3], %0, %1, a[%c2:%c3]" :: "v"(f < 13 ? o.fs[f >= 4 && f < 8][J] : o.fb[op_vr(f) & 31]), "v"(B), "i"(ac), "i"(ac + 3));
  }
}
template <int U> DI void op_gate(OPS& o) {
  constexpr int wd = U / 9, pt = U % 9;
  if constexpr (pt == 0)      asm volatile("v_lshlrev_b32_e32 %0, 16, %2\n\tv_and_b32_e32 %1, 0xffff0000, %2" : "=&v"(o.g0), "=&v"(o.g1) : "v"(o.gv[wd >> 2][wd & 3]));
  else if constexpr (pt == 1) asm volatile("v_mul_f32_e32 %0, 0xbfb8aa3b, %2\n\tv_mul_f32_e32 %1, 0xbfb8aa3b, %3" : "=&v"(o.t0), "=&v"(o.t1) : "v"(o.g0), "v"(o.g1));
  else if constexpr (pt == 2) asm volatile("v_exp_f32_e32 %0, %0" : "+v"(o.t0));
  else if constexpr (pt == 3) asm volatile("v_exp_f32_e32 %0, %0" : "+v"(o.t1));
  else if constexpr (pt == 4) asm volatile("v_add_f32_e32 %0, 1.0, %0\n\tv_add_f32_e32 %1, 1.0, %1" : "+v"(o.t0), "+v"(o.t1));
  else if constexpr (pt == 5) asm volatile("v_rcp_f32_e32 %0, %0" : "+v"(o.t0));
  else if constexpr (pt == 6) asm volatile("v_rcp_f32_e32 %0, %0" : "+v"(o.t1));
  else if constexpr (pt == 7) asm volatile("v_mul_f32_e32 %0, %0, %2\n\tv_mul_f32_e32 %1, %1, %3" : "+v"(o.t0), "+v"(o.t1) : "v"(o.g0), "v"(o.g1));
  else { unsigned r; asm volatile("v_cvt_pk_bf16_f32 %0, %1, %2" : "=v"(r) : "v"(o.t0), "v"(o.t1)); o.gv[wd >> 2][wd & 3] = r; }
}
template <int I, int J, int OFF> DI void op_wr(const unsigned ad) { asm volatile("ds_write_b128 %0, a[%c1:%c2] offset:%c3" :: "v"(ad), "i"(16 * I + 4 * J), "i"(16 * I + 4 * J + 3), "i"(OFF + 64 * J) : "memory"); }
template <int PASS, int I, int U> DI void op_wrs(OPS& o) {
  constexpr int r = 64 + 16 * PASS + 4 * I, off = (PASS ? 64 * I * RSTR : 4608 * (I & 1)) + 256;
  if constexpr (U < 2) asm volatile("v_accvgpr_read_b32 %0, a[%c2]\n\tv_accvgpr_read_b32 %1, a[%c3]" : "=v"(o.h[2 * U]), "=v"(o.h[2 * U + 1]) : "i"(r + 2 * U), "i"(r + 2 * U + 1));
  else if constexpr (U < 4) asm volatile("v_permlane32_swap_b32_e32 %0, %1" : "+v"(o.h[2 * U - 4]), "+v"(o.h[2 * U - 3]));
  else if constexpr (U == 4) asm volatile("v_add_f32_e32 %0, %0, %1\n\tv_add_f32_e32 %2, %2, %3" : "+v"(o.h[0]), "+v"(o.h[1]), "+v"(o.h[2]), "+v"(o.h[3]));
  else asm volatile("ds_write_b32 %0, %1 offset:%3\n\tds_write_b32 %0, %2 offset:%4" :: "v"((PASS || I < 2 ? o.ra : o.rb) + o.hs), "v"(o.h[0]), "v"(o.h[2]), "i"(off - 28), "i"(off - 20) : "memory");
}
template <int ST, int Q, int CODE> DI void op_t(OPS& o) {
  constexpr int kind = CODE / 1000, a = CODE % 1000;
  if constexpr (kind == 0) op_mfma<ST, Q>(o);
  else if constexpr (kind == 1) op_wr<a / 5, a % 5, 4608 * ((a / 5) & 1)>(a < 10 ? o.ra : o.rb);
  else if constexpr (kind == 2) asm volatile("s_waitcnt lgkmcnt(%0)" :: "i"(a) : "memory");
  else if constexpr (kind == 3) asm volatile("s_waitcnt lgkmcnt(0)\n\ts_barrier" ::: "memory");
  else if constexpr (kind == 4) {
    constexpr int j = a >> 3, w = (a >> 1) & 3, h = a & 1;
    if constexpr (j == 1) asm volatile("ds_read_b128 %0, %1 offset:%2" : "=v"(o.m[j][a & 7]) : "v"(o.ma[1] + w * o.ms1), "i"(16 * h) : "memory");
    else if constexpr (j == 0) asm volatile("ds_read_b128 %0, %1 offset:%2" : "=v"(o.m[j][a & 7]) : "v"(o.ma[0] + (w >> 1) * 2u * BP_W), "i"(16 * h + (w & 1) * BP_W) : "memory");
    else asm volatile("ds_read_b128 %0, %1 offset:%2" : "=v"(o.m[j][a & 7]) : "v"(o.ma[2]), "i"(16 * h + w * 9216) : "memory");
  } else if constexpr (kind == 5) {
    constexpr int j = a / 100;
    asm volatile("s_waitcnt lgkmcnt(%8)" : "+v"(o.m[j][0]), "+v"(o.m[j][1]), "+v"(o.m[j][2]), "+v"(o.m[j][3]), "+v"(o.m[j][4]), "+v"(o.m[j][5]), "+v"(o.m[j][6]), "+v"(o.m[j][7]) : "i"(a % 100) : "memory");
  } else if constexpr (kind == 6) {
    constexpr int j = a / 100, e = a % 100, h = (e >> 2) & 1, c = e & 3;
    float v = o.m[j][h][c]; add_v(v, o.m[j][2 * ((e >> 3) + 1) + h][c]); o.m[j][h][c] = v;
  } else if constexpr (kind == 7)
    asm volatile("buffer_store_dwordx4 %0, %1, %2, %3 offen offset:%c4 sc1\n\ts_nop 1" :: "v"(o.m[a >> 1][a & 1]), "v"(o.va), "s"(o.srdE), "s"(o.sw + 8192u * (a >> 1)), "i"(1024 * (a & 1)) : "memory");
  else if constexpr (kind == 8) op_reqA1<ST + OP_LA, a>(o);
  else if constexpr (kind == 9) op_reqB<a>(o);
  else if constexpr (kind == 10) {
    if constexpr (a < 3) asm volatile("buffer_load_dwordx4 %0, %1, %2, %3 offen sc1" : "=v"(o.gv[a]) : "v"(o.an[a]), "s"(o.srdN), "s"(o.sn) : "memory");
    else                 asm volatile("buffer_load_dwordx4 %0, %1, %2, %3 offen" : "=v"(o.xv[a - 3]) : "v"(o.ax[a - 3]), "s"(o.srdX), "s"(o.sn) : "memory");
  } else if constexpr (kind == 11) op_gate<a>(o);
  else if constexpr (kind == 12) op_wr<a / 5, a % 5, 64 * (a / 5) * RSTR>(o.ra);
  else if constexpr (kind == 13) op_stage<(a >> 3), ((a >> 2) & 1), (a & 3)>(o);
  else if constexpr (kind == 15) op_wrs<a / 100, (a / 10) % 10, a % 10>(o);
  else if constexpr (kind == 14) asm volatile("s_barrier" ::: "memory");
}
template <int ST, int Q, int... Is> DI void op_ts(OPS& o, iseq<Is...>) { (op_t<ST, Q, TT[TS[18 * ST + Q] + Is]>(o), ...); }
template <int... Is> DI void op_tail(OPS& o, iseq<Is...>) { ((op_t<32, 0, TT[TS[576] + Is]>(o), real_op(), real_op()), ...); }
template <int ST, int... Qs> DI void op_qs(OPS& o, iseq<Qs...>) { (op_ts<ST, Qs>(o, make_iseq<TS[18 * ST + Qs + 1] - TS[18 * ST + Qs]>{}), ...); }

template <typename F, int... Is> DI void for_seq(F& f, iseq<Is...>) { (f(std::integral_constant<int, Is>{}), ...); }
DI void wait_flag(const unsigned* p, const unsigned tag) {
#pragma nounroll
  for (unsigned spin = 0; spin < (1u << 20); ++spin) {
    unsigned v;
    asm volatile("global_load_dword %0, %1, off sc1\n\ts_waitcnt vmcnt(0)" : "=v"(v) : "v"(p) : "memory");
    if (__all(v == tag)) break;
    __builtin_amdgcn_s_sleep(1);
  }
}
DI unsigned op_k0(const unsigned blk, const unsigned w) { return ((blk >> 3) & 7u) * 128u + (~blk & 4u) * 16u + 16u * w; }

constexpr int NFIN = 200, NBL = 49, FVW = 30, FVS = 0;
constexpr short FU[] = {0,4,1,5,2,6,3,7,8,9,10,11,12,13,14,15,};
constexpr short FT[] = {5064,14049,5001,23000,16000,5002,14050,5003,23001,16001,5071,14051,5006,23002,16008,5005,14052,5004,23003,16009,5072,14053,5009,23004,16016,5010,14054,5011,23005,16017,5079,31000,25003,5014,23006,
  16024,5013,14055,5012,23007,16025,5080,31000,25002,5017,23008,16002,5018,14056,5019,23009,16003,5099,31000,25001,5034,23010,16010,5033,14057,5032,23011,16011,5112,31000,25000,5049,23012,16018,5050,14058,5051,23013,
  16019,5087,31000,25004,5022,23014,16026,5021,14059,5020,23015,16027,5088,31000,25005,5025,23016,16004,5026,14060,5027,23017,16005,5095,31000,25006,5030,23018,16012,5029,14061,5028,23019,16013,5100,31000,25007,5037,
  23020,16020,5038,14062,5039,23021,16021,5119,31000,25011,5054,23022,16028,5053,14063,5052,23023,16029,5104,31000,25010,5041,23024,16006,5042,16007,16014,5043,23025,16015,5111,31000,25009,5046,23026,16022,5045,16023,
  16030,5044,31000,23027,23028,5120,31000,23029,23030,5057,23031,16031,5058,31000,25008,5059,13000,5127,13001,5062,15000,5061,31000,25015,5060,13002,13003,29013,1000,27000,15001,22000,1001,31000,26000,13016,1003,13017,
  13018,1002,31000,26001,1004,15008,13019,1005,15009,13032,1007,31000,26002,13033,1006,15016,13034,1008,13035,13048,1009,31000,26003,1011,27001,15017,13049,1010,15024,13050,1012,31000,26004,13051,1013,15025,13004,1015,
  13005,13006,1014,31000,26005,3000,15002,13007,1016,15003,13020,1017,31000,26006,13021,1019,15010,13022,1018,13023,13036,1020,31000,26007,1021,27002,15011,13037,1023,15018,13038,1022,31000,26008,13039,1024,15019,13052,
  1025,31000,24000,24001,24002,1027,31000,24003,13053,1026,15026,13054,13055,1028,15027,1029,31000,24004,24005,24006,24007,1031,31000,25014,1030,31000,3001,31000,4000,31000,24008,24009,24010,24011,4001,31000,25013,30008,
  2000,31000,12000,13008,2001,31000,12001,13009,2017,31000,24012,24013,24014,24015,25012,2016,31000,17000,17001,17002,17003,15004,30024,2002,13010,13011,13024,2003,31000,25019,15005,13025,2019,31000,24016,24017,24018,
  24019,17004,2018,31000,25018,17005,17006,17007,30040,2004,31000,18000,18001,18002,18003,18004,2005,31000,25017,18005,18006,18007,2021,31000,24020,24021,24022,24023,19000,2020,31000,25016,19001,19002,19003,30056,2006,
  15012,13026,13027,2007,31000,28000,17008,17009,17010,2023,31000,24024,24025,24026,24027,17011,2022,31000,25020,17012,17013,17014,30072,2008,31000,17015,18008,18009,18010,18011,2009,31000,25021,18012,18013,18014,2025,
  31000,24028,24029,24030,24031,2024,31000,18015,19004,19005,19006,19007,30088,2010,31000,28001,17016,17017,2011,31000,17018,17019,17020,17021,17022,2027,31000,17023,18016,18017,18018,2026,31000,25022,18019,18020,18021,
  30100,2012,31000,18022,18023,19008,19009,2013,19010,19011,15013,13040,2029,31000,28002,17024,17025,2028,31000,17026,17027,17028,17029,17030,30112,2014,31000,17031,18024,18025,18026,2015,31000,25023,18027,18028,18029,
  2031,31000,18030,18031,19012,19013,2030,19014,19015,13041,13042,1032,31000,28003,17032,17033,1033,31000,17034,17035,17036,17037,1035,31000,17038,17039,18032,18033,1034,31000,25026,18034,18035,18036,1036,31000,18037,
  18038,18039,19016,1037,19017,19018,19019,15020,13043,1039,31000,28004,17040,17041,1038,31000,17042,17043,17044,17045,1040,31000,17046,17047,18040,18041,1041,31000,25025,18042,18043,18044,1043,31000,18045,18046,18047,
  19020,1042,19021,19022,19023,15021,13056,1044,31000,28005,17048,17049,1045,31000,17050,17051,17052,17053,1047,31000,17054,17055,18048,18049,1046,31000,25024,18050,18051,18052,3002,31000,18053,18054,18055,1048,19024,
  19025,19026,19027,13057,1049,31000,28006,17056,17057,1051,31000,17058,17059,17060,17061,17062,1050,31000,17063,18056,18057,18058,1052,31000,18059,18060,18061,18062,18063,1053,19028,19029,19030,19031,1055,31000,28007,
  15028,13058,1054,13059,13012,1056,15029,13013,13014,1057,15006,13015,13028,1059,15007,13029,13030,1058,15014,13031,13044,1060,15015,13045,13046,1061,15022,13047,13060,1063,15023,13061,13062,1062,15030,13063,3003,15031,
  4002,31000,2032,31000,2033,31000,12002,2035,31000,2034,31000,17064,17065,17066,17067,2036,31000,2037,31000,17068,17069,17070,17071,18064,2039,31000,18065,18066,18067,18068,18069,2038,31000,18070,18071,19032,19033,
  19034,2040,19035,2041,31000,28008,17072,17073,17074,2043,31000,17075,17076,17077,17078,17079,2042,31000,18072,18073,18074,18075,18076,2044,31000,18077,18078,18079,19036,19037,2045,19038,19039,2047,31000,28009,17080,
  17081,17082,2046,31000,17083,17084,17085,17086,17087,18080,4003,31000,18081,18082,18083,18084,2048,31000,18085,18086,18087,12003,2049,19040,19041,19042,19043,2051,31000,28010,17088,17089,17090,17091,2050,31000,17092,
  17093,17094,17095,18088,18089,2052,31000,18090,18091,18092,18093,18094,18095,2053,19044,19045,19046,19047,2055,31000,28011,17096,17097,17098,17099,2054,31000,17100,17101,17102,17103,18096,18097,2056,31000,18098,18099,
  18100,18101,18102,18103,2057,19048,19049,19050,19051,2059,31000,28012,17104,17105,17106,17107,2058,31000,17108,17109,17110,17111,18104,18105,2060,31000,18106,18107,18108,18109,18110,18111,2061,19052,19053,19054,19055,
  2063,31000,28013,17112,17113,17114,17115,2062,31000,17116,17117,17118,17119,18112,18113,18114,18115,18116,18117,18118,18119,19056,19057,19058,19059,28014,17120,17121,17122,17123,17124,17125,17126,17127,18120,18121,
  18122,18123,18124,18125,18126,18127,19060,19061,19062,19063,28015,};
constexpr short FS[] = {0,2,5,7,10,12,15,17,20,22,25,27,30,33,36,38,41,44,47,49,52,55,58,60,63,66,69,71,74,77,80,82,85,88,91,93,96,99,102,104,107,110,113,115,118,121,124,126,129,132,135,138,141,144,147,150,154,158,161,
  164,166,168,170,173,176,181,185,188,191,194,197,201,204,207,210,214,217,221,224,227,230,233,236,240,243,246,249,253,256,260,263,268,272,276,278,284,287,289,291,297,300,305,309,316,323,328,333,340,346,354,360,367,373,
  378,384,391,397,405,411,417,424,430,437,443,449,456,461,466,473,480,486,492,497,502,508,514,520,526,532,537,543,549,555,561,567,572,578,584,590,595,601,606,613,619,626,631,636,639,643,647,651,655,659,663,667,670,672,
  674,676,679,681,687,689,696,703,710,712,718,725,732,739,742,748,756,762,768,773,780,788,796,801,808,816,824,829,836,844,852,857,864,904,};
struct OST { u32x4 w, P[4][2]; float o[8], il[4], ls[4]; unsigned vo, so[4]; __amdgpu_buffer_rsrc_t srd; bf16x4 lo[8][2], up[8][2]; };
template <int U> DI void o_store(const OST& t) {
  asm volatile("buffer_store_dwordx4 %0, %1, %2, %3 offen sc1" :: "v"(t.w), "v"(t.vo), "s"(t.srd), "s"(t.so[U & 3] + 131072u * (U >> 2)) : "memory");
}
template <int CODE> DI void fin_op(W4C& c, OST& t, OPS& o) {
  constexpr int kind = CODE / 1000, a = CODE % 1000;
  if constexpr (kind == 1) pv16<AO_((a >> 1) & 7, a >> 4), false>(c.Vf[(a >> 1) & 7][a & 1], c.Pk[a >> 4][a & 1]);
  else if constexpr (kind == 2) pv16<AO_((a >> 1) & 7, a >> 4), false>(__builtin_shufflevector(t.lo[(a >> 1) & 7][a & 1], t.up[(a >> 1) & 7][a & 1], 0, 1, 2, 3, 4, 5, 6, 7), t.P[a >> 4][a & 1]);
  else if constexpr (kind == 3) sum64(c, a, c.Pk[a]);
  else if constexpr (kind == 4) sum64(c, a, t.P[a]);
  else if constexpr (kind == 5) { constexpr int kb = (a >> 4) & 3, qb = (a >> 2) & 3; qk16<kb, qb, (a & 3), (a >> 6)>(c.S[1][kb][qb], c.NEGM[qb]); }
  else if constexpr (kind == 14) exp_e<0, a>(c);
  else if constexpr (kind == 16) pack_e<0, a>(c);
  else if constexpr (kind == 22) asm volatile("" :: "v"(c.NEGM[0]), "v"(c.NEGM[1]), "v"(c.NEGM[2]), "v"(c.NEGM[3]));
  else if constexpr (kind == 23) tr_read_<a, 0>(c);
  else if constexpr (kind == 29) phaseA_end<a>(c);
  else if constexpr (kind == 12) asm volatile("v_add_f32_e32 %0, %2, %3\n\tv_rcp_f32_e32 %1, %0" : "=&v"(t.ls[a]), "=v"(t.il[a]) : "v"(c.L[a][0]), "v"(c.L[a][1]), "v"(c.L[a]));
  else if constexpr (kind == 13) exp_e<1, a>(c);
  else if constexpr (kind == 15) pack_to<1, a>(c, t.P);
  else if constexpr (kind == 17) t.o[a & 7] = acc_read<8 * FU[a >> 3] + (a & 7)>();
  else if constexpr (kind == 18) {
    asm volatile("v_mul_f32_e32 %0, %0, %1" : "+v"(t.o[a & 7]) : "v"(t.il[FU[a >> 3] >> 2]));
    if constexpr (a >= 8 && (a & 7) == 0) asm volatile("" :: "v"(t.w));
  } else if constexpr (kind == 19) { GAP(); const unsigned w = cvtpk(t.o[2 * (a & 3)], t.o[2 * (a & 3) + 1]); asm volatile("" :: "v"(w)); t.w[a & 3] = w; GAP(); }
  else if constexpr (kind == 24) tr_rd<a, 0x4000>(c, t.lo, t.up);
  else if constexpr (kind == 25) op_reqB<32 + a>(o);
  else if constexpr (kind == 26) asm volatile("buffer_load_dwordx4 %0, %1, %2 offen offset:%c3 sc1 lds" :: "v"(c.pfv), "s"(c.srdP), "s"(c.pfo + 4096u * (a >> 2)), "i"(1024 * (a & 3)) : "memory");
  else if constexpr (kind == 27) asm volatile("s_mov_b32 m0, %0" :: "s"(c.pfm0 + 4096u * a) : "m0", "memory");
  else if constexpr (kind == 28) o_store<FU[a]>(t);
  else if constexpr (kind == 31) real_op();
  else if constexpr (a == 200) asm volatile("s_nop 0");
  else { constexpr int d = a >> 4; asm volatile("s_waitcnt lgkmcnt(%4)" : "+v"(t.lo[d][0]), "+v"(t.lo[d][1]), "+v"(t.up[d][0]), "+v"(t.up[d][1]) : "i"(a & 15) : "memory"); }
}
template <int G, int... Is> DI void fin_gap(W4C& c, OST& t, OPS& o, iseq<Is...>) { (fin_op<FT[FS[G] + Is]>(c, t, o), ...); GAP(); }
template <int... Gs> DI void fin_gaps(W4C& c, OST& t, OPS& o, iseq<Gs...>) { (fin_gap<Gs>(c, t, o, make_iseq<FS[Gs + 1] - FS[Gs]>{}), ...); }

template <int J, int H> DI void redo_q(const __amdgpu_buffer_rsrc_t srdW, const __amdgpu_buffer_rsrc_t srdN, const unsigned vw, const unsigned vx, const unsigned sw, const unsigned sx) {
  f32x4 q[4] = {};
#pragma nounroll
  for (unsigned ks = 0; ks < DM / 32; ++ks) {
    u32x4 w;
#pragma unroll
    for (int e = 0; e < 4; ++e) {
      const unsigned so = sw + (64u * J + 8u * H) + WRS * (32u * ks + 2u * e);
      w[e] = (unsigned)__builtin_amdgcn_raw_buffer_load_b16(srdW, vw, so, 0) | ((unsigned)__builtin_amdgcn_raw_buffer_load_b16(srdW, vw, so + WRS, 0) << 16);
    }
    asm volatile("s_nop 1" : "+v"(w), "+v"(q[0]), "+v"(q[1]), "+v"(q[2]), "+v"(q[3]));
#pragma unroll
    for (int qb = 0; qb < 4; ++qb) {
      u32x4 x;
      asm volatile("buffer_load_dwordx4 %0, %1, %2, %3 offen sc1\n\ts_waitcnt vmcnt(0)" : "=v"(x) : "v"(vx), "s"(srdN), "s"(sx + 64u * ks + 16u * XRS * qb) : "memory");
      sum16<false>(q[qb], w, x);
    }
  }
  asm volatile("s_nop 7" : "+v"(q[0]), "+v"(q[1]), "+v"(q[2]), "+v"(q[3]));
  const float C = 0.12751743082f;
  unsigned r[8];
#pragma unroll
  for (int qb = 0; qb < 4; ++qb) { r[2 * qb] = cvtpk(C * q[qb][0], C * q[qb][1]); r[2 * qb + 1] = cvtpk(C * q[qb][2], C * q[qb][3]); }
  asm volatile("s_nop 1" : "+v"(r[0]), "+v"(r[1]), "+v"(r[2]), "+v"(r[3]), "+v"(r[4]), "+v"(r[5]), "+v"(r[6]), "+v"(r[7]));
  acc_write<AQ_(0, J) + 2 * H>(r[0]); acc_write<AQ_(0, J) + 2 * H + 1>(r[1]); acc_write<AQ_(1, J) + 2 * H>(r[2]); acc_write<AQ_(1, J) + 2 * H + 1>(r[3]);
  acc_write<AQ_(2, J) + 2 * H>(r[4]); acc_write<AQ_(2, J) + 2 * H + 1>(r[5]); acc_write<AQ_(3, J) + 2 * H>(r[6]); acc_write<AQ_(3, J) + 2 * H + 1>(r[7]);
}
template <int I> DI void redo_kload(const unsigned vo, const __amdgpu_buffer_rsrc_t srd, const unsigned so) {
  asm volatile("buffer_load_dwordx4 a[%c3:%c4], %0, %1, %2 offen offset:%c5" :: "v"(vo), "s"(srd), "s"(so + 4096u * (I >> 2)), "i"(192 + 4 * I), "i"(195 + 4 * I), "i"(64 * (I & 3)) : "memory");
}
template <int CM, int... Ms> DI void redo_s(W4C& c, const unsigned vk, const unsigned so, f32x4 (&T)[2][4], iseq<Ms...>) {
  redo_kload<0>(vk, c.srdK, so); redo_kload<1>(vk, c.srdK, so); redo_kload<2>(vk, c.srdK, so); redo_kload<3>(vk, c.srdK, so);
  redo_kload<4>(vk, c.srdK, so); redo_kload<5>(vk, c.srdK, so); redo_kload<6>(vk, c.srdK, so); redo_kload<7>(vk, c.srdK, so);
  asm volatile("s_waitcnt vmcnt(0)" ::: "memory");
  (qk16<(Ms >> 4), ((Ms >> 2) & 3), (Ms & 3), ((Ms & 3) ? 0 : CM)>(T[Ms >> 4][(Ms >> 2) & 3], c.NEGM[(Ms >> 2) & 3]), ...);
  asm volatile("s_nop 7" : "+v"(T[0][0]), "+v"(T[0][1]), "+v"(T[0][2]), "+v"(T[0][3]), "+v"(T[1][0]), "+v"(T[1][1]), "+v"(T[1][2]), "+v"(T[1][3]));
}
template <int DB> DI void redo_pv(const W4C& c, const unsigned vv, const unsigned so, const u32x4 (&P)[4]) {
  u32x4 v;
#pragma unroll
  for (int e = 0; e < 4; ++e) {
    const unsigned s = so + (896u + 32u * DB) + KRS * (16u * (e >> 1) + 2u * (e & 1));
    v[e] = (unsigned)__builtin_amdgcn_raw_buffer_load_b16(c.srdV, vv, s, 0) | ((unsigned)__builtin_amdgcn_raw_buffer_load_b16(c.srdV, vv, s + KRS, 0) << 16);
  }
  asm volatile("s_nop 1" : "+v"(v));
  const bf16x8 vf = __builtin_bit_cast(bf16x8, v);
  pv16<AO_(DB, 0), false>(vf, P[0]); pv16<AO_(DB, 1), false>(vf, P[1]); pv16<AO_(DB, 2), false>(vf, P[2]); pv16<AO_(DB, 3), false>(vf, P[3]);
  GAP();
}
template <int U> DI void redo_unit(OST& t) {
  t.o[0] = acc_read<8 * U>(); t.o[1] = acc_read<8 * U + 1>(); t.o[2] = acc_read<8 * U + 2>(); t.o[3] = acc_read<8 * U + 3>();
  t.o[4] = acc_read<8 * U + 4>(); t.o[5] = acc_read<8 * U + 5>(); t.o[6] = acc_read<8 * U + 6>(); t.o[7] = acc_read<8 * U + 7>();
  const float il = t.il[U >> 2];
  t.w = u32x4{cvtpk(t.o[0] * il, t.o[1] * il), cvtpk(t.o[2] * il, t.o[3] * il), cvtpk(t.o[4] * il, t.o[5] * il), cvtpk(t.o[6] * il, t.o[7] * il)};
  o_store<U>(t);
  asm volatile("s_nop 1" :: "v"(t.w));
}
template <int R0, int... Is> DI void redo_zero(const unsigned z, iseq<Is...>) { (acc_write<R0 + Is>(z), ...); }
template <int... Is> DI void redo_units(OST& t, iseq<Is...>) { (redo_unit<Is>(t), ...); }
template <int... Is> DI void redo_reqs(OPS& o, iseq<Is...>) { (op_reqB<32 + Is>(o), ...); }
DI void redo_rows(W4C& c, OST& t, OPS& o, const u16* xn, const u16* wq, const unsigned long long* keys, const unsigned row0, const unsigned h, const unsigned tag) {
  unsigned l;
  asm volatile("s_waitcnt vmcnt(0)\n\tv_mbcnt_lo_u32_b32 %0, -1, 0\n\tv_mbcnt_hi_u32_b32 %0, -1, %0" : "=v"(l) :: "memory");
  const unsigned c16 = l & 15u, g = l >> 4;
  wait_flag((const unsigned*)(keys + row0 + l) + 1, tag);
  {
    const __amdgpu_buffer_rsrc_t srdW = mk_srd(wq, DM * HQ * DH * 2), srdN = mk_srd(xn, SEQ * DM * 2);
    const unsigned vw = WRS * 8u * g + 16u * (c16 >> 2) + 2u * (c16 & 3u), vx = XRS * c16 + 16u * g, sw = 256u * h, sx = XRS * row0;
    redo_q<0, 0>(srdW, srdN, vw, vx, sw, sx); redo_q<0, 1>(srdW, srdN, vw, vx, sw, sx); redo_q<1, 0>(srdW, srdN, vw, vx, sw, sx); redo_q<1, 1>(srdW, srdN, vw, vx, sw, sx);
    redo_q<2, 0>(srdW, srdN, vw, vx, sw, sx); redo_q<2, 1>(srdW, srdN, vw, vx, sw, sx); redo_q<3, 0>(srdW, srdN, vw, vx, sw, sx); redo_q<3, 1>(srdW, srdN, vw, vx, sw, sx);
  }
  const unsigned vk = KRS * c16 + 16u * g, vv = KRS * 4u * g + 2u * c16;
  f32x4 T[2][4];
  float m[4] = {-__builtin_inff(), -__builtin_inff(), -__builtin_inff(), -__builtin_inff()};
#pragma nounroll
  for (unsigned u = 0; u < SEQ / 32; ++u) {
    redo_s<2>(c, vk, 32u * KRS * u, T, make_iseq<32>{});
#pragma unroll
    for (int q = 0; q < 4; ++q)
#pragma unroll
      for (int e = 0; e < 8; ++e) m[q] = max2f(m[q], T[e >> 2][q][e & 3]);
  }
#pragma unroll
  for (int q = 0; q < 4; ++q) {
    asm volatile("s_nop 1" : "+v"(m[q]));
    auto p1 = __builtin_amdgcn_permlane16_swap(__float_as_uint(m[q]), __float_as_uint(m[q]), false, false);
    m[q] = max2f(__uint_as_float(p1[0]), __uint_as_float(p1[1]));
    asm volatile("s_nop 1" : "+v"(m[q]));
    auto p2 = __builtin_amdgcn_permlane32_swap(__float_as_uint(m[q]), __float_as_uint(m[q]), false, false);
    m[q] = -max2f(__uint_as_float(p2[0]), __uint_as_float(p2[1]));
    c.NEGM[q] = f32x4{m[q], m[q], m[q], m[q]};
    c.L[q] = f32x4{0.f, 0.f, 0.f, 0.f};
  }
  redo_zero<0>(0u, make_iseq<128>{});
  asm volatile("s_nop 1" : "+v"(c.NEGM[0]), "+v"(c.NEGM[1]), "+v"(c.NEGM[2]), "+v"(c.NEGM[3]), "+v"(c.L[0]), "+v"(c.L[1]), "+v"(c.L[2]), "+v"(c.L[3]));
#pragma nounroll
  for (unsigned u = 0; u < SEQ / 32; ++u) {
    redo_s<1>(c, vk, 32u * KRS * u, T, make_iseq<32>{});
    u32x4 P[4];
#pragma unroll
    for (int q = 0; q < 4; ++q)
#pragma unroll
      for (int e = 0; e < 4; ++e) P[q][e] = cvtpk(__builtin_amdgcn_exp2f(T[e >> 1][q][2 * (e & 1)]), __builtin_amdgcn_exp2f(T[e >> 1][q][2 * (e & 1) + 1]));
    asm volatile("s_nop 1" : "+v"(P[0]), "+v"(P[1]), "+v"(P[2]), "+v"(P[3]));
#pragma unroll
    for (int q = 0; q < 4; ++q) sum16<false>(c.L[q], c.ONE, P[q]);
    const unsigned so = 32u * KRS * u;
    redo_pv<0>(c, vv, so, P); redo_pv<1>(c, vv, so, P); redo_pv<2>(c, vv, so, P); redo_pv<3>(c, vv, so, P);
    redo_pv<4>(c, vv, so, P); redo_pv<5>(c, vv, so, P); redo_pv<6>(c, vv, so, P); redo_pv<7>(c, vv, so, P);
  }
  asm volatile("s_nop 7" : "+v"(c.L[0]), "+v"(c.L[1]), "+v"(c.L[2]), "+v"(c.L[3]));
#pragma unroll
  for (int q = 0; q < 4; ++q) t.il[q] = __builtin_amdgcn_rcpf(c.L[q][0]);
  redo_units(t, make_iseq<16>{});
  redo_reqs(o, make_iseq<OP_NR>{});
  asm volatile("s_waitcnt vmcnt(0)" ::: "memory");
}

DI void oproj_phase(OPS& o, char* lds, const u16* __restrict__ Oa, const u16* __restrict__ wot, const u16* __restrict__ x, const u16* __restrict__ xn,
                    unsigned* __restrict__ hand, float* __restrict__ exch, u16* __restrict__ out, const unsigned tag, const unsigned myflag  ) {
  unsigned lane;
  asm volatile("v_mbcnt_lo_u32_b32 %0, -1, 0\n\tv_mbcnt_hi_u32_b32 %0, -1, %0" : "=v"(lane));
  const unsigned wid = rfl(threadIdx.x >> 6), tid = 64u * wid + lane, l16 = lane & 15u, g16 = lane >> 4;
  const unsigned blk = blockIdx.x, xg = blk & 7u, ii = blk >> 3;
  const unsigned mt = 8u * (xg & 3u) + 2u * (ii >> 3) + (xg >> 2), nt = ii & 7u, kq = (~blk & 4u) * 16u + 16u * wid;
  const unsigned ldsb = (unsigned)(size_t)lds;
  o.srdA = mk_srd(Oa, SEQ * HQ * DH * 2);
#pragma unroll
  for (unsigned p = 0; p < 4u; ++p) o.sa[p] = (4u * (mt ^ 1u) * 128u + kq + (p ^ ((mt ^ 1u) & 3u))) * 1024u;
  asm volatile("" : "+s"(o.sa[0]), "+s"(o.sa[1]), "+s"(o.sa[2]), "+s"(o.sa[3]));
  o.lb5 = ldsb + SD_BASE + BP_W * wid + o.va;
  const __amdgpu_buffer_rsrc_t srdH = mk_srd(hand, 65536u), srdF = mk_srd(hand + myflag, 4u);
  const unsigned fxw = H_X + 18u * nt + (lane < 18u ? lane : 0u);
  unsigned selr, fx, fe;
  unsigned fo;
  const unsigned fow = H_O + 256u * (mt >> 1) + 2u * kq + (lane & 7u);
  asm volatile("buffer_load_dword %0, %1, %2, 0 offen sc1" : "=&v"(fo) : "v"(4u * fow), "s"(srdH) : "memory");
  GAP();
  asm volatile("s_waitcnt vmcnt(%2)\n\tds_read_b32 %0, %1\n\ts_waitcnt lgkmcnt(0)" : "=v"(selr) : "v"(ldsb + WF_FL + 4u * l16), "i"(FVW + 1) : "memory");
  static_assert(FVS == 0, "the last request of the final phase is a store");
  {
    const unsigned lb2 = ldsb + 9216u * wid + o.va;
    op_stage<0, 0, 0>(o); op_stage<0, 0, 1>(o); op_stage<0, 0, 2>(o); op_stage<0, 0, 3>(o); op_stage<1, 1, 0>(o); op_stage<1, 1, 1>(o); op_stage<1, 1, 2>(o); op_stage<1, 1, 3>(o);
    op_ldsB<8>(o, lb2); op_ldsBs<13>(o, lb2, make_iseq<19>{});
  }
  o.ma[0] = ldsb + SD_BASE + 13824u + 32u * tid;
  o.ma[1] = ldsb + (tid < 32u ? SD_BASE + 13824u + 8192u + 32u * tid : 32u * (tid - 32u)); o.ms1 = tid < 32u ? BP_W : 9216u;
  o.ma[2] = ldsb + 7168u + 32u * lane;
  { const unsigned rl = (l16 * 72u + 4u * g16) * 4u; o.ra = ldsb + SD_BASE + BP_W * wid + 13824u + rl; o.rb = ldsb + 9216u * wid + rl; }
  o.sw = 2048u * wid; o.srdE = mk_srd(exch + blk * 4608u, 18432u);
  o.srdN = mk_srd(xn, (SEQ + NSEL) * DM * 2); o.srdX = mk_srd(x, SEQ * DM * 2); o.sn = 144u * nt;
#pragma unroll
  for (int j = 0; j < 3; ++j) {
    const unsigned ci = tid + 256u * j, r = ci / 9u, ch = ci - 9u * r;
    o.ax[j] = (64u * mt + (r < 64u ? r : 63u)) * XRS_ + 16u * ch;
    asm volatile("" : "+v"(o.ax[j]));
  }
  asm volatile("" : "+v"(o.ma[0]), "+v"(o.ma[1]), "+v"(o.ma[2]), "+v"(o.ms1), "+v"(o.ra), "+v"(o.rb));
  GAP();
  asm volatile("s_waitcnt vmcnt(1)\n\tbuffer_store_dword %0, %1, %2, 0 offen sc1\n\ts_waitcnt vmcnt(1)" :: "v"(tag), "v"(o.va), "s"(srdF) : "memory");
  asm volatile("" : "+v"(fo));
  if (!__all(fo == tag)) wait_flag(hand + fow, tag);
  GAP();
  op_reqA0(o, make_iseq<4 * OP_LA>{});
  GAP();
  redo_zero<64>(0u, make_iseq<32>{});
  o.idx = (lane & 8u) ? 0xeeeeeeeeu : 0x44444444u; o.hs = lane < 32u ? 28u : 0u;
  asm volatile("s_waitcnt lgkmcnt(0)" : "+v"(o.fb[0]), "+v"(o.fb[1]), "+v"(o.fb[2]), "+v"(o.fb[3]), "+v"(o.fb[4]), "+v"(o.fb[5]), "+v"(o.fb[6]), "+v"(o.fb[7]), "+v"(o.fb[8]), "+v"(o.fb[9]),
               "+v"(o.fb[10]), "+v"(o.fb[11]), "+v"(o.fb[12]), "+v"(o.fb[13]), "+v"(o.fb[14]), "+v"(o.fb[15]), "+v"(o.fb[16]), "+v"(o.fb[17]), "+v"(o.fb[18]), "+v"(o.fb[19]), "+v"(o.idx) :: "memory");
  GAP();
#pragma nounroll
  for (unsigned spin = 0; spin < (1u << 20) && !__all((lane & 1u) == 0u || selr == tag); ++spin) {
    __builtin_amdgcn_s_sleep(6);
    asm volatile("buffer_load_dword %0, %1, %2, 0 offen sc1\n\ts_waitcnt vmcnt(0)" : "=v"(selr) : "v"(4u * l16), "s"(srdH) : "memory");
  }
  const unsigned tmask = (unsigned)(__ballot((lane & 1u) == 0u && lane < 16u && ((selr & 0xffffu) - 64u * mt) < 64u));
  asm volatile("s_nop 4\n\tbuffer_load_dword %0, %1, %2, 0 offen sc1" : "=&v"(fx) : "v"(4u * fxw), "s"(mk_srd(hand, ((0u - tmask) >> 31) * 8192u)) : "memory");
  GAP();
  const __amdgpu_buffer_rsrc_t srdEi = mk_srd(exch + (blk ^ 4u) * 4608u, 18432u);
  f32x4 pv[3][2];
  auto step = [&](auto ST) __attribute__((always_inline)) {
    constexpr int st = decltype(ST)::value, A = st % (OP_LA + 1);
    if constexpr (st == 16 - OP_LA) {
      const unsigned sd = (mt - (mt ^ 1u)) * 524288u;
      o.sa[0] += sd; o.sa[1] += sd; o.sa[2] += sd; o.sa[3] += sd;
      asm volatile("" : "+s"(o.sa[0]), "+s"(o.sa[1]), "+s"(o.sa[2]), "+s"(o.sa[3]));
    }
    if constexpr (st == 16) {
    }
    if constexpr (st == 4) {
      asm volatile("s_waitcnt vmcnt(%1)" : "+v"(fx) : "i"(OPW[4] + 4) : "memory");
      o.an[0] = o.ax[0]; o.an[1] = o.ax[1]; o.an[2] = o.ax[2];
      if (__builtin_expect(tmask != 0u, 0)) {
        if (!__all(fx == tag)) wait_flag(hand + fxw, tag);
#pragma nounroll
        for (unsigned mk = tmask; mk; mk &= mk - 1u) {
          const unsigned t2 = (unsigned)__builtin_ctz(mk), ro = ((unsigned)__builtin_amdgcn_readlane(selr, t2) & 0xffffu) * XRS_;
#pragma unroll
          for (int j = 0; j < 3; ++j) { const unsigned d = o.ax[j] - ro; o.an[j] = d < XRS_ ? (SEQ + (t2 >> 1)) * XRS_ + d : o.an[j]; }
        }
      }
    }
    asm volatile("s_waitcnt vmcnt(%4)" : "+v"(o.fa[A][0]), "+v"(o.fa[A][1]), "+v"(o.fa[A][2]), "+v"(o.fa[A][3]) : "i"(OPW[st]) : "memory");
    if constexpr (st == 0 || st == 24) asm volatile("s_waitcnt lgkmcnt(0)" : "+v"(o.fs[0][0]), "+v"(o.fs[0][1]), "+v"(o.fs[0][2]), "+v"(o.fs[0][3]), "+v"(o.fs[1][0]), "+v"(o.fs[1][1]), "+v"(o.fs[1][2]), "+v"(o.fs[1][3]) :: "memory");
    if constexpr (st == 4 || st == 20) asm volatile("s_waitcnt lgkmcnt(0)" : "+v"(o.fs[0][0]), "+v"(o.fs[0][1]), "+v"(o.fs[0][2]), "+v"(o.fs[0][3]) :: "memory");
    if constexpr (st == 9) asm volatile("" : "+v"(o.gv[0]), "+v"(o.gv[1]), "+v"(o.gv[2]));
    if constexpr (st == OP_SF) {
      const __amdgpu_buffer_rsrc_t sf = mk_srd(hand + H_E + 32u * blk + wid, 4u); const unsigned tg = tag, va = o.va;
      asm volatile("buffer_store_dword %0, %1, %2, 0 offen sc1" :: "v"(tg), "v"(va), "s"(sf) : "memory");
    }
    if constexpr (st == OP_SD) {
      asm volatile("s_waitcnt vmcnt(%1)" : "+v"(fe) : "i"(OP_FEW) : "memory");
      if (__builtin_expect(!__all(fe == tag), 0)) wait_flag(hand + H_E + 32u * (blk ^ 4u) + wid, tag);
      const __amdgpu_buffer_rsrc_t se = srdEi; const unsigned va = o.va, sw = o.sw;
#pragma unroll
      for (int j = 0; j < 3; ++j)
        asm volatile("buffer_load_dwordx4 %0, %2, %3, %4 offen sc1\n\tbuffer_load_dwordx4 %1, %2, %3, %4 offen offset:1024 sc1" : "=&v"(pv[j][0]), "=&v"(pv[j][1]) : "v"(va), "s"(se), "s"(8192u * j + sw) : "memory");
    }
    GAP();
    op_qs<st>(o, make_iseq<18>{});
    GAP();
    if constexpr (st == OP_SP) { const __amdgpu_buffer_rsrc_t sh = srdH; const unsigned so = 4u * (H_E + 32u * (blk ^ 4u) + wid); asm volatile("buffer_load_dword %0, off, %1, %2 sc1" : "=v"(fe) : "s"(sh), "s"(so) : "memory"); }
    if constexpr (st == 29) {
      o.ra = ldsb + SD_BASE + BP_W * wid + (l16 * RSTR + 4u * g16) * 4u;
      asm volatile("" : "+v"(o.ra));
    }
    GAP();
  };
  for_seq(step, make_iseq<32>{});
  asm volatile("s_nop 7" ::: "memory");
  op_tail(o, make_iseq<TS[577] - TS[576]>{});
  asm volatile("s_waitcnt lgkmcnt(0)\n\ts_barrier" ::: "memory");
  f32x4 s[3][2];
#pragma unroll
  for (int j = 0; j < 3; ++j) {
    const unsigned ci = tid + 256u * j, r = ci / 9u, ch = ci - 9u * r;
    const float* rp = (const float*)(lds + SD_BASE) + (r & 63u) * RSTR + 8u * ch;
    s[j][0] = *(const f32x4*)rp; s[j][1] = *(const f32x4*)(rp + 4);
#pragma unroll
    for (int w = 1; w < 4; ++w) { s[j][0] += *(const f32x4*)(rp + w * (BP_W / 4)); s[j][1] += *(const f32x4*)(rp + w * (BP_W / 4) + 4); }
  }
  asm volatile("s_waitcnt vmcnt(0)" : "+v"(pv[0][0]), "+v"(pv[0][1]), "+v"(pv[1][0]), "+v"(pv[1][1]), "+v"(pv[2][0]), "+v"(pv[2][1]), "+v"(o.xv[0]), "+v"(o.xv[1]), "+v"(o.xv[2]),
               "+v"(s[0][0]), "+v"(s[0][1]), "+v"(s[1][0]), "+v"(s[1][1]), "+v"(s[2][0]), "+v"(s[2][1]) :: "memory");
#pragma unroll
  for (int j = 0; j < 3; ++j) {
    const unsigned ci = tid + 256u * j, r = ci / 9u, ch = ci - 9u * r;
    if (r < 64u) {
      const f32x4 s0 = s[j][0] + pv[j][0], s1 = s[j][1] + pv[j][1];
      const u32x4 g4 = o.gv[j];
      u32x4 ov;
#pragma unroll
      for (int w = 0; w < 4; ++w) {
        const unsigned sp = w < 2 ? cvtpk(s0[2 * w], s0[2 * w + 1]) : cvtpk(s1[2 * (w - 2)], s1[2 * (w - 2) + 1]);
        const unsigned pp = cvtpk(bflo(sp) * bflo(g4[w]), bfhi(sp) * bfhi(g4[w]));
        ov[w] = cvtpk(bflo(o.xv[j][w]) + bflo(pp), bfhi(o.xv[j][w]) + bfhi(pp));
      }
      *(u32x4*)(out + (size_t)(64u * mt + r) * DM + 72u * nt + 8u * ch) = ov;
    }
  }
}

extern "C" __global__ __launch_bounds__(256, 1)
void k_attn(u16* __restrict__ xn, const u16* __restrict__ wq, const u16* __restrict__ Kc, const u16* __restrict__ Vc, u16* __restrict__ O,
            const u16* __restrict__ wo, const u16* __restrict__ wg, const u16* __restrict__ wu, const u16* __restrict__ wd, u16* __restrict__ wot,
            unsigned long long* __restrict__ keys, float* __restrict__ ypart, int* __restrict__ sel, const u16* __restrict__ xres, u16* __restrict__ out,
            const u16* __restrict__ w_r  ) {
  extern __shared__ __attribute__((aligned(16))) char lds[];
  const unsigned tid = threadIdx.x, lane = tid & 63u, wid = tid >> 6, l32 = lane & 31u, hi = lane >> 5;
  constexpr int T = NT;
  const float C = 0.12751743082f;
  W4C c;
  c.ldsb = (unsigned)(size_t)lds; c.pdum = c.ldsb + PF_DUM;
  asm volatile("" : "+s"(c.pdum));
  c.m0k = rfl(c.ldsb + KRING + 4096u * wid); c.m0v = rfl(c.ldsb + VRING + 4096u * wid);
  c.sofk = rfl(KRS * 16u * wid) + (32u / 1) * KTILEB * ((blockIdx.x >> 3) & 0u);

  const unsigned blk = blockIdx.x, xg = blk & 7u, ii = blk >> 3;
  const unsigned kvh = 2u * xg + (ii >> 4), h = 2u * kvh + ((ii >> 3) & 1u), yb = ii & 7u;
  const unsigned row0 = 256u * yb + 64u * wid;
  const bool moe = blk < (unsigned)MOE_WG;
  const bool ret = !moe && blk < 208u;
  unsigned tag = 0u, gen;
  const unsigned lrow = 8u * blk + 2u * wid + hi;
  QFT qf;
  {
    QP p; LNJ lj; u32x4 cw; u32x2 g0, g1; unsigned pb0, pb1, pb2, pb3, csw; unsigned long long pbase;
    lj.three = l32 < 8u; lj.up = hi != 0u;
    const unsigned vl = 16u * l32 + XRS * hi, vl3 = lj.three ? vl + 1024u : 0x40000000u, srow = XRS * 8u * blk + XRS * 2u * rfl(wid);
    {
      const unsigned r8 = (lane >> 2) & 7u, vws = WRS * ((r8 & 3u) + 8u * (r8 >> 2)) + 64u * hi + 16u * (lane & 3u);
      p.vw[0] = vws + 896u; p.vw[1] = vws; p.vw[2] = vws + (4u * WRS - 2048u + 896u); p.vw[3] = vws + (4u * WRS - 2048u);
#pragma unroll
      for (int q = 0; q < 4; ++q) p.vx[q] = XRS * (lane >> 3) + 16u * ((lane & 7u) ^ (4u * (q & 1) + (lane >> 4))) + (8u * XRS - 1024u) * q;
    }
    p.m0x = rfl(c.ldsb + 0x4000u + 8192u * wid);
    p.sofw = rfl(WRS * 16u * wid + 256u * h);
    p.sofx = rfl(XRS * row0);
    p.kk = (yb ^ (ii >> 3)) & 1u;
    asm volatile("s_nop 4" : "+s"(c.m0k), "+s"(c.m0v), "+s"(c.sofk), "+s"(p.m0x), "+s"(p.sofw), "+s"(p.sofx), "+s"(p.kk));
    asm volatile("" : "+v"(p.vw[0]), "+v"(p.vw[1]), "+v"(p.vw[2]), "+v"(p.vw[3]), "+v"(p.vx[0]), "+v"(p.vx[1]), "+v"(p.vx[2]), "+v"(p.vx[3]));
    GAP();
    p.m0w = c.m0k;
    p.srdW = __builtin_amdgcn_make_buffer_rsrc((void*)((const char*)wq - 896), (short)0, DM * HQ * DH * 2 + 896, SRD_WORD3);
    p.srdX = __builtin_amdgcn_make_buffer_rsrc((void*)xres, (short)0, SEQ * DM * 2, SRD_WORD3);
    auto vm = [&](auto I_) __attribute__((always_inline)) {
      constexpr int I = decltype(I_)::value; constexpr VOp o = VT.v[I]; constexpr bool nm = vq_newm0(I); constexpr int q = o.q & 3;
      if constexpr (o.kind == VK_S) {
        if constexpr (o.q == 0 && o.st < 2) qp_next<o.st>(p);
        if constexpr (o.q < 4) lds_dma<nm, 1024 * q>(p.m0w + QP_STG * (o.st % 3), p.vw[q], p.srdW, p.sw);
        else {
          if constexpr (o.q == 8) { p.sx += 32u * XRS; asm volatile("" : "+s"(p.sx)); }
          lds_dma<nm, 1024 * q>(p.m0x + QP_STG * (o.st % 3) + (o.q >= 8 ? 4096u : 0u), p.vx[q], p.srdX, p.sx);
        }
      } else if constexpr (o.kind == VK_W8) lds_dma<nm, 1024 * q>(p.m0w + QP_W8, p.vw[q], p.srdW, p.sw);
      else if constexpr (o.kind == VK_KV) {
        if constexpr (o.q < 8) lds_dma<nm, 1024 * q>(c.m0k + 0x4000u * (o.q >> 2), (q & 2) ? c.ksx[q & 1] : c.vksrc[q & 1], c.srdK, KTILEB * (o.q >> 2) + c.sofk);
        else lds_dma<true, 0>(c.m0v + 1024u * q, c.vvsrc, c.srdV, c.sofk + (896u + KRS * 8u * (q >> 1) + 128u * (q & 1)));
      } else if constexpr (o.kind == VK_V1) lds_dma<nm, 1024 * q>(c.m0v + 0x4000u, (q & 1) ? c.vvsrc : c.vvsrc0, c.srdV, KTILEB + c.sofk);
      else if constexpr (o.kind == VK_K2) lds_dma<nm, 1024 * q>(c.m0k, (q & 2) ? c.ksx[q & 1] : c.vksrc[q & 1], c.srdK, (KTILEB * 2u + c.sofk) & KMASK);
      else if constexpr (o.kind == VK_PF)
        asm volatile("s_mov_b32 m0, %0\n\ts_add_u32 %1, %1, %4\n\tbuffer_load_dwordx4 %2, %3, %1 offen sc1 lds\n\ts_add_u32 %0, %0, 0x400"
                     : "+s"(c.pfm0), "+s"(c.pfo) : "v"(c.pfv), "s"(c.srdP), "s"(c.pfst) : "m0", "memory", "scc");
      else if constexpr (o.kind == VK_CW) {
        unsigned long long pa, po;
        asm volatile("s_getpc_b64 s[20:21]\n\ts_add_u32 s20, s20, k_attn@rel32@lo+4\n\ts_addc_u32 s21, s21, k_attn@rel32@hi+12\n\ts_mov_b64 %0, s[20:21]" : "=s"(pa) :: "scc", "s20", "s21");
        asm volatile("s_getpc_b64 s[20:21]\n\ts_add_u32 s20, s20, k_attn_end@rel32@lo+4\n\ts_addc_u32 s21, s21, k_attn_end@rel32@hi+12\n\ts_mov_b64 %0, s[20:21]" : "=s"(po) :: "scc", "s20", "s21");
        const __amdgpu_buffer_rsrc_t srdC = __builtin_amdgcn_make_buffer_rsrc((void*)pa, (short)0, (int)(po - pa), SRD_WORD3);
        asm volatile("buffer_load_dwordx4 %0, %1, %2, %3 offen" : "=&v"(cw) : "v"(16u * tid), "s"(srdC), "s"((blk >> 3) * 4096u) : "memory");
      } else if constexpr (o.kind == VK_GEN)
        asm volatile("buffer_load_dword %0, off, %1, 0 offset:%c2 sc1" : "=&v"(gen) : "s"(__builtin_amdgcn_make_buffer_rsrc((void*)sel, (short)0, 8192, SRD_WORD3)), "i"(4 * H_G) : "memory");
      else if constexpr (o.kind == VK_A)
        asm volatile("buffer_load_dwordx4 %0, %1, %2, %3 offen offset:%c4" : "=&v"(lj.a[o.q]) : "v"(o.q == 2 ? vl3 : vl), "s"(p.srdX), "s"(srow), "i"(o.q == 1 ? 512 : 0) : "memory");
      else if constexpr (o.kind == VK_WR)
        asm volatile("buffer_load_dwordx4 %0, %1, %2, 0 offen offset:%c3" : "=&v"(lj.w[o.q]) : "v"(16u * l32), "s"(__builtin_amdgcn_make_buffer_rsrc((void*)w_r, (short)0, DM * 2, SRD_WORD3)), "i"(512 * o.q) : "memory");
      else if constexpr (o.kind == VK_STS || o.kind == VK_KEY) {
        const u32x2 g = {o.kind == VK_STS ? lj.stat : lj.key, tag};
        asm volatile("buffer_store_dwordx2 %0, %1, %2, %3 offen sc1" :: "v"(g), "v"(l32 == 0u ? 8u * lrow : 0x40000000u),
                     "s"(__builtin_amdgcn_make_buffer_rsrc((void*)keys, (short)0, SEQ * 16, SRD_WORD3)), "s"(o.kind == VK_STS ? SEQ * 8u : 0u) : "memory");
      } else if constexpr (o.kind == VK_GAT) {
        asm volatile("buffer_load_dwordx2 %0, %1, %2, %3 offen offset:%c4 sc1" : "=&v"(o.q ? g1 : g0) : "v"(8u * (row0 + l32)),
                     "s"(__builtin_amdgcn_make_buffer_rsrc((void*)keys, (short)0, SEQ * 16, SRD_WORD3)), "s"(SEQ * 8u), "i"(256 * o.q) : "memory");
      } else if constexpr (o.kind == VK_XNS) {
        const __amdgpu_buffer_rsrc_t srdN = __builtin_amdgcn_make_buffer_rsrc((void*)xn, (short)0, SEQ * DM * 2, SRD_WORD3);
        asm volatile("buffer_store_dwordx4 %0, %1, %2, %3 offen offset:%c4 sc1\n\ts_nop 1" :: "v"(lj.a[o.q]), "v"(o.q == 2 ? vl3 : vl), "s"(srdN), "s"(srow), "i"(o.q == 1 ? 512 : 0) : "memory");
      }
    };
    auto hook = [&](auto P_) __attribute__((always_inline)) {
      constexpr int pos = decltype(P_)::value;
      if constexpr (pos == 1) {
        asm volatile("s_waitcnt vmcnt(%5)" : "+v"(gen), "+v"(lj.a[0]), "+v"(lj.a[1]), "+v"(lj.a[2]), "+v"(cw) : "i"(vq_wait(VK_CW, 0, pos + 1)) : "memory");
        tag = rfl(gen) * 1664525u + 1013904223u;
        asm volatile("" : "+s"(tag));
      } else if constexpr (pos == 32 + 13) asm volatile("" : "+v"(lj.w[0]), "+v"(lj.w[1]), "+v"(lj.w[2]));
      else if constexpr (pos == 32 + 1) {
        c.vkb[0] = c.ldsb + KRING + 256u * (lane & 15u) + 16u * ((lane >> 4) ^ (lane & 15u));
        asm volatile("" : "+v"(c.vkb[0]));
      } else if constexpr (pos == 32 + 3) {
#pragma unroll
        for (int j = 1; j < 4; ++j) c.vkb[j] = c.vkb[0] ^ (64u * j);
        asm volatile("" : "+v"(c.vkb[1]), "+v"(c.vkb[2]), "+v"(c.vkb[3]));
      } else if constexpr (pos == 32 + 5) {
        c.vvb[0] = c.ldsb + VRING + (((lane & 3u) << 3) | (((lane >> 2) & 3u) << 6) | (((lane >> 4) & 1u) * 288u) | (hi << 11));
        c.vvb[1] = c.vvb[0] ^ 32u;
        asm volatile("" : "+v"(c.vvb[0]), "+v"(c.vvb[1]));
      } else if constexpr (pos == 32 + 7) {
#pragma unroll
        for (int par = 0; par < 2; ++par) c.vksrc[par] = KRS * (lane >> 4) + 16u * ((lane & 15u) ^ (4u * par + (lane >> 4)));
        c.ksx[0] = c.vksrc[0] ^ 128u; c.ksx[1] = c.vksrc[1] ^ 128u;
        asm volatile("" : "+v"(c.vksrc[0]), "+v"(c.vksrc[1]), "+v"(c.ksx[0]), "+v"(c.ksx[1]));
      } else if constexpr (pos == 32 + 9) {
        c.vvsrc = KRS * ((lane >> 2) & 7u) + 64u * hi + 16u * ((lane & 3u) ^ ((lane >> 3) & 2u)); c.vvsrc0 = c.vvsrc + 896u;
        asm volatile("" : "+v"(c.vvsrc), "+v"(c.vvsrc0));
      } else if constexpr (pos == 32 + 0) {
        const unsigned rs = moe ? FF * 2u : ret ? DM * 2u : 64u;
        const unsigned r16 = ret ? ((lane >> 2) & 3u) | (lane & 16u) | ((lane >> 3) & 4u) : lane >> 2;
        c.pfv = rs * r16 + 16u * ((lane & 3u) ^ ((lane >> 4) & 2u));
        asm volatile("" : "+v"(c.pfv));
      } else if constexpr (pos == 29) {
        const unsigned w = rfl(wid), fb = (blk & 7u) * 18u + (blk >> 3);
        pb0 = 64u * fb + 16u * FF * 2u * (w & 1u); pb1 = ((blk - 144u) * 4u + w) * 18432u - 9216u * (w & 1u); pb2 = ((blk - 208u) * 4u + w) * 27648u; pb3 = 18432u * w;
        const u16 *a0 = wg, *a1 = wu;
        asm volatile("s_cmp_lt_u32 %1, 2\n\ts_cselect_b64 %0, %2, %3" : "=s"(pbase) : "s"(w), "s"(a0), "s"(a1) : "scc");
        asm volatile("" : "+s"(pb0), "+s"(pb1), "+s"(pb2), "+s"(pb3));
      } else if constexpr (pos == 31) {
        const u16 *a0 = wo, *a1 = wd; const unsigned b = blk;
        unsigned long long pb; unsigned nb, st, np;
        asm volatile("s_cmp_lt_u32 %8, 208\n\ts_cselect_b64 %0, %9, %10\n\ts_cselect_b32 %5, %5, %6\n\ts_mov_b32 %1, %12\n\ts_cselect_b32 %2, 64, %13\n\ts_cselect_b32 %7, %7, 0\n\ts_cselect_b32 %3, %11, %14\n\t"
                     "s_cmp_lt_u32 %8, 144\n\ts_cselect_b64 %0, %15, %0\n\ts_cselect_b32 %5, %4, %5\n\ts_cselect_b32 %1, %16, %1\n\ts_cselect_b32 %2, %17, %2"
                     : "=&s"(pb), "=&s"(nb), "=&s"(st), "=&s"(np), "+s"(pb0), "+s"(pb1), "+s"(pb2), "+s"(pb3)
                     : "s"(b), "s"(a0), "s"(a1), "s"(18432u), "s"(27648u), "s"(1024u), "s"(20480u), "s"(pbase), "s"(559u * (FF * 2u) + 64u), "s"(FF * 2u * 32u) : "scc");
        pbase = pb; pb0 = nb; pb2 = np; c.pfst = st;
      } else if constexpr (pos == 32 + 15) {
        c.srdP = __builtin_amdgcn_make_buffer_rsrc((void*)(pbase + pb1), (short)0, (int)pb0, SRD_WORD3);
        c.pfo = 0u - c.pfst;
        c.pfm0 = c.ldsb + SD_BASE + pb3; c.pfend = c.pfm0 + pb2;
        asm volatile("" : "+s"(c.srdP), "+s"(c.pfst), "+s"(c.pfo), "+s"(c.pfm0), "+s"(c.pfend));
      } else if constexpr (pos == 32 + 17) {
        unsigned kvo = kvh; asm volatile("" : "+s"(kvo));
        c.srdK = __builtin_amdgcn_make_buffer_rsrc((void*)((const char*)Kc + (size_t)kvo * SEQ * 256), (short)0, SEQ * 256, SRD_WORD3);
        c.srdV = __builtin_amdgcn_make_buffer_rsrc((void*)((const char*)Vc + (size_t)kvo * SEQ * 256 - 896), (short)0, SEQ * 256 + 896, SRD_WORD3);
        asm volatile("" : "+s"(c.srdK), "+s"(c.srdV));
      } else if constexpr (pos == 64 + 1) {
        unsigned kvo = kvh; asm volatile("" : "+s"(kvo));
        const unsigned id = ((blk >> 3) & 15u) * 4u + rfl(wid);
        const u16 *a0 = Kc, *a1 = Vc; unsigned long long pb;
        asm volatile("s_cmp_lt_u32 %1, 32\n\ts_cselect_b64 %0, %2, %3" : "=s"(pb) : "s"(id), "s"(a0), "s"(a1) : "scc");
        c.srdF = __builtin_amdgcn_make_buffer_rsrc((void*)(pb + (size_t)kvo * SEQ * 256), (short)0, SEQ * 256, SRD_WORD3);
        c.sfo = (c.sofk & ~(KTILEB - 1u)) + KTILEB * (LAH + ((id >> 4) & 1u) - (id >> 5)) + 1024u * (id & 15u);
        asm volatile("" : "+s"(c.srdF), "+s"(c.sfo));
      } else if constexpr (pos == 192 + 0) {
        asm volatile("s_waitcnt vmcnt(%2)" : "+v"(g0), "+v"(g1) : "i"(vq_wait(VK_GAT, 0, pos + 1)) : "memory");
        if (__builtin_expect(!__all(g0[1] == tag && g1[1] == tag), 0)) {
          const unsigned long long* stg = keys + SEQ + row0 + l32;
#pragma nounroll
          for (unsigned spin = 0; spin < (1u << 20) && !__all(g0[1] == tag && g1[1] == tag); ++spin) {
            __builtin_amdgcn_s_sleep(6);
            asm volatile("global_load_dwordx2 %0, %2, off sc1\n\tglobal_load_dwordx2 %1, %2, off offset:256 sc1\n\ts_waitcnt vmcnt(0)" : "=&v"(g0), "=&v"(g1) : "v"(stg) : "memory");
          }
        }
      } else if constexpr (pos == 192 + 2 || pos == 192 + 3) {
        constexpr int q = pos == 192 + 3;
        qf.a[q] = C * rcp_nr(bflo((q ? g1 : g0)[0]));
        asm volatile("" : "+v"(qf.a[q]));
      } else if constexpr (pos == 192 + 5 || pos == 192 + 25) {
        constexpr int q = pos == 192 + 25;
        const unsigned n = hi == 0u ? ((q ? g1 : g0)[0] >> 16) ^ 0x8000u : 0u;
        qf.nb[q] = u32x4{n | (n << 16), n, 0u, 0u};
        asm volatile("" : "+v"(qf.nb[q]));
      } else if constexpr (pos == 192 + 30) {
        const f32x4 cs = p.cs;
        asm volatile("ds_write_b128 %0, %1" :: "v"(csw + 0u), "v"(cs) : "memory");
      } else if constexpr (pos == 192 + 22) {
        unsigned a0 = 128u * wid + 64u * (lane & 1u) + 16u * (lane >> 4), a1 = 1024u + 16u * tid;
        asm volatile("" : "+v"(a0), "+v"(a1));
        csw = c.ldsb + QP_CS + ((lane & 14u) == 0u ? a0 : a1);
        asm volatile("" : "+v"(csw));
      }
    };
    asm volatile("" :: "s"(xn), "s"(Kc), "s"(Vc), "s"(O), "s"(wo), "s"(wg), "s"(wu), "s"(wd), "s"(wot), "s"(keys), "s"(ypart), "s"(out), "s"(w_r));
    vq_issue<-1>(vm);
    p.vwb = c.ldsb + ((((lane & 1u) << 5) | ((lane & 2u) << 2)) | (((lane >> 2) & 3u) << 6) | (lane & 16u) | (hi << 8));
#pragma unroll
    for (int ks = 0; ks < 4; ++ks) p.vxb[ks] = c.ldsb + 0x4000u + 8192u * wid + 128u * l32 + 16u * ((2u * ks + hi) ^ ((l32 >> 1) & 7u));
    p.vwc = p.vwb + 512u * wid;
    { const unsigned on = (lane & 15u) == ((lane >> 4) & 1u) ? 0x3f803f80u : 0u; p.sel = u32x4{on, on, on, on}; }
    GAP();
    asm volatile("s_waitcnt vmcnt(%0)\n\ts_barrier" :: "i"(vq_wait(VK_S, 0, 0)) : "memory");
    qp_rds<0, 8>(p, make_iseq<20>{});
    qp_iters(p, vm, hook, lj, lrow, make_iseq<7>{});
    asm volatile("s_waitcnt lgkmcnt(5)" ::: "memory");
#pragma unroll
    for (int d = 0; d < 4; ++d) qf.ca[d] = u32x4{0u, 0u, 0u, 0u};
    GAP();
    qj_gaps(p, c, qf, vm, c.ldsb + QP_CS + 4u * l32, make_iseq<NJ>{});
  }
  {
    const unsigned on = (lane & 2u) == 0u ? 0x3f803f80u : 0u; c.ONE = u32x4{on, on, on, on}; c.IDX = (lane & 1u) ? 0xeeeeeeeeu : 0x44444444u;
    c.L[0] = c.L[1] = c.L[2] = c.L[3] = f32x4{0.f, 0.f, 0.f, 0.f};
    asm volatile("" : "+v"(c.ONE), "+v"(c.IDX), "+v"(c.L[0]), "+v"(c.L[1]), "+v"(c.L[2]), "+v"(c.L[3]));
  }
  unsigned rstate = 3u;
  auto route = [&]() __attribute__((always_inline)) {
    unsigned lq;
    asm volatile("v_mbcnt_lo_u32_b32 %0, -1, 0\n\tv_mbcnt_hi_u32_b32 %0, -1, %0" : "=v"(lq));
    const unsigned w = rfl(wid), kb_ = c.ldsb + MO_KEY + 4096u * w;
    bool req = rstate == 0u;
    if (rstate == 1u) {
      u32x4 g[4];
      asm volatile("ds_read_b128 %0, %4\n\tds_read_b128 %1, %4 offset:1024\n\tds_read_b128 %2, %4 offset:2048\n\tds_read_b128 %3, %4 offset:3072\n\ts_waitcnt lgkmcnt(0)"
                   : "=&v"(g[0]), "=&v"(g[1]), "=&v"(g[2]), "=&v"(g[3]) : "v"(kb_ + 16u * lq) : "memory");
      bool ok = true;
#pragma unroll
      for (int j = 0; j < 4; ++j) ok = ok && g[j][1] == tag && g[j][3] == tag;
      if (__all(ok)) {
        unsigned k[8] = {g[0][0], g[0][2], g[1][0], g[1][2], g[2][0], g[2][2], g[3][0], g[3][2]};
        ce(k[0], k[1]); ce(k[2], k[3]); ce(k[4], k[5]); ce(k[6], k[7]);
        ce(k[0], k[2]); ce(k[1], k[3]); ce(k[4], k[6]); ce(k[5], k[7]);
        ce(k[1], k[2]); ce(k[5], k[6]); ce(k[0], k[4]); ce(k[3], k[7]);
        ce(k[1], k[5]); ce(k[2], k[6]);
        ce(k[1], k[4]); ce(k[3], k[6]);
        ce(k[2], k[4]); ce(k[3], k[5]);
        ce(k[3], k[4]);
        unsigned cnd = 0u;
#pragma nounroll
        for (unsigned r = 0; r < (unsigned)NSEL; ++r) {
          unsigned m = k[0], t;
          t = (unsigned)__builtin_amdgcn_update_dpp(0, (int)m, 0xB1, 0xf, 0xf, false); m = t > m ? t : m;
          t = (unsigned)__builtin_amdgcn_update_dpp(0, (int)m, 0x4E, 0xf, 0xf, false); m = t > m ? t : m;
          t = (unsigned)__builtin_amdgcn_update_dpp(0, (int)m, 0x141, 0xf, 0xf, false); m = t > m ? t : m;
          t = (unsigned)__builtin_amdgcn_update_dpp(0, (int)m, 0x140, 0xf, 0xf, false); m = t > m ? t : m;
          cnd = (lq & 15u) == (unsigned)r ? m : cnd;
          const bool pp = k[0] == m;
#pragma unroll
          for (int i = 0; i < 7; ++i) k[i] = pp ? k[i + 1] : k[i];
        }
        lds_st32(c.ldsb + MO_RS + ((lq & 8u) == 0u ? 64u + 128u * w + 2u * (lq & 48u) + 4u * (lq & 7u) : 640u + 4u * lq), cnd);
        asm volatile("s_waitcnt lgkmcnt(0)" ::: "memory");
        lds_st32(c.ldsb + MO_RS + 4u * w, 1u);
        rstate = 2u;
      } else req = true;
    } else if (rstate == 2u) {
      u32x4 fl;
      asm volatile("ds_read_b128 %0, %1\n\ts_waitcnt lgkmcnt(0)" : "=v"(fl) : "v"(c.ldsb + MO_RS) : "memory");
      if (rfl(fl[0] & fl[1] & fl[2] & fl[3]) != 0u) {
        unsigned cv, rank = 0u;
        asm volatile("ds_read_b32 %0, %1" : "=v"(cv) : "v"(c.ldsb + MO_RS + 64u + 128u * w + 4u * (lq & 31u)) : "memory");
#pragma nounroll
        for (unsigned h = 0; h < 4u; ++h) {
          u32x4 c4v[4];
          asm volatile("ds_read_b128 %0, %5\n\tds_read_b128 %1, %5 offset:16\n\tds_read_b128 %2, %5 offset:32\n\tds_read_b128 %3, %5 offset:48\n\ts_waitcnt lgkmcnt(0)"
                       : "=&v"(c4v[0]), "=&v"(c4v[1]), "=&v"(c4v[2]), "=&v"(c4v[3]), "+v"(cv) : "v"(c.ldsb + MO_RS + 64u + 256u * (lq >> 5) + 64u * h) : "memory");
#pragma unroll
          for (int j = 0; j < 4; ++j)
#pragma unroll
            for (int e = 0; e < 4; ++e) rank += c4v[j][e] > cv ? 1u : 0u;
        }
        auto pr = __builtin_amdgcn_permlane32_swap(rank, rank, false, false);
        rank = pr[0] + pr[1];
        {
          const float rs = rbf(__builtin_amdgcn_rcpf(1.f + __builtin_amdgcn_exp2f(key_val(cv) * -1.4426950408889634f)));
          const u32x2 gr = {(__float_as_uint(rs) & 0xffff0000u) | (unsigned)key_row(cv), tag};
          const unsigned vo = lq < 32u && rank < 8u ? 8u * rank : 0x40000000u, vg = (lq | w) == 0u ? 4u * H_G : 0x40000000u;
          asm volatile("s_nop 4\n\tbuffer_store_dwordx2 %0, %1, %3, 0 offen sc1\n\tbuffer_store_dword %4, %2, %3, 0 offen sc1\n\ts_nop 1" :: "v"(gr), "v"(vo), "v"(vg), "s"(mk_srd(sel, 8192u)), "v"(gr[1]) : "memory");
        }
        rstate = 3u;
      }
    }
    if (req) {
      const __amdgpu_buffer_rsrc_t srdK = mk_srd((keys + 512u * w), 4096);
#pragma unroll
      for (int j = 0; j < 4; ++j)
        asm volatile("s_mov_b32 m0, %0\n\ts_nop 4\n\tbuffer_load_dwordx4 %1, %2, %3 offen sc1 lds" :: "s"(kb_ + 1024u * j), "v"(16u * lq), "s"(srdK), "s"(1024u * j) : "m0", "memory");
      rstate = 1u;
    }
  };
  const unsigned wids = rfl(wid);
  auto opq = [&](unsigned v) __attribute__((always_inline)) { asm volatile("" : "+s"(v)); return v; };
  auto lane_id = [&]() __attribute__((always_inline)) { unsigned lq; asm volatile("v_mbcnt_lo_u32_b32 %0, -1, 0\n\tv_mbcnt_hi_u32_b32 %0, -1, %0" : "=v"(lq)); return lq; };
  auto hand_req = [&](const unsigned m0, const unsigned word0, const unsigned nb) __attribute__((always_inline)) {
    const unsigned vo = c.vksrc[0];
    asm volatile("s_mov_b32 m0, %0\n\ts_nop 0\n\tbuffer_load_dwordx4 %1, %2, 0 offen sc1 lds" :: "s"(m0), "v"(vo), "s"(mk_srd(sel + word0, nb)) : "m0", "memory");
  };
  auto flag1 = [&](const unsigned word, const unsigned on) __attribute__((always_inline)) {
    const unsigned vo = c.vvsrc, tg = tag;
    asm volatile("buffer_store_dword %0, %1, %2, 0 offen sc1" :: "v"(tg), "v"(vo), "s"(mk_srd(sel + word, on)) : "memory");
  };
  auto pf1 = [&]() __attribute__((always_inline)) {
    asm volatile("s_mov_b32 m0, %0\n\ts_add_u32 %1, %1, %4\n\tbuffer_load_dwordx4 %2, %3, %1 offen sc1 lds\n\ts_add_u32 %0, %0, 0x400"
                 : "+s"(c.pfm0), "+s"(c.pfo) : "v"(c.pfv), "s"(c.srdP), "s"(c.pfst) : "m0", "memory", "scc");
  };
  auto m0_set = [&](const unsigned m0) __attribute__((always_inline)) { asm volatile("s_mov_b32 m0, %0" :: "s"(m0) : "m0", "memory"); };
  auto dma16 = [&](const __amdgpu_buffer_rsrc_t sr, const unsigned vo) __attribute__((always_inline)) { asm volatile("buffer_load_dwordx4 %0, %1, 0 offen sc1 lds" :: "v"(vo), "s"(sr) : "memory"); };
  auto flag_st = [&](const __amdgpu_buffer_rsrc_t sr, const unsigned tg) __attribute__((always_inline)) { const unsigned vo = c.vvsrc; asm volatile("buffer_store_dword %0, %1, %2, 0 offen sc1" :: "v"(tg), "v"(vo), "s"(sr) : "memory"); };
  struct CW { unsigned long long pa; unsigned so; __amdgpu_buffer_rsrc_t sr; };
  auto warm_step = [&](CW& k, auto I_) __attribute__((always_inline)) {
    constexpr int I = decltype(I_)::value;
    if constexpr (I == 0) asm volatile("s_getpc_b64 s[20:21]\n\ts_add_u32 s20, s20, k_attn@rel32@lo+4\n\ts_addc_u32 s21, s21, k_attn@rel32@hi+12\n\ts_mov_b64 %0, s[20:21]" : "=s"(k.pa) :: "scc", "s20", "s21");
    if constexpr (I == 1) { unsigned nb; asm volatile("s_mov_b32 %0, k_attn_end-k_attn" : "=s"(nb)); k.sr = __builtin_amdgcn_make_buffer_rsrc((void*)k.pa, (short)0, (int)nb, SRD_WORD3); asm volatile("" : "+s"(k.sr)); }
    if constexpr (I == 2) { k.so = (opq(blk) >> 3) * 4096u + (c.m0k - c.ldsb) / 4u; asm volatile("" : "+s"(k.so)); }
    if constexpr (I == 3) m0_set(c.pdum);
    if constexpr (I == 4) { const unsigned vo = c.vksrc[0], so = k.so; const __amdgpu_buffer_rsrc_t sr = k.sr; asm volatile("buffer_load_dwordx4 %0, %1, %2 offen lds" :: "v"(vo), "s"(sr), "s"(so) : "memory"); }
  };
  auto warm_hook = [&](CW& k, auto M_) __attribute__((always_inline)) {
    constexpr int M = decltype(M_)::value, i = M == FB(0) ? 0 : M == FB(1) ? 1 : M == FB(2) ? 2 : M == 39 ? 3 : M == 40 ? 4 : -1;
    if constexpr (i >= 0) warm_step(k, std::integral_constant<int, i>{});
  };
  phaseA_end<2>(c);
  {
    CW k;
    auto hook = [&](auto M_) __attribute__((always_inline)) { warm_hook(k, M_); };
    phaseB_<1, true, true, 2>(c, hook, make_iseq<NGB>{});
  }
  GAP();
  if (__builtin_expect(opq(blk) == 255u, 0)) {
    {
      const unsigned w = opq(wids), kb_ = c.ldsb + MO_KEY + 4096u * w;
      RTS r; __amdgpu_buffer_rsrc_t sk;
      auto hookA2 = [&](auto M_) __attribute__((always_inline)) {
        constexpr int M = decltype(M_)::value, n = fa_of(M);
        if constexpr (n == 0) { r.lq = lane_id(); asm volatile("" : "+v"(r.lq)); }
        if constexpr (n == 1) { sk = mk_srd(keys + 512u * w, 4096); asm volatile("" : "+s"(sk)); }
        if constexpr (n == 2) { r.va = 16u * r.lq; m0_set(kb_); asm volatile("" : "+v"(r.va)); }
        if constexpr (n == 4 || n == 5 || n == 7 || n == 9) {
          constexpr int j = n == 4 ? 0 : n == 5 ? 1 : n == 7 ? 2 : 3;
          const unsigned v = r.va; const __amdgpu_buffer_rsrc_t sr = sk;
          asm volatile("buffer_load_dwordx4 %0, %1, 0 offen offset:%c2 sc1 lds" :: "v"(v), "s"(sr), "i"(1024 * j) : "memory");
        }
      };
      phaseAh<0, 7>(c, 2, hookA2);
      phaseB<0, 2>(c);
      auto hookA3 = [&](auto M_) __attribute__((always_inline)) { if constexpr (decltype(M_)::value == FA(0)) { r.va += kb_; asm volatile("" : "+v"(r.va)); } };
      phaseAh<1, 2>(c, 3, hookA3);
      auto steps = [&](auto I0_, auto G0_, auto M_) __attribute__((always_inline)) {
        constexpr int M = decltype(M_)::value, I = decltype(I0_)::value + M - 100 - decltype(G0_)::value;
        if constexpr (M >= 100 + decltype(G0_)::value && I < RT_END) rt_step<I>(r, tag);
      };
      auto hookB3 = [&](auto M_) __attribute__((always_inline)) {
        constexpr int M = decltype(M_)::value;
        if constexpr (M == 2 || M == 4 || M == 5 || M == 7) {
          constexpr int j = M == 2 ? 0 : M == 7 ? 3 : M - 3;
          u32x4& d = r.g[j]; const unsigned v = r.va;
          asm volatile("ds_read_b128 %0, %1 offset:%2" : "=v"(d) : "v"(v), "i"(1024 * j) : "memory");
        }
        if constexpr (M == FB(0)) { u32x4 (&g)[4] = r.g; asm volatile("s_waitcnt lgkmcnt(%4)" : "+v"(g[0]), "+v"(g[1]), "+v"(g[2]), "+v"(g[3]) : "i"(kyo(7, FB(0))) : "memory"); }
        steps(IC(0), IC(FB(0)), M_);
      };
      phaseBh<1, 2>(c, hookB3);
      auto hookA4 = [&](auto M_) __attribute__((always_inline)) {
        constexpr int M = decltype(M_)::value;
        steps(IC(NGB - FB(0)), IC(0), M_);
        if constexpr (M == FA(6)) { r.va = (r.lq & 8u) == 0u ? 64u + 128u * w + 2u * (r.lq & 48u) + 4u * (r.lq & 7u) : 640u + 4u * r.lq; asm volatile("" : "+v"(r.va)); }
        if constexpr (M == FA(8)) lds_st32(c.ldsb + MO_RS + r.va, r.cnd);
        if constexpr (M == FA(9)) lds_st32(c.ldsb + MO_RS + 4u * w, __all(r.bad == 0u) ? 1u : 0u);
        if constexpr (M == FA(10)) { r.va = c.ldsb + MO_RS + 64u + 256u * (r.lq >> 5); r.m = c.ldsb + MO_RS + 64u + 128u * w + 4u * (r.lq & 31u); r.rank = 0u; asm volatile("" : "+v"(r.va), "+v"(r.m), "+v"(r.rank)); }
      };
      static_assert(NGB - FB(0) + FA(6) >= RT_END, "");
      phaseAh<0, 3>(c, 4, hookA4);
      auto hookB4 = [&](auto M_) __attribute__((always_inline)) {
        constexpr int M = decltype(M_)::value;
        if constexpr (M == 0) { unsigned& d = r.cv; u32x4& f = r.g[3]; const unsigned v = r.m, v2 = c.ldsb + MO_RS; asm volatile("ds_read_b32 %0, %2\n\tds_read_b128 %1, %3" : "=&v"(d), "=&v"(f) : "v"(v), "v"(v2) : "memory"); }
        if constexpr (M < 60 && M % 4 == 0) { u32x4& d = r.g[(M >> 2) % 3]; const unsigned v = r.va; asm volatile("ds_read_b128 %0, %1 offset:%2" : "=v"(d) : "v"(v), "i"(4 * M) : "memory"); }
        if constexpr (M >= 5 && M < 65 && M % 4 == 1) { u32x4 &d = r.g[((M - 5) >> 2) % 3], &f = r.g[3]; unsigned& cv = r.cv; asm volatile("s_waitcnt lgkmcnt(%3)" : "+v"(d), "+v"(cv), "+v"(f) : "i"(kyo(M - 5, M) + (M < 61)) : "memory"); }
        if constexpr (M >= 105 && M < 165) { unsigned long long& pm = r.pm[M & 1]; const unsigned a = r.g[((M - 105) >> 2) % 3][(M - 105) & 3], cv = r.cv; asm volatile("v_cmp_gt_u32_e64 %0, %1, %2" : "=s"(pm) : "v"(a), "v"(cv)); }
        if constexpr (M >= 106 && M < 166) cnt_s(r.rank, r.pm[(M - 1) & 1]);
        if constexpr (M == 165) { r.bad = r.g[3][0] & r.g[3][1] & r.g[3][2] & r.g[3][3]; asm volatile("" : "+v"(r.bad)); }
        if constexpr (M == 66) { u32x4& d = r.g[0]; const unsigned v = r.va; asm volatile("ds_read_b128 %0, %1 offset:240" : "=v"(d) : "v"(v) : "memory"); }
      };
      phaseBh<0, 2>(c, hookB4);
      auto hookA5 = [&](auto M_) __attribute__((always_inline)) {
        constexpr int M = decltype(M_)::value;
        if constexpr (M == 4) { u32x4& d = r.g[0]; asm volatile("s_waitcnt lgkmcnt(3)" : "+v"(d) :: "memory"); }
        if constexpr (M >= 104 && M < 108) { r.rank += r.g[0][M - 104] > r.cv ? 1u : 0u; asm volatile("" : "+v"(r.rank)); }
        if constexpr (M == FA(1)) { auto pr = __builtin_amdgcn_permlane32_swap(r.rank, r.rank, false, false); r.rank = pr[0] + pr[1]; asm volatile("" : "+v"(r.rank)); }
        if constexpr (M == FA(2)) { r.rs = key_val(r.cv) * -1.4426950408889634f; asm volatile("" : "+v"(r.rs)); }
        if constexpr (M == FA(3)) { r.rs = 1.f + __builtin_amdgcn_exp2f(r.rs); asm volatile("" : "+v"(r.rs)); }
        if constexpr (M == FA(4)) { r.rs = rbf(__builtin_amdgcn_rcpf(r.rs)); asm volatile("" : "+v"(r.rs)); }
        if constexpr (M == FA(5)) { r.m = (__float_as_uint(r.rs) & 0xffff0000u) | (unsigned)key_row(r.cv); r.bad = (r.bad - 1u) & 0x40000000u; asm volatile("" : "+v"(r.m), "+v"(r.bad)); }
        if constexpr (M == FA(7)) { r.va = (r.lq < 32u && r.rank < 8u ? 8u * r.rank : 0x40000000u) | r.bad; asm volatile("" : "+v"(r.va)); }
        if constexpr (M == 100 + FA(8)) { r.cnd = ((r.lq | w) == 0u ? 4u * H_G : 0x40000000u) | r.bad; sk = mk_srd(sel, 8192u); asm volatile("" : "+v"(r.cnd), "+s"(sk)); }
        if constexpr (M == FA(9)) {
          const u32x2 gr = {r.m, tag}; const unsigned vo = r.va, vg = r.cnd; const __amdgpu_buffer_rsrc_t sr = sk;
          asm volatile("buffer_store_dwordx2 %0, %1, %3, 0 offen sc1\n\tbuffer_store_dword %4, %2, %3, 0 offen sc1" :: "v"(gr), "v"(vo), "v"(vg), "s"(sr), "v"(gr[1]) : "memory");
        }
      };
      phaseAh<1, 4>(c, 5, hookA5);
      rstate = rfl(r.bad) ? 0u : 3u;
      if (rstate == 0u) lds_st32(c.ldsb + MO_RS + 4u * w, 0u);
      phaseB<1, 2>(c);
    }
#pragma nounroll
    for (int t = 6; t < 14; t += 2) {
      phaseA<0, 3>(c, t);     phaseB<0, 2>(c);
      phaseA<1, 2>(c, t + 1); phaseB<1, 2>(c);
      if (rstate != 3u) {
        route();
        if (t == 12)
#pragma nounroll
          for (unsigned spin = 0; spin < (1u << 20) && rstate != 3u; ++spin) {
            asm volatile("s_waitcnt vmcnt(0)" ::: "memory");
            route();
            __builtin_amdgcn_s_sleep(8);
          }
      }
      GAP();
    }
  }
  GAP();
  {
    const unsigned b = opq(blk);
    const int t1 = 12 - 6 * (int)((b - 144u) >> 31) - 4 * (int)(((b - 208u) & ~(b - 144u)) >> 31) - 12 * (int)((254u - b) >> 31);
#pragma nounroll
    for (int t = 0; __builtin_expect(t != t1, 1);) {
      t += 2;
      phaseA<0, 3>(c, t);     phaseB<0, 2>(c);
      phaseA<1, 2>(c, t + 1); phaseB<1, 2>(c);
    }
  }
  GAP();
  constexpr auto SG = [](int i) constexpr { constexpr int g[10] = {14, 20, 26, 31, 36, 41, 46, 52, 57, 61}; return g[i + 1]; };
  constexpr auto sg_of = [](int m) constexpr { constexpr int g[10] = {14, 20, 26, 31, 36, 41, 46, 52, 57, 61}; for (int i = 0; i < 10; ++i) if (g[i] == m) return i - 1; return -2; };
  if (__builtin_expect(opq(blk) < 144u, 1)) {
    const unsigned w = opq(wids), b = opq(blk), fbb = (b & 7u) * 18u + (b >> 3);
    phaseA<0, 3>(c, 8);
    {
      __amdgpu_buffer_rsrc_t sr; unsigned m0;
      auto hook = [&](auto M_) __attribute__((always_inline)) {
        constexpr int M = decltype(M_)::value;
        if constexpr (M == FB(1)) { sr = mk_srd(sel, 64u); asm volatile("" : "+s"(sr)); }
        if constexpr (M == FB(2)) { m0 = c.ldsb + MO_C; asm volatile("" : "+s"(m0)); }
        if constexpr (M == 39) m0_set(m0);
        if constexpr (M == 40) dma16(sr, c.vksrc[0]);
      };
      phaseBh<0, 2>(c, hook);
    }
    {
      unsigned va, vo; u32x2 sv; __amdgpu_buffer_rsrc_t srdX;
      constexpr auto yo = [](int g0, int g1) constexpr { return 2 * (pcnt(PA0, 'T', g1) - pcnt(PA0, 'T', g0)) + pcnt(PA0, 'X', g1) - pcnt(PA0, 'X', g0); };
      auto hook = [&](auto M_) __attribute__((always_inline)) {
        constexpr int M = decltype(M_)::value;
        if constexpr (M == FA(0)) { va = c.ldsb + MO_C + ((c.vksrc[0] >> 1) & 0x38u); asm volatile("" : "+v"(va)); }
        if constexpr (M == FA(2)) { vo = ((c.vksrc[0] >> 1) & 64u) | ((c.vksrc[0] >> 4) & 0x30u); asm volatile("" : "+v"(vo)); }
        if constexpr (M == FA(4)) { srdX = mk_srd(xn, SEQ * DM * 2); asm volatile("" : "+s"(srdX)); }
        if constexpr (M == FA(5)) asm volatile("s_waitcnt vmcnt(2)\n\tds_read_b64 %0, %1" : "=v"(sv) : "v"(va) : "memory");
        if constexpr (M == 100 + FA(6)) {
          asm volatile("s_waitcnt lgkmcnt(%1)" : "+v"(sv) : "i"(yo(FA(5), FA(6))) : "memory");
          if (__builtin_expect(!__all(sv[1] == tag), 0)) {
#pragma nounroll
            for (unsigned spin = 0; spin < (1u << 20) && !__all(sv[1] == tag); ++spin) {
              __builtin_amdgcn_s_sleep(8);
              hand_req(c.ldsb + MO_C, 0u, 64u);
              asm volatile("s_waitcnt vmcnt(0)\n\tds_read_b64 %0, %1\n\ts_waitcnt lgkmcnt(0)" : "=v"(sv) : "v"(va) : "memory");
            }
          }
        }
        if constexpr (M == FA(7)) { vo += (sv[0] & 0xffffu) * XRS; asm volatile("" : "+v"(vo)); }
        if constexpr (M == 100 + FA(8) || M == FA(9) || M == FA(10)) {
          constexpr unsigned i = M == FA(9) ? 1 : M == FA(10) ? 2 : 0;
          const unsigned v = vo, m0 = c.ldsb + MO_XR + 1024u * w + 4096u * i, so = 128u * w + 512u * i; const __amdgpu_buffer_rsrc_t sx = srdX;
          asm volatile("s_mov_b32 m0, %0\n\ts_nop 0\n\tbuffer_load_dwordx4 %1, %2, %3 offen sc1 lds" :: "s"(m0), "v"(v), "s"(sx), "s"(so) : "m0", "memory");
        }
      };
      phaseAh<1, 5>(c, 9, hook);
    }
    phaseB<1, 0>(c);
    f32x4 gu; unsigned vw, vx;
    auto gate_up = [&](auto H_) __attribute__((always_inline)) {
      constexpr int HH = decltype(H_)::value;
      bf16x4 wl[2], wh[2]; bf16x8 xf[2];
      auto hook = [&](auto M_) __attribute__((always_inline)) {
        constexpr int M = decltype(M_)::value, j = sg_of(M % 100);
        if constexpr (M < 100 && j >= 0) asm volatile("s_waitcnt lgkmcnt(%3)" : "+v"(wl[j & 1]), "+v"(wh[j & 1]), "+v"(xf[j & 1]) : "i"(kyo(SG(j - 1) + 1, M)) : "memory");
        if constexpr (M >= 100 && j >= 0) {
          const bf16x8 wfr = __builtin_shufflevector(wl[j & 1], wh[j & 1], 0, 1, 2, 3, 4, 5, 6, 7);
          if constexpr (HH == 0 && j == 0) asm volatile("v_mfma_f32_16x16x32_bf16 %0, %1, %2, 0" : "=&v"(gu) : "v"(wfr), "v"(xf[j & 1]));
          else                             asm volatile("v_mfma_f32_16x16x32_bf16 %0, %1, %2, %0" : "+v"(gu) : "v"(wfr), "v"(xf[j & 1]));
        }
        if constexpr (M >= 100 && j >= -1 && j < 8) {
          constexpr int n = (j + 1) & 1, i = 9 * HH + j + 1;
          asm volatile("ds_read_b64_tr_b16 %0, %1 offset:%2" : "=v"(wl[n]) : "v"(vw), "i"(1024 * i) : "memory");
          asm volatile("ds_read_b64_tr_b16 %0, %1 offset:%2" : "=v"(wh[n]) : "v"(vw), "i"(1024 * i + 256) : "memory");
          asm volatile("ds_read_b128 %0, %1 offset:%2" : "=v"(xf[n]) : "v"(vx), "i"(1024 * (i >> 1) + 128 * (i & 1)) : "memory");
        }
        if constexpr (HH == 1) {
          if constexpr (M == DB(8) || M == DB(9) || M == FB(6) || M == FB(8)) pf1();
          if constexpr (M == FB(9)) { vx += 1024u * w; asm volatile("" : "+v"(vx)); }
          if constexpr (M == 72) { const f32x4 g4 = gu; asm volatile("ds_write_b128 %0, %1 offset:%2" :: "v"(vx), "v"(g4), "i"(MO_H - MO_XR) : "memory"); }
        }
      };
      phaseBh<HH, 0>(c, hook);
    };
    {
      auto hook = [&](auto M_) __attribute__((always_inline)) {
        constexpr int M = decltype(M_)::value;
        if constexpr (M == FA(0)) { vw = c.vvb[0] & 0xffu; vx = c.vvb[0] & 32u; asm volatile("" : "+v"(vw), "+v"(vx)); }
        if constexpr (M == FA(1)) { vw = (vw + 16u * vx) ^ (32u * (w & 1u)); asm volatile("" : "+v"(vw)); }
        if constexpr (M == FA(2)) { vx = 9u * (c.vvb[0] & 2048u); asm volatile("" : "+v"(vx)); }
        if constexpr (M == FA(3)) { vw += vx + (c.ldsb + SD_BASE + 36864u * (w >> 1)); asm volatile("" : "+v"(vw)); }
        if constexpr (M == FA(4)) { vx = c.vksrc[0] + (c.ldsb + MO_XR); asm volatile("" : "+v"(vx)); }
      };
      phaseAh<0, 1>(c, 10, hook);
    }
    gate_up(std::integral_constant<int, 0>{});
    {
      unsigned lq;
      auto hook = [&](auto M_) __attribute__((always_inline)) {
        constexpr int M = decltype(M_)::value;
        if constexpr (M == FA(0)) { lq = lane_id(); asm volatile("" : "+v"(lq)); }
        if constexpr (M == FA(2)) { c.pfv = 16u * (lq & 3u); asm volatile("" : "+v"(c.pfv)); }
        if constexpr (M == FA(4)) { c.pfv += XRS * ((lq >> 2) & 3u); asm volatile("" : "+v"(c.pfv)); }
        if constexpr (M == FA(5)) { c.pfv += 2u * (lq & 32u); asm volatile("" : "+v"(c.pfv)); }
        if constexpr (M == FA(9)) { c.pfv += XRS * (lq & 16u); asm volatile("" : "+v"(c.pfv)); }
        if constexpr (M == 3) { c.srdP = mk_srd(wd, DM * FF * 2); asm volatile("" : "+s"(c.srdP)); }
        if constexpr (M == FA(1)) { c.pfo = 32u * fbb + 4u * w; asm volatile("" : "+s"(c.pfo)); }
        if constexpr (M == FA(3)) { c.pfst = 128u; c.pfo = XRS * c.pfo - 128u; asm volatile("" : "+s"(c.pfst), "+s"(c.pfo)); }
        if constexpr (M == FA(7)) { c.pfm0 = c.ldsb + SD_BASE + 18432u * w; asm volatile("" : "+s"(c.pfm0)); }
      };
      phaseAh<1, 0>(c, 11, hook);
    }
    gate_up(std::integral_constant<int, 1>{});
    {
      auto hook = [&](auto M_) __attribute__((always_inline)) { constexpr int M = decltype(M_)::value; if constexpr (M == FA(0) || M == FA(2) || M == FA(4) || M == FA(5) || M == FA(9)) pf1(); };
      phaseAh<0, 10>(c, 12, hook);
    }
    {
      unsigned vh; f32x2 g, u, s;
      auto hook = [&](auto M_) __attribute__((always_inline)) {
        constexpr int M = decltype(M_)::value;
        if constexpr (M == 109) { vh = c.vksrc[0] + (c.ldsb + MO_H + 1024u * (w >> 1) + 8u * (w & 1u)); asm volatile("" : "+v"(vh)); }
        if constexpr (M == FB(0)) asm volatile("ds_read_b64 %0, %2\n\tds_read_b64 %1, %2 offset:2048" : "=&v"(g), "=&v"(u) : "v"(vh) : "memory");
        if constexpr (M == 116) { vh = c.vksrc[0] + (c.ldsb + MO_HH + 4u * w); asm volatile("" : "+v"(vh)); }
        if constexpr (M == FB(1)) {
          asm volatile("s_waitcnt lgkmcnt(%2)" : "+v"(g), "+v"(u) : "i"(kyo(FB(0), FB(1))) : "memory");
          const unsigned pg = cvtpk(g[0], g[1]);
          g[0] = bflo(pg); g[1] = bfhi(pg);
          asm volatile("" : "+v"(g));
        }
        if constexpr (M == FB(2)) { const unsigned pu = cvtpk(u[0], u[1]); u[0] = bflo(pu); u[1] = bfhi(pu); asm volatile("" : "+v"(u)); }
        if constexpr (M == FB(3)) { s[0] = g[0] * -1.4426950408889634f; s[1] = g[1] * -1.4426950408889634f; asm volatile("" : "+v"(s)); }
        if constexpr (M == DB(8) || M == FB(4)) { constexpr int e = M == FB(4); s[e] = __builtin_amdgcn_exp2f(s[e]); asm volatile("" : "+v"(s)); }
        if constexpr (M == DB(9)) { float s0 = s[0], s1 = s[1]; add_v(s0, 1.f); add_v(s1, 1.f); s = f32x2{s0, s1}; }
        if constexpr (M == FB(5) || M == FB(6)) { constexpr int e = M == FB(6); s[e] = __builtin_amdgcn_rcpf(s[e]); asm volatile("" : "+v"(s)); }
        if constexpr (M == FB(7)) { const unsigned ps = cvtpk(g[0] * s[0], g[1] * s[1]); s[0] = __uint_as_float(ps); asm volatile("" : "+v"(s)); }
        if constexpr (M == FB(8)) { const unsigned ps = __float_as_uint(s[0]); u[0] *= bflo(ps); u[1] *= bfhi(ps); asm volatile("" : "+v"(u)); }
        if constexpr (M == FB(9)) { const unsigned hv = cvtpk(u[0], u[1]); asm volatile("ds_write_b32 %0, %1" :: "v"(vh), "v"(hv) : "memory"); }
      };
      phaseBh<0, 0>(c, hook);
    }
    {
      unsigned vd, vh, vyo, lq;
      __amdgpu_buffer_rsrc_t srdYs;
      bf16x4 dl[2], dh[2]; f32x4 y[2]; u32x4 hwv;
      auto hookA = [&](auto M_) __attribute__((always_inline)) {
        constexpr int M = decltype(M_)::value;
        if constexpr (M == FA(0)) { vh = c.vksrc[0] + (c.ldsb + MO_HH); vd = c.vksrc[0] & 0x300u; asm volatile("" : "+v"(vh), "+v"(vd)); }
        if constexpr (M == FA(1)) { vd = 72u * vd + (c.vvb[0] & 0xdfu); asm volatile("" : "+v"(vd)); }
        if constexpr (M == FA(2)) { vd += c.ldsb + SD_BASE + 512u * (w >> 1) + 32u * (w & 1u); asm volatile("" : "+v"(vd)); }
        if constexpr (M == FA(3)) { lq = lane_id(); asm volatile("" : "+v"(lq)); }
        if constexpr (M == FA(4)) { vyo = (lq & 48u) << 3; asm volatile("" : "+v"(vyo)); }
        if constexpr (M == FA(5)) { vyo |= (lq & 7u) << 4; asm volatile("" : "+v"(vyo)); }
        if constexpr (M == FA(7)) { vyo |= (lq & 8u) << 27; asm volatile("" : "+v"(vyo)); }
        if constexpr (M == FA(9)) { srdYs = mk_srd((const char*)ypart + fbb * 18432u, 18432u); asm volatile("" : "+s"(srdYs)); }
      };
      phaseAh<1, 0>(c, 13, hookA);
      auto hook = [&](auto M_) __attribute__((always_inline)) {
        constexpr int M = decltype(M_)::value, i = sg_of(M % 100), is = sg_of(M - 3);
        if constexpr (M == FB(0)) asm volatile("ds_read_b128 %0, %1" : "=v"(hwv) : "v"(vh) : "memory");
        if constexpr (M < 100 && i >= 0) asm volatile("s_waitcnt lgkmcnt(%3)" : "+v"(dl[i & 1]), "+v"(dh[i & 1]), "+v"(hwv) : "i"(kyo(SG(i - 1) + 1, M)) : "memory");
        if constexpr (M >= 100 && i >= 0) {
          const bf16x8 d8 = __builtin_shufflevector(dl[i & 1], dh[i & 1], 0, 1, 2, 3, 4, 5, 6, 7);
          asm volatile("v_mfma_f32_16x16x32_bf16 %0, %1, %2, 0" : "=&v"(y[i & 1]) : "v"(d8), "v"(hwv));
        }
        if constexpr (M >= 100 && i >= -1 && i < 8) {
          asm volatile("ds_read_b64_tr_b16 %0, %1 offset:%2" : "=v"(dl[(i + 1) & 1]) : "v"(vd), "i"(1024 * (i + 1)) : "memory");
          asm volatile("ds_read_b64_tr_b16 %0, %1 offset:%2" : "=v"(dh[(i + 1) & 1]) : "v"(vd), "i"(1024 * (i + 1) + 256) : "memory");
        }
        if constexpr (M < 100 && is >= 0) {
          const unsigned vo = vyo; const __amdgpu_buffer_rsrc_t sr = srdYs; const f32x4 yv = y[is & 1]; const unsigned so = 512u * w + 2048u * is;
          asm volatile("buffer_store_dwordx4 %0, %1, %2, %3 offen sc1\n\ts_nop 1" :: "v"(yv), "v"(vo), "s"(sr), "s"(so) : "memory");
        }
      };
      phaseBh<1, 0>(c, hook);
    }
  }
  GAP();
  if (__builtin_expect(opq(blk) - 144u < 64u, 0)) {
    const unsigned w = opq(wids), b = opq(blk), job = (b - 144u) * 4u + w;
    constexpr unsigned NTS = 128u * 4608u;
    unsigned vw[2], v0, v1, vs;
    __amdgpu_buffer_rsrc_t srdT;
    bf16x4 wl[2], wh[2];
    auto rd = [&](auto I_) __attribute__((always_inline)) {
      constexpr int i = decltype(I_)::value;
      bf16x4 &l = wl[i & 1], &h = wh[i & 1]; const unsigned v = vw[i & 1];
      asm volatile("ds_read_b64_tr_b16 %0, %1 offset:%2" : "=v"(l) : "v"(v), "i"(1024 * (i >> 1)) : "memory");
      asm volatile("ds_read_b64_tr_b16 %0, %1 offset:%2" : "=v"(h) : "v"(v), "i"(1024 * (i >> 1) + 256) : "memory");
    };
    auto st = [&](auto I_, auto Y_) __attribute__((always_inline)) {
      constexpr int i = decltype(I_)::value, u = 2 * i, r = u % 9;
      constexpr auto at = [](int x) constexpr { return (unsigned)(x / 9) * NTS + 1024u * ((x % 9) >> 1) + 128u * ((x % 9) & 1); };
      asm volatile("s_waitcnt lgkmcnt(%2)" : "+v"(wl[i & 1]), "+v"(wh[i & 1]) : "i"(decltype(Y_)::value) : "memory");
      const u32x4 x = __builtin_bit_cast(u32x4, __builtin_shufflevector(wl[i & 1], wh[i & 1], 0, 1, 2, 3, 4, 5, 6, 7));
      const __amdgpu_buffer_rsrc_t sr = srdT; const unsigned up = v1;
      const unsigned vo = r == 8 ? v0 | ((up ^ 1u) << 30) : r == 7 ? v0 | (up << 30) : v0 + up * ((r & 1) ? 896u : 128u);
      asm volatile("buffer_store_dwordx4 %0, %1, %2, %3 offen sc1\n\ts_nop 1" :: "v"(x), "v"(vo), "s"(sr), "s"(at(r == 8 ? u + 1 : u)) : "memory");
      if constexpr (r >= 7) {
        const u32x2 e = {x[0], x[2]}, o2 = {x[1], x[3]}; const unsigned v2 = vs | ((r == 8 ? up : up ^ 1u) << 30);
        asm volatile("buffer_store_dwordx2 %0, %2, %3, %4 offen sc1\n\tbuffer_store_dwordx2 %1, %2, %3, %4 offen offset:128 sc1" :: "v"(e), "v"(o2), "v"(v2), "s"(sr), "s"((unsigned)(u / 9) * NTS) : "memory");
      }
    };
    auto hookB = [&](auto I0_, auto M_) __attribute__((always_inline)) {
      constexpr int M = decltype(M_)::value, i0 = decltype(I0_)::value;
      constexpr auto fr = [](int g) constexpr { return fb_of(g) >= 0 || g == DB(8) || g == DB(9); };
      constexpr auto cnt = [=](int g) constexpr { int n = 0; for (int x = 0; x < g; ++x) n += fr(x); return n; };
      constexpr auto prev = [=](int g) constexpr { int x = g - 1; while (x > 0 && !fr(x)) --x; return x; };
      if constexpr (M < NGB) { if constexpr (fr(M)) {
        if constexpr (cnt(M) % 2 == 0) rd(std::integral_constant<int, i0 + cnt(M) / 2>{});
        else st(std::integral_constant<int, i0 + cnt(M) / 2>{}, std::integral_constant<int, kyo(prev(M), M)>{});
      } }
    };
    {
      auto hook = [&](auto M_) __attribute__((always_inline)) {
        constexpr int M = decltype(M_)::value;
        if constexpr (M == FA(0)) { vw[0] = c.vvb[0] & 0xffu; v0 = c.vvb[0] & 32u; asm volatile("" : "+v"(vw[0]), "+v"(v0)); }
        if constexpr (M == FA(1)) { vw[0] += 16u * v0; v0 = c.vvb[0] & 2048u; asm volatile("" : "+v"(vw[0]), "+v"(v0)); }
        if constexpr (M == FA(2)) { vw[0] += 9u * v0; asm volatile("" : "+v"(vw[0])); }
        if constexpr (M == FA(3)) { vw[0] += c.ldsb + SD_BASE + 36864u * (w >> 1) + 9216u * (w & 1u); asm volatile("" : "+v"(vw[0])); }
        if constexpr (M == FA(4)) { v1 = lane_id(); asm volatile("" : "+v"(v1)); }
        if constexpr (M == FA(5)) { vw[1] = vw[0] ^ 32u; v0 = (v1 & 7u) << 4; asm volatile("" : "+v"(vw[1]), "+v"(v0)); }
        if constexpr (M == FA(7)) { vs = v0 | ((v1 & 32u) << 3) | ((v1 & 16u) >> 1); asm volatile("" : "+v"(vs)); }
        if constexpr (M == FA(9)) { v0 = (v0 | ((v1 & 48u) << 4)) + 4096u * ((job >> 1) & 1u); asm volatile("" : "+v"(v0)); }
        if constexpr (M == FA(10)) { vs += 8192u + 512u * ((job >> 1) & 1u); v1 = (v1 >> 3) & 1u; asm volatile("" : "+v"(vs), "+v"(v1)); }
        if constexpr (M == 100 + FA(6)) { srdT = mk_srd((const char*)wot + ((job & 1u) * 256u + (job >> 2)) * 9216u, 3u * NTS + 9216u); asm volatile("" : "+s"(srdT)); }
      };
      phaseAh<0, 3>(c, 10, hook);
    }
    { auto hook = [&](auto M_) __attribute__((always_inline)) { hookB(std::integral_constant<int, 0>{}, M_); }; phaseBh<0, 0>(c, hook); }
    {
      auto hook = [&](auto M_) __attribute__((always_inline)) {
        constexpr int M = decltype(M_)::value;
        if constexpr (M < 64) { if constexpr (fa_of(M) >= 0) {
          constexpr int n = fa_of(M), p = n ? FA(n - 1) : 0, y = 2 * (pcnt(PA0, 'T', M) - pcnt(PA0, 'T', p)) + pcnt(PA0, 'X', M) - pcnt(PA0, 'X', p);
          if constexpr (n % 2 == 0) rd(std::integral_constant<int, 6 + n / 2>{});
          else st(std::integral_constant<int, 6 + n / 2>{}, std::integral_constant<int, y>{});
        } }
      };
      phaseAh<1, 15>(c, 11, hook);
    }
    { auto hook = [&](auto M_) __attribute__((always_inline)) { hookB(std::integral_constant<int, 12>{}, M_); }; phaseBh<1, 0>(c, hook); }
    phaseA<0>(c, 12);
    {
      auto hook = [&](auto M_) __attribute__((always_inline)) { if constexpr (decltype(M_)::value == FB(0)) flag1(H_W + (b - 144u), ((w - 1u) >> 31) * 4u); };
      phaseBh<0, 0>(c, hook);
    }
    phaseA<1>(c, 13); phaseB<1, 0>(c);
  }
  {
    const unsigned w = opq(wids), b = opq(blk);
    {
      __amdgpu_buffer_rsrc_t sf, sh; unsigned tg, m0; CW k;
      auto hookA = [&](auto M_) __attribute__((always_inline)) {
        constexpr int M = decltype(M_)::value;
        if constexpr (M == FA(0)) { m0 = (((w - 1u) & (b - 144u)) >> 31) * 4u; asm volatile("" : "+s"(m0)); }
        if constexpr (M == FA(1) || M == FA(2)) { if constexpr (M == FA(2)) { sf = mk_srd(sel + H_Y + b, m0); asm volatile("" : "+s"(sf)); } }
        if constexpr (M == FA(3)) { tg = tag; m0 = c.ldsb + WF_FL; asm volatile("" : "+v"(tg), "+s"(m0)); }
        if constexpr (M == FA(4)) { sh = mk_srd(sel + H_W, 256u); asm volatile("" : "+s"(sh)); }
      };
      phaseAh<0, 0>(c, 14, hookA);
      auto hook = [&](auto M_) __attribute__((always_inline)) {
        constexpr int M = decltype(M_)::value;
        if constexpr (M == FB(0)) flag_st(sf, tg);
        if constexpr (M == FB(1)) warm_step(k, std::integral_constant<int, 0>{});
        if constexpr (M == FB(2)) warm_step(k, std::integral_constant<int, 1>{});
        if constexpr (M == FB(3)) warm_step(k, std::integral_constant<int, 2>{});
        if constexpr (M == FB(4)) warm_step(k, std::integral_constant<int, 3>{});
        if constexpr (M == DB(9)) warm_step(k, std::integral_constant<int, 4>{});
        if constexpr (M == 39) m0_set(m0);
        if constexpr (M == 40) dma16(sh, c.vksrc[0]);
      };
      phaseBh<0, 0>(c, hook);
    }
    {
      unsigned va; u32x4 f;
      auto hookA = [&](auto M_) __attribute__((always_inline)) {
        constexpr int M = decltype(M_)::value;
        if constexpr (M == FA(0)) { va = c.ldsb + WF_FL + (c.vksrc[0] & 0xf0u); asm volatile("" : "+v"(va)); }
        if constexpr (M == FA(1)) { c.pfv = lane_id(); asm volatile("" : "+v"(c.pfv)); }
        if constexpr (M == FA(2)) { c.pfv *= 16u; c.pfo = op_k0(b, w); asm volatile("" : "+v"(c.pfv), "+s"(c.pfo)); }
        if constexpr (M == FA(3)) { c.pfo *= 4608u; asm volatile("" : "+s"(c.pfo)); }
        if constexpr (M == FA(4)) { c.srdP = mk_srd((const char*)wot + c.pfo, 16 * 4608); asm volatile("" : "+s"(c.srdP)); }
        if constexpr (M == FA(5)) { c.pfst = 1024u; c.pfo = 0u - 1024u; c.pfm0 = c.ldsb + SD_BASE + BP_W * w; asm volatile("" : "+s"(c.pfst), "+s"(c.pfo), "+s"(c.pfm0)); }
        if constexpr (M == FA(9)) { c.pfend = c.pfm0 + BP_W; asm volatile("" : "+s"(c.pfend)); }
      };
      phaseAh<1, 1>(c, 15, hookA);
      auto hook = [&](auto M_) __attribute__((always_inline)) {
        constexpr int M = decltype(M_)::value;
        if constexpr (M == FB(0)) asm volatile("ds_read_b128 %0, %1" : "=v"(f) : "v"(va) : "memory");
        if constexpr (M == FB(1)) { asm volatile("s_waitcnt lgkmcnt(%1)" : "+v"(f) : "i"(kyo(FB(0), FB(1))) : "memory"); f[0] = (f[0] ^ f[1]) | (f[2] ^ f[3]) | (f[0] ^ f[2]); asm volatile("" : "+v"(f)); }
        if constexpr (M == FB(2)) {
          if (__builtin_expect(!__all((f[0] | (f[1] ^ tag)) == 0u), 0)) {
#pragma nounroll
            for (unsigned spin = 0; spin < (1u << 20); ++spin) {
              __builtin_amdgcn_s_sleep(8);
              hand_req(c.ldsb + WF_FL, H_W, 256u);
              asm volatile("s_waitcnt vmcnt(0)\n\tds_read_b128 %0, %1\n\ts_waitcnt lgkmcnt(0)" : "=v"(f) : "v"(va) : "memory");
              if (__all(((f[0] ^ tag) | (f[1] ^ tag) | (f[2] ^ tag) | (f[3] ^ tag)) == 0u)) break;
            }
            asm volatile("s_mov_b32 m0, %0" :: "s"(c.pm0[1]) : "m0", "memory");
          }
        }
      };
      phaseBh<1, 2>(c, hook);
    }
  }
  GAP();
  if (__builtin_expect(((RED0 - 1u - opq(blk)) & (opq(blk) - RED1)) >> 31, 0)) {
    const unsigned job = (opq(blk) - RED0) * 4u + opq(wids);
    unsigned va; u32x4 f; u32x2 sv, nv; f32x4 y[4]; float a[4]; __amdgpu_buffer_rsrc_t sr;
    auto req_fl = [&](auto W_) __attribute__((always_inline)) {
      u32x4& f_ = f; u32x2& s_ = sv; const unsigned v0 = c.pfv, v1 = (c.pfv >> 1) & 56u; const __amdgpu_buffer_rsrc_t r0 = mk_srd(sel + H_Y, 4u * MOE_WG), r1 = mk_srd(sel, 64u);
      if constexpr (decltype(W_)::value) asm volatile("buffer_load_dwordx4 %0, %2, %4, 0 offen sc1\n\tbuffer_load_dwordx2 %1, %3, %5, 0 offen sc1\n\ts_waitcnt vmcnt(0)" : "=&v"(f_), "=&v"(s_) : "v"(v0), "v"(v1), "s"(r0), "s"(r1) : "memory");
      else                               asm volatile("buffer_load_dwordx4 %0, %2, %4, 0 offen sc1\n\tbuffer_load_dwordx2 %1, %3, %5, 0 offen sc1" : "=&v"(f_), "=&v"(s_) : "v"(v0), "v"(v1), "s"(r0), "s"(r1) : "memory");
    };
    auto bad = [&]() __attribute__((always_inline)) { return vsel(c.pfv >= 16u * (MOE_WG / 4), 0u, (f[0] ^ tag) | (f[1] ^ tag) | (f[2] ^ tag) | (f[3] ^ tag)) | (sv[1] ^ tag); };
#define PINA asm volatile("" : "+v"(a[0]), "+v"(a[1]), "+v"(a[2]), "+v"(a[3]))
    auto batch = [&](auto B_, auto M_) __attribute__((always_inline)) {
      constexpr int B = decltype(B_)::value, M = decltype(M_)::value, g = M - 100, i = g / 9;
      if constexpr (B > 0 && g >= 2 && g < 36 && (g % 9 == 2 || g % 9 == 5) && 4 * (B - 1) + i < 18) {
        constexpr int e = g % 9 == 2 ? 0 : 2; const f32x4 v = y[i];
        if constexpr (B == 1 && i == 0) { a[e] = v[e]; a[e + 1] = v[e + 1]; PIN2(a[e], a[e + 1]); } else { add_v(a[e], v[e]); add_v(a[e + 1], v[e + 1]); }
      }
      if constexpr (M >= 7 && M < 36 && M % 9 == 7 && 4 * B + M / 9 < 18) {
        f32x4& d = y[M / 9]; const unsigned v = va; const __amdgpu_buffer_rsrc_t r = sr;
        asm volatile("buffer_load_dwordx4 %0, %1, %2, %3 offen sc1" : "=v"(d) : "v"(v), "s"(r), "s"(8u * NSEL * DM * 4u * (4 * B + M / 9)) : "memory");
      }
    };
#define PINY asm volatile("" : "+v"(y[0]), "+v"(y[1]), "+v"(y[2]), "+v"(y[3]))
    phaseA<0, 3>(c, 16); phaseB<0, 2>(c);
    phaseA<1, 2>(c, 17);
    { auto hook = [&](auto M_) __attribute__((always_inline)) { if constexpr (decltype(M_)::value == 40) req_fl(IC(0)); }; phaseBh<1, 2>(c, hook); }
    {
      auto hook = [&](auto M_) __attribute__((always_inline)) {
        constexpr int M = decltype(M_)::value;
        if constexpr (M == FA(0)) { va = (c.pfv >> 7) * (unsigned)(NSEL * DM * 4); asm volatile("" : "+v"(va)); }
        if constexpr (M == FA(2)) { va += c.pfv & 112u; asm volatile("" : "+v"(va)); }
        if constexpr (M == FA(4)) { sr = mk_srd(ypart + 32u * job, MOE_WG * NSEL * DM * 4 - 128u * job); asm volatile("" : "+s"(sr)); }
      };
      phaseAh<0, 3>(c, 18, hook);
    }
    asm volatile("" : "+v"(f), "+v"(sv));
    {
      auto hook = [&](auto M_) __attribute__((always_inline)) {
        constexpr int M = decltype(M_)::value;
        if constexpr (M == 104) {
          if (__builtin_expect(!__all(bad() == 0u), 0)) {
#pragma nounroll
            for (unsigned spin = 0; spin < (1u << 20) && !__all(bad() == 0u); ++spin) { __builtin_amdgcn_s_sleep(8); req_fl(IC(1)); }
          }
        }
        batch(IC(0), M_);
      };
      phaseBh<0, 2>(c, hook);
    }
    phaseA<1, 2>(c, 19);
    PINY; { auto hook = [&](auto M_) __attribute__((always_inline)) { batch(IC(1), M_); }; phaseBh<1, 2>(c, hook); }
    phaseA<0, 3>(c, 20);
    PINY; { auto hook = [&](auto M_) __attribute__((always_inline)) { batch(IC(2), M_); }; phaseBh<0, 2>(c, hook); }
    phaseA<1, 2>(c, 21);
    PINY; { auto hook = [&](auto M_) __attribute__((always_inline)) { batch(IC(3), M_); }; phaseBh<1, 2>(c, hook); }
    phaseA<0, 3>(c, 22);
    PINY;
    {
      auto hook = [&](auto M_) __attribute__((always_inline)) {
        batch(IC(4), M_);
        if constexpr (decltype(M_)::value == 40) {
          u32x2& n_ = nv; const unsigned v = (sv[0] & 0xffffu) * XRS, o2 = 8u * job; const __amdgpu_buffer_rsrc_t r = mk_srd(xn, SEQ * DM * 2);
          asm volatile("buffer_load_dwordx2 %0, %1, %2, %3 offen sc1" : "=v"(n_) : "v"(v), "s"(r), "s"(o2) : "memory");
        }
      };
      phaseBh<0, 2>(c, hook);
    }
    phaseA<1, 2>(c, 23);
    asm volatile("" : "+v"(y[0]), "+v"(y[1]), "+v"(nv));
    {
      auto hook = [&](auto M_) __attribute__((always_inline)) {
        constexpr int M = decltype(M_)::value;
        batch(IC(5), M_);
        if constexpr (M == FB(1)) { _Pragma("unroll") for (int e = 0; e < 4; ++e) a[e] = dpp_add<0x128>(a[e]); PINA; }
        if constexpr (M == FB(2))       { a[0] = red_step<4>(a[0]); a[1] = red_step<4>(a[1]); PINA; }
        if constexpr (M == 100 + FB(3)) { a[2] = red_step<4>(a[2]); a[3] = red_step<4>(a[3]); PINA; }
        if constexpr (M == 100 + FB(4) || M == FB(5)) {
          constexpr int e0 = (M == FB(5)) * 2;
#pragma unroll
          for (int e = e0; e < e0 + 2; ++e) {
            auto pr = __builtin_amdgcn_permlane32_swap(__float_as_uint(a[e]), __float_as_uint(a[e]), false, false);
            a[e] = __uint_as_float(pr[0]) + __uint_as_float(pr[1]);
          }
          PINA;
        }
        if constexpr (M == 153 || M == FB(7) || M == FB(8) || M == FB(9)) {
          constexpr int e = M == 153 ? 0 : fb_of(M) - 6;
          a[e] = ((e & 1) ? bfhi(nv[e >> 1]) : bflo(nv[e >> 1])) + rbf(rbf(a[e]) * bfhi(sv[0])); PINA;
        }
        if constexpr (M == 120) { va = (c.pfv >> 4) * XRS; asm volatile("" : "+v"(va)); }
        if constexpr (M == 72) {
          const u32x2 o2 = {cvtpk(a[0], a[1]), cvtpk(a[2], a[3])};
          const unsigned vo = va; const __amdgpu_buffer_rsrc_t r = mk_srd(xn + SEQ * DM + 4u * job, 7u * XRS + 8u);
          asm volatile("buffer_store_dwordx2 %0, %1, %2, 0 offen sc1" :: "v"(o2), "v"(vo), "s"(r) : "memory");
        }
      };
      phaseBh<1, 2>(c, hook);
    }
  }
  GAP();
  {
    int t = 14 + 8 * (int)(((RED0 - 1u - opq(blk)) & (opq(blk) - RED1)) >> 31);
#pragma nounroll
    do {
      t += 2;
      phaseA<0, 3>(c, t);     phaseB<0, 2>(c);
      phaseA<1, 2>(c, t + 1); phaseB<1, 2>(c);
    } while (__builtin_expect(t != T - 4, 1));
  }
  GAP();
  OPS o; OST ot;
  {
    const unsigned w = opq(wids), b = opq(blk);
    __amdgpu_buffer_rsrc_t sf, sh; unsigned on, tg, m0;
    auto hookA = [&](auto M_) __attribute__((always_inline)) {
      constexpr int M = decltype(M_)::value;
      if constexpr (M == FA(0)) { on = ((RED0 - 1u - b) & (b - RED1)) >> 31; asm volatile("" : "+s"(on)); }
      if constexpr (M == FA(1)) { m0 = on * ((b - RED0) * 4u + w); asm volatile("" : "+s"(m0)); }
      if constexpr (M == FA(2)) { sf = mk_srd(sel + H_X + m0, on * 4u); asm volatile("" : "+s"(sf)); }
      if constexpr (M == FA(5)) { sh = mk_srd(sel, 64u); tg = tag; asm volatile("" : "+s"(sh), "+v"(tg)); }
      if constexpr (M == FA(7)) { ot.srd = mk_srd(O, SEQ * HQ * DH * 2); m0 = c.ldsb + WF_FL; asm volatile("" : "+s"(ot.srd), "+s"(m0)); }
      if constexpr (M == FA(9)) { ot.so[0] = ((b >> 3) & 7u) * 2097152u + w * (524288u + 1024u); asm volatile("" : "+s"(ot.so[0])); }
      if constexpr (M == FA(10)) { ot.so[0] += (4u * (b & 7u) + (b >> 6)) * 4096u; asm volatile("" : "+s"(ot.so[0])); }
    };
    phaseAh<0, 1>(c, T - 2, hookA);
    o.srdB = c.srdP; o.va = c.pfv;
    auto hook = [&](auto M_) __attribute__((always_inline)) {
      constexpr int M = decltype(M_)::value;
      constexpr int f = M == 50 ? 3 : M < NGB && PB2[M < NGB ? M : 0] == 'F' ? pcnt(PB2, 'F', M) : -1;
      if constexpr (f == 0) flag_st(sf, tg);
      if constexpr (M == ppos(PB2, 'F', 1) - 1) m0_set(m0);
      if constexpr (f == 1) dma16(sh, c.vksrc[0]);
      if constexpr (f == 2) { ot.so[1] = ot.so[0] ^ 1024u; ot.so[2] = ot.so[0] ^ 2048u; ot.so[3] = ot.so[0] ^ 3072u; asm volatile("" : "+s"(ot.so[1]), "+s"(ot.so[2]), "+s"(ot.so[3])); }
      if constexpr (f == 3) {
        c.pfo = 23u * 1024u; c.pfm0 = c.ldsb + 9216u * w;
        asm volatile("" : "+s"(c.pfo), "+s"(c.pfm0));
      }
    };
    static_assert(pcnt(PB2, 'E', NGB) == NBL && ppos(PB2, 'D', 0) == DB(4), "");
    phaseB_<0, false, false, 0>(c, hook, make_iseq<NGB>{});
  }
  ot.vo = c.pfv;
  fin_gaps(c, ot, o, make_iseq<NFIN>{});
  asm volatile("s_nop 1" :: "v"(ot.w));
  if (__builtin_expect(!__all((ot.ls[0] + ot.ls[1]) + (ot.ls[2] + ot.ls[3]) <= LIM), 0))
    redo_rows(c, ot, o, xn, wq, keys, 256u * ((opq(blk) >> 3) & 7u) + 64u * opq(wids), 4u * (opq(blk) & 7u) + (opq(blk) >> 6), tag);
  oproj_phase(o, lds, O, wot, xres, xn, (unsigned*)sel, ypart + MOE_WG * NSEL * DM, out, tag, H_O + 256u * (2u * yb + (wids >> 1)) + 32u * xg + 4u * (wids & 1u) + (h & 3u)  );
}
extern "C" __global__ void k_attn_end() {}

extern "C" void kernel_launch(void* const* d_in, const int* in_sizes, int n_in,
                              void* d_out, int out_size, void* d_ws, size_t ws_size,
                              hipStream_t stream) {
  const u16* x   = (const u16*)d_in[0];
  const u16* kc  = (const u16*)d_in[1];
  const u16* vc  = (const u16*)d_in[2];
  const u16* wq  = (const u16*)d_in[3];
  const u16* wo  = (const u16*)d_in[4];
  const u16* w_r = (const u16*)d_in[6];
  const u16* wg  = (const u16*)d_in[7];
  const u16* wu  = (const u16*)d_in[8];
  const u16* wd  = (const u16*)d_in[9];
  char* ws = (char*)d_ws;
  u16* xn = (u16*)(ws + WS_XN); unsigned long long* keys = (unsigned long long*)(ws + WS_KEY); int* sel = (int*)(ws + WS_SEL);
  u16* o = (u16*)(ws + WS_O); u16* wot = (u16*)(ws + WS_WOT); float* yp = (float*)(ws + WS_YP);
  (void)hipFuncSetAttribute((const void*)k_attn, hipFuncAttributeMaxDynamicSharedMemorySize, (int)SHM_ATTN);
  hipLaunchKernelGGL(k_attn, dim3(256), dim3(256), SHM_ATTN, stream, xn, wq, kc, vc, o, wo, wg, wu, wd, wot, keys, yp, sel, x, (u16*)d_out, w_r  );
}
```

```cpp
#include <hip/hip_runtime.h>
#include <hip/hip_bf16.h>
#include <utility>
#include <type_traits>

constexpr int SEQ = 2048, DM = 576, HQ = 32, HKV = 16, DH = 128, FF = 4608, NSEL = 8;
constexpr int NT = SEQ / 64;
constexpr float LIM = 1073741824.0f;

typedef float    f32x16 __attribute__((ext_vector_type(16)));
typedef float    f32x2  __attribute__((ext_vector_type(2)));
typedef float    f32x4  __attribute__((ext_vector_type(4)));
typedef __bf16   bf16x8 __attribute__((ext_vector_type(8)));
typedef __bf16   bf16x4 __attribute__((ext_vector_type(4)));
typedef __bf16   bf16x2 __attribute__((ext_vector_type(2)));
typedef unsigned u32x8  __attribute__((ext_vector_type(8)));
typedef unsigned u32x4  __attribute__((ext_vector_type(4)));
typedef unsigned u32x2  __attribute__((ext_vector_type(2)));
typedef unsigned short u16;
template <int... Is> using iseq = std::integer_sequence<int, Is...>;
template <int N> using make_iseq = std::make_integer_sequence<int, N>;

#define DI __device__ __forceinline__
#define GAP() __builtin_amdgcn_sched_barrier(0)
#define IC(x) std::integral_constant<int, (x)>{}
#define SRD_WORD3 0x00020000

DI unsigned rfl(unsigned x) { return __builtin_amdgcn_readfirstlane(x); }
DI unsigned cvtpk(float lo, float hi) { f32x2 v = {lo, hi}; bf16x2 b = __builtin_convertvector(v, bf16x2); return __builtin_bit_cast(unsigned, b); }
DI __amdgpu_buffer_rsrc_t mk_srd(const void* p, const unsigned bytes) {
  asm volatile("" : "+s"(p));
  return __builtin_amdgcn_make_buffer_rsrc((void*)p, (short)0, (int)bytes, SRD_WORD3);
}
DI unsigned vsel(const bool c, unsigned a, const unsigned b) { asm volatile("" : "+v"(a)); return c ? a : b; }
DI float bflo(unsigned w) { return __uint_as_float(w << 16); }
DI float bfhi(unsigned w) { return __uint_as_float(w & 0xffff0000u); }
DI float rbf(float x) { return bflo(cvtpk(x, 0.f)); }
DI u16 f2bf(float x) { return (u16)(cvtpk(x, 0.f) & 0xffffu); }

constexpr size_t WS_XN   = 0;
constexpr size_t WS_KEY  = WS_XN + (size_t)(SEQ + NSEL) * DM * 2;
constexpr size_t WS_ST   = WS_KEY + (size_t)SEQ * 8;
constexpr size_t WS_SEL  = WS_ST + (size_t)SEQ * 8;
constexpr unsigned H_G = 32u, H_W = 64u, H_Y = 128u, H_O = 4096u, H_X = 1536u, H_E = 8192u;
constexpr size_t WS_O    = WS_SEL + 65536;
constexpr size_t WS_WOT  = WS_O + (size_t)SEQ * HQ * DH * 2;
constexpr int    MOE_WG  = 144;
constexpr size_t WS_YP   = WS_WOT + (size_t)HQ * DH * DM * 2;
constexpr size_t WS_END  = WS_YP + (size_t)MOE_WG * NSEL * DM * 4 + 256u * 20480u;

DI void ce(unsigned& a, unsigned& b) { const unsigned hi_ = a > b ? a : b, lo_ = a > b ? b : a; a = hi_; b = lo_; }
DI int key_row(unsigned k) { return 0xffff - (int)(k & 0xffffu); }
DI float key_val(unsigned k) { const unsigned mono = k >> 16; const unsigned rb = (mono & 0x8000u) ? (mono & 0x7fffu) : (~mono & 0xffffu); return __uint_as_float(rb << 16); }

struct LNJ { u32x4 a[3], w[3]; float x0, s[3], q[3], mb, den, rden, t0, t1, t2, t3; unsigned stat, key; bool three, up; };
DI float rcp_nr(float d) { const float r = __builtin_amdgcn_rcpf(d); return __builtin_fmaf(__builtin_fmaf(-d, r, 1.f), r, r); }
template <int CTRL> DI float dpp_add(float v) { return v + __int_as_float(__builtin_amdgcn_update_dpp(0, __float_as_int(v), CTRL, 0xf, 0xf, false)); }
template <int K> DI float red_step(float v) {
  if constexpr (K == 0) return dpp_add<0xB1>(v);
  else if constexpr (K == 1) return dpp_add<0x4E>(v);
  else if constexpr (K == 2) return dpp_add<0x141>(v);
  else if constexpr (K == 3) return dpp_add<0x140>(v);
  else { auto pr = __builtin_amdgcn_permlane16_swap(__float_as_uint(v), __float_as_uint(v), false, false); return __uint_as_float(pr[0]) + __uint_as_float(pr[1]); }
}
#define PIN2(a, b) asm volatile("" : "+v"(a), "+v"(b))
constexpr int LN_STAT = 34, LN_N0 = 40, LN_XN = LN_N0 + 96, LN_KEY = LN_XN + 7;
template <int I> DI void ln_step(LNJ& j, const unsigned row) {
  if constexpr (I == 0) {
    const float xa = bflo((unsigned)__builtin_amdgcn_readlane((int)j.a[0][0], 0)), xb = bflo((unsigned)__builtin_amdgcn_readlane((int)j.a[0][0], 32));
    j.x0 = j.up ? xb : xa;
    asm volatile("" : "+v"(j.x0));
  } else if constexpr (I <= 24) {
    constexpr int e = I - 1, c = e >> 3, i = (e >> 1) & 3;
    const float d = ((e & 1) ? bfhi(j.a[c][i]) : bflo(j.a[c][i])) - j.x0;
    if constexpr ((e & 7) == 0) { j.s[c] = d; j.q[c] = d * d; } else { j.s[c] += d; j.q[c] = __builtin_fmaf(d, d, j.q[c]); }
    PIN2(j.s[c], j.q[c]);
  } else if constexpr (I == 25) {
    j.t0 = (j.s[0] + j.s[1]) + (j.three ? j.s[2] : 0.f); j.t1 = (j.q[0] + j.q[1]) + (j.three ? j.q[2] : 0.f);
    PIN2(j.t0, j.t1);
  } else if constexpr (I <= 30) {
    j.t0 = red_step<I - 26>(j.t0); j.t1 = red_step<I - 26>(j.t1);
    PIN2(j.t0, j.t1);
  } else if constexpr (I == 31) {
    j.t0 *= 1.0f / DM; j.t1 = __builtin_fmaxf(__builtin_fmaf(-j.t0, j.t0, j.t1 * (1.0f / DM)), 0.f);
    PIN2(j.t0, j.t1);
  } else if constexpr (I == 32) {
    j.mb = rbf(j.x0 + j.t0); j.t1 = rbf(j.t1) + 1e-5f;
    PIN2(j.mb, j.t1);
  } else if constexpr (I == 33) {
    j.den = rbf(__builtin_amdgcn_sqrtf(rbf(j.t1)));
    j.stat = (__float_as_uint(j.mb) & 0xffff0000u) | (__float_as_uint(j.den) >> 16);
    PIN2(j.den, j.stat);
  } else if constexpr (I < LN_N0) {
    if constexpr (I == 34) { j.rden = rcp_nr(j.den); j.s[0] = 0.f; j.s[1] = 0.f; asm volatile("" : "+v"(j.rden)); }
  } else if constexpr (I < LN_XN) {
    constexpr int e = (I - LN_N0) >> 3, st = (I - LN_N0) & 7, c = e >> 2, i = e & 3;
    if constexpr (st == 0)      { j.t0 = bflo(j.a[c][i]); j.t1 = bfhi(j.a[c][i]); }
    else if constexpr (st == 1) { j.t0 -= j.mb; j.t1 -= j.mb; }
    else if constexpr (st == 2) { const unsigned pk = cvtpk(j.t0, j.t1); j.t0 = bflo(pk); j.t1 = __uint_as_float(pk); }
    else if constexpr (st == 3) { j.t0 *= j.rden; j.t1 = bfhi(__float_as_uint(j.t1)); }
    else if constexpr (st == 4) { unsigned pk = cvtpk(j.t0, j.t1 * j.rden); asm volatile("" : "+v"(pk)); j.a[c][i] = pk; }
    else if constexpr (st == 5) { j.t0 = bflo(j.a[c][i]); j.t1 = bflo(j.w[c][i]); }
    else if constexpr (st == 6) { j.s[0] = __builtin_fmaf(j.t0, j.t1, j.s[0]); j.t0 = bfhi(j.a[c][i]); asm volatile("" : "+v"(j.s[0])); }
    else                        { j.s[1] = __builtin_fmaf(j.t0, bfhi(j.w[c][i]), j.s[1]); asm volatile("" : "+v"(j.s[1])); }
    PIN2(j.t0, j.t1);
  } else if constexpr (I == LN_XN) {
    j.t0 = j.s[0] + j.s[1]; asm volatile("" : "+v"(j.t0));
  } else if constexpr (I < LN_XN + 6) {
    j.t0 = red_step<I - LN_XN - 1>(j.t0); asm volatile("" : "+v"(j.t0));
  } else if constexpr (I == LN_XN + 6) {
    const unsigned rb = f2bf(j.t0);
    const unsigned mono = (rb & 0x8000u) ? (~rb & 0xffffu) : (rb | 0x8000u);
    j.key = (mono << 16) | (0xffffu - row);
    asm volatile("" : "+v"(j.key));
  }
}

struct RTS { unsigned k[8], bad, m, cnd, lq, va, cv, rank; u32x4 g[4]; float rs; unsigned long long pm[2]; };
DI void cnd_s(unsigned& d, const unsigned a, const unsigned long long m) { asm volatile("v_cndmask_b32_e64 %0, %0, %1, %2" : "+v"(d) : "v"(a), "s"(m)); }
DI void cnt_s(unsigned& d, const unsigned long long m) { unsigned long long co; asm volatile("v_addc_co_u32_e64 %0, %1, 0, %0, %2" : "+v"(d), "=s"(co) : "s"(m)); }
constexpr int RT_SORT = 3, RT_POP = RT_SORT + 19, RT_END = RT_POP + 64;
template <int CTRL> DI unsigned dpp_umax(unsigned v) { const unsigned t = (unsigned)__builtin_amdgcn_update_dpp(0, (int)v, CTRL, 0xf, 0xf, false); return t > v ? t : v; }
template <int I> DI void rt_step(RTS& r, const unsigned tag) {
  constexpr int net[19][2] = {{0, 1}, {2, 3}, {4, 5}, {6, 7}, {0, 2}, {1, 3}, {4, 6}, {5, 7}, {1, 2}, {5, 6}, {0, 4}, {3, 7}, {1, 5}, {2, 6}, {1, 4}, {3, 6}, {2, 4}, {3, 5}, {3, 4}};
  if constexpr (I == 0) { r.bad = (r.g[0][1] ^ tag) | (r.g[0][3] ^ tag) | (r.g[1][1] ^ tag); asm volatile("" : "+v"(r.bad)); }
  else if constexpr (I == 1) { r.bad |= (r.g[1][3] ^ tag) | (r.g[2][1] ^ tag) | (r.g[2][3] ^ tag); asm volatile("" : "+v"(r.bad)); }
  else if constexpr (I == 2) {
    r.bad |= (r.g[3][1] ^ tag) | (r.g[3][3] ^ tag); r.cnd = 0u; r.cv = r.lq & 15u;
#pragma unroll
    for (int j = 0; j < 8; ++j) r.k[j] = r.g[j >> 1][2 * (j & 1)];
    asm volatile("" : "+v"(r.bad));
  } else if constexpr (I < RT_POP) { ce(r.k[net[I - RT_SORT][0]], r.k[net[I - RT_SORT][1]]); PIN2(r.k[net[I - RT_SORT][0]], r.k[net[I - RT_SORT][1]]); }
  else if constexpr (I < RT_END) {
    constexpr int n = (I - RT_POP) >> 3, p = (I - RT_POP) & 7;
    if constexpr (p == 0) r.m = dpp_umax<0xB1>(r.k[0]);
    else if constexpr (p == 1) { r.m = dpp_umax<0x4E>(r.m); asm volatile("v_cmp_eq_u32_e64 %0, %1, %2" : "=s"(r.pm[1]) : "i"(n), "v"(r.cv)); }
    else if constexpr (p == 2) r.m = dpp_umax<0x141>(r.m);
    else if constexpr (p == 3) r.m = dpp_umax<0x140>(r.m);
    else if constexpr (p == 4) { asm volatile("v_cmp_eq_u32_e64 %0, %1, %2" : "=s"(r.pm[0]) : "v"(r.m), "v"(r.k[0])); cnd_s(r.cnd, r.m, r.pm[1]); }
    else {
#pragma unroll
      for (int i = p == 5 ? 0 : 2 * p - 9; i < 2 * p - 7 && i < 7 - n; ++i) cnd_s(r.k[i], r.k[i + 1], r.pm[0]);
    }
    asm volatile("" : "+v"(r.m));
  }
}

constexpr unsigned PF_DUM = 0x27C00u;
#define KRING 0x0000u
#define VRING 0x8000u
constexpr unsigned SD_BASE = 0x10000u, MO_XR = 0x22000u, MO_H = 0x25200u, MO_C = 0x26400u;
constexpr unsigned RTB = 254u;
constexpr unsigned RED0 = 208u, RED1 = 244u;
constexpr unsigned MO_HH = 0x26C00u;
constexpr unsigned WF_FL = 0x27000u;
constexpr unsigned MO_RS = 0x26800u, MO_KEY = 0x20000u;
constexpr unsigned BP_W = 23552u;
constexpr size_t SHM_ATTN = 0x28000;
constexpr unsigned KRS = 256u, KTILEB = 64u * KRS;
constexpr unsigned LAH = 3u;
constexpr unsigned KMASK = SEQ * KRS - 1u;
constexpr unsigned WRS = HQ * DH * 2u;
constexpr unsigned XRS = DM * 2u;

#define W4_ACLOB_HI "a128","a129","a130","a131","a132","a133","a134","a135","a136","a137","a138","a139","a140","a141","a142","a143","a144","a145","a146","a147","a148","a149","a150","a151","a152","a153","a154","a155","a156","a157","a158","a159","a160","a161","a162","a163","a164","a165","a166","a167","a168","a169","a170","a171","a172","a173","a174","a175","a176","a177","a178","a179","a180","a181","a182","a183","a184","a185","a186","a187","a188","a189","a190","a191","a192","a193","a194","a195","a196","a197","a198","a199","a200","a201","a202","a203","a204","a205","a206","a207","a208","a209","a210","a211","a212","a213","a214","a215","a216","a217","a218","a219","a220","a221","a222","a223","a224","a225","a226","a227","a228","a229","a230","a231","a232","a233","a234","a235","a236","a237","a238","a239","a240","a241","a242","a243","a244","a245","a246","a247","a248","a249","a250","a251","a252","a253","a254","a255"
#define W4_ACLOB "a0","a1","a2","a3","a4","a5","a6","a7","a8","a9","a10","a11","a12","a13","a14","a15","a16","a17","a18","a19","a20","a21","a22","a23","a24","a25","a26","a27","a28","a29","a30","a31","a32","a33","a34","a35","a36","a37","a38","a39","a40","a41","a42","a43","a44","a45","a46","a47","a48","a49","a50","a51","a52","a53","a54","a55","a56","a57","a58","a59","a60","a61","a62","a63","a64","a65","a66","a67","a68","a69","a70","a71","a72","a73","a74","a75","a76","a77","a78","a79","a80","a81","a82","a83","a84","a85","a86","a87","a88","a89","a90","a91","a92","a93","a94","a95","a96","a97","a98","a99","a100","a101","a102","a103","a104","a105","a106","a107","a108","a109","a110","a111","a112","a113","a114","a115","a116","a117","a118","a119","a120","a121","a122","a123","a124","a125","a126","a127"

DI float max2f(float a, float b) { float r; asm("v_max_f32_e32 %0, %1, %2" : "=v"(r) : "v"(a), "v"(b)); return r; }
DI void lds_st32(unsigned addr, unsigned v) { asm volatile("ds_write_b32 %0, %1" :: "v"(addr), "v"(v) : "memory"); }

struct W4C {
  unsigned ksx[2];
  unsigned vkb[4], vvb[2], vksrc[2], vvsrc, vvsrc0, m0k, m0v, sofk, ldsb;
  __amdgpu_buffer_rsrc_t srdK, srdV, srdF; unsigned sfo;
  f32x4 S[2][4][4];
  f32x4 NEGM[4], L[4];
  u32x4 ONE; unsigned IDX;
  bf16x8 Vf[8][2];
  u32x4 Pk[4][2];
  bf16x4 vlo[8][2], vup[8][2];
  unsigned pm0[2], psf[2];
  __amdgpu_buffer_rsrc_t srdP; unsigned pfo, pfm0, pfv, pfst, pfend, pdum;
};

constexpr unsigned FCT = 0x27400u;
constexpr int AO_(int db, int qb) { return 32 * qb + 4 * db; }
constexpr int AQ_(int qb, int j) { return 128 + 16 * qb + 4 * j; }
constexpr int AK_(int kb, int j) { return 192 + 16 * kb + 4 * j; }

template <int I> DI float acc_read() { float r; asm volatile("v_accvgpr_read_b32 %0, a[%c1]" : "=v"(r) : "i"(I)); return r; }
template <int I> DI void acc_write(const unsigned v) { asm volatile("v_accvgpr_write_b32 a[%c1], %0" :: "v"(v), "i"(I)); }

template <int I, int VSLOT> DI void tr_rd(const W4C& c, bf16x4 (&lo)[8][2], bf16x4 (&up)[8][2]) {
  constexpr int db = I >> 2, k2 = (I >> 1) & 1, off = VSLOT + 8192 * k2 + 4096 * (I & 1) + 512 * (db >> 1);
  if constexpr ((I & 1) == 0) asm volatile("ds_read_b64_tr_b16 %0, %1 offset:%2" : "=v"(lo[db][k2]) : "v"(c.vvb[db & 1]), "i"(off) : "memory");
  else                        asm volatile("ds_read_b64_tr_b16 %0, %1 offset:%2" : "=v"(up[db][k2]) : "v"(c.vvb[db & 1]), "i"(off) : "memory");
}
template <int I, int VSLOT> DI void tr_read_(W4C& c) { tr_rd<I, VSLOT>(c, c.vlo, c.vup); }
template <int I, int SLOT> DI void k_read(W4C& c) {
  asm volatile("ds_read_b128 a[%c1:%c2], %0 offset:%c3" :: "v"(c.vkb[I & 3]), "i"(192 + 4 * I), "i"(195 + 4 * I), "i"(SLOT + 4096 * (I >> 2)) : "memory");
}
template <int KB, int QB, int J, int CM> DI void qk16(f32x4& s, const f32x4& negm) {
  constexpr int ak = AK_(KB, J), aq = AQ_(QB, J);
  if constexpr (CM == 2)      asm volatile("v_mfma_f32_16x16x32_bf16 %0, a[%c1:%c2], a[%c3:%c4], 0" : "=&v"(s) : "i"(ak), "i"(ak + 3), "i"(aq), "i"(aq + 3));
  else if constexpr (CM == 1) asm volatile("v_mfma_f32_16x16x32_bf16 %0, a[%c2:%c3], a[%c4:%c5], %1" : "=&v"(s) : "v"(negm), "i"(ak), "i"(ak + 3), "i"(aq), "i"(aq + 3));
  else                        asm volatile("v_mfma_f32_16x16x32_bf16 %0, a[%c1:%c2], a[%c3:%c4], %0" : "+v"(s) : "i"(ak), "i"(ak + 3), "i"(aq), "i"(aq + 3));
}
template <int X, int M> DI void qk_mfma(W4C& c) { constexpr int kb = M >> 4, qb = (M >> 2) & 3, j = (qb & 1) ? 3 - (M & 3) : (M & 3); qk16<kb, qb, j, ((M & 3) == 0)>(c.S[X][kb][qb], c.NEGM[qb]); }
template <int AO, bool ZERO> DI void pv16(const bf16x8& A, const u32x4& B) {
  if constexpr (ZERO) asm volatile("v_mfma_f32_16x16x32_bf16 a[%c2:%c3], %0, %1, 0" :: "v"(A), "v"(B), "i"(AO), "i"(AO + 3));
  else                asm volatile("v_mfma_f32_16x16x32_bf16 a[%c2:%c3], %0, %1, a[%c2:%c3]" :: "v"(A), "v"(B), "i"(AO), "i"(AO + 3));
}
template <bool ZERO> DI void sum16(f32x4& l, const u32x4& one, const u32x4& B) {
  if constexpr (ZERO) asm volatile("v_mfma_f32_16x16x32_bf16 %0, %1, %2, 0" : "=&v"(l) : "v"(one), "v"(B));
  else                asm volatile("v_mfma_f32_16x16x32_bf16 %0, %1, %2, %0" : "+v"(l) : "v"(one), "v"(B));
}
DI void sum64(W4C& c, const int qb, const u32x4 (&P)[2]) {
  const u32x8 B = __builtin_shufflevector(P[0], P[1], 0, 1, 2, 3, 4, 5, 6, 7);
  asm volatile("v_smfmac_f32_16x16x64_bf16 %0, %1, %2, %3" : "+v"(c.L[qb]) : "v"(c.ONE), "v"(B), "v"(c.IDX));
}
constexpr int NGB = 68;
template <int G, bool ZERO> DI void pv_mfma(W4C& c) {
  constexpr int qb = G / 17, r = G % 17, db = (r >> 1) & 7, k2 = (r ^ db) & 1;
  if constexpr (r < 16) pv16<AO_(db, qb), (ZERO && (r & 1) == 0)>(c.Vf[db][k2], c.Pk[qb][k2]);
  else sum64(c, qb, c.Pk[qb]);
}
DI void real_op() { GAP(); __builtin_amdgcn_s_setprio(0); GAP(); }
DI void add_v(float& acc, float x) { asm volatile("v_add_f32_e32 %0, %0, %1" : "+v"(acc) : "v"(x)); }
template <int X, int E> DI void exp_e(W4C& c) {
  GAP();
  const float e = __builtin_amdgcn_exp2f(c.S[X][E >> 4][(E >> 2) & 3][E & 3]);
  asm volatile("" :: "v"(e));
  c.S[X][E >> 4][(E >> 2) & 3][E & 3] = e;
  GAP();
}
template <int X, int U> DI void pack_to(W4C& c, u32x4 (&P)[4][2]) {
  constexpr int kb = U >> 3, qb = (U >> 1) & 3, h = U & 1;
  GAP();
  const unsigned w = cvtpk(c.S[X][kb][qb][2 * h], c.S[X][kb][qb][2 * h + 1]);
  asm volatile("" :: "v"(w));
  P[qb][kb >> 1][2 * (kb & 1) + h] = w;
  GAP();
}
template <int X, int U> DI void pack_e(W4C& c) { pack_to<X, U>(c, c.Pk); }

constexpr int NBE = 40;
constexpr char PA0[] = "EXETEXFTEXETEXFT" "EXETEXFTEXETEXFT" "EXXEXFEXXEXEXEXF" "ECCECFECCECECECF";
constexpr char PB2[] = "EFEKEEEEKEEFEKEES" "EEKEEDEKEEDEKEEDS" "EKEDEKEEEFEEEEEES" "EEEEEEEEEEEEEEEES";
constexpr char PB0[] = "EDEKEEDEKEEDEKFES" "DEKEEDEKFEDEKEEDS" "EKEDEKEEFDEEEFDES" "EEEEEFEEEEEFEEEES";
constexpr int pcnt(const char* p, char ch, int g) { int n = 0; for (int i = 0; i < g; ++i) n += p[i] == ch; return n; }
constexpr int ppos(const char* p, char ch, int n) { int i = 0; while (p[i] != ch || n-- > 0) ++i; return i; }
constexpr int FA(int n) { constexpr int g[12] = {6, 10, 14, 18, 22, 30, 37, 41, 47, 53, 57, 63}; return g[n]; }
constexpr int FB(int n) { constexpr int g[10] = {14, 25, 33, 42, 47, 50, 56, 59, 62, 67}; return g[n]; }
constexpr int fa_of(int m) { for (int n = 0; n < 12; ++n) if (FA(n) == m) return n; return -1; }
constexpr int fb_of(int m) { for (int n = 0; n < 10; ++n) if (FB(n) == m) return n; return -1; }
constexpr int DB(int n) { return ppos(PB0, 'D', n); }
constexpr int kyo(int g0, int g1) { const int n = 2 * (pcnt(PB0, 'K', g1) - pcnt(PB0, 'K', g0)); return n < 15 ? n : 15; }
static_assert(pcnt(PA0, 'E', 64) == 64 - NBE && pcnt(PB0, 'E', NGB) == NBE && pcnt(PB0, 'D', NGB) == 10 && pcnt(PB0, 'K', NGB) == 8 && FB(3) == DB(8) - 1 && FB(4) == DB(9) - 1 && PA0[FA(6)] == 'F' && PA0[FA(8)] == 'F', "");
template <int X, int M, typename H> DI void gapA(W4C& c, const int t, H& hook) {
  constexpr int Xp = X ^ 1;
  constexpr const char* pa = PA0;
  constexpr char ty = pa[M];
  constexpr int nt = 2 * pcnt(pa, 'T', M) + pcnt(pa, 'X', M), np = pcnt(pa, 'X', M) + 2 * pcnt(pa, 'C', M);
  if constexpr (M == 0) asm volatile("" ::: W4_ACLOB, W4_ACLOB_HI);
  qk_mfma<X, M>(c);
  hook(std::integral_constant<int, M>{});
  if constexpr (ty == 'T' || ty == 'X') tr_read_<nt, Xp * 0x4000>(c);
  if constexpr (ty == 'T') tr_read_<nt + 1, Xp * 0x4000>(c);
  if constexpr (ty == 'X' || ty == 'C') pack_e<Xp, np>(c);
  if constexpr (ty == 'C') pack_e<Xp, np + 1>(c);
  if constexpr (ty == 'E') exp_e<Xp, NBE + pcnt(pa, 'E', M)>(c);
  {
    if constexpr (M == FA(6))      { c.pm0[0] = c.m0k + (unsigned)X * 0x4000u; c.pm0[1] = c.m0v + (unsigned)(X ^ 1) * 0x4000u; asm volatile("" : "+s"(c.pm0[0]), "+s"(c.pm0[1])); }
    else if constexpr (M == FA(8)) asm volatile("s_mov_b32 %1, %0\n\ts_add_u32 %0, %0, %2\n\ts_and_b32 %0, %0, %3" : "+s"(c.psf[0]), "=&s"(c.psf[1]) : "i"(KTILEB), "i"(KMASK) : "scc");
  }
  hook(std::integral_constant<int, 100 + M>{});
  GAP();
}
#define W4_V8(a, k) "+v"(a[0][k]), "+v"(a[1][k]), "+v"(a[2][k]), "+v"(a[3][k]), "+v"(a[4][k]), "+v"(a[5][k]), "+v"(a[6][k]), "+v"(a[7][k])
template <int PFW> DI void phaseA_end(W4C& c) {
  asm volatile("s_waitcnt vmcnt(%16) lgkmcnt(0)" : W4_V8(c.vlo, 0), W4_V8(c.vlo, 1) : "i"(PFW) : "memory");
  asm volatile("" : W4_V8(c.vup, 0), W4_V8(c.vup, 1) : "v"(c.NEGM[0]), "v"(c.NEGM[1]), "v"(c.NEGM[2]), "v"(c.NEGM[3]) : "memory");
  GAP();
  __builtin_amdgcn_s_barrier();
  asm volatile("" ::: "memory");
  GAP();
#pragma unroll
  for (int k2 = 0; k2 < 2; ++k2)
#pragma unroll
    for (int db = 0; db < 8; ++db) c.Vf[db][k2] = __builtin_shufflevector(c.vlo[db][k2], c.vup[db][k2], 0, 1, 2, 3, 4, 5, 6, 7);
}
template <int X, int PFW, typename H, int... Ms> DI void phaseA_(W4C& c, const int t, H& hook, iseq<Ms...>) {
  (gapA<X, Ms>(c, t, hook), ...);
  phaseA_end<PFW>(c);
}
struct NoHook { template <typename T> DI void operator()(T) const {} };
template <int X, int PFW = 0> DI void phaseA(W4C& c, const int t) { NoHook h; phaseA_<X, PFW>(c, t, h, make_iseq<64>{}); }
template <int X, int PFW, typename H> DI void phaseAh(W4C& c, const int t, H& hook) { phaseA_<X, PFW>(c, t, hook, make_iseq<64>{}); }

template <int X, bool ZERO1, bool DK, int PF, int M, typename H> DI void gapB(W4C& c, H& hook) {
  constexpr const char* pb = DK ? PB0 : PB2;
  constexpr char ty = pb[M];
  constexpr int n = pcnt(pb, ty, M) + (!DK && ty == 'D' ? 4 : 0), d8 = DB(8), d9 = DB(9);
  pv_mfma<M, ZERO1>(c);
  if constexpr (DK && M == DB(0) - 1) asm volatile("s_mov_b32 m0, %0" :: "s"(c.pm0[0]) : "m0", "memory");
  if constexpr (M == DB(4) - 1) asm volatile("s_mov_b32 m0, %0" :: "s"(c.pm0[1]) : "m0", "memory");
  if constexpr (M == 57) asm volatile("s_waitcnt lgkmcnt(0)" ::: "memory");
  if constexpr (PF && (M == d8 - 1 || M == d9 - 1))
    asm volatile("s_cmp_lt_u32 %0, %1\n\ts_cselect_b32 m0, %0, %2\n\ts_add_u32 %0, %0, 0x400" : "+s"(c.pfm0) : "s"(c.pfend), "s"(c.pdum) : "m0", "memory", "scc");
  if constexpr (PF && (M == d8 - 2 || M == d9 - 2)) asm volatile("s_add_u32 %0, %0, %1" : "+s"(c.pfo) : "s"(c.pfst) : "scc");
  constexpr int lg = PF ? 56 : 40;
  if constexpr (X == 1 && M == lg - 5) asm volatile("s_add_u32 %0, %0, %1" : "+s"(c.sfo) : "i"(2 * KTILEB) : "scc");
  if constexpr (X == 1 && M == lg - 4) asm volatile("s_and_b32 %0, %0, %1" : "+s"(c.sfo) : "i"(KMASK) : "scc");
  if constexpr (X == 1 && M == lg - 2) asm volatile("s_mov_b32 m0, %0" :: "s"(c.pdum) : "m0", "memory");
  if constexpr (X == 1 && M == lg) asm volatile("buffer_load_dwordx4 %0, %1, %2 offen lds" :: "v"(c.vksrc[0]), "s"(c.srdF), "s"(c.sfo) : "memory");
  hook(std::integral_constant<int, M>{});
  if constexpr (ty == 'K') { k_read<2 * n, (X ^ 1) * 0x4000>(c); k_read<2 * n + 1, (X ^ 1) * 0x4000>(c); }
  if constexpr (ty == 'D') {
    if constexpr (n < 4) { asm volatile("buffer_load_dwordx4 %0, %1, %2 offen offset:%c3 lds" :: "v"((n & 2) ? c.ksx[n & 1] : c.vksrc[n & 1]), "s"(c.srdK),  "s"(c.psf[0]), "i"(1024 * (n & 3)) : "memory"); }
    else if constexpr (n < 8) asm volatile("buffer_load_dwordx4 %0, %1, %2 offen offset:%c3 lds" :: "v"((n & 1) ? c.vvsrc : c.vvsrc0), "s"(c.srdV),  "s"(c.psf[1]), "i"(1024 * (n & 3)) : "memory");
    else if constexpr (PF) asm volatile("buffer_load_dwordx4 %0, %1, %2 offen sc1 lds" :: "v"(c.pfv), "s"(c.srdP), "s"(c.pfo) : "memory");
  }
  if constexpr (ty == 'E') exp_e<X, n>(c);
  hook(std::integral_constant<int, 100 + M>{});
  GAP();
}
template <int X, bool ZERO1, bool DK, int PF, typename H, int... Ms> DI void phaseB_(W4C& c, H& hook, iseq<Ms...>) {
  GAP();
  (gapB<X, ZERO1, DK, PF, Ms>(c, hook), ...);
  hook(std::integral_constant<int, 72>{});
  GAP();
}
template <int X, int PF> DI void phaseB(W4C& c) { NoHook h; phaseB_<X, false, true, PF>(c, h, make_iseq<NGB>{}); }
template <int X, int PF, typename H> DI void phaseBh(W4C& c, H& hook) { phaseB_<X, false, true, PF>(c, hook, make_iseq<NGB>{}); }

constexpr unsigned QP_STG = 0xC000u, QP_W8 = 0x24000u, QP_CS = 0x18000u;
constexpr int PR0 = 1, PR1 = 8;
constexpr int PJ = 224;
constexpr int NJ = 200, JK2 = 79, JR0 = 103;
constexpr int JT[] = {1000,20004,20005,32000,1001,20006,20007,32000,31000,1002,22000,32000,1003,21000,21001,32000,1016,34000,21002,21003,32000,1017,34000,21004,21005,32000,1018,34000,21006,21007,30000,32000,16000,1019,
  34000,32000,16001,30001,1004,34000,20008,20009,32000,16002,1005,34000,20010,20011,32000,16003,1006,34000,20012,20013,32000,16004,1007,34000,20014,20015,32000,16005,1008,34000,32000,16006,1020,34000,32000,16007,1021,
  34000,32000,10000,10001,10002,10003,10004,10005,1022,34000,32000,10006,10007,10008,10009,10010,10011,1023,34000,32000,10012,10013,10014,10015,10016,10017,1024,34000,32000,10018,10019,10020,10021,10022,10023,1032,34000,
  32000,10024,10025,10026,1033,34000,32000,10027,10028,10029,10030,10031,1034,34000,32000,10032,10033,10034,10035,10036,10037,1035,34000,32000,10038,10039,10040,10041,10042,10043,30002,1036,34000,32000,10044,10045,10046,
  10047,10048,10049,1037,34000,32000,10050,10051,10052,10053,10054,10055,1038,34000,32000,10056,10057,10058,10059,10060,1039,34000,32000,10061,10062,10063,1040,34000,32000,10064,10065,10066,10067,10068,10069,1048,34000,
  20016,32000,10070,10071,1049,34000,20017,32000,10072,10073,10074,10075,10076,10077,1050,34000,20018,32000,10078,10079,10080,10081,10082,10083,1051,34000,20019,32000,10084,10085,10086,10087,10088,10089,1052,34000,20020,
  32000,10090,10091,10092,10093,10094,10095,1053,34000,20021,32000,10096,10097,10098,1054,34000,20022,32000,10099,10100,10101,10102,10103,1055,34000,20023,32000,10104,10105,10106,10107,1056,34000,32000,1064,34000,32000,
  1065,34000,32000,10108,10109,10110,10111,10112,10113,1066,34000,32000,10114,10115,10116,10117,10118,10119,1067,34000,32000,10120,10121,10122,10123,10124,10125,30003,1068,34000,32000,10126,10127,10128,10129,10130,10131,
  1069,34000,32000,10132,10133,10134,1070,34000,32000,10135,10136,10137,10138,10139,1071,34000,32000,10140,10141,10142,10143,1072,34000,20024,32000,1080,34000,20025,32000,1081,34000,20026,32000,10144,10145,10146,10147,
  10148,10149,1082,34000,20027,32000,10150,10151,10152,10153,10154,10155,1083,34000,20028,32000,10156,10157,10158,10159,10160,10161,1084,34000,20029,32000,10162,10163,10164,10165,10166,10167,1085,34000,20030,32000,10168,
  10169,10170,1086,34000,20031,32000,10171,10172,10173,10174,10175,1087,34000,32000,10176,10177,10178,10179,1088,34000,32000,1096,34000,32000,1097,34000,32000,10180,10181,10182,10183,10184,10185,31001,1098,34000,32000,
  10186,10187,10188,10189,10190,10191,1099,34000,32000,10192,10193,10194,10195,30004,1100,34000,23000,23001,32000,10196,10197,10198,10199,10200,1101,34000,23002,23003,32000,10201,10202,10203,10204,1102,34000,23004,23005,
  24000,32000,1103,34000,23006,23007,32000,10205,10206,1104,34000,23008,32000,10207,10208,10209,10210,10211,1112,34000,23009,32000,10212,10213,10214,10215,1113,34000,23010,32000,10216,10217,10218,10219,10220,10221,1114,
  34000,23011,32000,10222,10223,10224,10225,10226,10227,1115,34000,23012,32000,10228,10229,10230,10231,1116,34000,23013,32000,10232,10233,10234,10235,10236,10237,1117,34000,23014,32000,10238,10239,10240,10241,1118,34000,
  23015,32000,10242,10243,1119,34000,32000,10244,10245,10246,10247,10248,10249,1120,34000,32000,10250,10251,30005,2128,34000,24001,24002,32000,2132,34000,32000,10252,10253,2136,34000,32000,10254,10255,2140,34000,23100,
  32000,10256,2001,34000,32000,10257,10258,2005,34000,32000,10259,10260,31002,2009,34000,32000,10261,10262,2013,32000,2002,34000,23101,32000,10263,2003,33000,32000,2006,34000,32000,10264,10265,2007,33001,32000,2010,
  34000,32000,11000,11001,2014,34000,32000,11002,11016,11017,11003,11004,11005,11018,23102,11006,11007,11008,11009,11010,11011,11012,2080,34000,32000,11013,11014,2017,34000,32000,11019,2018,34000,32000,11020,11021,2019,
  34000,32000,11022,2096,34000,32000,11023,11024,2033,34000,32000,2034,34000,32000,11025,11026,2035,34000,32000,11027,11028,2112,34000,32000,11029,11030,2049,34000,32000,10266,10267,2050,34000,32000,10268,10269,2051,
  34000,32000,2087,34000,32000,10270,10271,2022,34000,32000,10272,10273,2021,34000,32000,10274,10275,2020,34000,32000,10276,2103,34000,32000,10277,2038,32000,2037,34000,32000,10278,2036,34000,32000,10279,2119,34000,
  32000,10280,10281,2054,34000,32000,10282,10283,2053,34000,32000,10284,10285,2052,32000,10286,10287,24003,24004,2011,32000,13000,2015,34000,23103,24005,32000,13001,13002,11032,11033,11034,13003,11048,11035,11036,11037,
  11049,11050,11038,11039,11040,11041,11042,11043,11044,2088,34000,32000,11045,11046,2025,34000,32000,11051,2026,34000,32000,11052,11053,2027,34000,32000,11054,2104,34000,32000,11055,11056,2041,34000,32000,11057,11058,
  2042,34000,32000,11059,11060,2043,34000,32000,11061,11062,2120,32000,13016,2057,34000,24006,32000,15000,2058,32000,13017,2059,32000,13018,2095,34000,24007,32000,15001,2030,32000,13019,2029,32000,13032,2028,34000,23104,
  32000,15008,2111,34000,23105,32000,15009,2046,34000,23106,23107,32000,2045,34000,24008,24009,32000,2044,34000,23108,23109,32000,2127,34000,23110,23111,32000,2062,34000,24010,24011,32000,2061,32000,13033,2060,34000,
  23112,32000,15016,30105,3064,34000,23113,23114,32000,3001,34000,23115,24012,32000,3002,32000,13034,3003,34000,24013,24014,32000,3071,32000,13035,3006,32000,13048,3005,34000,24015,32000,15017,3004,32000,13049,3072,
  32000,13050,3009,34000,24016,32000,15024,3010,32000,13051,3011,32000,13004,3079,34000,24017,32000,15025,3014,32000,13005,3013,32000,13006,3012,34000,24018,32000,15002,3080,32000,13007,3017,32000,13020,3018,34000,24019,
  32000,15003,3019,32000,13021,3087,32000,13022,3022,34000,24020,32000,15010,3021,32000,13023,3020,32000,13036,3088,34000,24021,32000,15011,3025,32000,13037,3026,32000,13038,3027,34000,24022,32000,15018,3095,32000,13039,
  3030,32000,13052,3029,34000,24023,32000,15019,3028,32000,13053,3096,32000,13054,3033,34000,24024,32000,15026,30102,3034,32000,13055,3035,32000,13008,3103,34000,24025,32000,15027,3038,32000,13009,3037,32000,13010,3036,
  34000,24026,32000,15004,3104,32000,13011,3041,32000,13024,3042,34000,24027,32000,15005,3043,32000,13025,3111,32000,13026,3046,34000,24028,32000,15012,3045,32000,13027,3044,32000,13040,3112,34000,24029,32000,15013,3049,
  32000,13041,3050,32000,13042,3051,34000,24030,32000,15020,3119,32000,13043,3054,34000,24031,32000,15021,3053,32000,13056,3052,32000,13057,3120,32000,13058,3057,32000,13059,3058,32000,13012,3059,32000,13013,3127,34000,
  32000,15028,15029,3062,32000,13014,3061,32000,13015,3060,34000,32000,15006,13028,13029,13030,15007,13031,13044,15014,13045,13046,15015,13047,13060,15022,13061,13062,15023,13063,15030,15031,};
constexpr short JS[] = {0,4,8,12,16,21,26,33,37,44,50,56,62,66,70,79,88,97,106,112,120,129,138,148,157,165,171,180,186,196,206,216,226,233,242,250,253,256,265,274,283,293,299,307,314,318,322,332,342,352,362,369,378,385,
  388,391,400,410,417,428,437,443,450,459,467,477,487,495,505,513,519,528,533,539,544,549,554,559,564,570,572,577,580,585,588,593,611,616,620,625,629,634,637,642,647,652,657,662,665,670,675,680,684,688,690,694,698,703,
  708,713,719,722,746,751,755,760,764,769,774,779,784,787,792,795,798,803,806,809,814,819,824,829,834,839,844,847,852,858,863,866,871,874,877,882,885,888,893,896,899,904,907,910,915,918,921,926,929,932,937,940,943,948,
  951,954,959,962,965,970,973,976,981,985,988,993,996,999,1004,1007,1010,1015,1018,1021,1026,1029,1032,1037,1040,1043,1048,1051,1056,1059,1062,1065,1068,1071,1074,1079,1082,1085,1108,};
enum : int { VK_CW, VK_GEN, VK_S, VK_W8, VK_A, VK_WR, VK_STS, VK_GAT, VK_XNS, VK_KV, VK_KEY, VK_V1, VK_K2, VK_PF };
struct VOp { int kind, st, q, pos; };
struct VTab { VOp v[192]; int n; };
constexpr VTab mk_vtab() {
  VTab t{}; int n = 0;
  auto add = [&](int kind, int st, int q, int pos) { t.v[n].kind = kind; t.v[n].st = st; t.v[n].q = q; t.v[n].pos = pos; ++n; };
  const int PS[12] = {7, 10, 12, 15, 18, 20, 23, 26, 28, 31, 33, 36}, P3[12] = {20, 22, 24, 26, 28, 30, 32, 34, 36, 38, 40, 42}, P4[12] = {12, 14, 16, 19, 21, 23, 26, 28, 30, 32, 34, 36};
  for (int q = 0; q < 12; ++q) add(VK_S, 0, q, -1);
  for (int q = 0; q < 3; ++q) add(VK_A, 0, q, -1);
  add(VK_GEN, 0, 0, -1); add(VK_CW, 0, 0, -1);
  for (int q = 0; q < 12; ++q) add(VK_S, 1, q, q < 8 ? -1 : q - 8);
  for (int q = 0; q < 12; ++q) add(VK_S, 2, q, 4 + q);
  add(VK_STS, 0, 0, 12);
  for (int q = 0; q < 3; ++q) add(VK_WR, 0, q, 16 + q);
  for (int s = 3; s < 9; ++s) for (int q = 0; q < 12; ++q) add(s == 8 && q < 4 ? VK_W8 : VK_S, s, q, 32 * (s - 3) + (s == 3 ? P3[q] : s == 4 ? P4[q] : PS[q]));
  add(VK_GAT, 0, 0, 128 + 9); add(VK_GAT, 0, 1, 128 + 14);
  add(VK_XNS, 0, 0, 160 + 22); add(VK_XNS, 0, 1, 160 + 25); add(VK_XNS, 0, 2, 160 + 30);
  for (int q = 0; q < 12; ++q) add(VK_KV, 0, q, 192 + PS[q]);
  add(VK_KEY, 0, 0, PJ + 57);
  for (int q = 0; q < 4; ++q) add(VK_V1, 0, q, PJ + 60 + 3 * q);
  for (int q = 0; q < 4; ++q) add(VK_K2, 0, q, PJ + JK2 + 6 * q);
  add(VK_PF, 0, 0, PJ + JR0); add(VK_PF, 0, 1, PJ + JR0 + 6);
  for (int i = 1; i < n; ++i) { const VOp x = t.v[i]; int j = i; while (j > 0 && t.v[j - 1].pos > x.pos) { t.v[j] = t.v[j - 1]; --j; } t.v[j] = x; }
  t.n = n;
  return t;
}
constexpr VTab VT = mk_vtab();
constexpr int vq_first(int pos) { int i = 0; while (i < VT.n && VT.v[i].pos < pos) ++i; return i; }
constexpr int vq_wait(int kind, int st, int pos) {
  int last = -1;
  for (int i = 0; i < VT.n; ++i) if (VT.v[i].kind == kind && VT.v[i].st == st) last = i;
  return vq_first(pos) - 1 - last;
}
constexpr int imin(int a, int b) { return a < b ? a : b; }
constexpr int vq_grp(int i) {
  const VOp o = VT.v[i];
  return o.kind == VK_S ? 3 * o.st + (o.q >> 2) : o.kind == VK_W8 ? 40 : o.kind == VK_KV ? 41 + (o.q >> 2) : o.kind == VK_V1 ? 44 : o.kind == VK_K2 ? 45 : o.kind == VK_PF ? 50 + o.q : -1;
}
constexpr bool vq_newm0(int i) { int j = i - 1; while (j >= 0 && vq_grp(j) < 0) --j; return j < 0 || vq_grp(j) != vq_grp(i); }
template <bool NEWM0, int OFF> DI void lds_dma(const unsigned m0, const unsigned vo, const __amdgpu_buffer_rsrc_t srd, const unsigned so) {
  if constexpr (NEWM0) asm volatile("s_mov_b32 m0, %0\n\ts_nop 0\n\tbuffer_load_dwordx4 %1, %2, %3 offen offset:%c4 lds" :: "s"(m0), "v"(vo), "s"(srd), "s"(so), "i"(OFF) : "m0", "memory");
  else                 asm volatile("buffer_load_dwordx4 %0, %1, %2 offen offset:%c3 lds" :: "v"(vo), "s"(srd), "s"(so), "i"(OFF) : "memory");
}
template <int I0, typename F, int... Is> DI void vq_run(F& vm, iseq<Is...>) { (vm(std::integral_constant<int, I0 + Is>{}), ...); }
template <int POS, typename F> DI void vq_issue(F& vm) { vq_run<vq_first(POS)>(vm, make_iseq<vq_first(POS + 1) - vq_first(POS)>{}); }

constexpr int AW_(int T, int db, int ks) { return ((T & 1) ? 192 : 0) + 16 * db + 4 * ks; }
constexpr int AX_(int T, int qb, int ks) { return ((T & 1) ? 96 : 64) + 16 * qb + 4 * ks; }
template <int T, int DB, int QB, int KS> DI void qp_mfma(f32x16& d) {
  constexpr int aw = AW_(T, DB, KS), ax = AX_(T, QB, KS);
  if constexpr (T == 0 && KS == 0) asm volatile("v_mfma_f32_32x32x16_bf16 %0, a[%c1:%c2], a[%c3:%c4], 0" : "=&v"(d) : "i"(aw), "i"(aw + 3), "i"(ax), "i"(ax + 3));
  else                             asm volatile("v_mfma_f32_32x32x16_bf16 %0, a[%c1:%c2], a[%c3:%c4], %0" : "+v"(d) : "i"(aw), "i"(aw + 3), "i"(ax), "i"(ax + 3));
}
struct QP {
  f32x16 A[4][2];
  bf16x4 cl[2][4], cu[2][4];
  f32x4 cs; u32x4 sel;
  unsigned vwb, vwc, vw[4], vx[4], vxb[4], m0w, m0x, sofw, sofx, kk, sw, sx;
  __amdgpu_buffer_rsrc_t srdW, srdX;
};
template <int S> DI void qp_next(QP& p) {
  constexpr int d = S < PR0 || S > PR1 ? 0 : ((S - PR0) & 1) ? -1 : 1;
  const unsigned k = (unsigned)S + (d == 0 ? 0u : d > 0 ? p.kk : 0u - p.kk);
  p.sw = p.sofw + (k << 19); p.sx = p.sofx + (k << 7);
  asm volatile("" : "+s"(p.sw), "+s"(p.sx));
}
constexpr unsigned qp_wbase(int T) { return T == 8 ? QP_W8 : QP_STG * (T % 3); }
template <int T, int DB, int KS, int U> DI void qp_tr(QP& p) {
  asm volatile("ds_read_b64_tr_b16 a[%c1:%c2], %0 offset:%c3" :: "v"(p.vwb + qp_wbase(T)), "i"(AW_(T, DB, KS) + 2 * U), "i"(AW_(T, DB, KS) + 2 * U + 1), "i"(4096 * KS + 2048 * U + 512 * DB) : "memory");
}
template <int T, int QB, int KS> DI void qp_xr(QP& p) {
  asm volatile("ds_read_b128 a[%c1:%c2], %0 offset:%c3" :: "v"(p.vxb[KS] + QP_STG * (T % 3)), "i"(AX_(T, QB, KS)), "i"(AX_(T, QB, KS) + 3), "i"(4096 * QB) : "memory");
}
template <int T, int I> DI void qp_csr(QP& p) {
  constexpr int ks = I >> 1;
  if constexpr ((I & 1) == 0) asm volatile("ds_read_b64_tr_b16 %0, %1 offset:%2" : "=v"(p.cl[T == 8][ks]) : "v"(p.vwc + qp_wbase(T)), "i"(4096 * ks) : "memory");
  else                        asm volatile("ds_read_b64_tr_b16 %0, %1 offset:%2" : "=v"(p.cu[T == 8][ks]) : "v"(p.vwc + qp_wbase(T)), "i"(4096 * ks + 2048) : "memory");
}
template <int KS, bool ZERO, int L = 0> DI void qp_csd(QP& p) {
  const bf16x8 w = __builtin_shufflevector(p.cl[L][KS], p.cu[L][KS], 0, 1, 2, 3, 4, 5, 6, 7);
  if constexpr (ZERO) asm volatile("v_mfma_f32_16x16x32_bf16 %0, %1, %2, 0" : "=&v"(p.cs) : "v"(w), "v"(p.sel));
  else                asm volatile("v_mfma_f32_16x16x32_bf16 %0, %1, %2, %0" : "+v"(p.cs) : "v"(w), "v"(p.sel));
}
template <int T, int R> DI void qp_rd(QP& p) {
  if constexpr (R < 8) qp_csr<T, R>(p);
  else {
    constexpr int ks = (R - 8) / 10, u = (R - 8) % 10;
    if constexpr (u == 0) qp_xr<T, 0, ks>(p);
    else if constexpr (u == 3) qp_xr<T, 1, ks>(p);
    else { constexpr int v = u < 3 ? u - 1 : u - 2; qp_tr<T, (v >> 1), ks, (v & 1)>(p); }
  }
}
template <int T, int R0, int... Rs> DI void qp_rds(QP& p, iseq<Rs...>) { (qp_rd<T, R0 + Rs>(p), ...); }
#define QP_CSV(L) "+v"(p.cl[L][0]), "+v"(p.cl[L][1]), "+v"(p.cl[L][2]), "+v"(p.cl[L][3]), "+v"(p.cu[L][0]), "+v"(p.cu[L][1]), "+v"(p.cu[L][2]), "+v"(p.cu[L][3])
constexpr int LN_P0 = 32 + 18, LN_P1 = 160 + 21;
constexpr int ln_slots(int pos) { int n = 0; for (int q = LN_P0; q < pos; ++q) if (vq_first(q + 1) == vq_first(q) && (q & 31) != 5) ++n; return n; }
constexpr int ln_lo(int pos) {
  if (pos <= 1) return 0;
  if (pos <= 9) return 3 * (pos - 1);
  if (pos <= 11) return 24 + 4 * (pos - 9);
  if (pos < LN_P0) return LN_N0;
  if (pos <= LN_P1) return LN_N0 + (ln_slots(pos) * 97) / ln_slots(LN_P1);
  const int s = LN_XN + 1 + (pos - LN_P1) / 3;
  return s < LN_KEY ? s : LN_KEY;
}
template <int I0, int... Is> DI void ln_steps(LNJ& j, const unsigned row, iseq<Is...>) { (ln_step<I0 + Is>(j, row), ...); }
template <int T, int M, typename FV, typename FH> DI void qp_gap(QP& p, FV& vm, FH& hook, LNJ& lj, const unsigned lrow) {
  constexpr int ks = M >> 3, db = (M >> 1) & 3, qb = M & 1, pos = 32 * T + M, BG = T == 0 ? 12 : 6;
  if constexpr (T == 0 && M == 0) asm volatile("s_waitcnt lgkmcnt(10)" ::: "memory", W4_ACLOB, W4_ACLOB_HI);
  if constexpr (T == 0 && M == 8) asm volatile("s_waitcnt lgkmcnt(0)" : QP_CSV(0) :: "memory");
  if constexpr (T > 0 && M == 0) asm volatile("s_waitcnt lgkmcnt(0)" ::: "memory");
  if constexpr (M == BG) {
    constexpr int w0 = vq_wait(VK_S, T + 1, pos);
    constexpr int w = T == 1 ? imin(w0, vq_wait(VK_WR, 0, pos)) : T == 6 ? imin(w0, vq_wait(VK_W8, 8, pos)) : w0;
    asm volatile("s_waitcnt vmcnt(%0)\n\ts_barrier" :: "i"(w) : "memory");
  }
  qp_mfma<T, db, qb, ks>(p.A[db][qb]);
  if constexpr (T == 0) {
    if constexpr (M < 5) qp_rds<0, 28 + 4 * M>(p, make_iseq<4>{});
    if constexpr (M == 5 || M == 6) qp_rds<0, 4 * (M - 5)>(p, make_iseq<4>{});
    if constexpr (M == 18) asm volatile("s_waitcnt lgkmcnt(10)" : QP_CSV(0) :: "memory");
    if constexpr (M >= 12 && M < 28) qp_rds<1, 3 * (M - 12)>(p, make_iseq<3>{});
  } else {
    if constexpr (M == 13) asm volatile("s_waitcnt lgkmcnt(%8)" : QP_CSV(0) : "i"(T == 6 ? 10 : 6) : "memory");
    if constexpr (T == 6 && M == 17) asm volatile("s_waitcnt lgkmcnt(6)" : QP_CSV(1) :: "memory");
    if constexpr (M >= 6 && M < 30) qp_rds<T + 1, 2 * (M - 6)>(p, make_iseq<2>{});
    if constexpr (T == 6 && M >= 6 && M < 14) qp_csr<8, M - 6>(p);
    if constexpr (T == 6 && M >= 30) { qp_tr<8, 0, M - 30, 0>(p); qp_tr<8, 0, M - 30, 1>(p); }
  }
  vq_issue<pos>(vm);
  if constexpr (pos == 3 || pos == 19 || pos == 32 + 11 || (M == 5 && T >= 2 && T <= 5)) qp_next<(pos == 3 ? 2 : pos == 19 ? 3 : T + 3)>(p);
  GAP();
  hook(std::integral_constant<int, pos>{});
  ln_steps<ln_lo(pos)>(lj, lrow, make_iseq<ln_lo(pos + 1) - ln_lo(pos)>{});
  if constexpr (T == 0 && M >= 8 && M < 12) qp_csd<M - 8, M == 8>(p);
  if constexpr (T == 0 && M >= 18 && M < 28 && M % 3 == 0) qp_csd<(M - 18) / 3, false>(p);
  if constexpr (T > 0 && M >= 13 && M < 23 && M % 3 == 1) qp_csd<(M - 13) / 3, false>(p);
  if constexpr (T == 6 && (M == 17 || M == 21 || M == 24 || M == 27)) qp_csd<(M == 17 ? 0 : (M - 18) / 3), false, 1>(p);
  GAP();
}
template <int T, typename FV, typename FH, int... Ms> DI void qp_gaps(QP& p, FV& vm, FH& hook, LNJ& lj, const unsigned lrow, iseq<Ms...>) { (qp_gap<T, Ms>(p, vm, hook, lj, lrow), ...); }
template <int T, typename FV, typename FH> DI void qp_iter(QP& p, FV& vm, FH& hook, LNJ& lj, const unsigned lrow) { qp_gaps<T>(p, vm, hook, lj, lrow, make_iseq<32>{}); }
template <typename FV, typename FH, int... Ts> DI void qp_iters(QP& p, FV& vm, FH& hook, LNJ& lj, const unsigned lrow, iseq<Ts...>) { (qp_iter<Ts>(p, vm, hook, lj, lrow), ...); }

struct QFT { float a[2], cv[4], r1, m[4], m2[4]; unsigned w[8]; u32x4 ca[4], nb[2]; };
template <int N> DI void q_fin(QP& p, QFT& t) {
  constexpr int tau = N / 36, n = N % 36, h = n / 12, u = n % 12, db = tau >> 1, qb = tau & 1;
  f32x16& d = p.A[db][qb];
  if constexpr (n >= 28) acc_write<AQ_(2 * qb + ((n - 28) >> 2), db) + (n & 3)>(t.w[n - 28]);
  else if constexpr (n >= 24) { GAP(); auto pr = __builtin_amdgcn_permlane16_swap(t.w[u], t.w[4 + u], false, false); t.w[u] = pr[0]; t.w[4 + u] = pr[1]; asm volatile("" :: "v"(t.w[u]), "v"(t.w[4 + u])); GAP(); }
  else if constexpr (u < 8) { float v = d[8 * h + u]; asm volatile("v_mul_f32_e32 %0, %0, %1" : "+v"(v) : "v"(t.a[qb])); d[8 * h + u] = v; }
  else { GAP(); t.w[4 * h + u - 8] = cvtpk(d[8 * h + 2 * (u - 8)], d[8 * h + 2 * (u - 8) + 1]); asm volatile("" :: "v"(t.w[4 * h + u - 8])); GAP(); }
}
template <int Q, int I> DI void ref_step(W4C& c, QFT& t) {
  f32x4& s = c.S[0][0][Q];
  if constexpr (I == 0) asm volatile("v_max3_f32 %0, %1, %2, %3" : "=v"(t.m[Q]) : "v"(s[0]), "v"(s[1]), "v"(s[2]));
  else if constexpr (I == 1) asm volatile("v_max_f32_e32 %0, %0, %1" : "+v"(t.m[Q]) : "v"(s[3]));
  else if constexpr (I == 2 || I == 5) asm volatile("v_mov_b32_e32 %0, %1" : "=v"(t.m2[Q]) : "v"(t.m[Q]));
  else if constexpr (I == 3) asm volatile("v_permlane16_swap_b32_e32 %0, %1" : "+v"(t.m[Q]), "+v"(t.m2[Q]));
  else if constexpr (I == 4) asm volatile("v_max_f32_e32 %0, %0, %1" : "+v"(t.m[Q]) : "v"(t.m2[Q]));
  else if constexpr (I == 6) asm volatile("v_permlane32_swap_b32_e32 %0, %1" : "+v"(t.m[Q]), "+v"(t.m2[Q]));
  else if constexpr (I < 11) { float n; asm volatile("v_min_f32_e64 %0, -%1, -%2" : "=v"(n) : "v"(t.m[Q]), "v"(t.m2[Q])); c.NEGM[Q][I - 7] = n; }
  else { float v = s[I - 11]; add_v(v, c.NEGM[Q][0]); s[I - 11] = v; }
}
template <int J, int CODE, typename FV> DI void qj_op(QP& p, W4C& c, QFT& t, FV& vm, const unsigned csa) {
  constexpr int kind = CODE / 1000, a = CODE % 1000;
  if constexpr (kind == 1) {
    constexpr int tau = a >> 4, u = a & 15, db = tau >> 1, qb = tau & 1;
    if constexpr (u < 8) qp_mfma<7 + (u >> 2), db, qb, (u & 3)>(p.A[db][qb]);
    else asm volatile("v_mfma_f32_32x32x16_bf16 %0, %1, %2, %0" : "+v"(p.A[db][qb]) : "v"(t.ca[db]), "v"(t.nb[qb]));
  } else if constexpr (kind == 2 || kind == 3) { constexpr int kb = (a >> 4) & 3, qb = (a >> 2) & 3; qk16<kb, qb, (a & 3), (a >> 6)>(c.S[kind - 2][kb][qb], c.NEGM[qb]); }
  else if constexpr (kind == 10) q_fin<a>(p, t);
  else if constexpr (kind == 11) ref_step<(a >> 4), (a & 15)>(c, t);
  else if constexpr (kind == 13) exp_e<0, a>(c);
  else if constexpr (kind == 15) pack_e<0, a>(c);
  else if constexpr (kind == 16) {
    constexpr int d = a >> 1;
    if constexpr ((a & 1) == 0) { t.r1 = t.cv[d] - rbf(t.cv[d]); t.ca[d][0] = cvtpk(t.cv[d], t.r1); asm volatile("" : "+v"(t.r1), "+v"(t.ca[d])); }
    else { t.ca[d][1] = cvtpk(t.r1 - bfhi(t.ca[d][0]), 0.f); asm volatile("" : "+v"(t.ca[d])); }
  } else if constexpr (kind == 20) qp_tr<8, (a >> 3), ((a >> 1) & 3), (a & 1)>(p);
  else if constexpr (kind == 21) qp_xr<8, (a >> 2), (a & 3)>(p);
  else if constexpr (kind == 22)
    asm volatile("ds_read_b32 %0, %4\n\tds_read_b32 %1, %4 offset:128\n\tds_read_b32 %2, %4 offset:256\n\tds_read_b32 %3, %4 offset:384"
                 : "=&v"(t.cv[0]), "=&v"(t.cv[1]), "=&v"(t.cv[2]), "=&v"(t.cv[3]) : "v"(csa) : "memory");
  else if constexpr (kind == 23) k_read<a % 100, (a >= 100 ? 0x4000 : 0)>(c);
  else if constexpr (kind == 24) tr_read_<a, 0>(c);
  else if constexpr (kind == 30) {
    if constexpr (a == 0) asm volatile("s_waitcnt lgkmcnt(8)" : "+v"(t.cv[0]), "+v"(t.cv[1]), "+v"(t.cv[2]), "+v"(t.cv[3]) :: "memory");
    else if constexpr (a == 200) asm volatile("s_nop 0");
    else asm volatile("s_waitcnt lgkmcnt(%0)" :: "i"(a >= 100 ? a - 100 : 0) : "memory");
  } else if constexpr (kind == 31) {
    if constexpr (a == 0) asm volatile("s_waitcnt vmcnt(%0) lgkmcnt(6)\n\ts_barrier" :: "i"(vq_wait(VK_S, 8, PJ + J)) : "memory");
    else if constexpr (a == 1) asm volatile("s_waitcnt vmcnt(%0)\n\ts_barrier" :: "i"(vq_wait(VK_KV, 0, PJ + J)) : "memory");
    else asm volatile("s_barrier" ::: "memory");
  } else if constexpr (kind == 32) { vq_issue<PJ + J>(vm); GAP(); }
  else if constexpr (kind == 34) real_op();
  else if constexpr (kind == 33) {
    if constexpr (a == 0) { c.pm0[0] = c.m0k + 0x4000u; c.psf[0] = (KTILEB * 3u + c.sofk) & KMASK; asm volatile("" : "+s"(c.pm0[0]), "+s"(c.psf[0])); }
    else { c.pm0[1] = c.m0v; c.psf[1] = (KTILEB * 2u + c.sofk) & KMASK; asm volatile("" : "+s"(c.pm0[1]), "+s"(c.psf[1])); }
  }
}
template <int J, typename FV, int... Is> DI void qj_gap(QP& p, W4C& c, QFT& t, FV& vm, const unsigned csa, iseq<Is...>) { (qj_op<J, JT[JS[J] + Is]>(p, c, t, vm, csa), ...); GAP(); }
template <typename FV, int... Js> DI void qj_gaps(QP& p, W4C& c, QFT& t, FV& vm, const unsigned csa, iseq<Js...>) { (qj_gap<Js>(p, c, t, vm, csa, make_iseq<JS[Js + 1] - JS[Js]>{}), ...); }

constexpr unsigned XRS_ = DM * 2u;
constexpr int op_vr(int f) { return f < 4 ? f : f < 9 ? f : f < 13 ? f - 9 : f - 4; }
constexpr int op_ar(int f) { return f < 32 ? 72 + 4 * (f - 26) : f < 64 ? (((f >> 2) & 1) ? 128 : 192) + 16 * ((f - 32) >> 3) + 4 * (f & 3) : 96 + 4 * (f - 64); }
constexpr int OP_LA = 4, OP_SF = 22, OP_SP = 25, OP_SD = 29, OP_FEW = 8, OP_NR = 27, OP_FXW = 24, OP_RING = 5;
constexpr short OPU[] = {1,2,3,0,9,10,11,8,5,6,7,4,13,14,15,12,17,18,19,16,21,22,23,20,25,29,26,30,27,31,24,28,5,6,7,11,9,10,13,14,15,3,1,2,17,18,19,23,21,22,25,29,26,30,27,31,0,8,12,16,20,24,28,4,};
constexpr short OPW[] = {19,21,23,25,18,18,18,17,16,14,12,12,12,12,12,12,12,14,16,17,15,14,12,13,13,13,13,13,13,14,9,5,};
constexpr short TT[] = {0,0,9059,0,0,8000,0,0,0,0,8001,0,0,0,9060,0,0,8002,0,0,0,0,8003,0,0,0,0,9061,0,8000,0,0,0,0,8001,0,0,0,0,0,8002,0,9062,0,0,0,8003,0,0,0,13004,13005,0,0,8000,0,9063,0,13006,13007,0,0,8001,0,0,0,0,
  0,8002,0,9064,0,0,0,8003,0,0,0,0,0,8000,0,0,9065,0,0,8001,0,0,0,0,0,8002,0,0,9066,0,0,8003,0,15000,0,0,0,0,8000,0,0,0,9067,0,8001,0,0,0,0,0,8002,0,0,0,0,8003,0,9068,0,0,0,0,8000,0,0,0,0,8001,0,9069,0,0,0,0,8002,0,0,0,
  0,8003,0,9070,0,0,0,0,8000,0,0,0,0,8001,0,0,0,9071,0,0,8002,0,0,0,0,8003,0,0,0,0,0,8000,0,0,0,0,8001,0,0,0,0,0,8002,0,0,0,0,8003,0,0,0,11000,0,11001,0,8000,0,11002,0,11003,0,11004,0,8001,0,11005,0,0,11006,0,11007,0,
  8002,0,11008,0,11009,0,11010,0,8003,0,11011,0,0,11012,0,11013,0,8000,0,11014,0,11015,0,11016,0,8001,0,11017,0,0,11018,0,11019,0,8002,0,11020,0,11021,0,11022,0,8003,0,11023,0,0,11024,0,11025,0,8000,0,11026,0,11027,0,
  11028,0,8001,0,11029,0,0,11030,0,11031,0,8002,0,11032,0,11033,0,11034,0,8003,0,11035,0,0,11036,0,11037,0,8000,0,11038,0,11039,0,11040,0,8001,0,11041,0,0,11042,0,11043,0,8002,0,11044,0,11045,0,11046,0,8003,0,11047,0,0,
  11048,0,11049,0,8000,0,11050,0,11051,0,11052,0,8001,0,1000,0,11053,0,11054,0,1001,0,8002,0,11055,0,1004,0,11056,0,8003,0,1002,0,0,11057,0,1003,0,8000,0,11058,0,11059,0,11060,0,8001,0,1005,0,0,11061,0,1006,0,8002,0,
  11062,0,1009,0,11063,0,8003,0,1007,0,0,11064,0,1008,0,8000,0,11065,0,11066,0,11067,0,8001,0,1010,0,0,11068,0,1011,0,8002,0,11069,0,1014,0,11070,0,8003,0,1012,0,11071,0,11072,0,1013,0,8000,0,11073,3000,0,4000,0,4105,0,
  8001,0,4210,0,4301,0,4406,0,11074,0,8002,5004,0,6000,6001,0,6002,6003,5103,0,4011,6100,0,8003,0,6101,6102,0,6103,5203,0,4104,6200,0,6201,6202,0,8000,5303,0,6203,6304,0,7000,4209,6305,0,6306,6307,0,8001,5403,0,4314,
  6404,0,6405,0,6406,6407,5003,0,4402,6004,0,8002,0,6005,6006,5103,0,6007,6116,0,7001,4007,6117,0,8003,0,6118,6119,0,4112,5204,0,6216,6217,0,6218,6219,0,8000,5303,0,4203,6316,0,6317,6318,5403,0,6319,6408,0,8001,0,7004,
  4308,6409,0,6410,5003,0,6411,6008,0,4413,6009,0,8002,0,6010,6011,5103,0,6108,6109,0,6110,6111,0,8003,5202,0,7002,6212,6213,0,6214,0,13000,13001,5303,0,6215,6312,0,8000,0,6313,6314,0,13002,13003,5404,0,6315,6412,0,8001,
  0,6413,6414,0,6415,0,7003,0,11075,0,8002,0,11076,0,11077,0,11078,0,8003,0,11079,0,11080,0,0,0,8000,0,0,0,0,8001,0,0,0,0,0,8002,0,0,0,0,8003,0,15000,0,0,0,0,8000,0,0,0,0,8001,0,0,0,0,0,8002,0,0,0,0,8003,0,0,0,0,0,8000,
  0,0,0,0,8001,0,0,0,0,0,8002,0,0,0,0,8003,0,0,14000,0,0,0,8000,0,0,0,0,8001,0,0,0,0,0,8002,0,0,0,0,8003,0,0,0,0,0,8000,0,0,0,0,8001,0,0,0,0,0,8002,0,0,0,0,8003,0,0,0,0,0,8000,0,0,0,0,8001,0,0,0,0,0,8002,0,0,0,0,8003,0,
  0,0,0,0,8000,0,0,0,0,8001,0,1100,0,0,0,1101,0,8002,0,0,1104,0,0,8003,0,1102,0,0,0,1103,0,8000,0,0,0,0,8001,0,1105,0,0,0,1106,0,8002,0,0,1109,0,0,8003,0,1107,0,0,0,1108,0,8000,0,0,0,0,8001,0,1110,0,0,0,1111,0,8002,0,0,
  1114,0,0,8003,0,1112,0,0,0,1113,0,0,3000,0,4900,0,4025,0,4130,0,4901,0,4226,0,4331,0,4904,0,4429,5006,12000,0,6020,6021,0,6022,6023,5105,0,4034,6120,0,4902,6121,0,6122,6123,0,4127,0,13004,13005,5208,12001,0,6224,6225,
  0,6226,6227,5307,0,4232,6324,0,13006,13007,0,4903,6325,0,6326,6327,5409,12004,0,4328,6436,0,6437,0,6438,6439,5009,0,4433,6036,0,6037,6038,5108,12002,0,6039,6128,0,6129,6130,5205,0,6131,6228,0,6229,6230,5301,12003,0,
  6231,6332,0,6333,0,6334,6335,5400,0,6432,6433,0,6434,6435,0,16000,0,16001,0,16002,0,16003,0,16004,0,0,16005,0,16006,0,16007,0,16008,0,16009,0,16010,0,16011,0,16012,15003,0,0,16013,0,16014,0,16015,0,16016,0,16017,0,0,
  17000,0,18000,18001,0,18002,0,18003,18004,0,18005,18006,0,18007,18016,0,18017,18018,0,18019,18008,0,18009,18010,0,18011,18012,0,18013,18014,18015,};
constexpr short TS[] = {0,1,3,4,6,7,8,9,11,12,13,15,16,18,19,20,21,23,24,25,26,28,30,31,32,33,35,36,37,38,39,41,43,44,45,47,48,49,52,53,55,57,60,61,63,64,65,66,67,69,71,72,73,75,76,77,78,79,81,82,84,85,87,88,89,90,91,93,
  94,96,97,99,100,102,103,104,106,107,108,110,112,113,114,115,116,118,119,120,121,123,125,126,127,128,130,131,132,133,135,137,138,139,140,142,143,144,145,147,149,150,151,152,154,155,156,157,159,160,161,163,164,166,167,
  168,169,171,172,173,174,175,177,178,179,180,182,183,184,185,186,188,189,190,191,193,194,195,197,199,201,203,205,207,209,211,212,214,216,218,220,222,224,226,228,229,231,233,235,237,239,241,243,245,246,248,250,252,254,
  256,258,260,262,263,265,267,269,271,273,275,277,279,280,282,284,286,288,290,292,294,296,297,299,301,303,305,307,309,311,313,314,316,318,320,322,324,326,328,330,331,333,335,337,339,341,343,345,347,349,351,353,355,357,
  359,361,363,365,366,368,370,372,374,376,378,380,382,383,385,387,389,391,393,395,397,399,400,402,404,406,408,410,412,414,416,417,419,421,423,425,427,429,431,433,435,437,439,441,443,446,448,450,452,454,456,458,460,464,
  467,471,473,476,478,482,485,487,491,495,498,500,504,506,509,513,515,518,522,526,528,531,533,537,540,542,546,549,553,555,559,561,565,568,570,573,577,580,582,587,589,592,596,598,601,604,608,610,613,615,617,619,621,623,
  625,627,629,631,633,634,635,637,638,639,640,642,643,644,645,646,648,649,650,651,653,654,656,657,658,660,661,662,663,665,666,667,668,669,671,672,673,674,676,677,678,679,680,682,683,684,685,687,688,689,690,691,693,694,
  695,696,698,699,700,702,703,705,706,707,708,710,711,712,713,714,716,717,718,719,721,722,723,724,725,727,728,729,730,732,733,734,735,736,738,739,740,741,743,744,745,746,747,749,750,751,752,754,755,756,757,758,760,761,
  762,763,765,766,767,768,769,771,772,773,774,776,778,779,780,782,784,785,787,788,790,792,793,794,796,798,799,800,801,803,805,806,807,809,811,812,814,815,817,819,820,821,823,825,826,827,828,830,832,833,834,836,838,839,
  841,842,844,846,847,848,850,851,852,855,857,859,861,863,865,867,869,874,877,881,884,887,889,892,897,900,904,907,910,913,918,920,923,927,930,935,938,942,945,950,952,955,959,962,964,966,968,970,972,973,975,977,979,981,
  983,985,987,989,991,993,995,997,999,1001,1002,1003,1007,1009,1012,1015,1018,1021,1024,1027,1030,1033,1034,};
constexpr bool op_first(int u) { for (int x = u & 32; x < u; ++x) if ((OPU[x] & 3) == (OPU[u] & 3)) return false; return true; }
struct OPS {
  u32x4 fa[OP_LA + 1][4], fb[22]; f32x4 D[2][5], T[5], m[OP_RING], pv[5]; float g0, g1, t0, t1, gx[36];
  u32x4 gv[2][2]; unsigned gs[2];
  unsigned ra[3], sw; __amdgpu_buffer_rsrc_t srdE;
  unsigned va, idx, sa[4], lb5, lb2;
  __amdgpu_buffer_rsrc_t srdA, srdB;
};
template <int F> DI void op_reqB(OPS& o) {
  asm volatile("buffer_load_dwordx4 a[%c3:%c4], %0, %1, %2 offen offset:%c5 sc1" :: "v"(o.va), "s"(o.srdB), "s"(1024u * (F & ~3)), "i"(op_ar(F)), "i"(op_ar(F) + 3), "i"(1024 * (F & 3)) : "memory");
}
template <int ST, int I> DI void op_reqA1(OPS& o) {
  constexpr int un = OPU[2 * ST + (I >> 1)], k = 2 * (un >> 2) + (I & 1);
  const unsigned so = o.sa[(k & 3) ^ (ST >> 4)];
  asm volatile("buffer_load_dwordx4 %0, %1, %2, %3 offen sc1" : "=v"(o.fa[ST % (OP_LA + 1)][I]) : "v"(o.va), "s"(o.srdA), "s"(so ^ (131072u * (un & 3) + 1024u * (k & ~3))) : "memory");
}
template <int... Qs> DI void op_reqA0(OPS& o, iseq<Qs...>) { (op_reqA1<Qs / 4, Qs % 4>(o), ...); }
template <int F> DI void op_ldsB(OPS& o) {
  if constexpr (F < 26) asm volatile("ds_read_b128 %0, %1 offset:%2" : "=v"(o.fb[op_vr(F)]) : "v"(F < 23 ? o.lb5 : o.lb2), "i"(1024 * (F < 23 ? F : F - 23)) : "memory");
  else                  asm volatile("ds_read_b128 a[%c1:%c2], %0 offset:%c3" :: "v"(o.lb2), "i"(op_ar(F)), "i"(op_ar(F) + 3), "i"(1024 * (F - 23)) : "memory");
}
template <int F0, int... Is> DI void op_ldsBs(OPS& o, iseq<Is...>) { (op_ldsB<F0 + Is>(o), ...); }
constexpr int op_ac(int pass, int i, int j) { return j < 4 ? 16 * (i - 1) + 4 * j : 48 + 12 * pass + 4 * (i - 1); }
template <int ST, int Q> DI void op_mfma(OPS& o) {
  constexpr int A = ST % (OP_LA + 1), P = ST >> 4;
  if constexpr (Q == 0 || Q == 9) {
    constexpr int u = Q == 9, un = OPU[2 * ST + u], i = un & 3, f = 9 * (un >> 2) + 8, ac = op_ac(P, i, 4), ar = op_ar(f);
    const u32x8 B = __builtin_shufflevector(o.fa[A][2 * u], o.fa[A][2 * u + 1], 0, 1, 2, 3, 4, 5, 6, 7);
    if constexpr (i == 0 && f < 26) asm volatile("v_smfmac_f32_16x16x64_bf16 %0, %1, %2, %3" : "+v"(o.D[P][4]) : "v"(o.fb[op_vr(f) % 22]), "v"(B), "v"(o.idx));
    else if constexpr (i == 0)      asm volatile("v_smfmac_f32_16x16x64_bf16 %0, a[%c3:%c4], %1, %2" : "+v"(o.D[P][4]) : "v"(B), "v"(o.idx), "i"(ar), "i"(ar + 3));
    else if constexpr (f < 26)      asm volatile("v_smfmac_f32_16x16x64_bf16 a[%c3:%c4], %0, %1, %2" :: "v"(o.fb[op_vr(f) % 22]), "v"(B), "v"(o.idx), "i"(ac), "i"(ac + 3));
    else                            asm volatile("v_smfmac_f32_16x16x64_bf16 a[%c2:%c3], a[%c4:%c5], %0, %1" :: "v"(B), "v"(o.idx), "i"(ac), "i"(ac + 3), "i"(ar), "i"(ar + 3));
  } else {
    constexpr int d = Q - 1 - (Q > 9), J = d >> 2, u = (d >> 1) & 1, kk = d & 1, un = OPU[2 * ST + u], i = un & 3, f = 9 * (un >> 2) + 4 * kk + J, ac = op_ac(P, i, J), ar = op_ar(f);
    constexpr bool z = kk == 0 && op_first(2 * ST + u);
    static_assert(!z || f < 26, "");
    const u32x4& B = o.fa[A][2 * u + kk];
    if constexpr (i == 0 && z)           asm volatile("v_mfma_f32_16x16x32_bf16 %0, %1, %2, 0" : "=&v"(o.D[P][J]) : "v"(o.fb[op_vr(f) % 22]), "v"(B));
    else if constexpr (i == 0 && f < 26) asm volatile("v_mfma_f32_16x16x32_bf16 %0, %1, %2, %0" : "+v"(o.D[P][J]) : "v"(o.fb[op_vr(f) % 22]), "v"(B));
    else if constexpr (i == 0)           asm volatile("v_mfma_f32_16x16x32_bf16 %0, a[%c2:%c3], %1, %0" : "+v"(o.D[P][J]) : "v"(B), "i"(ar), "i"(ar + 3));
    else if constexpr (z)                asm volatile("v_mfma_f32_16x16x32_bf16 a[%c2:%c3], %0, %1, 0" :: "v"(o.fb[op_vr(f) % 22]), "v"(B), "i"(ac), "i"(ac + 3));
    else if constexpr (f < 26)           asm volatile("v_mfma_f32_16x16x32_bf16 a[%c2:%c3], %0, %1, a[%c2:%c3]" :: "v"(o.fb[op_vr(f) % 22]), "v"(B), "i"(ac), "i"(ac + 3));
    else                                 asm volatile("v_mfma_f32_16x16x32_bf16 a[%c1:%c2], a[%c3:%c4], %0, a[%c1:%c2]" :: "v"(B), "i"(ac), "i"(ac + 3), "i"(ar), "i"(ar + 3));
  }
}
template <int X, int WD> DI unsigned op_word(const OPS& o) { if constexpr (WD < 8) return o.gv[X][WD >> 2][WD & 3]; else return o.gs[X]; }
template <int U> DI void op_gate(OPS& o) {
  constexpr int wd = U / 9, pt = U % 9;
  if constexpr (pt == 0)      asm volatile("v_lshlrev_b32_e32 %0, 16, %2\n\tv_and_b32_e32 %1, 0xffff0000, %2" : "=&v"(o.g0), "=&v"(o.g1) : "v"(op_word<0, wd>(o)));
  else if constexpr (pt == 1) asm volatile("v_mul_f32_e32 %0, 0xbfb8aa3b, %2\n\tv_mul_f32_e32 %1, 0xbfb8aa3b, %3" : "=&v"(o.t0), "=&v"(o.t1) : "v"(o.g0), "v"(o.g1));
  else if constexpr (pt == 2) asm volatile("v_exp_f32_e32 %0, %0" : "+v"(o.t0));
  else if constexpr (pt == 3) asm volatile("v_exp_f32_e32 %0, %0" : "+v"(o.t1));
  else if constexpr (pt == 4) asm volatile("v_add_f32_e32 %0, 1.0, %0\n\tv_add_f32_e32 %1, 1.0, %1" : "+v"(o.t0), "+v"(o.t1));
  else if constexpr (pt == 5) asm volatile("v_rcp_f32_e32 %0, %0" : "+v"(o.t0));
  else if constexpr (pt == 6) asm volatile("v_rcp_f32_e32 %0, %0" : "+v"(o.t1));
  else if constexpr (pt == 7) asm volatile("v_mul_f32_e32 %0, %0, %2\n\tv_mul_f32_e32 %1, %1, %3" : "+v"(o.t0), "+v"(o.t1) : "v"(o.g0), "v"(o.g1));
  else { unsigned r; asm volatile("v_cvt_pk_bf16_f32 %0, %1, %2" : "=v"(r) : "v"(o.t0), "v"(o.t1)); if constexpr (wd < 8) o.gv[0][wd >> 2][wd & 3] = r; else o.gs[0] = r; }
}
template <int ST, int Q, int CODE> DI void op_t(OPS& o) {
  constexpr int kind = CODE / 1000, a = CODE % 1000;
  if constexpr (kind == 0) op_mfma<ST, Q>(o);
  else if constexpr (kind == 1) {
    constexpr int n = a % 100, ac = op_ac(a / 100, 1 + n / 5, n % 5);
    asm volatile("ds_write_b128 %0, a[%c1:%c2] offset:%c3" :: "v"(n < 10 ? o.lb5 : o.lb2), "i"(ac), "i"(ac + 3), "i"(n < 10 ? 13312 + 1024 * n : 1024 * (n - 10)) : "memory");
  } else if constexpr (kind == 2) asm volatile("s_waitcnt lgkmcnt(%0)" :: "i"(a) : "memory");
  else if constexpr (kind == 3) asm volatile("s_waitcnt lgkmcnt(0)\n\ts_barrier" ::: "memory");
  else if constexpr (kind == 4) {
    constexpr int sl = a / 100, n = a % 20;
    asm volatile("ds_read_b128 %0, %1 offset:%2" : "=v"(sl == 9 ? o.T[n % 5] : o.m[sl % OP_RING]) : "v"(o.ra[n / 5]), "i"(n < 10 ? 13312 + 1024 * n : 1024 * (n - 10)) : "memory");
  } else if constexpr (kind == 5) asm volatile("s_waitcnt lgkmcnt(%1)" : "+v"(o.m[a / 100]) : "i"(a % 100) : "memory");
  else if constexpr (kind == 6 || kind == 18) {
    constexpr int j = (a % 20) >> 2, e = a & 3;
    f32x4& s = kind == 6 && a % 100 < 20 ? o.D[0][j] : o.T[j];
    float v = s[e]; add_v(v, kind == 6 ? o.m[a / 100][e] : o.pv[j][e]); s[e] = v;
  } else if constexpr (kind == 7)
    asm volatile("buffer_store_dwordx4 %0, %1, %2, %3 offen offset:%c4 sc1" :: "v"(o.D[0][a]), "v"(o.va), "s"(o.srdE), "s"(o.sw + 1024u * (a & 4)), "i"(1024 * (a & 3)) : "memory");
  else if constexpr (kind == 8) op_reqA1<ST + OP_LA, a>(o);
  else if constexpr (kind == 9) op_reqB<a>(o);
  else if constexpr (kind == 11) op_gate<a>(o);
  else if constexpr (kind == 12) asm volatile("" : "+v"(o.T[a]));
  else if constexpr (kind == 13) op_ldsB<9 * (a >> 2) + (a & 3)>(o);
  else if constexpr (kind == 14) asm volatile("s_barrier" ::: "memory");
  else if constexpr (kind == 15) asm volatile("s_waitcnt lgkmcnt(%4)" : "+v"(o.fb[0]), "+v"(o.fb[1]), "+v"(o.fb[2]), "+v"(o.fb[3]) : "i"(a) : "memory");
  else if constexpr (kind == 16) asm volatile("v_lshlrev_b32_e32 %0, 16, %2\n\tv_and_b32_e32 %1, 0xffff0000, %2" : "=&v"(o.gx[2 * a]), "=v"(o.gx[2 * a + 1]) : "v"(op_word<a / 9, a % 9>(o)));
  else if constexpr (kind == 17) asm volatile("s_waitcnt vmcnt(0)" : "+v"(o.pv[0]), "+v"(o.pv[1]), "+v"(o.pv[2]), "+v"(o.pv[3]), "+v"(o.pv[4]) :: "memory");
}
template <int ST, int Q, int... Is> DI void op_ts(OPS& o, iseq<Is...>) { (op_t<ST, Q, TT[TS[18 * ST + Q] + Is]>(o), ...); }
template <int... Is> DI void op_tail(OPS& o, iseq<Is...>) { (op_t<32, 0, TT[TS[576] + Is]>(o), ...); }
template <int ST, int... Qs> DI void op_qs(OPS& o, iseq<Qs...>) { (op_ts<ST, Qs>(o, make_iseq<TS[18 * ST + Qs + 1] - TS[18 * ST + Qs]>{}), ...); }

template <typename F, int... Is> DI void for_seq(F& f, iseq<Is...>) { (f(std::integral_constant<int, Is>{}), ...); }
DI void wait_flag(const unsigned* p, const unsigned tag) {
#pragma nounroll
  for (unsigned spin = 0; spin < (1u << 20); ++spin) {
    unsigned v;
    asm volatile("global_load_dword %0, %1, off sc1\n\ts_waitcnt vmcnt(0)" : "=v"(v) : "v"(p) : "memory");
    if (__all(v == tag)) break;
    __builtin_amdgcn_s_sleep(1);
  }
}
DI unsigned op_k0(const unsigned blk, const unsigned w) { return ((blk >> 3) & 7u) * 128u + (~blk & 4u) * 16u + 16u * w; }

constexpr int NFIN = 200, NBL = 49, FVW = 30, FVS = 0;
constexpr short FU[] = {0,4,1,5,2,6,3,7,8,9,10,11,12,13,14,15,};
constexpr short FT[] = {5064,14049,5001,23000,16000,5002,14050,5003,23001,16001,5071,14051,5006,23002,16008,5005,14052,5004,23003,16009,5072,14053,5009,23004,16016,5010,14054,5011,23005,16017,5079,31000,25003,5014,23006,
  16024,5013,14055,5012,23007,16025,5080,31000,25002,5017,23008,16002,5018,14056,5019,23009,16003,5099,31000,25001,5034,23010,16010,5033,14057,5032,23011,16011,5112,31000,25000,5049,23012,16018,5050,14058,5051,23013,
  16019,5087,31000,25004,5022,23014,16026,5021,14059,5020,23015,16027,5088,31000,25005,5025,23016,16004,5026,14060,5027,23017,16005,5095,31000,25006,5030,23018,16012,5029,14061,5028,23019,16013,5100,31000,25007,5037,
  23020,16020,5038,14062,5039,23021,16021,5119,31000,25011,5054,23022,16028,5053,14063,5052,23023,16029,5104,31000,25010,5041,23024,16006,5042,16007,16014,5043,23025,16015,5111,31000,25009,5046,23026,16022,5045,16023,
  16030,5044,31000,23027,23028,5120,31000,23029,23030,5057,23031,16031,5058,31000,25008,5059,13000,5127,13001,5062,15000,5061,31000,25015,5060,13002,13003,29013,1000,27000,15001,22000,1001,31000,26000,13016,1003,13017,
  13018,1002,31000,26001,1004,15008,13019,1005,15009,13032,1007,31000,26002,13033,1006,15016,13034,1008,13035,13048,1009,31000,26003,1011,27001,15017,13049,1010,15024,13050,1012,31000,26004,13051,1013,15025,13004,1015,
  13005,13006,1014,31000,26005,3000,15002,13007,1016,15003,13020,1017,31000,26006,13021,1019,15010,13022,1018,13023,13036,1020,31000,26007,1021,27002,15011,13037,1023,15018,13038,1022,31000,26008,13039,1024,15019,13052,
  1025,31000,24000,24001,24002,1027,31000,24003,13053,1026,15026,13054,13055,1028,15027,1029,31000,24004,24005,24006,24007,1031,31000,25014,1030,31000,3001,31000,4000,31000,24008,24009,24010,24011,4001,31000,25013,30008,
  2000,31000,12000,13008,2001,31000,12001,13009,2017,31000,24012,24013,24014,24015,25012,2016,31000,17000,17001,17002,17003,15004,30024,2002,13010,13011,13024,2003,31000,25019,15005,13025,2019,31000,24016,24017,24018,
  24019,17004,2018,31000,25018,17005,17006,17007,30040,2004,31000,18000,18001,18002,18003,18004,2005,31000,25017,18005,18006,18007,2021,31000,24020,24021,24022,24023,19000,2020,31000,25016,19001,19002,19003,30056,2006,
  15012,13026,13027,2007,31000,28000,17008,17009,17010,2023,31000,24024,24025,24026,24027,17011,2022,31000,25020,17012,17013,17014,30072,2008,31000,17015,18008,18009,18010,18011,2009,31000,25021,18012,18013,18014,2025,
  31000,24028,24029,24030,24031,2024,31000,18015,19004,19005,19006,19007,30088,2010,31000,28001,17016,17017,2011,31000,17018,17019,17020,17021,17022,2027,31000,17023,18016,18017,18018,2026,31000,25022,18019,18020,18021,
  30100,2012,31000,18022,18023,19008,19009,2013,19010,19011,15013,13040,2029,31000,28002,17024,17025,2028,31000,17026,17027,17028,17029,17030,30112,2014,31000,17031,18024,18025,18026,2015,31000,25023,18027,18028,18029,
  2031,31000,18030,18031,19012,19013,2030,19014,19015,13041,13042,1032,31000,28003,17032,17033,1033,31000,17034,17035,17036,17037,1035,31000,17038,17039,18032,18033,1034,31000,25026,18034,18035,18036,1036,31000,18037,
  18038,18039,19016,1037,19017,19018,19019,15020,13043,1039,31000,28004,17040,17041,1038,31000,17042,17043,17044,17045,1040,31000,17046,17047,18040,18041,1041,31000,25025,18042,18043,18044,1043,31000,18045,18046,18047,
  19020,1042,19021,19022,19023,15021,13056,1044,31000,28005,17048,17049,1045,31000,17050,17051,17052,17053,1047,31000,17054,17055,18048,18049,1046,31000,25024,18050,18051,18052,3002,31000,18053,18054,18055,1048,19024,
  19025,19026,19027,13057,1049,31000,28006,17056,17057,1051,31000,17058,17059,17060,17061,17062,1050,31000,17063,18056,18057,18058,1052,31000,18059,18060,18061,18062,18063,1053,19028,19029,19030,19031,1055,31000,28007,
  15028,13058,1054,13059,13012,1056,15029,13013,13014,1057,15006,13015,13028,1059,15007,13029,13030,1058,15014,13031,13044,1060,15015,13045,13046,1061,15022,13047,13060,1063,15023,13061,13062,1062,15030,13063,3003,15031,
  4002,31000,2032,31000,2033,31000,12002,2035,31000,2034,31000,17064,17065,17066,17067,2036,31000,2037,31000,17068,17069,17070,17071,18064,2039,31000,18065,18066,18067,18068,18069,2038,31000,18070,18071,19032,19033,
  19034,2040,19035,2041,31000,28008,17072,17073,17074,2043,31000,17075,17076,17077,17078,17079,2042,31000,18072,18073,18074,18075,18076,2044,31000,18077,18078,18079,19036,19037,2045,19038,19039,2047,31000,28009,17080,
  17081,17082,2046,31000,17083,17084,17085,17086,17087,18080,4003,31000,18081,18082,18083,18084,2048,31000,18085,18086,18087,12003,2049,19040,19041,19042,19043,2051,31000,28010,17088,17089,17090,17091,2050,31000,17092,
  17093,17094,17095,18088,18089,2052,31000,18090,18091,18092,18093,18094,18095,2053,19044,19045,19046,19047,2055,31000,28011,17096,17097,17098,17099,2054,31000,17100,17101,17102,17103,18096,18097,2056,31000,18098,18099,
  18100,18101,18102,18103,2057,19048,19049,19050,19051,2059,31000,28012,17104,17105,17106,17107,2058,31000,17108,17109,17110,17111,18104,18105,2060,31000,18106,18107,18108,18109,18110,18111,2061,19052,19053,19054,19055,
  2063,31000,28013,17112,17113,17114,17115,2062,31000,17116,17117,17118,17119,18112,18113,18114,18115,18116,18117,18118,18119,19056,19057,19058,19059,28014,17120,17121,17122,17123,17124,17125,17126,17127,18120,18121,
  18122,18123,18124,18125,18126,18127,19060,19061,19062,19063,28015,};
constexpr short FS[] = {0,2,5,7,10,12,15,17,20,22,25,27,30,33,36,38,41,44,47,49,52,55,58,60,63,66,69,71,74,77,80,82,85,88,91,93,96,99,102,104,107,110,113,115,118,121,124,126,129,132,135,138,141,144,147,150,154,158,161,
  164,166,168,170,173,176,181,185,188,191,194,197,201,204,207,210,214,217,221,224,227,230,233,236,240,243,246,249,253,256,260,263,268,272,276,278,284,287,289,291,297,300,305,309,316,323,328,333,340,346,354,360,367,373,
  378,384,391,397,405,411,417,424,430,437,443,449,456,461,466,473,480,486,492,497,502,508,514,520,526,532,537,543,549,555,561,567,572,578,584,590,595,601,606,613,619,626,631,636,639,643,647,651,655,659,663,667,670,672,
  674,676,679,681,687,689,696,703,710,712,718,725,732,739,742,748,756,762,768,773,780,788,796,801,808,816,824,829,836,844,852,857,864,904,};
struct OST { u32x4 w, P[4][2]; float o[8], il[4], ls[4]; unsigned vo, so[4]; __amdgpu_buffer_rsrc_t srd; bf16x4 lo[8][2], up[8][2]; };
template <int U> DI void o_store(const OST& t) {
  asm volatile("buffer_store_dwordx4 %0, %1, %2, %3 offen sc1" :: "v"(t.w), "v"(t.vo), "s"(t.srd), "s"(t.so[U & 3] + 131072u * (U >> 2)) : "memory");
}
template <int CODE> DI void fin_op(W4C& c, OST& t, OPS& o) {
  constexpr int kind = CODE / 1000, a = CODE % 1000;
  if constexpr (kind == 1) pv16<AO_((a >> 1) & 7, a >> 4), false>(c.Vf[(a >> 1) & 7][a & 1], c.Pk[a >> 4][a & 1]);
  else if constexpr (kind == 2) pv16<AO_((a >> 1) & 7, a >> 4), false>(__builtin_shufflevector(t.lo[(a >> 1) & 7][a & 1], t.up[(a >> 1) & 7][a & 1], 0, 1, 2, 3, 4, 5, 6, 7), t.P[a >> 4][a & 1]);
  else if constexpr (kind == 3) sum64(c, a, c.Pk[a]);
  else if constexpr (kind == 4) sum64(c, a, t.P[a]);
  else if constexpr (kind == 5) { constexpr int kb = (a >> 4) & 3, qb = (a >> 2) & 3; qk16<kb, qb, (a & 3), (a >> 6)>(c.S[1][kb][qb], c.NEGM[qb]); }
  else if constexpr (kind == 14) exp_e<0, a>(c);
  else if constexpr (kind == 16) pack_e<0, a>(c);
  else if constexpr (kind == 22) asm volatile("" :: "v"(c.NEGM[0]), "v"(c.NEGM[1]), "v"(c.NEGM[2]), "v"(c.NEGM[3]));
  else if constexpr (kind == 23) tr_read_<a, 0>(c);
  else if constexpr (kind == 29) phaseA_end<a>(c);
  else if constexpr (kind == 12) asm volatile("v_add_f32_e32 %0, %2, %3\n\tv_rcp_f32_e32 %1, %0" : "=&v"(t.ls[a]), "=v"(t.il[a]) : "v"(c.L[a][0]), "v"(c.L[a][1]), "v"(c.L[a]));
  else if constexpr (kind == 13) exp_e<1, a>(c);
  else if constexpr (kind == 15) pack_to<1, a>(c, t.P);
  else if constexpr (kind == 17) t.o[a & 7] = acc_read<8 * FU[a >> 3] + (a & 7)>();
  else if constexpr (kind == 18) {
    asm volatile("v_mul_f32_e32 %0, %0, %1" : "+v"(t.o[a & 7]) : "v"(t.il[FU[a >> 3] >> 2]));
    if constexpr (a >= 8 && (a & 7) == 0) asm volatile("" :: "v"(t.w));
  } else if constexpr (kind == 19) { GAP(); const unsigned w = cvtpk(t.o[2 * (a & 3)], t.o[2 * (a & 3) + 1]); asm volatile("" :: "v"(w)); t.w[a & 3] = w; GAP(); }
  else if constexpr (kind == 24) tr_rd<a, 0x4000>(c, t.lo, t.up);
  else if constexpr (kind == 25) op_reqB<32 + a>(o);
  else if constexpr (kind == 26) asm volatile("buffer_load_dwordx4 %0, %1, %2 offen offset:%c3 sc1 lds" :: "v"(c.pfv), "s"(c.srdP), "s"(c.pfo + 4096u * (a >> 2)), "i"(1024 * (a & 3)) : "memory");
  else if constexpr (kind == 27) asm volatile("s_mov_b32 m0, %0" :: "s"(c.pfm0 + 4096u * a) : "m0", "memory");
  else if constexpr (kind == 28) o_store<FU[a]>(t);
  else if constexpr (kind == 31) real_op();
  else if constexpr (a == 200) asm volatile("s_nop 0");
  else { constexpr int d = a >> 4; asm volatile("s_waitcnt lgkmcnt(%4)" : "+v"(t.lo[d][0]), "+v"(t.lo[d][1]), "+v"(t.up[d][0]), "+v"(t.up[d][1]) : "i"(a & 15) : "memory"); }
}
template <int G, int... Is> DI void fin_gap(W4C& c, OST& t, OPS& o, iseq<Is...>) { (fin_op<FT[FS[G] + Is]>(c, t, o), ...); GAP(); }
template <int... Gs> DI void fin_gaps(W4C& c, OST& t, OPS& o, iseq<Gs...>) { (fin_gap<Gs>(c, t, o, make_iseq<FS[Gs + 1] - FS[Gs]>{}), ...); }

template <int J, int H> DI void redo_q(const __amdgpu_buffer_rsrc_t srdW, const __amdgpu_buffer_rsrc_t srdN, const unsigned vw, const unsigned vx, const unsigned sw, const unsigned sx) {
  f32x4 q[4] = {};
#pragma nounroll
  for (unsigned ks = 0; ks < DM / 32; ++ks) {
    u32x4 w;
#pragma unroll
    for (int e = 0; e < 4; ++e) {
      const unsigned so = sw + (64u * J + 8u * H) + WRS * (32u * ks + 2u * e);
      w[e] = (unsigned)__builtin_amdgcn_raw_buffer_load_b16(srdW, vw, so, 0) | ((unsigned)__builtin_amdgcn_raw_buffer_load_b16(srdW, vw, so + WRS, 0) << 16);
    }
    asm volatile("s_nop 1" : "+v"(w), "+v"(q[0]), "+v"(q[1]), "+v"(q[2]), "+v"(q[3]));
#pragma unroll
    for (int qb = 0; qb < 4; ++qb) {
      u32x4 x;
      asm volatile("buffer_load_dwordx4 %0, %1, %2, %3 offen sc1\n\ts_waitcnt vmcnt(0)" : "=v"(x) : "v"(vx), "s"(srdN), "s"(sx + 64u * ks + 16u * XRS * qb) : "memory");
      sum16<false>(q[qb], w, x);
    }
  }
  asm volatile("s_nop 7" : "+v"(q[0]), "+v"(q[1]), "+v"(q[2]), "+v"(q[3]));
  const float C = 0.12751743082f;
  unsigned r[8];
#pragma unroll
  for (int qb = 0; qb < 4; ++qb) { r[2 * qb] = cvtpk(C * q[qb][0], C * q[qb][1]); r[2 * qb + 1] = cvtpk(C * q[qb][2], C * q[qb][3]); }
  asm volatile("s_nop 1" : "+v"(r[0]), "+v"(r[1]), "+v"(r[2]), "+v"(r[3]), "+v"(r[4]), "+v"(r[5]), "+v"(r[6]), "+v"(r[7]));
  acc_write<AQ_(0, J) + 2 * H>(r[0]); acc_write<AQ_(0, J) + 2 * H + 1>(r[1]); acc_write<AQ_(1, J) + 2 * H>(r[2]); acc_write<AQ_(1, J) + 2 * H + 1>(r[3]);
  acc_write<AQ_(2, J) + 2 * H>(r[4]); acc_write<AQ_(2, J) + 2 * H + 1>(r[5]); acc_write<AQ_(3, J) + 2 * H>(r[6]); acc_write<AQ_(3, J) + 2 * H + 1>(r[7]);
}
template <int I> DI void redo_kload(const unsigned vo, const __amdgpu_buffer_rsrc_t srd, const unsigned so) {
  asm volatile("buffer_load_dwordx4 a[%c3:%c4], %0, %1, %2 offen offset:%c5" :: "v"(vo), "s"(srd), "s"(so + 4096u * (I >> 2)), "i"(192 + 4 * I), "i"(195 + 4 * I), "i"(64 * (I & 3)) : "memory");
}
template <int CM, int... Ms> DI void redo_s(W4C& c, const unsigned vk, const unsigned so, f32x4 (&T)[2][4], iseq<Ms...>) {
  redo_kload<0>(vk, c.srdK, so); redo_kload<1>(vk, c.srdK, so); redo_kload<2>(vk, c.srdK, so); redo_kload<3>(vk, c.srdK, so);
  redo_kload<4>(vk, c.srdK, so); redo_kload<5>(vk, c.srdK, so); redo_kload<6>(vk, c.srdK, so); redo_kload<7>(vk, c.srdK, so);
  asm volatile("s_waitcnt vmcnt(0)" ::: "memory");
  (qk16<(Ms >> 4), ((Ms >> 2) & 3), (Ms & 3), ((Ms & 3) ? 0 : CM)>(T[Ms >> 4][(Ms >> 2) & 3], c.NEGM[(Ms >> 2) & 3]), ...);
  asm volatile("s_nop 7" : "+v"(T[0][0]), "+v"(T[0][1]), "+v"(T[0][2]), "+v"(T[0][3]), "+v"(T[1][0]), "+v"(T[1][1]), "+v"(T[1][2]), "+v"(T[1][3]));
}
template <int DB> DI void redo_pv(const W4C& c, const unsigned vv, const unsigned so, const u32x4 (&P)[4]) {
  u32x4 v;
#pragma unroll
  for (int e = 0; e < 4; ++e) {
    const unsigned s = so + (896u + 32u * DB) + KRS * (16u * (e >> 1) + 2u * (e & 1));
    v[e] = (unsigned)__builtin_amdgcn_raw_buffer_load_b16(c.srdV, vv, s, 0) | ((unsigned)__builtin_amdgcn_raw_buffer_load_b16(c.srdV, vv, s + KRS, 0) << 16);
  }
  asm volatile("s_nop 1" : "+v"(v));
  const bf16x8 vf = __builtin_bit_cast(bf16x8, v);
  pv16<AO_(DB, 0), false>(vf, P[0]); pv16<AO_(DB, 1), false>(vf, P[1]); pv16<AO_(DB, 2), false>(vf, P[2]); pv16<AO_(DB, 3), false>(vf, P[3]);
  GAP();
}
template <int U> DI void redo_unit(OST& t) {
  t.o[0] = acc_read<8 * U>(); t.o[1] = acc_read<8 * U + 1>(); t.o[2] = acc_read<8 * U + 2>(); t.o[3] = acc_read<8 * U + 3>();
  t.o[4] = acc_read<8 * U + 4>(); t.o[5] = acc_read<8 * U + 5>(); t.o[6] = acc_read<8 * U + 6>(); t.o[7] = acc_read<8 * U + 7>();
  const float il = t.il[U >> 2];
  t.w = u32x4{cvtpk(t.o[0] * il, t.o[1] * il), cvtpk(t.o[2] * il, t.o[3] * il), cvtpk(t.o[4] * il, t.o[5] * il), cvtpk(t.o[6] * il, t.o[7] * il)};
  o_store<U>(t);
  asm volatile("s_nop 1" :: "v"(t.w));
}
template <int R0, int... Is> DI void redo_zero(const unsigned z, iseq<Is...>) { (acc_write<R0 + Is>(z), ...); }
template <int... Is> DI void redo_units(OST& t, iseq<Is...>) { (redo_unit<Is>(t), ...); }
template <int... Is> DI void redo_reqs(OPS& o, iseq<Is...>) { (op_reqB<32 + Is>(o), ...); }
DI void redo_rows(W4C& c, OST& t, OPS& o, const u16* xn, const u16* wq, const unsigned long long* keys, const unsigned row0, const unsigned h, const unsigned tag) {
  unsigned l;
  asm volatile("s_waitcnt vmcnt(0)\n\tv_mbcnt_lo_u32_b32 %0, -1, 0\n\tv_mbcnt_hi_u32_b32 %0, -1, %0" : "=v"(l) :: "memory");
  const unsigned c16 = l & 15u, g = l >> 4;
  wait_flag((const unsigned*)(keys + row0 + l) + 1, tag);
  {
    const __amdgpu_buffer_rsrc_t srdW = mk_srd(wq, DM * HQ * DH * 2), srdN = mk_srd(xn, SEQ * DM * 2);
    const unsigned vw = WRS * 8u * g + 16u * (c16 >> 2) + 2u * (c16 & 3u), vx = XRS * c16 + 16u * g, sw = 256u * h, sx = XRS * row0;
    redo_q<0, 0>(srdW, srdN, vw, vx, sw, sx); redo_q<0, 1>(srdW, srdN, vw, vx, sw, sx); redo_q<1, 0>(srdW, srdN, vw, vx, sw, sx); redo_q<1, 1>(srdW, srdN, vw, vx, sw, sx);
    redo_q<2, 0>(srdW, srdN, vw, vx, sw, sx); redo_q<2, 1>(srdW, srdN, vw, vx, sw, sx); redo_q<3, 0>(srdW, srdN, vw, vx, sw, sx); redo_q<3, 1>(srdW, srdN, vw, vx, sw, sx);
  }
  const unsigned vk = KRS * c16 + 16u * g, vv = KRS * 4u * g + 2u * c16;
  f32x4 T[2][4];
  float m[4] = {-__builtin_inff(), -__builtin_inff(), -__builtin_inff(), -__builtin_inff()};
#pragma nounroll
  for (unsigned u = 0; u < SEQ / 32; ++u) {
    redo_s<2>(c, vk, 32u * KRS * u, T, make_iseq<32>{});
#pragma unroll
    for (int q = 0; q < 4; ++q)
#pragma unroll
      for (int e = 0; e < 8; ++e) m[q] = max2f(m[q], T[e >> 2][q][e & 3]);
  }
#pragma unroll
  for (int q = 0; q < 4; ++q) {
    asm volatile("s_nop 1" : "+v"(m[q]));
    auto p1 = __builtin_amdgcn_permlane16_swap(__float_as_uint(m[q]), __float_as_uint(m[q]), false, false);
    m[q] = max2f(__uint_as_float(p1[0]), __uint_as_float(p1[1]));
    asm volatile("s_nop 1" : "+v"(m[q]));
    auto p2 = __builtin_amdgcn_permlane32_swap(__float_as_uint(m[q]), __float_as_uint(m[q]), false, false);
    m[q] = -max2f(__uint_as_float(p2[0]), __uint_as_float(p2[1]));
    c.NEGM[q] = f32x4{m[q], m[q], m[q], m[q]};
    c.L[q] = f32x4{0.f, 0.f, 0.f, 0.f};
  }
  redo_zero<0>(0u, make_iseq<128>{});
  asm volatile("s_nop 1" : "+v"(c.NEGM[0]), "+v"(c.NEGM[1]), "+v"(c.NEGM[2]), "+v"(c.NEGM[3]), "+v"(c.L[0]), "+v"(c.L[1]), "+v"(c.L[2]), "+v"(c.L[3]));
#pragma nounroll
  for (unsigned u = 0; u < SEQ / 32; ++u) {
    redo_s<1>(c, vk, 32u * KRS * u, T, make_iseq<32>{});
    u32x4 P[4];
#pragma unroll
    for (int q = 0; q < 4; ++q)
#pragma unroll
      for (int e = 0; e < 4; ++e) P[q][e] = cvtpk(__builtin_amdgcn_exp2f(T[e >> 1][q][2 * (e & 1)]), __builtin_amdgcn_exp2f(T[e >> 1][q][2 * (e & 1) + 1]));
    asm volatile("s_nop 1" : "+v"(P[0]), "+v"(P[1]), "+v"(P[2]), "+v"(P[3]));
#pragma unroll
    for (int q = 0; q < 4; ++q) sum16<false>(c.L[q], c.ONE, P[q]);
    const unsigned so = 32u * KRS * u;
    redo_pv<0>(c, vv, so, P); redo_pv<1>(c, vv, so, P); redo_pv<2>(c, vv, so, P); redo_pv<3>(c, vv, so, P);
    redo_pv<4>(c, vv, so, P); redo_pv<5>(c, vv, so, P); redo_pv<6>(c, vv, so, P); redo_pv<7>(c, vv, so, P);
  }
  asm volatile("s_nop 7" : "+v"(c.L[0]), "+v"(c.L[1]), "+v"(c.L[2]), "+v"(c.L[3]));
#pragma unroll
  for (int q = 0; q < 4; ++q) t.il[q] = __builtin_amdgcn_rcpf(c.L[q][0]);
  redo_units(t, make_iseq<16>{});
  redo_reqs(o, make_iseq<OP_NR>{});
  asm volatile("s_waitcnt vmcnt(0)" ::: "memory");
}

DI void oproj_phase(OPS& o, char* lds, const u16* __restrict__ Oa, const u16* __restrict__ wot, const u16* __restrict__ x, const u16* __restrict__ xn,
                    unsigned* __restrict__ hand, float* __restrict__ exch, u16* __restrict__ out, const unsigned tag, const unsigned myflag  ) {
  unsigned lane;
  asm volatile("v_mbcnt_lo_u32_b32 %0, -1, 0\n\tv_mbcnt_hi_u32_b32 %0, -1, %0" : "=v"(lane));
  const unsigned wid = rfl(threadIdx.x >> 6), l16 = lane & 15u, g16 = lane >> 4;
  const unsigned blk = blockIdx.x, xg = blk & 7u, ii = blk >> 3;
  const unsigned mt = 8u * (xg & 3u) + 2u * (ii >> 3) + (xg >> 2), nt = ii & 7u, kq = (~blk & 4u) * 16u + 16u * wid;
  const unsigned ldsb = (unsigned)(size_t)lds;
  o.srdA = mk_srd(Oa, SEQ * HQ * DH * 2);
#pragma unroll
  for (unsigned p = 0; p < 4u; ++p) o.sa[p] = ((4u * (mt ^ 1u) + wid) * 128u + kq + (p ^ ((mt ^ 1u) & 3u))) * 1024u;
  asm volatile("" : "+s"(o.sa[0]), "+s"(o.sa[1]), "+s"(o.sa[2]), "+s"(o.sa[3]));
  o.lb5 = ldsb + SD_BASE + BP_W * wid + o.va; o.lb2 = ldsb + 9216u * wid + o.va;
  const __amdgpu_buffer_rsrc_t srdH = mk_srd(hand, 65536u), srdF = mk_srd(hand + myflag, 4u);
  const unsigned fxw = H_X + 18u * nt + (lane < 18u ? lane : 0u);
  unsigned selr, fx, fe;
  unsigned fo;
  const unsigned fow = H_O + 256u * (mt >> 1) + 2u * kq + (lane & 7u);
  asm volatile("buffer_load_dword %0, %1, %2, 0 offen sc1" : "=&v"(fo) : "v"(4u * fow), "s"(srdH) : "memory");
  GAP();
  asm volatile("s_waitcnt vmcnt(%2)\n\tds_read_b32 %0, %1" : "=v"(selr) : "v"(ldsb + WF_FL + 4u * l16), "i"(FVW + 1) : "memory");
  static_assert(FVS == 0, "the last request of the final phase is a store");
  op_ldsBs<0>(o, make_iseq<9>{}); op_ldsBs<13>(o, make_iseq<19>{});
  o.ra[0] = ldsb + SD_BASE + BP_W * (wid ^ 1u) + o.va; o.ra[1] = ldsb + SD_BASE + BP_W * (wid ^ 2u) + o.va; o.ra[2] = ldsb + 9216u * (wid ^ 3u) + o.va;
  o.sw = 5120u * wid; o.srdE = mk_srd(exch + blk * 5120u, 20480u);
  const unsigned row0 = 64u * mt + 16u * wid, sn = 144u * nt;
  unsigned ax[2] = {(row0 + l16) * XRS_ + 16u * g16, (row0 + l16) * XRS_ + 128u + 8u * (g16 & 1u) + 2u * (g16 & 2u)};
  asm volatile("" : "+v"(ax[0]), "+v"(ax[1]));
  const __amdgpu_buffer_rsrc_t srdN = mk_srd(xn, (SEQ + NSEL) * DM * 2);
#define OP_LD3(POL) "buffer_load_dwordx4 %0, %3, %5, %6 offen" POL "\n\tbuffer_load_dwordx4 %1, %3, %5, %6 offen offset:64" POL "\n\tbuffer_load_dword %2, %4, %5, %6 offen" POL
  o.idx = (lane & 8u) ? 0xeeeeeeeeu : 0x44444444u; o.D[0][4] = f32x4{0.f, 0.f, 0.f, 0.f};
  asm volatile("" : "+v"(o.ra[0]), "+v"(o.ra[1]), "+v"(o.ra[2]), "+v"(o.idx), "+v"(o.D[0][4]));
  redo_zero<48>(0u, make_iseq<24>{});
  asm volatile("s_waitcnt lgkmcnt(0)" : "+v"(o.fb[0]), "+v"(o.fb[1]), "+v"(o.fb[2]), "+v"(o.fb[3]), "+v"(o.fb[4]), "+v"(o.fb[5]), "+v"(o.fb[6]), "+v"(o.fb[7]), "+v"(o.fb[8]), "+v"(o.fb[9]), "+v"(o.fb[10]),
               "+v"(o.fb[11]), "+v"(o.fb[12]), "+v"(o.fb[13]), "+v"(o.fb[14]), "+v"(o.fb[15]), "+v"(o.fb[16]), "+v"(o.fb[17]), "+v"(o.fb[18]), "+v"(o.fb[19]), "+v"(o.fb[20]), "+v"(o.fb[21]), "+v"(selr) :: "memory");
  GAP();
  asm volatile("s_waitcnt vmcnt(1)\n\tbuffer_store_dword %0, %1, %2, 0 offen sc1\n\ts_waitcnt vmcnt(1)" :: "v"(tag), "v"(o.va), "s"(srdF) : "memory");
  asm volatile("" : "+v"(fo));
  if (!__all(fo == tag)) wait_flag(hand + fow, tag);
  GAP();
  op_reqA0(o, make_iseq<4 * OP_LA>{});
  GAP();
#pragma nounroll
  for (unsigned spin = 0; spin < (1u << 20) && !__all((lane & 1u) == 0u || selr == tag); ++spin) {
    __builtin_amdgcn_s_sleep(6);
    asm volatile("buffer_load_dword %0, %1, %2, 0 offen sc1\n\ts_waitcnt vmcnt(0)" : "=v"(selr) : "v"(4u * l16), "s"(srdH) : "memory");
  }
  asm volatile(OP_LD3(" sc1") : "=&v"(o.gv[0][0]), "=&v"(o.gv[0][1]), "=&v"(o.gs[0]) : "v"(ax[0]), "v"(ax[1]), "s"(srdN), "s"(sn) : "memory");
  asm volatile(OP_LD3("") : "=&v"(o.gv[1][0]), "=&v"(o.gv[1][1]), "=&v"(o.gs[1]) : "v"(ax[0]), "v"(ax[1]), "s"(mk_srd(x, SEQ * DM * 2)), "s"(sn) : "memory");
  const unsigned tmask = (unsigned)(__ballot((lane & 1u) == 0u && lane < 16u && ((selr & 0xffffu) - row0) < 16u));
  asm volatile("s_nop 4\n\tbuffer_load_dword %0, %1, %2, 0 offen sc1" : "=&v"(fx) : "v"(4u * fxw), "s"(mk_srd(hand, ((0u - tmask) >> 31) * 8192u)) : "memory");
  GAP();
  const __amdgpu_buffer_rsrc_t srdEi = mk_srd(exch + (blk ^ 4u) * 5120u, 20480u);
  auto step = [&](auto ST) __attribute__((always_inline)) {
    constexpr int st = decltype(ST)::value, A = st % (OP_LA + 1);
    if constexpr (st == 16 - OP_LA) {
      const unsigned sd = (mt - (mt ^ 1u)) * 524288u;
      o.sa[0] += sd; o.sa[1] += sd; o.sa[2] += sd; o.sa[3] += sd;
      asm volatile("" : "+s"(o.sa[0]), "+s"(o.sa[1]), "+s"(o.sa[2]), "+s"(o.sa[3]));
    }
    if constexpr (st == 16) {
    }
    if constexpr (st == 24) {
    }
    if constexpr (st == 30) {
    }
    if constexpr (st == 4) {
      asm volatile("s_waitcnt vmcnt(%1)" : "+v"(fx) : "i"(OP_FXW) : "memory");
      unsigned an[2] = {ax[0], ax[1]};
      if (__builtin_expect(tmask != 0u, 0)) {
        if (!__all(fx == tag)) wait_flag(hand + fxw, tag);
#pragma nounroll
        for (unsigned mk = tmask; mk; mk &= mk - 1u) {
          const unsigned t2 = (unsigned)__builtin_ctz(mk), ro = ((unsigned)__builtin_amdgcn_readlane(selr, t2) & 0xffffu) * XRS_;
#pragma unroll
          for (int j = 0; j < 2; ++j) { const unsigned d = ax[j] - ro; an[j] = d < XRS_ ? (SEQ + (t2 >> 1)) * XRS_ + d : an[j]; }
        }
      }
      const __amdgpu_buffer_rsrc_t sr = srdN; const unsigned so = sn, tm = tmask;
      asm volatile("s_cmp_eq_u32 %7, 0\n\ts_cbranch_scc1 1f\n\t" OP_LD3(" sc1") "\n\t1:" : "+v"(o.gv[0][0]), "+v"(o.gv[0][1]), "+v"(o.gs[0]) : "v"(an[0]), "v"(an[1]), "s"(sr), "s"(so), "s"(tm) : "scc", "memory");
    }
    if constexpr (st == OP_SD) {
      asm volatile("s_waitcnt vmcnt(%1)" : "+v"(fe) : "i"(OP_FEW) : "memory");
      if (__builtin_expect(!__all(fe == tag), 0)) wait_flag(hand + H_E + 32u * (blk ^ 4u) + wid, tag);
      const __amdgpu_buffer_rsrc_t se = srdEi; const unsigned va = o.va, sw = o.sw;
      asm volatile("buffer_load_dwordx4 %0, %5, %6, %7 offen sc1\n\tbuffer_load_dwordx4 %1, %5, %6, %7 offen offset:1024 sc1\n\tbuffer_load_dwordx4 %4, %5, %6, %8 offen sc1\n\t"
                   "buffer_load_dwordx4 %2, %5, %6, %7 offen offset:2048 sc1\n\tbuffer_load_dwordx4 %3, %5, %6, %7 offen offset:3072 sc1"
                   : "=&v"(o.pv[0]), "=&v"(o.pv[1]), "=&v"(o.pv[2]), "=&v"(o.pv[3]), "=&v"(o.pv[4]) : "v"(va), "s"(se), "s"(sw), "s"(sw + 4096u) : "memory");
    }
    if constexpr (st == 28) { o.D[1][4] = f32x4{0.f, 0.f, 0.f, 0.f}; asm volatile("s_nop 0" : "+v"(o.D[1][4])); }
    asm volatile("s_waitcnt vmcnt(%4)" : "+v"(o.fa[A][0]), "+v"(o.fa[A][1]), "+v"(o.fa[A][2]), "+v"(o.fa[A][3]) : "i"(OPW[st]) : "memory");
    if constexpr (st == 4 || st == 8) { constexpr int X = st == 4; asm volatile("" : "+v"(o.gv[X][0]), "+v"(o.gv[X][1]), "+v"(o.gs[X])); }
    if constexpr (st == OP_SF) {
      const __amdgpu_buffer_rsrc_t sf = mk_srd(hand + H_E + 32u * blk + wid, 4u); const unsigned tg = tag, va = o.va;
      asm volatile("buffer_store_dword %0, %1, %2, 0 offen sc1" :: "v"(tg), "v"(va), "s"(sf) : "memory");
    }
    GAP();
    op_qs<st>(o, make_iseq<18>{});
    GAP();
    if constexpr (st == OP_SP) { const __amdgpu_buffer_rsrc_t sh = srdH; const unsigned so = 4u * (H_E + 32u * (blk ^ 4u) + wid); asm volatile("buffer_load_dword %0, off, %1, %2 sc1" : "=v"(fe) : "s"(sh), "s"(so) : "memory"); }
    if constexpr (st == 27) { o.srdE = mk_srd(out, SEQ * DM * 2); asm volatile("" : "+s"(o.srdE)); }
    GAP();
  };
  for_seq(step, make_iseq<32>{});
  op_tail(o, make_iseq<TS[577] - TS[576]>{});
  auto fin2 = [&](const float s0, const float s1, const int wd) __attribute__((always_inline)) {
    const unsigned sp = cvtpk(s0, s1), pp = cvtpk(bflo(sp) * o.gx[2 * wd], bfhi(sp) * o.gx[2 * wd + 1]);
    return cvtpk(o.gx[18 + 2 * wd] + bflo(pp), o.gx[19 + 2 * wd] + bfhi(pp));
  };
  auto fin = [&](auto J_) __attribute__((always_inline)) {
    constexpr int j = decltype(J_)::value;
    const f32x4 s = o.D[1][2 * j] + o.T[2 * j];
    const __amdgpu_buffer_rsrc_t so = o.srdE; const unsigned sc = sn, va = ax[j >> 1];
    if constexpr (j < 2) {
      const f32x4 r = o.D[1][2 * j + 1] + o.T[2 * j + 1];
      const u32x4 ov = {fin2(s[0], s[1], 4 * j), fin2(s[2], s[3], 4 * j + 1), fin2(r[0], r[1], 4 * j + 2), fin2(r[2], r[3], 4 * j + 3)};
      asm volatile("buffer_store_dwordx4 %0, %1, %2, %3 offen offset:%c4\n\ts_nop 1" :: "v"(ov), "v"(va), "s"(so), "s"(sc), "i"(64 * j) : "memory");
    } else {
      auto p0 = __builtin_amdgcn_permlane32_swap(__float_as_uint(s[0]), __float_as_uint(s[2]), false, false), p1 = __builtin_amdgcn_permlane32_swap(__float_as_uint(s[1]), __float_as_uint(s[3]), false, false);
      const unsigned ov = fin2(__uint_as_float(p0[0]) + __uint_as_float(p0[1]), __uint_as_float(p1[0]) + __uint_as_float(p1[1]), 8);
      asm volatile("buffer_store_dword %0, %1, %2, %3 offen" :: "v"(ov), "v"(va), "s"(so), "s"(sc) : "memory");
    }
  };
  fin(IC(2)); fin(IC(0));
  GAP();
  fin(IC(1));
}

extern "C" __global__ __launch_bounds__(256, 1)
void k_attn(u16* __restrict__ xn, const u16* __restrict__ wq, const u16* __restrict__ Kc, const u16* __restrict__ Vc, u16* __restrict__ O,
            const u16* __restrict__ wo, const u16* __restrict__ wg, const u16* __restrict__ wu, const u16* __restrict__ wd, u16* __restrict__ wot,
            unsigned long long* __restrict__ keys, float* __restrict__ ypart, int* __restrict__ sel, const u16* __restrict__ xres, u16* __restrict__ out,
            const u16* __restrict__ w_r  ) {
  extern __shared__ __attribute__((aligned(16))) char lds[];
  const unsigned tid = threadIdx.x, lane = tid & 63u, wid = tid >> 6, l32 = lane & 31u, hi = lane >> 5;
  constexpr int T = NT;
  const float C = 0.12751743082f;
  W4C c;
  c.ldsb = (unsigned)(size_t)lds; c.pdum = c.ldsb + PF_DUM;
  asm volatile("" : "+s"(c.pdum));
  c.m0k = rfl(c.ldsb + KRING + 4096u * wid); c.m0v = rfl(c.ldsb + VRING + 4096u * wid);
  c.sofk = rfl(KRS * 16u * wid) + (32u / 1) * KTILEB * ((blockIdx.x >> 3) & 0u);

  const unsigned blk = blockIdx.x, xg = blk & 7u, ii = blk >> 3;
  const unsigned kvh = 2u * xg + (ii >> 4), h = 2u * kvh + ((ii >> 3) & 1u), yb = ii & 7u;
  const unsigned row0 = 256u * yb + 64u * wid;
  const bool moe = blk < (unsigned)MOE_WG;
  const bool ret = !moe && blk < 208u;
  unsigned tag = 0u, gen;
  const unsigned lrow = 8u * blk + 2u * wid + hi;
  QFT qf;
  {
    QP p; LNJ lj; u32x4 cw; u32x2 g0, g1; unsigned pb0, pb1, pb2, pb3, csw; unsigned long long pbase;
    lj.three = l32 < 8u; lj.up = hi != 0u;
    const unsigned vl = 16u * l32 + XRS * hi, vl3 = lj.three ? vl + 1024u : 0x40000000u, srow = XRS * 8u * blk + XRS * 2u * rfl(wid);
    {
      const unsigned r8 = (lane >> 2) & 7u, vws = WRS * ((r8 & 3u) + 8u * (r8 >> 2)) + 64u * hi + 16u * (lane & 3u);
      p.vw[0] = vws + 896u; p.vw[1] = vws; p.vw[2] = vws + (4u * WRS - 2048u + 896u); p.vw[3] = vws + (4u * WRS - 2048u);
#pragma unroll
      for (int q = 0; q < 4; ++q) p.vx[q] = XRS * (lane >> 3) + 16u * ((lane & 7u) ^ (4u * (q & 1) + (lane >> 4))) + (8u * XRS - 1024u) * q;
    }
    p.m0x = rfl(c.ldsb + 0x4000u + 8192u * wid);
    p.sofw = rfl(WRS * 16u * wid + 256u * h);
    p.sofx = rfl(XRS * row0);
    p.kk = (yb ^ (ii >> 3)) & 1u;
    asm volatile("s_nop 4" : "+s"(c.m0k), "+s"(c.m0v), "+s"(c.sofk), "+s"(p.m0x), "+s"(p.sofw), "+s"(p.sofx), "+s"(p.kk));
    asm volatile("" : "+v"(p.vw[0]), "+v"(p.vw[1]), "+v"(p.vw[2]), "+v"(p.vw[3]), "+v"(p.vx[0]), "+v"(p.vx[1]), "+v"(p.vx[2]), "+v"(p.vx[3]));
    GAP();
    p.m0w = c.m0k;
    p.srdW = __builtin_amdgcn_make_buffer_rsrc((void*)((const char*)wq - 896), (short)0, DM * HQ * DH * 2 + 896, SRD_WORD3);
    p.srdX = __builtin_amdgcn_make_buffer_rsrc((void*)xres, (short)0, SEQ * DM * 2, SRD_WORD3);
    auto vm = [&](auto I_) __attribute__((always_inline)) {
      constexpr int I = decltype(I_)::value; constexpr VOp o = VT.v[I]; constexpr bool nm = vq_newm0(I); constexpr int q = o.q & 3;
      if constexpr (o.kind == VK_S) {
        if constexpr (o.q == 0 && o.st < 2) qp_next<o.st>(p);
        if constexpr (o.q < 4) lds_dma<nm, 1024 * q>(p.m0w + QP_STG * (o.st % 3), p.vw[q], p.srdW, p.sw);
        else {
          if constexpr (o.q == 8) { p.sx += 32u * XRS; asm volatile("" : "+s"(p.sx)); }
          lds_dma<nm, 1024 * q>(p.m0x + QP_STG * (o.st % 3) + (o.q >= 8 ? 4096u : 0u), p.vx[q], p.srdX, p.sx);
        }
      } else if constexpr (o.kind == VK_W8) lds_dma<nm, 1024 * q>(p.m0w + QP_W8, p.vw[q], p.srdW, p.sw);
      else if constexpr (o.kind == VK_KV) {
        if constexpr (o.q < 8) lds_dma<nm, 1024 * q>(c.m0k + 0x4000u * (o.q >> 2), (q & 2) ? c.ksx[q & 1] : c.vksrc[q & 1], c.srdK, KTILEB * (o.q >> 2) + c.sofk);
        else lds_dma<true, 0>(c.m0v + 1024u * q, c.vvsrc, c.srdV, c.sofk + (896u + KRS * 8u * (q >> 1) + 128u * (q & 1)));
      } else if constexpr (o.kind == VK_V1) lds_dma<nm, 1024 * q>(c.m0v + 0x4000u, (q & 1) ? c.vvsrc : c.vvsrc0, c.srdV, KTILEB + c.sofk);
      else if constexpr (o.kind == VK_K2) lds_dma<nm, 1024 * q>(c.m0k, (q & 2) ? c.ksx[q & 1] : c.vksrc[q & 1], c.srdK, (KTILEB * 2u + c.sofk) & KMASK);
      else if constexpr (o.kind == VK_PF)
        asm volatile("s_mov_b32 m0, %0\n\ts_add_u32 %1, %1, %4\n\tbuffer_load_dwordx4 %2, %3, %1 offen sc1 lds\n\ts_add_u32 %0, %0, 0x400"
                     : "+s"(c.pfm0), "+s"(c.pfo) : "v"(c.pfv), "s"(c.srdP), "s"(c.pfst) : "m0", "memory", "scc");
      else if constexpr (o.kind == VK_CW) {
        unsigned long long pa, po;
        asm volatile("s_getpc_b64 s[20:21]\n\ts_add_u32 s20, s20, k_attn@rel32@lo+4\n\ts_addc_u32 s21, s21, k_attn@rel32@hi+12\n\ts_mov_b64 %0, s[20:21]" : "=s"(pa) :: "scc", "s20", "s21");
        asm volatile("s_getpc_b64 s[20:21]\n\ts_add_u32 s20, s20, k_attn_end@rel32@lo+4\n\ts_addc_u32 s21, s21, k_attn_end@rel32@hi+12\n\ts_mov_b64 %0, s[20:21]" : "=s"(po) :: "scc", "s20", "s21");
        const __amdgpu_buffer_rsrc_t srdC = __builtin_amdgcn_make_buffer_rsrc((void*)pa, (short)0, (int)(po - pa), SRD_WORD3);
        asm volatile("buffer_load_dwordx4 %0, %1, %2, %3 offen" : "=&v"(cw) : "v"(16u * tid), "s"(srdC), "s"((blk >> 3) * 4096u) : "memory");
      } else if constexpr (o.kind == VK_GEN)
        asm volatile("buffer_load_dword %0, off, %1, 0 offset:%c2 sc1" : "=&v"(gen) : "s"(__builtin_amdgcn_make_buffer_rsrc((void*)sel, (short)0, 8192, SRD_WORD3)), "i"(4 * H_G) : "memory");
      else if constexpr (o.kind == VK_A)
        asm volatile("buffer_load_dwordx4 %0, %1, %2, %3 offen offset:%c4" : "=&v"(lj.a[o.q]) : "v"(o.q == 2 ? vl3 : vl), "s"(p.srdX), "s"(srow), "i"(o.q == 1 ? 512 : 0) : "memory");
      else if constexpr (o.kind == VK_WR)
        asm volatile("buffer_load_dwordx4 %0, %1, %2, 0 offen offset:%c3" : "=&v"(lj.w[o.q]) : "v"(16u * l32), "s"(__builtin_amdgcn_make_buffer_rsrc((void*)w_r, (short)0, DM * 2, SRD_WORD3)), "i"(512 * o.q) : "memory");
      else if constexpr (o.kind == VK_STS || o.kind == VK_KEY) {
        const u32x2 g = {o.kind == VK_STS ? lj.stat : lj.key, tag};
        asm volatile("buffer_store_dwordx2 %0, %1, %2, %3 offen sc1" :: "v"(g), "v"(l32 == 0u ? 8u * lrow : 0x40000000u),
                     "s"(__builtin_amdgcn_make_buffer_rsrc((void*)keys, (short)0, SEQ * 16, SRD_WORD3)), "s"(o.kind == VK_STS ? SEQ * 8u : 0u) : "memory");
      } else if constexpr (o.kind == VK_GAT) {
        asm volatile("buffer_load_dwordx2 %0, %1, %2, %3 offen offset:%c4 sc1" : "=&v"(o.q ? g1 : g0) : "v"(8u * (row0 + l32)),
                     "s"(__builtin_amdgcn_make_buffer_rsrc((void*)keys, (short)0, SEQ * 16, SRD_WORD3)), "s"(SEQ * 8u), "i"(256 * o.q) : "memory");
      } else if constexpr (o.kind == VK_XNS) {
        const __amdgpu_buffer_rsrc_t srdN = __builtin_amdgcn_make_buffer_rsrc((void*)xn, (short)0, SEQ * DM * 2, SRD_WORD3);
        asm volatile("buffer_store_dwordx4 %0, %1, %2, %3 offen offset:%c4 sc1\n\ts_nop 1" :: "v"(lj.a[o.q]), "v"(o.q == 2 ? vl3 : vl), "s"(srdN), "s"(srow), "i"(o.q == 1 ? 512 : 0) : "memory");
      }
    };
    auto hook = [&](auto P_) __attribute__((always_inline)) {
      constexpr int pos = decltype(P_)::value;
      if constexpr (pos == 1) {
        asm volatile("s_waitcnt vmcnt(%5)" : "+v"(gen), "+v"(lj.a[0]), "+v"(lj.a[1]), "+v"(lj.a[2]), "+v"(cw) : "i"(vq_wait(VK_CW, 0, pos + 1)) : "memory");
        tag = rfl(gen) * 1664525u + 1013904223u;
        asm volatile("" : "+s"(tag));
      } else if constexpr (pos == 32 + 13) asm volatile("" : "+v"(lj.w[0]), "+v"(lj.w[1]), "+v"(lj.w[2]));
      else if constexpr (pos == 32 + 1) {
        c.vkb[0] = c.ldsb + KRING + 256u * (lane & 15u) + 16u * ((lane >> 4) ^ (lane & 15u));
        asm volatile("" : "+v"(c.vkb[0]));
      } else if constexpr (pos == 32 + 3) {
#pragma unroll
        for (int j = 1; j < 4; ++j) c.vkb[j] = c.vkb[0] ^ (64u * j);
        asm volatile("" : "+v"(c.vkb[1]), "+v"(c.vkb[2]), "+v"(c.vkb[3]));
      } else if constexpr (pos == 32 + 5) {
        c.vvb[0] = c.ldsb + VRING + (((lane & 3u) << 3) | (((lane >> 2) & 3u) << 6) | (((lane >> 4) & 1u) * 288u) | (hi << 11));
        c.vvb[1] = c.vvb[0] ^ 32u;
        asm volatile("" : "+v"(c.vvb[0]), "+v"(c.vvb[1]));
      } else if constexpr (pos == 32 + 7) {
#pragma unroll
        for (int par = 0; par < 2; ++par) c.vksrc[par] = KRS * (lane >> 4) + 16u * ((lane & 15u) ^ (4u * par + (lane >> 4)));
        c.ksx[0] = c.vksrc[0] ^ 128u; c.ksx[1] = c.vksrc[1] ^ 128u;
        asm volatile("" : "+v"(c.vksrc[0]), "+v"(c.vksrc[1]), "+v"(c.ksx[0]), "+v"(c.ksx[1]));
      } else if constexpr (pos == 32 + 9) {
        c.vvsrc = KRS * ((lane >> 2) & 7u) + 64u * hi + 16u * ((lane & 3u) ^ ((lane >> 3) & 2u)); c.vvsrc0 = c.vvsrc + 896u;
        asm volatile("" : "+v"(c.vvsrc), "+v"(c.vvsrc0));
      } else if constexpr (pos == 32 + 0) {
        const unsigned rs = moe ? FF * 2u : ret ? DM * 2u : 64u;
        const unsigned r16 = ret ? ((lane >> 2) & 3u) | (lane & 16u) | ((lane >> 3) & 4u) : lane >> 2;
        c.pfv = rs * r16 + 16u * ((lane & 3u) ^ ((lane >> 4) & 2u));
        asm volatile("" : "+v"(c.pfv));
      } else if constexpr (pos == 29) {
        const unsigned w = rfl(wid), fb = (blk & 7u) * 18u + (blk >> 3);
        pb0 = 64u * fb + 16u * FF * 2u * (w & 1u); pb1 = ((blk - 144u) * 4u + w) * 18432u - 9216u * (w & 1u); pb2 = ((blk - 208u) * 4u + w) * 27648u; pb3 = 18432u * w;
        const u16 *a0 = wg, *a1 = wu;
        asm volatile("s_cmp_lt_u32 %1, 2\n\ts_cselect_b64 %0, %2, %3" : "=s"(pbase) : "s"(w), "s"(a0), "s"(a1) : "scc");
        asm volatile("" : "+s"(pb0), "+s"(pb1), "+s"(pb2), "+s"(pb3));
      } else if constexpr (pos == 31) {
        const u16 *a0 = wo, *a1 = wd; const unsigned b = blk;
        unsigned long long pb; unsigned nb, st, np;
        asm volatile("s_cmp_lt_u32 %8, 208\n\ts_cselect_b64 %0, %9, %10\n\ts_cselect_b32 %5, %5, %6\n\ts_mov_b32 %1, %12\n\ts_cselect_b32 %2, 64, %13\n\ts_cselect_b32 %7, %7, 0\n\ts_cselect_b32 %3, %11, %14\n\t"
                     "s_cmp_lt_u32 %8, 144\n\ts_cselect_b64 %0, %15, %0\n\ts_cselect_b32 %5, %4, %5\n\ts_cselect_b32 %1, %16, %1\n\ts_cselect_b32 %2, %17, %2"
                     : "=&s"(pb), "=&s"(nb), "=&s"(st), "=&s"(np), "+s"(pb0), "+s"(pb1), "+s"(pb2), "+s"(pb3)
                     : "s"(b), "s"(a0), "s"(a1), "s"(18432u), "s"(27648u), "s"(1024u), "s"(20480u), "s"(pbase), "s"(559u * (FF * 2u) + 64u), "s"(FF * 2u * 32u) : "scc");
        pbase = pb; pb0 = nb; pb2 = np; c.pfst = st;
      } else if constexpr (pos == 32 + 15) {
        c.srdP = __builtin_amdgcn_make_buffer_rsrc((void*)(pbase + pb1), (short)0, (int)pb0, SRD_WORD3);
        c.pfo = 0u - c.pfst;
        c.pfm0 = c.ldsb + SD_BASE + pb3; c.pfend = c.pfm0 + pb2;
        asm volatile("" : "+s"(c.srdP), "+s"(c.pfst), "+s"(c.pfo), "+s"(c.pfm0), "+s"(c.pfend));
      } else if constexpr (pos == 32 + 17) {
        unsigned kvo = kvh; asm volatile("" : "+s"(kvo));
        c.srdK = __builtin_amdgcn_make_buffer_rsrc((void*)((const char*)Kc + (size_t)kvo * SEQ * 256), (short)0, SEQ * 256, SRD_WORD3);
        c.srdV = __builtin_amdgcn_make_buffer_rsrc((void*)((const char*)Vc + (size_t)kvo * SEQ * 256 - 896), (short)0, SEQ * 256 + 896, SRD_WORD3);
        asm volatile("" : "+s"(c.srdK), "+s"(c.srdV));
      } else if constexpr (pos == 64 + 1) {
        unsigned kvo = kvh; asm volatile("" : "+s"(kvo));
        const unsigned id = ((blk >> 3) & 15u) * 4u + rfl(wid);
        const u16 *a0 = Kc, *a1 = Vc; unsigned long long pb;
        asm volatile("s_cmp_lt_u32 %1, 32\n\ts_cselect_b64 %0, %2, %3" : "=s"(pb) : "s"(id), "s"(a0), "s"(a1) : "scc");
        c.srdF = __builtin_amdgcn_make_buffer_rsrc((void*)(pb + (size_t)kvo * SEQ * 256), (short)0, SEQ * 256, SRD_WORD3);
        c.sfo = (c.sofk & ~(KTILEB - 1u)) + KTILEB * (LAH + ((id >> 4) & 1u) - (id >> 5)) + 1024u * (id & 15u);
        asm volatile("" : "+s"(c.srdF), "+s"(c.sfo));
      } else if constexpr (pos == 192 + 0) {
        asm volatile("s_waitcnt vmcnt(%2)" : "+v"(g0), "+v"(g1) : "i"(vq_wait(VK_GAT, 0, pos + 1)) : "memory");
        if (__builtin_expect(!__all(g0[1] == tag && g1[1] == tag), 0)) {
          const unsigned long long* stg = keys + SEQ + row0 + l32;
#pragma nounroll
          for (unsigned spin = 0; spin < (1u << 20) && !__all(g0[1] == tag && g1[1] == tag); ++spin) {
            __builtin_amdgcn_s_sleep(6);
            asm volatile("global_load_dwordx2 %0, %2, off sc1\n\tglobal_load_dwordx2 %1, %2, off offset:256 sc1\n\ts_waitcnt vmcnt(0)" : "=&v"(g0), "=&v"(g1) : "v"(stg) : "memory");
          }
        }
      } else if constexpr (pos == 192 + 2 || pos == 192 + 3) {
        constexpr int q = pos == 192 + 3;
        qf.a[q] = C * rcp_nr(bflo((q ? g1 : g0)[0]));
        asm volatile("" : "+v"(qf.a[q]));
      } else if constexpr (pos == 192 + 5 || pos == 192 + 25) {
        constexpr int q = pos == 192 + 25;
        const unsigned n = hi == 0u ? ((q ? g1 : g0)[0] >> 16) ^ 0x8000u : 0u;
        qf.nb[q] = u32x4{n | (n << 16), n, 0u, 0u};
        asm volatile("" : "+v"(qf.nb[q]));
      } else if constexpr (pos == 192 + 30) {
        const f32x4 cs = p.cs;
        asm volatile("ds_write_b128 %0, %1" :: "v"(csw + 0u), "v"(cs) : "memory");
      } else if constexpr (pos == 192 + 22) {
        unsigned a0 = 128u * wid + 64u * (lane & 1u) + 16u * (lane >> 4), a1 = 1024u + 16u * tid;
        asm volatile("" : "+v"(a0), "+v"(a1));
        csw = c.ldsb + QP_CS + ((lane & 14u) == 0u ? a0 : a1);
        asm volatile("" : "+v"(csw));
      }
    };
    asm volatile("" :: "s"(xn), "s"(Kc), "s"(Vc), "s"(O), "s"(wo), "s"(wg), "s"(wu), "s"(wd), "s"(wot), "s"(keys), "s"(ypart), "s"(out), "s"(w_r));
    vq_issue<-1>(vm);
    p.vwb = c.ldsb + ((((lane & 1u) << 5) | ((lane & 2u) << 2)) | (((lane >> 2) & 3u) << 6) | (lane & 16u) | (hi << 8));
#pragma unroll
    for (int ks = 0; ks < 4; ++ks) p.vxb[ks] = c.ldsb + 0x4000u + 8192u * wid + 128u * l32 + 16u * ((2u * ks + hi) ^ ((l32 >> 1) & 7u));
    p.vwc = p.vwb + 512u * wid;
    { const unsigned on = (lane & 15u) == ((lane >> 4) & 1u) ? 0x3f803f80u : 0u; p.sel = u32x4{on, on, on, on}; }
    GAP();
    asm volatile("s_waitcnt vmcnt(%0)\n\ts_barrier" :: "i"(vq_wait(VK_S, 0, 0)) : "memory");
    qp_rds<0, 8>(p, make_iseq<20>{});
    qp_iters(p, vm, hook, lj, lrow, make_iseq<7>{});
    asm volatile("s_waitcnt lgkmcnt(5)" ::: "memory");
#pragma unroll
    for (int d = 0; d < 4; ++d) qf.ca[d] = u32x4{0u, 0u, 0u, 0u};
    GAP();
    qj_gaps(p, c, qf, vm, c.ldsb + QP_CS + 4u * l32, make_iseq<NJ>{});
  }
  {
    const unsigned on = (lane & 2u) == 0u ? 0x3f803f80u : 0u; c.ONE = u32x4{on, on, on, on}; c.IDX = (lane & 1u) ? 0xeeeeeeeeu : 0x44444444u;
    c.L[0] = c.L[1] = c.L[2] = c.L[3] = f32x4{0.f, 0.f, 0.f, 0.f};
    asm volatile("" : "+v"(c.ONE), "+v"(c.IDX), "+v"(c.L[0]), "+v"(c.L[1]), "+v"(c.L[2]), "+v"(c.L[3]));
  }
  unsigned rstate = 3u;
  auto route = [&]() __attribute__((always_inline)) {
    unsigned lq;
    asm volatile("v_mbcnt_lo_u32_b32 %0, -1, 0\n\tv_mbcnt_hi_u32_b32 %0, -1, %0" : "=v"(lq));
    const unsigned w = rfl(wid), kb_ = c.ldsb + MO_KEY + 4096u * w;
    bool req = rstate == 0u;
    if (rstate == 1u) {
      u32x4 g[4];
      asm volatile("ds_read_b128 %0, %4\n\tds_read_b128 %1, %4 offset:1024\n\tds_read_b128 %2, %4 offset:2048\n\tds_read_b128 %3, %4 offset:3072\n\ts_waitcnt lgkmcnt(0)"
                   : "=&v"(g[0]), "=&v"(g[1]), "=&v"(g[2]), "=&v"(g[3]) : "v"(kb_ + 16u * lq) : "memory");
      bool ok = true;
#pragma unroll
      for (int j = 0; j < 4; ++j) ok = ok && g[j][1] == tag && g[j][3] == tag;
      if (__all(ok)) {
        unsigned k[8] = {g[0][0], g[0][2], g[1][0], g[1][2], g[2][0], g[2][2], g[3][0], g[3][2]};
        ce(k[0], k[1]); ce(k[2], k[3]); ce(k[4], k[5]); ce(k[6], k[7]);
        ce(k[0], k[2]); ce(k[1], k[3]); ce(k[4], k[6]); ce(k[5], k[7]);
        ce(k[1], k[2]); ce(k[5], k[6]); ce(k[0], k[4]); ce(k[3], k[7]);
        ce(k[1], k[5]); ce(k[2], k[6]);
        ce(k[1], k[4]); ce(k[3], k[6]);
        ce(k[2], k[4]); ce(k[3], k[5]);
        ce(k[3], k[4]);
        unsigned cnd = 0u;
#pragma nounroll
        for (unsigned r = 0; r < (unsigned)NSEL; ++r) {
          unsigned m = k[0], t;
          t = (unsigned)__builtin_amdgcn_update_dpp(0, (int)m, 0xB1, 0xf, 0xf, false); m = t > m ? t : m;
          t = (unsigned)__builtin_amdgcn_update_dpp(0, (int)m, 0x4E, 0xf, 0xf, false); m = t > m ? t : m;
          t = (unsigned)__builtin_amdgcn_update_dpp(0, (int)m, 0x141, 0xf, 0xf, false); m = t > m ? t : m;
          t = (unsigned)__builtin_amdgcn_update_dpp(0, (int)m, 0x140, 0xf, 0xf, false); m = t > m ? t : m;
          cnd = (lq & 15u) == (unsigned)r ? m : cnd;
          const bool pp = k[0] == m;
#pragma unroll
          for (int i = 0; i < 7; ++i) k[i] = pp ? k[i + 1] : k[i];
        }
        lds_st32(c.ldsb + MO_RS + ((lq & 8u) == 0u ? 64u + 128u * w + 2u * (lq & 48u) + 4u * (lq & 7u) : 640u + 4u * lq), cnd);
        asm volatile("s_waitcnt lgkmcnt(0)" ::: "memory");
        lds_st32(c.ldsb + MO_RS + 4u * w, 1u);
        rstate = 2u;
      } else req = true;
    } else if (rstate == 2u) {
      u32x4 fl;
      asm volatile("ds_read_b128 %0, %1\n\ts_waitcnt lgkmcnt(0)" : "=v"(fl) : "v"(c.ldsb + MO_RS) : "memory");
      if (rfl(fl[0] & fl[1] & fl[2] & fl[3]) != 0u) {
        unsigned cv, rank = 0u;
        asm volatile("ds_read_b32 %0, %1" : "=v"(cv) : "v"(c.ldsb + MO_RS + 64u + 128u * w + 4u * (lq & 31u)) : "memory");
#pragma nounroll
        for (unsigned h = 0; h < 4u; ++h) {
          u32x4 c4v[4];
          asm volatile("ds_read_b128 %0, %5\n\tds_read_b128 %1, %5 offset:16\n\tds_read_b128 %2, %5 offset:32\n\tds_read_b128 %3, %5 offset:48\n\ts_waitcnt lgkmcnt(0)"
                       : "=&v"(c4v[0]), "=&v"(c4v[1]), "=&v"(c4v[2]), "=&v"(c4v[3]), "+v"(cv) : "v"(c.ldsb + MO_RS + 64u + 256u * (lq >> 5) + 64u * h) : "memory");
#pragma unroll
          for (int j = 0; j < 4; ++j)
#pragma unroll
            for (int e = 0; e < 4; ++e) rank += c4v[j][e] > cv ? 1u : 0u;
        }
        auto pr = __builtin_amdgcn_permlane32_swap(rank, rank, false, false);
        rank = pr[0] + pr[1];
        {
          const float rs = rbf(__builtin_amdgcn_rcpf(1.f + __builtin_amdgcn_exp2f(key_val(cv) * -1.4426950408889634f)));
          const u32x2 gr = {(__float_as_uint(rs) & 0xffff0000u) | (unsigned)key_row(cv), tag};
          const unsigned vo = lq < 32u && rank < 8u ? 8u * rank : 0x40000000u, vg = (lq | w) == 0u ? 4u * H_G : 0x40000000u;
          asm volatile("s_nop 4\n\tbuffer_store_dwordx2 %0, %1, %3, 0 offen sc1\n\tbuffer_store_dword %4, %2, %3, 0 offen sc1\n\ts_nop 1" :: "v"(gr), "v"(vo), "v"(vg), "s"(mk_srd(sel, 8192u)), "v"(gr[1]) : "memory");
        }
        rstate = 3u;
      }
    }
    if (req) {
      const __amdgpu_buffer_rsrc_t srdK = mk_srd((keys + 512u * w), 4096);
#pragma unroll
      for (int j = 0; j < 4; ++j)
        asm volatile("s_mov_b32 m0, %0\n\ts_nop 4\n\tbuffer_load_dwordx4 %1, %2, %3 offen sc1 lds" :: "s"(kb_ + 1024u * j), "v"(16u * lq), "s"(srdK), "s"(1024u * j) : "m0", "memory");
      rstate = 1u;
    }
  };
  const unsigned wids = rfl(wid);
  auto opq = [&](unsigned v) __attribute__((always_inline)) { asm volatile("" : "+s"(v)); return v; };
  auto lane_id = [&]() __attribute__((always_inline)) { unsigned lq; asm volatile("v_mbcnt_lo_u32_b32 %0, -1, 0\n\tv_mbcnt_hi_u32_b32 %0, -1, %0" : "=v"(lq)); return lq; };
  auto hand_req = [&](const unsigned m0, const unsigned word0, const unsigned nb) __attribute__((always_inline)) {
    const unsigned vo = c.vksrc[0];
    asm volatile("s_mov_b32 m0, %0\n\ts_nop 0\n\tbuffer_load_dwordx4 %1, %2, 0 offen sc1 lds" :: "s"(m0), "v"(vo), "s"(mk_srd(sel + word0, nb)) : "m0", "memory");
  };
  auto flag1 = [&](const unsigned word, const unsigned on) __attribute__((always_inline)) {
    const unsigned vo = c.vvsrc, tg = tag;
    asm volatile("buffer_store_dword %0, %1, %2, 0 offen sc1" :: "v"(tg), "v"(vo), "s"(mk_srd(sel + word, on)) : "memory");
  };
  auto pf1 = [&]() __attribute__((always_inline)) {
    asm volatile("s_mov_b32 m0, %0\n\ts_add_u32 %1, %1, %4\n\tbuffer_load_dwordx4 %2, %3, %1 offen sc1 lds\n\ts_add_u32 %0, %0, 0x400"
                 : "+s"(c.pfm0), "+s"(c.pfo) : "v"(c.pfv), "s"(c.srdP), "s"(c.pfst) : "m0", "memory", "scc");
  };
  auto m0_set = [&](const unsigned m0) __attribute__((always_inline)) { asm volatile("s_mov_b32 m0, %0" :: "s"(m0) : "m0", "memory"); };
  auto dma16 = [&](const __amdgpu_buffer_rsrc_t sr, const unsigned vo) __attribute__((always_inline)) { asm volatile("buffer_load_dwordx4 %0, %1, 0 offen sc1 lds" :: "v"(vo), "s"(sr) : "memory"); };
  auto flag_st = [&](const __amdgpu_buffer_rsrc_t sr, const unsigned tg) __attribute__((always_inline)) { const unsigned vo = c.vvsrc; asm volatile("buffer_store_dword %0, %1, %2, 0 offen sc1" :: "v"(tg), "v"(vo), "s"(sr) : "memory"); };
  struct CW { unsigned long long pa; unsigned so; __amdgpu_buffer_rsrc_t sr; };
  auto warm_step = [&](CW& k, auto I_) __attribute__((always_inline)) {
    constexpr int I = decltype(I_)::value;
    if constexpr (I == 0) asm volatile("s_getpc_b64 s[20:21]\n\ts_add_u32 s20, s20, k_attn@rel32@lo+4\n\ts_addc_u32 s21, s21, k_attn@rel32@hi+12\n\ts_mov_b64 %0, s[20:21]" : "=s"(k.pa) :: "scc", "s20", "s21");
    if constexpr (I == 1) { unsigned nb; asm volatile("s_mov_b32 %0, k_attn_end-k_attn" : "=s"(nb)); k.sr = __builtin_amdgcn_make_buffer_rsrc((void*)k.pa, (short)0, (int)nb, SRD_WORD3); asm volatile("" : "+s"(k.sr)); }
    if constexpr (I == 2) { k.so = (opq(blk) >> 3) * 4096u + (c.m0k - c.ldsb) / 4u; asm volatile("" : "+s"(k.so)); }
    if constexpr (I == 3) m0_set(c.pdum);
    if constexpr (I == 4) { const unsigned vo = c.vksrc[0], so = k.so; const __amdgpu_buffer_rsrc_t sr = k.sr; asm volatile("buffer_load_dwordx4 %0, %1, %2 offen lds" :: "v"(vo), "s"(sr), "s"(so) : "memory"); }
  };
  auto warm_hook = [&](CW& k, auto M_) __attribute__((always_inline)) {
    constexpr int M = decltype(M_)::value, i = M == FB(0) ? 0 : M == FB(1) ? 1 : M == FB(2) ? 2 : M == 39 ? 3 : M == 40 ? 4 : -1;
    if constexpr (i >= 0) warm_step(k, std::integral_constant<int, i>{});
  };
  phaseA_end<2>(c);
  {
    CW k;
    auto hook = [&](auto M_) __attribute__((always_inline)) { warm_hook(k, M_); };
    phaseB_<1, true, true, 2>(c, hook, make_iseq<NGB>{});
  }
  GAP();
  if (__builtin_expect(opq(blk) == RTB, 0)) {
    {
      const unsigned w = opq(wids), kb_ = c.ldsb + MO_KEY + 4096u * w;
      RTS r; __amdgpu_buffer_rsrc_t sk;
      auto hookA2 = [&](auto M_) __attribute__((always_inline)) {
        constexpr int M = decltype(M_)::value, n = fa_of(M);
        if constexpr (n == 0) { r.lq = lane_id(); asm volatile("" : "+v"(r.lq)); }
        if constexpr (n == 1) { sk = mk_srd(keys + 512u * w, 4096); asm volatile("" : "+s"(sk)); }
        if constexpr (n == 2) { r.va = 16u * r.lq; m0_set(kb_); asm volatile("" : "+v"(r.va)); }
        if constexpr (n == 4 || n == 5 || n == 7 || n == 9) {
          constexpr int j = n == 4 ? 0 : n == 5 ? 1 : n == 7 ? 2 : 3;
          const unsigned v = r.va; const __amdgpu_buffer_rsrc_t sr = sk;
          asm volatile("buffer_load_dwordx4 %0, %1, 0 offen offset:%c2 sc1 lds" :: "v"(v), "s"(sr), "i"(1024 * j) : "memory");
        }
      };
      phaseAh<0, 7>(c, 2, hookA2);
      phaseB<0, 2>(c);
      auto hookA3 = [&](auto M_) __attribute__((always_inline)) { if constexpr (decltype(M_)::value == FA(0)) { r.va += kb_; asm volatile("" : "+v"(r.va)); } };
      phaseAh<1, 2>(c, 3, hookA3);
      auto steps = [&](auto I0_, auto G0_, auto M_) __attribute__((always_inline)) {
        constexpr int M = decltype(M_)::value, I = decltype(I0_)::value + M - 100 - decltype(G0_)::value;
        if constexpr (M >= 100 + decltype(G0_)::value && I < RT_END) rt_step<I>(r, tag);
      };
      auto hookB3 = [&](auto M_) __attribute__((always_inline)) {
        constexpr int M = decltype(M_)::value;
        if constexpr (M == 2 || M == 4 || M == 5 || M == 7) {
          constexpr int j = M == 2 ? 0 : M == 7 ? 3 : M - 3;
          u32x4& d = r.g[j]; const unsigned v = r.va;
          asm volatile("ds_read_b128 %0, %1 offset:%2" : "=v"(d) : "v"(v), "i"(1024 * j) : "memory");
        }
        if constexpr (M == FB(0)) { u32x4 (&g)[4] = r.g; asm volatile("s_waitcnt lgkmcnt(%4)" : "+v"(g[0]), "+v"(g[1]), "+v"(g[2]), "+v"(g[3]) : "i"(kyo(7, FB(0))) : "memory"); }
        steps(IC(0), IC(FB(0)), M_);
      };
      phaseBh<1, 2>(c, hookB3);
      auto hookA4 = [&](auto M_) __attribute__((always_inline)) {
        constexpr int M = decltype(M_)::value;
        steps(IC(NGB - FB(0)), IC(0), M_);
        if constexpr (M == FA(6)) { r.va = (r.lq & 8u) == 0u ? 64u + 128u * w + 2u * (r.lq & 48u) + 4u * (r.lq & 7u) : 640u + 4u * r.lq; asm volatile("" : "+v"(r.va)); }
        if constexpr (M == FA(8)) lds_st32(c.ldsb + MO_RS + r.va, r.cnd);
        if constexpr (M == FA(9)) lds_st32(c.ldsb + MO_RS + 4u * w, __all(r.bad == 0u) ? 1u : 0u);
        if constexpr (M == FA(10)) { r.va = c.ldsb + MO_RS + 64u + 256u * (r.lq >> 5); r.m = c.ldsb + MO_RS + 64u + 128u * w + 4u * (r.lq & 31u); r.rank = 0u; asm volatile("" : "+v"(r.va), "+v"(r.m), "+v"(r.rank)); }
      };
      static_assert(NGB - FB(0) + FA(6) >= RT_END, "");
      phaseAh<0, 3>(c, 4, hookA4);
      auto hookB4 = [&](auto M_) __attribute__((always_inline)) {
        constexpr int M = decltype(M_)::value;
        if constexpr (M == 0) { unsigned& d = r.cv; u32x4& f = r.g[3]; const unsigned v = r.m, v2 = c.ldsb + MO_RS; asm volatile("ds_read_b32 %0, %2\n\tds_read_b128 %1, %3" : "=&v"(d), "=&v"(f) : "v"(v), "v"(v2) : "memory"); }
        if constexpr (M < 60 && M % 4 == 0) { u32x4& d = r.g[(M >> 2) % 3]; const unsigned v = r.va; asm volatile("ds_read_b128 %0, %1 offset:%2" : "=v"(d) : "v"(v), "i"(4 * M) : "memory"); }
        if constexpr (M >= 5 && M < 65 && M % 4 == 1) { u32x4 &d = r.g[((M - 5) >> 2) % 3], &f = r.g[3]; unsigned& cv = r.cv; asm volatile("s_waitcnt lgkmcnt(%3)" : "+v"(d), "+v"(cv), "+v"(f) : "i"(kyo(M - 5, M) + (M < 61)) : "memory"); }
        if constexpr (M >= 105 && M < 165) { unsigned long long& pm = r.pm[M & 1]; const unsigned a = r.g[((M - 105) >> 2) % 3][(M - 105) & 3], cv = r.cv; asm volatile("v_cmp_gt_u32_e64 %0, %1, %2" : "=s"(pm) : "v"(a), "v"(cv)); }
        if constexpr (M >= 106 && M < 166) cnt_s(r.rank, r.pm[(M - 1) & 1]);
        if constexpr (M == 165) { r.bad = r.g[3][0] & r.g[3][1] & r.g[3][2] & r.g[3][3]; asm volatile("" : "+v"(r.bad)); }
        if constexpr (M == 66) { u32x4& d = r.g[0]; const unsigned v = r.va; asm volatile("ds_read_b128 %0, %1 offset:240" : "=v"(d) : "v"(v) : "memory"); }
      };
      phaseBh<0, 2>(c, hookB4);
      auto hookA5 = [&](auto M_) __attribute__((always_inline)) {
        constexpr int M = decltype(M_)::value;
        if constexpr (M == 4) { u32x4& d = r.g[0]; asm volatile("s_waitcnt lgkmcnt(3)" : "+v"(d) :: "memory"); }
        if constexpr (M >= 104 && M < 108) { r.rank += r.g[0][M - 104] > r.cv ? 1u : 0u; asm volatile("" : "+v"(r.rank)); }
        if constexpr (M == FA(1)) { auto pr = __builtin_amdgcn_permlane32_swap(r.rank, r.rank, false, false); r.rank = pr[0] + pr[1]; asm volatile("" : "+v"(r.rank)); }
        if constexpr (M == FA(2)) { r.rs = key_val(r.cv) * -1.4426950408889634f; asm volatile("" : "+v"(r.rs)); }
        if constexpr (M == FA(3)) { r.rs = 1.f + __builtin_amdgcn_exp2f(r.rs); asm volatile("" : "+v"(r.rs)); }
        if constexpr (M == FA(4)) { r.rs = rbf(__builtin_amdgcn_rcpf(r.rs)); asm volatile("" : "+v"(r.rs)); }
        if constexpr (M == FA(5)) { r.m = (__float_as_uint(r.rs) & 0xffff0000u) | (unsigned)key_row(r.cv); r.bad = (r.bad - 1u) & 0x40000000u; asm volatile("" : "+v"(r.m), "+v"(r.bad)); }
        if constexpr (M == FA(7)) { r.va = (r.lq < 32u && r.rank < 8u ? 8u * r.rank : 0x40000000u) | r.bad; asm volatile("" : "+v"(r.va)); }
        if constexpr (M == 100 + FA(8)) { r.cnd = ((r.lq | w) == 0u ? 4u * H_G : 0x40000000u) | r.bad; sk = mk_srd(sel, 8192u); asm volatile("" : "+v"(r.cnd), "+s"(sk)); }
        if constexpr (M == FA(9)) {
          const u32x2 gr = {r.m, tag}; const unsigned vo = r.va, vg = r.cnd; const __amdgpu_buffer_rsrc_t sr = sk;
          asm volatile("buffer_store_dwordx2 %0, %1, %3, 0 offen sc1\n\tbuffer_store_dword %4, %2, %3, 0 offen sc1" :: "v"(gr), "v"(vo), "v"(vg), "s"(sr), "v"(gr[1]) : "memory");
        }
      };
      phaseAh<1, 4>(c, 5, hookA5);
      rstate = rfl(r.bad) ? 0u : 3u;
      if (rstate == 0u) lds_st32(c.ldsb + MO_RS + 4u * w, 0u);
      phaseB<1, 2>(c);
    }
#pragma nounroll
    for (int t = 6; t < 14; t += 2) {
      phaseA<0, 3>(c, t);     phaseB<0, 2>(c);
      phaseA<1, 2>(c, t + 1); phaseB<1, 2>(c);
      if (rstate != 3u) {
        route();
        if (t == 12)
#pragma nounroll
          for (unsigned spin = 0; spin < (1u << 20) && rstate != 3u; ++spin) {
            asm volatile("s_waitcnt vmcnt(0)" ::: "memory");
            route();
            __builtin_amdgcn_s_sleep(8);
          }
      }
      GAP();
    }
  }
  GAP();
  {
    const unsigned b = opq(blk);
    const int t1 = 12 - 6 * (int)((b - 144u) >> 31) - 4 * (int)(((b - 208u) & ~(b - 144u)) >> 31) - 12 * (int)(((b ^ RTB) - 1u) >> 31);
#pragma nounroll
    for (int t = 0; __builtin_expect(t != t1, 1);) {
      t += 2;
      phaseA<0, 3>(c, t);     phaseB<0, 2>(c);
      phaseA<1, 2>(c, t + 1); phaseB<1, 2>(c);
    }
  }
  GAP();
  constexpr auto SG = [](int i) constexpr { constexpr int g[10] = {14, 20, 26, 31, 36, 41, 46, 52, 57, 61}; return g[i + 1]; };
  constexpr auto sg_of = [](int m) constexpr { constexpr int g[10] = {14, 20, 26, 31, 36, 41, 46, 52, 57, 61}; for (int i = 0; i < 10; ++i) if (g[i] == m) return i - 1; return -2; };
  if (__builtin_expect(opq(blk) < 144u, 1)) {
    const unsigned w = opq(wids), b = opq(blk), fbb = (b & 7u) * 18u + (b >> 3);
    phaseA<0, 3>(c, 8);
    {
      __amdgpu_buffer_rsrc_t sr; unsigned m0;
      auto hook = [&](auto M_) __attribute__((always_inline)) {
        constexpr int M = decltype(M_)::value;
        if constexpr (M == FB(1)) { sr = mk_srd(sel, 64u); asm volatile("" : "+s"(sr)); }
        if constexpr (M == FB(2)) { m0 = c.ldsb + MO_C; asm volatile("" : "+s"(m0)); }
        if constexpr (M == 39) m0_set(m0);
        if constexpr (M == 40) dma16(sr, c.vksrc[0]);
      };
      phaseBh<0, 2>(c, hook);
    }
    {
      unsigned va, vo; u32x2 sv; __amdgpu_buffer_rsrc_t srdX;
      constexpr auto yo = [](int g0, int g1) constexpr { return 2 * (pcnt(PA0, 'T', g1) - pcnt(PA0, 'T', g0)) + pcnt(PA0, 'X', g1) - pcnt(PA0, 'X', g0); };
      auto hook = [&](auto M_) __attribute__((always_inline)) {
        constexpr int M = decltype(M_)::value;
        if constexpr (M == FA(0)) { va = c.ldsb + MO_C + ((c.vksrc[0] >> 1) & 0x38u); asm volatile("" : "+v"(va)); }
        if constexpr (M == FA(2)) { vo = ((c.vksrc[0] >> 1) & 64u) | ((c.vksrc[0] >> 4) & 0x30u); asm volatile("" : "+v"(vo)); }
        if constexpr (M == FA(4)) { srdX = mk_srd(xn, SEQ * DM * 2); asm volatile("" : "+s"(srdX)); }
        if constexpr (M == FA(5)) asm volatile("s_waitcnt vmcnt(2)\n\tds_read_b64 %0, %1" : "=v"(sv) : "v"(va) : "memory");
        if constexpr (M == 100 + FA(6)) {
          asm volatile("s_waitcnt lgkmcnt(%1)" : "+v"(sv) : "i"(yo(FA(5), FA(6))) : "memory");
          if (__builtin_expect(!__all(sv[1] == tag), 0)) {
#pragma nounroll
            for (unsigned spin = 0; spin < (1u << 20) && !__all(sv[1] == tag); ++spin) {
              __builtin_amdgcn_s_sleep(8);
              hand_req(c.ldsb + MO_C, 0u, 64u);
              asm volatile("s_waitcnt vmcnt(0)\n\tds_read_b64 %0, %1\n\ts_waitcnt lgkmcnt(0)" : "=v"(sv) : "v"(va) : "memory");
            }
          }
        }
        if constexpr (M == FA(7)) { vo += (sv[0] & 0xffffu) * XRS; asm volatile("" : "+v"(vo)); }
        if constexpr (M == 100 + FA(8) || M == FA(9) || M == FA(10)) {
          constexpr unsigned i = M == FA(9) ? 1 : M == FA(10) ? 2 : 0;
          const unsigned v = vo, m0 = c.ldsb + MO_XR + 1024u * w + 4096u * i, so = 128u * w + 512u * i; const __amdgpu_buffer_rsrc_t sx = srdX;
          asm volatile("s_mov_b32 m0, %0\n\ts_nop 0\n\tbuffer_load_dwordx4 %1, %2, %3 offen sc1 lds" :: "s"(m0), "v"(v), "s"(sx), "s"(so) : "m0", "memory");
        }
      };
      phaseAh<1, 5>(c, 9, hook);
    }
    phaseB<1, 0>(c);
    f32x4 gu; unsigned vw, vx;
    auto gate_up = [&](auto H_) __attribute__((always_inline)) {
      constexpr int HH = decltype(H_)::value;
      bf16x4 wl[2], wh[2]; bf16x8 xf[2];
      auto hook = [&](auto M_) __attribute__((always_inline)) {
        constexpr int M = decltype(M_)::value, j = sg_of(M % 100);
        if constexpr (M < 100 && j >= 0) asm volatile("s_waitcnt lgkmcnt(%3)" : "+v"(wl[j & 1]), "+v"(wh[j & 1]), "+v"(xf[j & 1]) : "i"(kyo(SG(j - 1) + 1, M)) : "memory");
        if constexpr (M >= 100 && j >= 0) {
          const bf16x8 wfr = __builtin_shufflevector(wl[j & 1], wh[j & 1], 0, 1, 2, 3, 4, 5, 6, 7);
          if constexpr (HH == 0 && j == 0) asm volatile("v_mfma_f32_16x16x32_bf16 %0, %1, %2, 0" : "=&v"(gu) : "v"(wfr), "v"(xf[j & 1]));
          else                             asm volatile("v_mfma_f32_16x16x32_bf16 %0, %1, %2, %0" : "+v"(gu) : "v"(wfr), "v"(xf[j & 1]));
        }
        if constexpr (M >= 100 && j >= -1 && j < 8) {
          constexpr int n = (j + 1) & 1, i = 9 * HH + j + 1;
          asm volatile("ds_read_b64_tr_b16 %0, %1 offset:%2" : "=v"(wl[n]) : "v"(vw), "i"(1024 * i) : "memory");
          asm volatile("ds_read_b64_tr_b16 %0, %1 offset:%2" : "=v"(wh[n]) : "v"(vw), "i"(1024 * i + 256) : "memory");
          asm volatile("ds_read_b128 %0, %1 offset:%2" : "=v"(xf[n]) : "v"(vx), "i"(1024 * (i >> 1) + 128 * (i & 1)) : "memory");
        }
        if constexpr (HH == 1) {
          if constexpr (M == DB(8) || M == DB(9) || M == FB(6) || M == FB(8)) pf1();
          if constexpr (M == FB(9)) { vx += 1024u * w; asm volatile("" : "+v"(vx)); }
          if constexpr (M == 72) { const f32x4 g4 = gu; asm volatile("ds_write_b128 %0, %1 offset:%2" :: "v"(vx), "v"(g4), "i"(MO_H - MO_XR) : "memory"); }
        }
      };
      phaseBh<HH, 0>(c, hook);
    };
    {
      auto hook = [&](auto M_) __attribute__((always_inline)) {
        constexpr int M = decltype(M_)::value;
        if constexpr (M == FA(0)) { vw = c.vvb[0] & 0xffu; vx = c.vvb[0] & 32u; asm volatile("" : "+v"(vw), "+v"(vx)); }
        if constexpr (M == FA(1)) { vw = (vw + 16u * vx) ^ (32u * (w & 1u)); asm volatile("" : "+v"(vw)); }
        if constexpr (M == FA(2)) { vx = 9u * (c.vvb[0] & 2048u); asm volatile("" : "+v"(vx)); }
        if constexpr (M == FA(3)) { vw += vx + (c.ldsb + SD_BASE + 36864u * (w >> 1)); asm volatile("" : "+v"(vw)); }
        if constexpr (M == FA(4)) { vx = c.vksrc[0] + (c.ldsb + MO_XR); asm volatile("" : "+v"(vx)); }
      };
      phaseAh<0, 1>(c, 10, hook);
    }
    gate_up(std::integral_constant<int, 0>{});
    {
      unsigned lq;
      auto hook = [&](auto M_) __attribute__((always_inline)) {
        constexpr int M = decltype(M_)::value;
        if constexpr (M == FA(0)) { lq = lane_id(); asm volatile("" : "+v"(lq)); }
        if constexpr (M == FA(2)) { c.pfv = 16u * (lq & 3u); asm volatile("" : "+v"(c.pfv)); }
        if constexpr (M == FA(4)) { c.pfv += XRS * ((lq >> 2) & 3u); asm volatile("" : "+v"(c.pfv)); }
        if constexpr (M == FA(5)) { c.pfv += 2u * (lq & 32u); asm volatile("" : "+v"(c.pfv)); }
        if constexpr (M == FA(9)) { c.pfv += XRS * (lq & 16u); asm volatile("" : "+v"(c.pfv)); }
        if constexpr (M == 3) { c.srdP = mk_srd(wd, DM * FF * 2); asm volatile("" : "+s"(c.srdP)); }
        if constexpr (M == FA(1)) { c.pfo = 32u * fbb + 4u * w; asm volatile("" : "+s"(c.pfo)); }
        if constexpr (M == FA(3)) { c.pfst = 128u; c.pfo = XRS * c.pfo - 128u; asm volatile("" : "+s"(c.pfst), "+s"(c.pfo)); }
        if constexpr (M == FA(7)) { c.pfm0 = c.ldsb + SD_BASE + 18432u * w; asm volatile("" : "+s"(c.pfm0)); }
      };
      phaseAh<1, 0>(c, 11, hook);
    }
    gate_up(std::integral_constant<int, 1>{});
    {
      auto hook = [&](auto M_) __attribute__((always_inline)) { constexpr int M = decltype(M_)::value; if constexpr (M == FA(0) || M == FA(2) || M == FA(4) || M == FA(5) || M == FA(9)) pf1(); };
      phaseAh<0, 10>(c, 12, hook);
    }
    {
      unsigned vh; f32x2 g, u, s;
      auto hook = [&](auto M_) __attribute__((always_inline)) {
        constexpr int M = decltype(M_)::value;
        if constexpr (M == 109) { vh = c.vksrc[0] + (c.ldsb + MO_H + 1024u * (w >> 1) + 8u * (w & 1u)); asm volatile("" : "+v"(vh)); }
        if constexpr (M == FB(0)) asm volatile("ds_read_b64 %0, %2\n\tds_read_b64 %1, %2 offset:2048" : "=&v"(g), "=&v"(u) : "v"(vh) : "memory");
        if constexpr (M == 116) { vh = c.vksrc[0] + (c.ldsb + MO_HH + 4u * w); asm volatile("" : "+v"(vh)); }
        if constexpr (M == FB(1)) {
          asm volatile("s_waitcnt lgkmcnt(%2)" : "+v"(g), "+v"(u) : "i"(kyo(FB(0), FB(1))) : "memory");
          const unsigned pg = cvtpk(g[0], g[1]);
          g[0] = bflo(pg); g[1] = bfhi(pg);
          asm volatile("" : "+v"(g));
        }
        if constexpr (M == FB(2)) { const unsigned pu = cvtpk(u[0], u[1]); u[0] = bflo(pu); u[1] = bfhi(pu); asm volatile("" : "+v"(u)); }
        if constexpr (M == FB(3)) { s[0] = g[0] * -1.4426950408889634f; s[1] = g[1] * -1.4426950408889634f; asm volatile("" : "+v"(s)); }
        if constexpr (M == DB(8) || M == FB(4)) { constexpr int e = M == FB(4); s[e] = __builtin_amdgcn_exp2f(s[e]); asm volatile("" : "+v"(s)); }
        if constexpr (M == DB(9)) { float s0 = s[0], s1 = s[1]; add_v(s0, 1.f); add_v(s1, 1.f); s = f32x2{s0, s1}; }
        if constexpr (M == FB(5) || M == FB(6)) { constexpr int e = M == FB(6); s[e] = __builtin_amdgcn_rcpf(s[e]); asm volatile("" : "+v"(s)); }
        if constexpr (M == FB(7)) { const unsigned ps = cvtpk(g[0] * s[0], g[1] * s[1]); s[0] = __uint_as_float(ps); asm volatile("" : "+v"(s)); }
        if constexpr (M == FB(8)) { const unsigned ps = __float_as_uint(s[0]); u[0] *= bflo(ps); u[1] *= bfhi(ps); asm volatile("" : "+v"(u)); }
        if constexpr (M == FB(9)) { const unsigned hv = cvtpk(u[0], u[1]); asm volatile("ds_write_b32 %0, %1" :: "v"(vh), "v"(hv) : "memory"); }
      };
      phaseBh<0, 0>(c, hook);
    }
    {
      unsigned vd, vh, vyo, lq;
      __amdgpu_buffer_rsrc_t srdYs;
      bf16x4 dl[2], dh[2]; f32x4 y[2]; u32x4 hwv;
      auto hookA = [&](auto M_) __attribute__((always_inline)) {
        constexpr int M = decltype(M_)::value;
        if constexpr (M == FA(0)) { vh = c.vksrc[0] + (c.ldsb + MO_HH); vd = c.vksrc[0] & 0x300u; asm volatile("" : "+v"(vh), "+v"(vd)); }
        if constexpr (M == FA(1)) { vd = 72u * vd + (c.vvb[0] & 0xdfu); asm volatile("" : "+v"(vd)); }
        if constexpr (M == FA(2)) { vd += c.ldsb + SD_BASE + 512u * (w >> 1) + 32u * (w & 1u); asm volatile("" : "+v"(vd)); }
        if constexpr (M == FA(3)) { lq = lane_id(); asm volatile("" : "+v"(lq)); }
        if constexpr (M == FA(4)) { vyo = (lq & 48u) << 3; asm volatile("" : "+v"(vyo)); }
        if constexpr (M == FA(5)) { vyo |= (lq & 7u) << 4; asm volatile("" : "+v"(vyo)); }
        if constexpr (M == FA(7)) { vyo |= (lq & 8u) << 27; asm volatile("" : "+v"(vyo)); }
        if constexpr (M == FA(9)) { srdYs = mk_srd((const char*)ypart + fbb * 18432u, 18432u); asm volatile("" : "+s"(srdYs)); }
      };
      phaseAh<1, 0>(c, 13, hookA);
      auto hook = [&](auto M_) __attribute__((always_inline)) {
        constexpr int M = decltype(M_)::value, i = sg_of(M % 100), is = sg_of(M - 3);
        if constexpr (M == FB(0)) asm volatile("ds_read_b128 %0, %1" : "=v"(hwv) : "v"(vh) : "memory");
        if constexpr (M < 100 && i >= 0) asm volatile("s_waitcnt lgkmcnt(%3)" : "+v"(dl[i & 1]), "+v"(dh[i & 1]), "+v"(hwv) : "i"(kyo(SG(i - 1) + 1, M)) : "memory");
        if constexpr (M >= 100 && i >= 0) {
          const bf16x8 d8 = __builtin_shufflevector(dl[i & 1], dh[i & 1], 0, 1, 2, 3, 4, 5, 6, 7);
          asm volatile("v_mfma_f32_16x16x32_bf16 %0, %1, %2, 0" : "=&v"(y[i & 1]) : "v"(d8), "v"(hwv));
        }
        if constexpr (M >= 100 && i >= -1 && i < 8) {
          asm volatile("ds_read_b64_tr_b16 %0, %1 offset:%2" : "=v"(dl[(i + 1) & 1]) : "v"(vd), "i"(1024 * (i + 1)) : "memory");
          asm volatile("ds_read_b64_tr_b16 %0, %1 offset:%2" : "=v"(dh[(i + 1) & 1]) : "v"(vd), "i"(1024 * (i + 1) + 256) : "memory");
        }
        if constexpr (M < 100 && is >= 0) {
          const unsigned vo = vyo; const __amdgpu_buffer_rsrc_t sr = srdYs; const f32x4 yv = y[is & 1]; const unsigned so = 512u * w + 2048u * is;
          asm volatile("buffer_store_dwordx4 %0, %1, %2, %3 offen sc1\n\ts_nop 1" :: "v"(yv), "v"(vo), "s"(sr), "s"(so) : "memory");
        }
      };
      phaseBh<1, 0>(c, hook);
    }
  }
  GAP();
  if (__builtin_expect(opq(blk) - 144u < 64u, 0)) {
    const unsigned w = opq(wids), b = opq(blk), job = (b - 144u) * 4u + w;
    constexpr unsigned NTS = 128u * 4608u;
    unsigned vw[2], v0, v1, vs;
    __amdgpu_buffer_rsrc_t srdT;
    bf16x4 wl[2], wh[2];
    auto rd = [&](auto I_) __attribute__((always_inline)) {
      constexpr int i = decltype(I_)::value;
      bf16x4 &l = wl[i & 1], &h = wh[i & 1]; const unsigned v = vw[i & 1];
      asm volatile("ds_read_b64_tr_b16 %0, %1 offset:%2" : "=v"(l) : "v"(v), "i"(1024 * (i >> 1)) : "memory");
      asm volatile("ds_read_b64_tr_b16 %0, %1 offset:%2" : "=v"(h) : "v"(v), "i"(1024 * (i >> 1) + 256) : "memory");
    };
    auto st = [&](auto I_, auto Y_) __attribute__((always_inline)) {
      constexpr int i = decltype(I_)::value, u = 2 * i, r = u % 9;
      constexpr auto at = [](int x) constexpr { return (unsigned)(x / 9) * NTS + 2048u * ((x % 9) >> 2) + 64u * ((x % 9) & 3); };
      asm volatile("s_waitcnt lgkmcnt(%2)" : "+v"(wl[i & 1]), "+v"(wh[i & 1]) : "i"(decltype(Y_)::value) : "memory");
      const u32x4 x = __builtin_bit_cast(u32x4, __builtin_shufflevector(wl[i & 1], wh[i & 1], 0, 1, 2, 3, 4, 5, 6, 7));
      const __amdgpu_buffer_rsrc_t sr = srdT; const unsigned up = v1;
      const unsigned vo = r == 8 ? v0 | ((up ^ 1u) << 30) : r == 7 ? v0 | (up << 30) : v0 + up * ((r & 3) == 3 ? 1856u : 64u);
      asm volatile("buffer_store_dwordx4 %0, %1, %2, %3 offen sc1\n\ts_nop 1" :: "v"(x), "v"(vo), "s"(sr), "s"(at(r == 8 ? u + 1 : u)) : "memory");
      if constexpr (r >= 7) {
        const u32x2 e = {x[0], x[2]}, o2 = {x[1], x[3]}; const unsigned v2 = vs | ((r == 8 ? up : up ^ 1u) << 30);
        asm volatile("buffer_store_dwordx2 %0, %2, %3, %4 offen sc1\n\tbuffer_store_dwordx2 %1, %2, %3, %4 offen offset:128 sc1" :: "v"(e), "v"(o2), "v"(v2), "s"(sr), "s"((unsigned)(u / 9) * NTS) : "memory");
      }
    };
    auto hookB = [&](auto I0_, auto M_) __attribute__((always_inline)) {
      constexpr int M = decltype(M_)::value, i0 = decltype(I0_)::value;
      constexpr auto fr = [](int g) constexpr { return fb_of(g) >= 0 || g == DB(8) || g == DB(9); };
      constexpr auto cnt = [=](int g) constexpr { int n = 0; for (int x = 0; x < g; ++x) n += fr(x); return n; };
      constexpr auto prev = [=](int g) constexpr { int x = g - 1; while (x > 0 && !fr(x)) --x; return x; };
      if constexpr (M < NGB) { if constexpr (fr(M)) {
        if constexpr (cnt(M) % 2 == 0) rd(std::integral_constant<int, i0 + cnt(M) / 2>{});
        else st(std::integral_constant<int, i0 + cnt(M) / 2>{}, std::integral_constant<int, kyo(prev(M), M)>{});
      } }
    };
    {
      auto hook = [&](auto M_) __attribute__((always_inline)) {
        constexpr int M = decltype(M_)::value;
        if constexpr (M == FA(0)) { vw[0] = c.vvb[0] & 0xffu; v0 = c.vvb[0] & 32u; asm volatile("" : "+v"(vw[0]), "+v"(v0)); }
        if constexpr (M == FA(1)) { vw[0] += 16u * v0; v0 = c.vvb[0] & 2048u; asm volatile("" : "+v"(vw[0]), "+v"(v0)); }
        if constexpr (M == FA(2)) { vw[0] += 9u * v0; asm volatile("" : "+v"(vw[0])); }
        if constexpr (M == FA(3)) { vw[0] += c.ldsb + SD_BASE + 36864u * (w >> 1) + 9216u * (w & 1u); asm volatile("" : "+v"(vw[0])); }
        if constexpr (M == FA(4)) { v1 = lane_id(); asm volatile("" : "+v"(v1)); }
        if constexpr (M == FA(5)) { vw[1] = vw[0] ^ 32u; v0 = (v1 & 7u) << 4; asm volatile("" : "+v"(vw[1]), "+v"(v0)); }
        if constexpr (M == FA(7)) { vs = v0 | ((v1 & 32u) << 3) | ((v1 & 16u) >> 1); asm volatile("" : "+v"(vs)); }
        if constexpr (M == FA(9)) { v0 = (((v1 & 51u) << 4) | ((v1 & 4u) << 8)) + 4096u * ((job >> 1) & 1u); asm volatile("" : "+v"(v0)); }
        if constexpr (M == FA(10)) { vs += 8192u + 512u * ((job >> 1) & 1u); v1 = (v1 >> 3) & 1u; asm volatile("" : "+v"(vs), "+v"(v1)); }
        if constexpr (M == 100 + FA(6)) { srdT = mk_srd((const char*)wot + ((job & 1u) * 256u + (job >> 2)) * 9216u, 3u * NTS + 9216u); asm volatile("" : "+s"(srdT)); }
      };
      phaseAh<0, 3>(c, 10, hook);
    }
    { auto hook = [&](auto M_) __attribute__((always_inline)) { hookB(std::integral_constant<int, 0>{}, M_); }; phaseBh<0, 0>(c, hook); }
    {
      auto hook = [&](auto M_) __attribute__((always_inline)) {
        constexpr int M = decltype(M_)::value;
        if constexpr (M < 64) { if constexpr (fa_of(M) >= 0) {
          constexpr int n = fa_of(M), p = n ? FA(n - 1) : 0, y = 2 * (pcnt(PA0, 'T', M) - pcnt(PA0, 'T', p)) + pcnt(PA0, 'X', M) - pcnt(PA0, 'X', p);
          if constexpr (n % 2 == 0) rd(std::integral_constant<int, 6 + n / 2>{});
          else st(std::integral_constant<int, 6 + n / 2>{}, std::integral_constant<int, y>{});
        } }
      };
      phaseAh<1, 15>(c, 11, hook);
    }
    { auto hook = [&](auto M_) __attribute__((always_inline)) { hookB(std::integral_constant<int, 12>{}, M_); }; phaseBh<1, 0>(c, hook); }
    phaseA<0>(c, 12);
    {
      auto hook = [&](auto M_) __attribute__((always_inline)) { if constexpr (decltype(M_)::value == FB(0)) flag1(H_W + (b - 144u), ((w - 1u) >> 31) * 4u); };
      phaseBh<0, 0>(c, hook);
    }
    phaseA<1>(c, 13); phaseB<1, 0>(c);
  }
  {
    const unsigned w = opq(wids), b = opq(blk);
    {
      __amdgpu_buffer_rsrc_t sf, sh; unsigned tg, m0; CW k;
      auto hookA = [&](auto M_) __attribute__((always_inline)) {
        constexpr int M = decltype(M_)::value;
        if constexpr (M == FA(0)) { m0 = (((w - 1u) & (b - 144u)) >> 31) * 4u; asm volatile("" : "+s"(m0)); }
        if constexpr (M == FA(1) || M == FA(2)) { if constexpr (M == FA(2)) { sf = mk_srd(sel + H_Y + b, m0); asm volatile("" : "+s"(sf)); } }
        if constexpr (M == FA(3)) { tg = tag; m0 = c.ldsb + WF_FL; asm volatile("" : "+v"(tg), "+s"(m0)); }
        if constexpr (M == FA(4)) { sh = mk_srd(sel + H_W, 256u); asm volatile("" : "+s"(sh)); }
      };
      phaseAh<0, 0>(c, 14, hookA);
      auto hook = [&](auto M_) __attribute__((always_inline)) {
        constexpr int M = decltype(M_)::value;
        if constexpr (M == FB(0)) flag_st(sf, tg);
        if constexpr (M == FB(1)) warm_step(k, std::integral_constant<int, 0>{});
        if constexpr (M == FB(2)) warm_step(k, std::integral_constant<int, 1>{});
        if constexpr (M == FB(3)) warm_step(k, std::integral_constant<int, 2>{});
        if constexpr (M == FB(4)) warm_step(k, std::integral_constant<int, 3>{});
        if constexpr (M == DB(9)) warm_step(k, std::integral_constant<int, 4>{});
        if constexpr (M == 39) m0_set(m0);
        if constexpr (M == 40) dma16(sh, c.vksrc[0]);
      };
      phaseBh<0, 0>(c, hook);
    }
    {
      unsigned va; u32x4 f;
      auto hookA = [&](auto M_) __attribute__((always_inline)) {
        constexpr int M = decltype(M_)::value;
        if constexpr (M == FA(0)) { va = c.ldsb + WF_FL + (c.vksrc[0] & 0xf0u); asm volatile("" : "+v"(va)); }
        if constexpr (M == FA(1)) { c.pfv = lane_id(); asm volatile("" : "+v"(c.pfv)); }
        if constexpr (M == FA(2)) { c.pfv *= 16u; c.pfo = op_k0(b, w); asm volatile("" : "+v"(c.pfv), "+s"(c.pfo)); }
        if constexpr (M == FA(3)) { c.pfo *= 4608u; asm volatile("" : "+s"(c.pfo)); }
        if constexpr (M == FA(4)) { c.srdP = mk_srd((const char*)wot + c.pfo, 16 * 4608); asm volatile("" : "+s"(c.srdP)); }
        if constexpr (M == FA(5)) { c.pfst = 1024u; c.pfo = 0u - 1024u; c.pfm0 = c.ldsb + SD_BASE + BP_W * w; asm volatile("" : "+s"(c.pfst), "+s"(c.pfo), "+s"(c.pfm0)); }
        if constexpr (M == FA(9)) { c.pfend = c.pfm0 + BP_W; asm volatile("" : "+s"(c.pfend)); }
      };
      phaseAh<1, 1>(c, 15, hookA);
      auto hook = [&](auto M_) __attribute__((always_inline)) {
        constexpr int M = decltype(M_)::value;
        if constexpr (M == FB(0)) asm volatile("ds_read_b128 %0, %1" : "=v"(f) : "v"(va) : "memory");
        if constexpr (M == FB(1)) { asm volatile("s_waitcnt lgkmcnt(%1)" : "+v"(f) : "i"(kyo(FB(0), FB(1))) : "memory"); f[0] = (f[0] ^ f[1]) | (f[2] ^ f[3]) | (f[0] ^ f[2]); asm volatile("" : "+v"(f)); }
        if constexpr (M == FB(2)) {
          if (__builtin_expect(!__all((f[0] | (f[1] ^ tag)) == 0u), 0)) {
#pragma nounroll
            for (unsigned spin = 0; spin < (1u << 20); ++spin) {
              __builtin_amdgcn_s_sleep(8);
              hand_req(c.ldsb + WF_FL, H_W, 256u);
              asm volatile("s_waitcnt vmcnt(0)\n\tds_read_b128 %0, %1\n\ts_waitcnt lgkmcnt(0)" : "=v"(f) : "v"(va) : "memory");
              if (__all(((f[0] ^ tag) | (f[1] ^ tag) | (f[2] ^ tag) | (f[3] ^ tag)) == 0u)) break;
            }
            asm volatile("s_mov_b32 m0, %0" :: "s"(c.pm0[1]) : "m0", "memory");
          }
        }
      };
      phaseBh<1, 2>(c, hook);
    }
  }
  GAP();
  if (__builtin_expect(((RED0 - 1u - opq(blk)) & (opq(blk) - RED1)) >> 31, 0)) {
    const unsigned job = (opq(blk) - RED0) * 4u + opq(wids);
    unsigned va; u32x4 f; u32x2 sv, nv; f32x4 y[4]; float a[4]; __amdgpu_buffer_rsrc_t sr;
    auto req_fl = [&](auto W_) __attribute__((always_inline)) {
      u32x4& f_ = f; u32x2& s_ = sv; const unsigned v0 = c.pfv, v1 = (c.pfv >> 1) & 56u; const __amdgpu_buffer_rsrc_t r0 = mk_srd(sel + H_Y, 4u * MOE_WG), r1 = mk_srd(sel, 64u);
      if constexpr (decltype(W_)::value) asm volatile("buffer_load_dwordx4 %0, %2, %4, 0 offen sc1\n\tbuffer_load_dwordx2 %1, %3, %5, 0 offen sc1\n\ts_waitcnt vmcnt(0)" : "=&v"(f_), "=&v"(s_) : "v"(v0), "v"(v1), "s"(r0), "s"(r1) : "memory");
      else                               asm volatile("buffer_load_dwordx4 %0, %2, %4, 0 offen sc1\n\tbuffer_load_dwordx2 %1, %3, %5, 0 offen sc1" : "=&v"(f_), "=&v"(s_) : "v"(v0), "v"(v1), "s"(r0), "s"(r1) : "memory");
    };
    auto bad = [&]() __attribute__((always_inline)) { return vsel(c.pfv >= 16u * (MOE_WG / 4), 0u, (f[0] ^ tag) | (f[1] ^ tag) | (f[2] ^ tag) | (f[3] ^ tag)) | (sv[1] ^ tag); };
#define PINA asm volatile("" : "+v"(a[0]), "+v"(a[1]), "+v"(a[2]), "+v"(a[3]))
    auto batch = [&](auto B_, auto M_) __attribute__((always_inline)) {
      constexpr int B = decltype(B_)::value, M = decltype(M_)::value, g = M - 100, i = g / 9;
      if constexpr (B > 0 && g >= 2 && g < 36 && (g % 9 == 2 || g % 9 == 5) && 4 * (B - 1) + i < 18) {
        constexpr int e = g % 9 == 2 ? 0 : 2; const f32x4 v = y[i];
        if constexpr (B == 1 && i == 0) { a[e] = v[e]; a[e + 1] = v[e + 1]; PIN2(a[e], a[e + 1]); } else { add_v(a[e], v[e]); add_v(a[e + 1], v[e + 1]); }
      }
      if constexpr (M >= 7 && M < 36 && M % 9 == 7 && 4 * B + M / 9 < 18) {
        f32x4& d = y[M / 9]; const unsigned v = va; const __amdgpu_buffer_rsrc_t r = sr;
        asm volatile("buffer_load_dwordx4 %0, %1, %2, %3 offen sc1" : "=v"(d) : "v"(v), "s"(r), "s"(8u * NSEL * DM * 4u * (4 * B + M / 9)) : "memory");
      }
    };
#define PINY asm volatile("" : "+v"(y[0]), "+v"(y[1]), "+v"(y[2]), "+v"(y[3]))
    phaseA<0, 3>(c, 16); phaseB<0, 2>(c);
    phaseA<1, 2>(c, 17);
    { auto hook = [&](auto M_) __attribute__((always_inline)) { if constexpr (decltype(M_)::value == 40) req_fl(IC(0)); }; phaseBh<1, 2>(c, hook); }
    {
      auto hook = [&](auto M_) __attribute__((always_inline)) {
        constexpr int M = decltype(M_)::value;
        if constexpr (M == FA(0)) { va = (c.pfv >> 7) * (unsigned)(NSEL * DM * 4); asm volatile("" : "+v"(va)); }
        if constexpr (M == FA(2)) { va += c.pfv & 112u; asm volatile("" : "+v"(va)); }
        if constexpr (M == FA(4)) { sr = mk_srd(ypart + 32u * job, MOE_WG * NSEL * DM * 4 - 128u * job); asm volatile("" : "+s"(sr)); }
      };
      phaseAh<0, 3>(c, 18, hook);
    }
    asm volatile("" : "+v"(f), "+v"(sv));
    {
      auto hook = [&](auto M_) __attribute__((always_inline)) {
        constexpr int M = decltype(M_)::value;
        if constexpr (M == 104) {
          if (__builtin_expect(!__all(bad() == 0u), 0)) {
#pragma nounroll
            for (unsigned spin = 0; spin < (1u << 20) && !__all(bad() == 0u); ++spin) { __builtin_amdgcn_s_sleep(8); req_fl(IC(1)); }
          }
        }
        batch(IC(0), M_);
      };
      phaseBh<0, 2>(c, hook);
    }
    phaseA<1, 2>(c, 19);
    PINY; { auto hook = [&](auto M_) __attribute__((always_inline)) { batch(IC(1), M_); }; phaseBh<1, 2>(c, hook); }
    phaseA<0, 3>(c, 20);
    PINY; { auto hook = [&](auto M_) __attribute__((always_inline)) { batch(IC(2), M_); }; phaseBh<0, 2>(c, hook); }
    phaseA<1, 2>(c, 21);
    PINY; { auto hook = [&](auto M_) __attribute__((always_inline)) { batch(IC(3), M_); }; phaseBh<1, 2>(c, hook); }
    phaseA<0, 3>(c, 22);
    PINY;
    {
      auto hook = [&](auto M_) __attribute__((always_inline)) {
        batch(IC(4), M_);
        if constexpr (decltype(M_)::value == 40) {
          u32x2& n_ = nv; const unsigned v = (sv[0] & 0xffffu) * XRS, o2 = 8u * job; const __amdgpu_buffer_rsrc_t r = mk_srd(xn, SEQ * DM * 2);
          asm volatile("buffer_load_dwordx2 %0, %1, %2, %3 offen sc1" : "=v"(n_) : "v"(v), "s"(r), "s"(o2) : "memory");
        }
      };
      phaseBh<0, 2>(c, hook);
    }
    phaseA<1, 2>(c, 23);
    asm volatile("" : "+v"(y[0]), "+v"(y[1]), "+v"(nv));
    {
      auto hook = [&](auto M_) __attribute__((always_inline)) {
        constexpr int M = decltype(M_)::value;
        batch(IC(5), M_);
        if constexpr (M == FB(1)) { _Pragma("unroll") for (int e = 0; e < 4; ++e) a[e] = dpp_add<0x128>(a[e]); PINA; }
        if constexpr (M == FB(2))       { a[0] = red_step<4>(a[0]); a[1] = red_step<4>(a[1]); PINA; }
        if constexpr (M == 100 + FB(3)) { a[2] = red_step<4>(a[2]); a[3] = red_step<4>(a[3]); PINA; }
        if constexpr (M == 100 + FB(4) || M == FB(5)) {
          constexpr int e0 = (M == FB(5)) * 2;
#pragma unroll
          for (int e = e0; e < e0 + 2; ++e) {
            auto pr = __builtin_amdgcn_permlane32_swap(__float_as_uint(a[e]), __float_as_uint(a[e]), false, false);
            a[e] = __uint_as_float(pr[0]) + __uint_as_float(pr[1]);
          }
          PINA;
        }
        if constexpr (M == 153 || M == FB(7) || M == FB(8) || M == FB(9)) {
          constexpr int e = M == 153 ? 0 : fb_of(M) - 6;
          a[e] = ((e & 1) ? bfhi(nv[e >> 1]) : bflo(nv[e >> 1])) + rbf(rbf(a[e]) * bfhi(sv[0])); PINA;
        }
        if constexpr (M == 120) { va = (c.pfv >> 4) * XRS; asm volatile("" : "+v"(va)); }
        if constexpr (M == 72) {
          const u32x2 o2 = {cvtpk(a[0], a[1]), cvtpk(a[2], a[3])};
          const unsigned vo = va; const __amdgpu_buffer_rsrc_t r = mk_srd(xn + SEQ * DM + 4u * job, 7u * XRS + 8u);
          asm volatile("buffer_store_dwordx2 %0, %1, %2, 0 offen sc1" :: "v"(o2), "v"(vo), "s"(r) : "memory");
        }
      };
      phaseBh<1, 2>(c, hook);
    }
  }
  GAP();
  {
    int t = 14 + 8 * (int)(((RED0 - 1u - opq(blk)) & (opq(blk) - RED1)) >> 31);
#pragma nounroll
    do {
      t += 2;
      phaseA<0, 3>(c, t);     phaseB<0, 2>(c);
      phaseA<1, 2>(c, t + 1); phaseB<1, 2>(c);
    } while (__builtin_expect(t != T - 4, 1));
  }
  GAP();
  OPS o; OST ot;
  {
    const unsigned w = opq(wids), b = opq(blk);
    __amdgpu_buffer_rsrc_t sf, sh; unsigned on, tg, m0;
    auto hookA = [&](auto M_) __attribute__((always_inline)) {
      constexpr int M = decltype(M_)::value;
      if constexpr (M == FA(0)) { on = ((RED0 - 1u - b) & (b - RED1)) >> 31; asm volatile("" : "+s"(on)); }
      if constexpr (M == FA(1)) { m0 = on * ((b - RED0) * 4u + w); asm volatile("" : "+s"(m0)); }
      if constexpr (M == FA(2)) { sf = mk_srd(sel + H_X + m0, on * 4u); asm volatile("" : "+s"(sf)); }
      if constexpr (M == FA(5)) { sh = mk_srd(sel, 64u); tg = tag; asm volatile("" : "+s"(sh), "+v"(tg)); }
      if constexpr (M == FA(7)) { ot.srd = mk_srd(O, SEQ * HQ * DH * 2); m0 = c.ldsb + WF_FL; asm volatile("" : "+s"(ot.srd), "+s"(m0)); }
      if constexpr (M == FA(9)) { ot.so[0] = ((b >> 3) & 7u) * 2097152u + w * (524288u + 1024u); asm volatile("" : "+s"(ot.so[0])); }
      if constexpr (M == FA(10)) { ot.so[0] += (4u * (b & 7u) + (b >> 6)) * 4096u; asm volatile("" : "+s"(ot.so[0])); }
    };
    phaseAh<0, 1>(c, T - 2, hookA);
    o.srdB = c.srdP; o.va = c.pfv;
    auto hook = [&](auto M_) __attribute__((always_inline)) {
      constexpr int M = decltype(M_)::value;
      constexpr int f = M == 50 ? 3 : M < NGB && PB2[M < NGB ? M : 0] == 'F' ? pcnt(PB2, 'F', M) : -1;
      if constexpr (f == 0) flag_st(sf, tg);
      if constexpr (M == ppos(PB2, 'F', 1) - 1) m0_set(m0);
      if constexpr (f == 1) dma16(sh, c.vksrc[0]);
      if constexpr (f == 2) { ot.so[1] = ot.so[0] ^ 1024u; ot.so[2] = ot.so[0] ^ 2048u; ot.so[3] = ot.so[0] ^ 3072u; asm volatile("" : "+s"(ot.so[1]), "+s"(ot.so[2]), "+s"(ot.so[3])); }
      if constexpr (f == 3) {
        c.pfo = 23u * 1024u; c.pfm0 = c.ldsb + 9216u * w;
        asm volatile("" : "+s"(c.pfo), "+s"(c.pfm0));
      }
    };
    static_assert(pcnt(PB2, 'E', NGB) == NBL && ppos(PB2, 'D', 0) == DB(4), "");
    phaseB_<0, false, false, 0>(c, hook, make_iseq<NGB>{});
  }
  ot.vo = c.pfv;
  fin_gaps(c, ot, o, make_iseq<NFIN>{});
  asm volatile("s_nop 1" :: "v"(ot.w));
  if (__builtin_expect(!__all((ot.ls[0] + ot.ls[1]) + (ot.ls[2] + ot.ls[3]) <= LIM), 0))
    redo_rows(c, ot, o, xn, wq, keys, 256u * ((opq(blk) >> 3) & 7u) + 64u * opq(wids), 4u * (opq(blk) & 7u) + (opq(blk) >> 6), tag);
  oproj_phase(o, lds, O, wot, xres, xn, (unsigned*)sel, ypart + MOE_WG * NSEL * DM, out, tag, H_O + 256u * (2u * yb + (wids >> 1)) + 32u * xg + 4u * (wids & 1u) + (h & 3u)  );
}
extern "C" __global__ void k_attn_end() {}

extern "C" void kernel_launch(void* const* d_in, const int* in_sizes, int n_in,
                              void* d_out, int out_size, void* d_ws, size_t ws_size,
                              hipStream_t stream) {
  const u16* x   = (const u16*)d_in[0];
  const u16* kc  = (const u16*)d_in[1];
  const u16* vc  = (const u16*)d_in[2];
  const u16* wq  = (const u16*)d_in[3];
  const u16* wo  = (const u16*)d_in[4];
  const u16* w_r = (const u16*)d_in[6];
  const u16* wg  = (const u16*)d_in[7];
  const u16* wu  = (const u16*)d_in[8];
  const u16* wd  = (const u16*)d_in[9];
  char* ws = (char*)d_ws;
  u16* xn = (u16*)(ws + WS_XN); unsigned long long* keys = (unsigned long long*)(ws + WS_KEY); int* sel = (int*)(ws + WS_SEL);
  u16* o = (u16*)(ws + WS_O); u16* wot = (u16*)(ws + WS_WOT); float* yp = (float*)(ws + WS_YP);
  (void)hipFuncSetAttribute((const void*)k_attn, hipFuncAttributeMaxDynamicSharedMemorySize, (int)SHM_ATTN);
  hipLaunchKernelGGL(k_attn, dim3(256), dim3(256), SHM_ATTN, stream, xn, wq, kc, vc, o, wo, wg, wu, wd, wot, keys, yp, sel, x, (u16*)d_out, w_r  );
}
```

```cpp
#include <hip/hip_runtime.h>
#include <hip/hip_bf16.h>
#include <utility>
#include <type_traits>

constexpr int SEQ = 2048, DM = 576, HQ = 32, HKV = 16, DH = 128, FF = 4608, NSEL = 8;
constexpr int NT = SEQ / 64;
constexpr float LIM = 1073741824.0f;

typedef float    f32x16 __attribute__((ext_vector_type(16)));
typedef float    f32x2  __attribute__((ext_vector_type(2)));
typedef float    f32x4  __attribute__((ext_vector_type(4)));
typedef __bf16   bf16x8 __attribute__((ext_vector_type(8)));
typedef __bf16   bf16x4 __attribute__((ext_vector_type(4)));
typedef __bf16   bf16x2 __attribute__((ext_vector_type(2)));
typedef unsigned u32x8  __attribute__((ext_vector_type(8)));
typedef unsigned u32x4  __attribute__((ext_vector_type(4)));
typedef unsigned u32x2  __attribute__((ext_vector_type(2)));
typedef unsigned short u16;
template <int... Is> using iseq = std::integer_sequence<int, Is...>;
template <int N> using make_iseq = std::make_integer_sequence<int, N>;

#define DI __device__ __forceinline__
#define GAP() __builtin_amdgcn_sched_barrier(0)
#define IC(x) std::integral_constant<int, (x)>{}
#define SRD_WORD3 0x00020000

DI unsigned rfl(unsigned x) { return __builtin_amdgcn_readfirstlane(x); }
DI unsigned cvtpk(float lo, float hi) { f32x2 v = {lo, hi}; bf16x2 b = __builtin_convertvector(v, bf16x2); return __builtin_bit_cast(unsigned, b); }
DI __amdgpu_buffer_rsrc_t mk_srd(const void* p, const unsigned bytes) {
  asm volatile("" : "+s"(p));
  return __builtin_amdgcn_make_buffer_rsrc((void*)p, (short)0, (int)bytes, SRD_WORD3);
}
DI u32x4 mk_srd4(const void* p, const unsigned bytes) { asm volatile("" : "+s"(p)); const unsigned long long a = (unsigned long long)p; return u32x4{(unsigned)a, (unsigned)(a >> 32) & 0xffffu, bytes, SRD_WORD3}; }
DI unsigned vsel(const bool c, unsigned a, const unsigned b) { asm volatile("" : "+v"(a)); return c ? a : b; }
DI float bflo(unsigned w) { return __uint_as_float(w << 16); }
DI float bfhi(unsigned w) { return __uint_as_float(w & 0xffff0000u); }
DI float rbf(float x) { return bflo(cvtpk(x, 0.f)); }
DI u16 f2bf(float x) { return (u16)(cvtpk(x, 0.f) & 0xffffu); }

#define PS_MACROS \
  ".macro psl n\n.Lps_\\n: s_cbranch_execz .Lps_ret\n.endm\n.set .Lpsl, 0\n" \
  ".macro ps\n.set .Lpso, (. - k_attn) >> 12\n.if .Lpso != .Lpsl\n.set .Lpsl, .Lpso\n.altmacro\npsl %%.Lpso\n.noaltmacro\n.endif\n.endm\n" \
  ".macro mf16 a, b, c, d\nv_mfma_f32_16x16x32_bf16 \\a, \\b, \\c, \\d\nps\n.endm\n" \
  ".macro mf32 a, b, c, d\nv_mfma_f32_32x32x16_bf16 \\a, \\b, \\c, \\d\nps\n.endm\n" \
  ".macro sm64 a, b, c, d\nv_smfmac_f32_16x16x64_bf16 \\a, \\b, \\c, \\d\nps\n.endm\n"
#define PS_IDS "0,1,2,3,4,5,6,7,8,9,10,11,12,13,14,15,16,17,18,19,20,21,22,23,24,25,26,27,28,29,30,31,32,33,34,35,36,37,38,39,40,41,42,43,44,45,46,47"
#define PS_AT(n) asm volatile("psl " #n)
#define PS_PAGE(d, n) asm volatile("s_mov_b32 %0, ((.Lps_" #n " - k_attn) >> 12) + 1" : "=s"(d))
DI void ps_touch(const unsigned n) {
  asm volatile("s_getpc_b64 s[20:21]\n\ts_lshl2_add_u32 vcc_lo, %0, .Lps_tab - .\n\ts_add_u32 s20, s20, vcc_lo\n\ts_addc_u32 s21, s21, -1\n\ts_mov_b64 exec, 0\n\ts_swappc_b64 vcc, s[20:21]\n\ts_mov_b64 exec, -1"
               :: "s"(n) : "vcc", "scc", "s20", "s21", "memory");
}
DI unsigned ps_sel(const bool c, const unsigned a, const unsigned b) { const unsigned m = 0u - (unsigned)c; return (a & m) | (b & ~m); }

constexpr size_t WS_XN   = 0;
constexpr size_t WS_KEY  = WS_XN + (size_t)(SEQ + NSEL) * DM * 2;
constexpr size_t WS_ST   = WS_KEY + (size_t)SEQ * 8;
constexpr size_t WS_SEL  = WS_ST + (size_t)SEQ * 8;
constexpr unsigned H_G = 32u, H_W = 64u, H_Y = 128u, H_O = 4096u, H_X = 1536u, H_E = 12288u;
constexpr size_t WS_O    = WS_SEL + 131072;
constexpr size_t WS_WOT  = WS_O + (size_t)SEQ * HQ * DH * 2;
constexpr int    MOE_WG  = 144;
constexpr size_t WS_YP   = WS_WOT + (size_t)HQ * DH * DM * 2;
constexpr size_t WS_END  = WS_YP + (size_t)MOE_WG * NSEL * DM * 4 + 256u * 20480u;

DI void ce(unsigned& a, unsigned& b) { const unsigned hi_ = a > b ? a : b, lo_ = a > b ? b : a; a = hi_; b = lo_; }
DI int key_row(unsigned k) { return 0xffff - (int)(k & 0xffffu); }
DI float key_val(unsigned k) { const unsigned mono = k >> 16; const unsigned rb = (mono & 0x8000u) ? (mono & 0x7fffu) : (~mono & 0xffffu); return __uint_as_float(rb << 16); }

struct LNJ { u32x4 a[3], w[3]; float x0, s[3], q[3], mb, den, rden, t0, t1, t2, t3; unsigned stat, key; bool three, up; };
DI float rcp_nr(float d) { const float r = __builtin_amdgcn_rcpf(d); return __builtin_fmaf(__builtin_fmaf(-d, r, 1.f), r, r); }
template <int CTRL> DI float dpp_add(float v) { return v + __int_as_float(__builtin_amdgcn_update_dpp(0, __float_as_int(v), CTRL, 0xf, 0xf, false)); }
template <int K> DI float red_step(float v) {
  if constexpr (K == 0) return dpp_add<0xB1>(v);
  else if constexpr (K == 1) return dpp_add<0x4E>(v);
  else if constexpr (K == 2) return dpp_add<0x141>(v);
  else if constexpr (K == 3) return dpp_add<0x140>(v);
  else { auto pr = __builtin_amdgcn_permlane16_swap(__float_as_uint(v), __float_as_uint(v), false, false); return __uint_as_float(pr[0]) + __uint_as_float(pr[1]); }
}
#define PIN2(a, b) asm volatile("" : "+v"(a), "+v"(b))
constexpr int LN_STAT = 34, LN_N0 = 40, LN_XN = LN_N0 + 96, LN_KEY = LN_XN + 7;
template <int I> DI void ln_step(LNJ& j, const unsigned row) {
  if constexpr (I == 0) {
    const float xa = bflo((unsigned)__builtin_amdgcn_readlane((int)j.a[0][0], 0)), xb = bflo((unsigned)__builtin_amdgcn_readlane((int)j.a[0][0], 32));
    j.x0 = j.up ? xb : xa;
    asm volatile("" : "+v"(j.x0));
  } else if constexpr (I <= 24) {
    constexpr int e = I - 1, c = e >> 3, i = (e >> 1) & 3;
    const float d = ((e & 1) ? bfhi(j.a[c][i]) : bflo(j.a[c][i])) - j.x0;
    if constexpr ((e & 7) == 0) { j.s[c] = d; j.q[c] = d * d; } else { j.s[c] += d; j.q[c] = __builtin_fmaf(d, d, j.q[c]); }
    PIN2(j.s[c], j.q[c]);
  } else if constexpr (I == 25) {
    j.t0 = (j.s[0] + j.s[1]) + (j.three ? j.s[2] : 0.f); j.t1 = (j.q[0] + j.q[1]) + (j.three ? j.q[2] : 0.f);
    PIN2(j.t0, j.t1);
  } else if constexpr (I <= 30) {
    j.t0 = red_step<I - 26>(j.t0); j.t1 = red_step<I - 26>(j.t1);
    PIN2(j.t0, j.t1);
  } else if constexpr (I == 31) {
    j.t0 *= 1.0f / DM; j.t1 = __builtin_fmaxf(__builtin_fmaf(-j.t0, j.t0, j.t1 * (1.0f / DM)), 0.f);
    PIN2(j.t0, j.t1);
  } else if constexpr (I == 32) {
    j.mb = rbf(j.x0 + j.t0); j.t1 = rbf(j.t1) + 1e-5f;
    PIN2(j.mb, j.t1);
  } else if constexpr (I == 33) {
    j.den = rbf(__builtin_amdgcn_sqrtf(rbf(j.t1)));
    j.stat = (__float_as_uint(j.mb) & 0xffff0000u) | (__float_as_uint(j.den) >> 16);
    PIN2(j.den, j.stat);
  } else if constexpr (I < LN_N0) {
    if constexpr (I == 34) { j.rden = rcp_nr(j.den); j.s[0] = 0.f; j.s[1] = 0.f; asm volatile("" : "+v"(j.rden)); }
  } else if constexpr (I < LN_XN) {
    constexpr int e = (I - LN_N0) >> 3, st = (I - LN_N0) & 7, c = e >> 2, i = e & 3;
    if constexpr (st == 0)      { j.t0 = bflo(j.a[c][i]); j.t1 = bfhi(j.a[c][i]); }
    else if constexpr (st == 1) { j.t0 -= j.mb; j.t1 -= j.mb; }
    else if constexpr (st == 2) { const unsigned pk = cvtpk(j.t0, j.t1); j.t0 = bflo(pk); j.t1 = __uint_as_float(pk); }
    else if constexpr (st == 3) { j.t0 *= j.rden; j.t1 = bfhi(__float_as_uint(j.t1)); }
    else if constexpr (st == 4) { unsigned pk = cvtpk(j.t0, j.t1 * j.rden); asm volatile("" : "+v"(pk)); j.a[c][i] = pk; }
    else if constexpr (st == 5) { j.t0 = bflo(j.a[c][i]); j.t1 = bflo(j.w[c][i]); }
    else if constexpr (st == 6) { j.s[0] = __builtin_fmaf(j.t0, j.t1, j.s[0]); j.t0 = bfhi(j.a[c][i]); asm volatile("" : "+v"(j.s[0])); }
    else                        { j.s[1] = __builtin_fmaf(j.t0, bfhi(j.w[c][i]), j.s[1]); asm volatile("" : "+v"(j.s[1])); }
    PIN2(j.t0, j.t1);
  } else if constexpr (I == LN_XN) {
    j.t0 = j.s[0] + j.s[1]; asm volatile("" : "+v"(j.t0));
  } else if constexpr (I < LN_XN + 6) {
    j.t0 = red_step<I - LN_XN - 1>(j.t0); asm volatile("" : "+v"(j.t0));
  } else if constexpr (I == LN_XN + 6) {
    const unsigned rb = f2bf(j.t0);
    const unsigned mono = (rb & 0x8000u) ? (~rb & 0xffffu) : (rb | 0x8000u);
    j.key = (mono << 16) | (0xffffu - row);
    asm volatile("" : "+v"(j.key));
  }
}

struct RTS { unsigned k[8], bad, m, cnd, lq, va, cv, rank; u32x4 g[4]; float rs; unsigned long long pm[2]; };
DI void cnd_s(unsigned& d, const unsigned a, const unsigned long long m) { asm volatile("v_cndmask_b32_e64 %0, %0, %1, %2" : "+v"(d) : "v"(a), "s"(m)); }
DI void cnt_s(unsigned& d, const unsigned long long m) { unsigned long long co; asm volatile("v_addc_co_u32_e64 %0, %1, 0, %0, %2" : "+v"(d), "=s"(co) : "s"(m)); }
constexpr int RT_SORT = 3, RT_POP = RT_SORT + 19, RT_END = RT_POP + 64;
template <int CTRL> DI unsigned dpp_umax(unsigned v) { const unsigned t = (unsigned)__builtin_amdgcn_update_dpp(0, (int)v, CTRL, 0xf, 0xf, false); return t > v ? t : v; }
template <int I> DI void rt_step(RTS& r, const unsigned tag) {
  constexpr int net[19][2] = {{0, 1}, {2, 3}, {4, 5}, {6, 7}, {0, 2}, {1, 3}, {4, 6}, {5, 7}, {1, 2}, {5, 6}, {0, 4}, {3, 7}, {1, 5}, {2, 6}, {1, 4}, {3, 6}, {2, 4}, {3, 5}, {3, 4}};
  if constexpr (I == 0) { r.bad = (r.g[0][1] ^ tag) | (r.g[0][3] ^ tag) | (r.g[1][1] ^ tag); asm volatile("" : "+v"(r.bad)); }
  else if constexpr (I == 1) { r.bad |= (r.g[1][3] ^ tag) | (r.g[2][1] ^ tag) | (r.g[2][3] ^ tag); asm volatile("" : "+v"(r.bad)); }
  else if constexpr (I == 2) {
    r.bad |= (r.g[3][1] ^ tag) | (r.g[3][3] ^ tag); r.cnd = 0u; r.cv = r.lq & 15u;
#pragma unroll
    for (int j = 0; j < 8; ++j) r.k[j] = r.g[j >> 1][2 * (j & 1)];
    asm volatile("" : "+v"(r.bad));
  } else if constexpr (I < RT_POP) { ce(r.k[net[I - RT_SORT][0]], r.k[net[I - RT_SORT][1]]); PIN2(r.k[net[I - RT_SORT][0]], r.k[net[I - RT_SORT][1]]); }
  else if constexpr (I < RT_END) {
    constexpr int n = (I - RT_POP) >> 3, p = (I - RT_POP) & 7;
    if constexpr (p == 0) r.m = dpp_umax<0xB1>(r.k[0]);
    else if constexpr (p == 1) { r.m = dpp_umax<0x4E>(r.m); asm volatile("v_cmp_eq_u32_e64 %0, %1, %2" : "=s"(r.pm[1]) : "i"(n), "v"(r.cv)); }
    else if constexpr (p == 2) r.m = dpp_umax<0x141>(r.m);
    else if constexpr (p == 3) r.m = dpp_umax<0x140>(r.m);
    else if constexpr (p == 4) { asm volatile("v_cmp_eq_u32_e64 %0, %1, %2" : "=s"(r.pm[0]) : "v"(r.m), "v"(r.k[0])); cnd_s(r.cnd, r.m, r.pm[1]); }
    else {
#pragma unroll
      for (int i = p == 5 ? 0 : 2 * p - 9; i < 2 * p - 7 && i < 7 - n; ++i) cnd_s(r.k[i], r.k[i + 1], r.pm[0]);
    }
    asm volatile("" : "+v"(r.m));
  }
}

constexpr unsigned PF_DUM = 0x27C00u;
#define KRING 0x0000u
#define VRING 0x8000u
constexpr unsigned SD_BASE = 0x10000u, MO_XR = 0x22000u, MO_H = 0x25200u, MO_C = 0x26400u;
constexpr unsigned RTB = 254u;
constexpr unsigned RED0 = 208u, RED1 = 244u;
constexpr unsigned MO_HH = 0x26C00u;
constexpr unsigned WF_FL = 0x27000u;
constexpr unsigned MO_RS = 0x26800u, MO_KEY = 0x20000u;
constexpr unsigned BP_W = 23552u;
constexpr size_t SHM_ATTN = 0x28000;
constexpr unsigned KRS = 256u, KTILEB = 64u * KRS;
constexpr unsigned LAH = 3u;
constexpr unsigned KMASK = SEQ * KRS - 1u;
constexpr unsigned WRS = HQ * DH * 2u;
constexpr unsigned XRS = DM * 2u;

#define W4_ACLOB_HI "a128","a129","a130","a131","a132","a133","a134","a135","a136","a137","a138","a139","a140","a141","a142","a143","a144","a145","a146","a147","a148","a149","a150","a151","a152","a153","a154","a155","a156","a157","a158","a159","a160","a161","a162","a163","a164","a165","a166","a167","a168","a169","a170","a171","a172","a173","a174","a175","a176","a177","a178","a179","a180","a181","a182","a183","a184","a185","a186","a187","a188","a189","a190","a191","a192","a193","a194","a195","a196","a197","a198","a199","a200","a201","a202","a203","a204","a205","a206","a207","a208","a209","a210","a211","a212","a213","a214","a215","a216","a217","a218","a219","a220","a221","a222","a223","a224","a225","a226","a227","a228","a229","a230","a231","a232","a233","a234","a235","a236","a237","a238","a239","a240","a241","a242","a243","a244","a245","a246","a247","a248","a249","a250","a251","a252","a253","a254","a255"
#define W4_ACLOB "a0","a1","a2","a3","a4","a5","a6","a7","a8","a9","a10","a11","a12","a13","a14","a15","a16","a17","a18","a19","a20","a21","a22","a23","a24","a25","a26","a27","a28","a29","a30","a31","a32","a33","a34","a35","a36","a37","a38","a39","a40","a41","a42","a43","a44","a45","a46","a47","a48","a49","a50","a51","a52","a53","a54","a55","a56","a57","a58","a59","a60","a61","a62","a63","a64","a65","a66","a67","a68","a69","a70","a71","a72","a73","a74","a75","a76","a77","a78","a79","a80","a81","a82","a83","a84","a85","a86","a87","a88","a89","a90","a91","a92","a93","a94","a95","a96","a97","a98","a99","a100","a101","a102","a103","a104","a105","a106","a107","a108","a109","a110","a111","a112","a113","a114","a115","a116","a117","a118","a119","a120","a121","a122","a123","a124","a125","a126","a127"

DI float max2f(float a, float b) { float r; asm("v_max_f32_e32 %0, %1, %2" : "=v"(r) : "v"(a), "v"(b)); return r; }
DI void lds_st32(unsigned addr, unsigned v) { asm volatile("ds_write_b32 %0, %1" :: "v"(addr), "v"(v) : "memory"); }

struct W4C {
  unsigned ksx[2];
  unsigned vkb[4], vvb[2], vksrc[2], vvsrc, vvsrc0, m0k, m0v, sofk, ldsb;
  __amdgpu_buffer_rsrc_t srdK, srdV, srdF; unsigned sfo;
  f32x4 S[2][4][4];
  f32x4 NEGM[4], L[4];
  u32x4 ONE; unsigned IDX;
  bf16x8 Vf[8][2];
  u32x4 Pk[4][2];
  bf16x4 vlo[8][2], vup[8][2];
  unsigned pm0[2], psf[2];
  __amdgpu_buffer_rsrc_t srdP; unsigned pfo, pfm0, pfv, pfst, pfend, pdum;
};

constexpr unsigned FCT = 0x27400u, SEL_C = 0x27800u;
constexpr int AO_(int db, int qb) { return 32 * qb + 4 * db; }
constexpr int AQ_(int qb, int j) { return 128 + 16 * qb + 4 * j; }
constexpr int AK_(int kb, int j) { return 192 + 16 * kb + 4 * j; }

template <int I> DI float acc_read() { float r; asm volatile("v_accvgpr_read_b32 %0, a[%c1]" : "=v"(r) : "i"(I)); return r; }
template <int I> DI void acc_write(const unsigned v) { asm volatile("v_accvgpr_write_b32 a[%c1], %0" :: "v"(v), "i"(I)); }

template <int I, int VSLOT> DI void tr_rd(const W4C& c, bf16x4 (&lo)[8][2], bf16x4 (&up)[8][2]) {
  constexpr int db = I >> 2, k2 = (I >> 1) & 1, off = VSLOT + 8192 * k2 + 4096 * (I & 1) + 512 * (db >> 1);
  if constexpr ((I & 1) == 0) asm volatile("ds_read_b64_tr_b16 %0, %1 offset:%2" : "=v"(lo[db][k2]) : "v"(c.vvb[db & 1]), "i"(off) : "memory");
  else                        asm volatile("ds_read_b64_tr_b16 %0, %1 offset:%2" : "=v"(up[db][k2]) : "v"(c.vvb[db & 1]), "i"(off) : "memory");
}
template <int I, int VSLOT> DI void tr_read_(W4C& c) { tr_rd<I, VSLOT>(c, c.vlo, c.vup); }
template <int I, int SLOT> DI void k_read(W4C& c) {
  asm volatile("ds_read_b128 a[%c1:%c2], %0 offset:%c3" :: "v"(c.vkb[I & 3]), "i"(192 + 4 * I), "i"(195 + 4 * I), "i"(SLOT + 4096 * (I >> 2)) : "memory");
}
template <int KB, int QB, int J, int CM> DI void qk16(f32x4& s, const f32x4& negm) {
  constexpr int ak = AK_(KB, J), aq = AQ_(QB, J);
  if constexpr (CM == 2)      asm volatile("mf16 %0, a[%c1:%c2], a[%c3:%c4], 0" : "=&v"(s) : "i"(ak), "i"(ak + 3), "i"(aq), "i"(aq + 3));
  else if constexpr (CM == 1) asm volatile("mf16 %0, a[%c2:%c3], a[%c4:%c5], %1" : "=&v"(s) : "v"(negm), "i"(ak), "i"(ak + 3), "i"(aq), "i"(aq + 3));
  else                        asm volatile("mf16 %0, a[%c1:%c2], a[%c3:%c4], %0" : "+v"(s) : "i"(ak), "i"(ak + 3), "i"(aq), "i"(aq + 3));
}
template <int X, int M> DI void qk_mfma(W4C& c) { constexpr int kb = M >> 4, qb = (M >> 2) & 3, j = (qb & 1) ? 3 - (M & 3) : (M & 3); qk16<kb, qb, j, ((M & 3) == 0)>(c.S[X][kb][qb], c.NEGM[qb]); }
template <int AO, bool ZERO> DI void pv16(const bf16x8& A, const u32x4& B) {
  if constexpr (ZERO) asm volatile("mf16 a[%c2:%c3], %0, %1, 0" :: "v"(A), "v"(B), "i"(AO), "i"(AO + 3));
  else                asm volatile("mf16 a[%c2:%c3], %0, %1, a[%c2:%c3]" :: "v"(A), "v"(B), "i"(AO), "i"(AO + 3));
}
template <bool ZERO> DI void sum16(f32x4& l, const u32x4& one, const u32x4& B) {
  if constexpr (ZERO) asm volatile("mf16 %0, %1, %2, 0" : "=&v"(l) : "v"(one), "v"(B));
  else                asm volatile("mf16 %0, %1, %2, %0" : "+v"(l) : "v"(one), "v"(B));
}
DI void sum64(W4C& c, const int qb, const u32x4 (&P)[2]) {
  const u32x8 B = __builtin_shufflevector(P[0], P[1], 0, 1, 2, 3, 4, 5, 6, 7);
  asm volatile("sm64 %0, %1, %2, %3" : "+v"(c.L[qb]) : "v"(c.ONE), "v"(B), "v"(c.IDX));
}
constexpr int NGB = 68;
template <int G, bool ZERO> DI void pv_mfma(W4C& c) {
  constexpr int qb = G / 17, r = G % 17, db = (r >> 1) & 7, k2 = (r ^ db) & 1;
  if constexpr (r < 16) pv16<AO_(db, qb), (ZERO && (r & 1) == 0)>(c.Vf[db][k2], c.Pk[qb][k2]);
  else sum64(c, qb, c.Pk[qb]);
}
DI void real_op() { GAP(); __builtin_amdgcn_s_setprio(0); GAP(); }
DI void add_v(float& acc, float x) { asm volatile("v_add_f32_e32 %0, %0, %1" : "+v"(acc) : "v"(x)); }
template <int X, int E> DI void exp_e(W4C& c) {
  GAP();
  const float e = __builtin_amdgcn_exp2f(c.S[X][E >> 4][(E >> 2) & 3][E & 3]);
  asm volatile("" :: "v"(e));
  c.S[X][E >> 4][(E >> 2) & 3][E & 3] = e;
  GAP();
}
template <int X, int U> DI void pack_to(W4C& c, u32x4 (&P)[4][2]) {
  constexpr int kb = U >> 3, qb = (U >> 1) & 3, h = U & 1;
  GAP();
  const unsigned w = cvtpk(c.S[X][kb][qb][2 * h], c.S[X][kb][qb][2 * h + 1]);
  asm volatile("" :: "v"(w));
  P[qb][kb >> 1][2 * (kb & 1) + h] = w;
  GAP();
}
template <int X, int U> DI void pack_e(W4C& c) { pack_to<X, U>(c, c.Pk); }

constexpr int NBE = 40;
constexpr char PA0[] = "EXETEXFTEXETEXFT" "EXETEXFTEXETEXFT" "EXXEXFEXXEXEXEXF" "ECCECFECCECECECF";
constexpr char PB2[] = "EDEKEEDEKEEDEKFES" "DEKEEWEKFEWEKEEWS" "EKEWEKEEFEEEEEEES" "EEEEEEEEEEEEEEEES";
constexpr char PB0[] = "EDEKEEDEKEEDEKFES" "DEKEEDEKFEDEKEEDS" "EKEDEKEEFDEEEFDES" "EEEEEFEEEEEFEEEES";
constexpr int pcnt(const char* p, char ch, int g) { int n = 0; for (int i = 0; i < g; ++i) n += p[i] == ch; return n; }
constexpr int ppos(const char* p, char ch, int n) { int i = 0; while (p[i] != ch || n-- > 0) ++i; return i; }
constexpr int FA(int n) { constexpr int g[12] = {6, 10, 14, 18, 22, 30, 37, 41, 47, 53, 57, 63}; return g[n]; }
constexpr int FB(int n) { constexpr int g[10] = {14, 25, 33, 42, 47, 50, 56, 59, 62, 67}; return g[n]; }
constexpr int fa_of(int m) { for (int n = 0; n < 12; ++n) if (FA(n) == m) return n; return -1; }
constexpr int fb_of(int m) { for (int n = 0; n < 10; ++n) if (FB(n) == m) return n; return -1; }
constexpr int DB(int n) { return ppos(PB0, 'D', n); }
constexpr int kyo(int g0, int g1) { const int n = 2 * (pcnt(PB0, 'K', g1) - pcnt(PB0, 'K', g0)); return n < 15 ? n : 15; }
static_assert(pcnt(PA0, 'E', 64) == 64 - NBE && pcnt(PB0, 'E', NGB) == NBE && pcnt(PB0, 'D', NGB) == 10 && pcnt(PB0, 'K', NGB) == 8 && FB(3) == DB(8) - 1 && FB(4) == DB(9) - 1 && PA0[FA(6)] == 'F' && PA0[FA(8)] == 'F', "");
template <int X, int M, typename H> DI void gapA(W4C& c, const int t, H& hook) {
  constexpr int Xp = X ^ 1;
  constexpr const char* pa = PA0;
  constexpr char ty = pa[M];
  constexpr int nt = 2 * pcnt(pa, 'T', M) + pcnt(pa, 'X', M), np = pcnt(pa, 'X', M) + 2 * pcnt(pa, 'C', M);
  if constexpr (M == 0) asm volatile("" ::: W4_ACLOB, W4_ACLOB_HI);
  qk_mfma<X, M>(c);
  hook(std::integral_constant<int, M>{});
  if constexpr (ty == 'T' || ty == 'X') tr_read_<nt, Xp * 0x4000>(c);
  if constexpr (ty == 'T') tr_read_<nt + 1, Xp * 0x4000>(c);
  if constexpr (ty == 'X' || ty == 'C') pack_e<Xp, np>(c);
  if constexpr (ty == 'C') pack_e<Xp, np + 1>(c);
  if constexpr (ty == 'E') exp_e<Xp, NBE + pcnt(pa, 'E', M)>(c);
  {
    if constexpr (M == FA(6))      { c.pm0[0] = c.m0k + (unsigned)X * 0x4000u; c.pm0[1] = c.m0v + (unsigned)(X ^ 1) * 0x4000u; asm volatile("" : "+s"(c.pm0[0]), "+s"(c.pm0[1])); }
    else if constexpr (M == FA(8)) asm volatile("s_mov_b32 %1, %0\n\ts_add_u32 %0, %0, %2\n\ts_and_b32 %0, %0, %3" : "+s"(c.psf[0]), "=&s"(c.psf[1]) : "i"(KTILEB), "i"(KMASK) : "scc");
  }
  hook(std::integral_constant<int, 100 + M>{});
  GAP();
}
#define W4_V8(a, k) "+v"(a[0][k]), "+v"(a[1][k]), "+v"(a[2][k]), "+v"(a[3][k]), "+v"(a[4][k]), "+v"(a[5][k]), "+v"(a[6][k]), "+v"(a[7][k])
template <int PFW> DI void phaseA_end(W4C& c) {
  asm volatile("s_waitcnt vmcnt(%16) lgkmcnt(0)" : W4_V8(c.vlo, 0), W4_V8(c.vlo, 1) : "i"(PFW) : "memory");
  asm volatile("" : W4_V8(c.vup, 0), W4_V8(c.vup, 1) : "v"(c.NEGM[0]), "v"(c.NEGM[1]), "v"(c.NEGM[2]), "v"(c.NEGM[3]) : "memory");
  GAP();
  __builtin_amdgcn_s_barrier();
  asm volatile("" ::: "memory");
  GAP();
#pragma unroll
  for (int k2 = 0; k2 < 2; ++k2)
#pragma unroll
    for (int db = 0; db < 8; ++db) c.Vf[db][k2] = __builtin_shufflevector(c.vlo[db][k2], c.vup[db][k2], 0, 1, 2, 3, 4, 5, 6, 7);
}
template <int X, int PFW, typename H, int... Ms> DI void phaseA_(W4C& c, const int t, H& hook, iseq<Ms...>) {
  (gapA<X, Ms>(c, t, hook), ...);
  phaseA_end<PFW>(c);
}
struct NoHook { template <typename T> DI void operator()(T) const {} };
template <int X, int PFW = 0> DI void phaseA(W4C& c, const int t) { NoHook h; phaseA_<X, PFW>(c, t, h, make_iseq<64>{}); }
template <int X, int PFW, typename H> DI void phaseAh(W4C& c, const int t, H& hook) { phaseA_<X, PFW>(c, t, hook, make_iseq<64>{}); }

template <int X, bool ZERO1, bool DK, int PF, int M, typename H> DI void gapB(W4C& c, H& hook) {
  constexpr const char* pb = DK ? PB0 : PB2;
  constexpr char ty = pb[M];
  constexpr int n = pcnt(pb, ty, M) + (!DK && ty == 'D' ? 4 : 0), d8 = DB(8), d9 = DB(9);
  pv_mfma<M, ZERO1>(c);
  if constexpr (M == DB(0) - 1) asm volatile("s_mov_b32 m0, %0" :: "s"(c.pm0[!DK]) : "m0", "memory");
  if constexpr (M == DB(4) - 1) asm volatile("s_mov_b32 m0, %0" :: "s"(c.pm0[DK]) : "m0", "memory");
  if constexpr (M == 57) asm volatile("s_waitcnt lgkmcnt(0)" ::: "memory");
  if constexpr (PF && (M == d8 - 1 || M == d9 - 1))
    asm volatile("s_cmp_lt_u32 %0, %1\n\ts_cselect_b32 m0, %0, %2\n\ts_add_u32 %0, %0, 0x400" : "+s"(c.pfm0) : "s"(c.pfend), "s"(c.pdum) : "m0", "memory", "scc");
  if constexpr (PF && (M == d8 - 2 || M == d9 - 2)) asm volatile("s_add_u32 %0, %0, %1" : "+s"(c.pfo) : "s"(c.pfst) : "scc");
  constexpr int lg = PF ? 56 : 40;
  if constexpr (X == 1 && M == lg - 5) asm volatile("s_add_u32 %0, %0, %1" : "+s"(c.sfo) : "i"(2 * KTILEB) : "scc");
  if constexpr (X == 1 && M == lg - 4) asm volatile("s_and_b32 %0, %0, %1" : "+s"(c.sfo) : "i"(KMASK) : "scc");
  if constexpr (X == 1 && M == lg - 2) asm volatile("s_mov_b32 m0, %0" :: "s"(c.pdum) : "m0", "memory");
  if constexpr (X == 1 && M == lg) asm volatile("buffer_load_dwordx4 %0, %1, %2 offen lds" :: "v"(c.vksrc[0]), "s"(c.srdF), "s"(c.sfo) : "memory");
  hook(std::integral_constant<int, M>{});
  if constexpr (ty == 'K') { k_read<2 * n, (X ^ 1) * 0x4000>(c); k_read<2 * n + 1, (X ^ 1) * 0x4000>(c); }
  if constexpr (ty == 'D') {
    if constexpr (n < 4) { asm volatile("buffer_load_dwordx4 %0, %1, %2 offen offset:%c3 lds" :: "v"((n & 2) ? c.ksx[n & 1] : c.vksrc[n & 1]), "s"(c.srdK),  "s"(c.psf[0]), "i"(1024 * (n & 3)) : "memory"); }
    else if constexpr (n < 8) asm volatile("buffer_load_dwordx4 %0, %1, %2 offen offset:%c3 lds" :: "v"((n & 1) ? c.vvsrc : c.vvsrc0), "s"(c.srdV),  "s"(c.psf[1]), "i"(1024 * (n & 3)) : "memory");
    else if constexpr (PF) asm volatile("buffer_load_dwordx4 %0, %1, %2 offen sc1 lds" :: "v"(c.pfv), "s"(c.srdP), "s"(c.pfo) : "memory");
  }
  if constexpr (ty == 'W') asm volatile("buffer_load_dwordx4 %0, %1, %2 offen offset:%c3 sc1 lds" :: "v"(c.pfv), "s"(c.srdP), "s"(c.pfo), "i"(1024 * n) : "memory");
  if constexpr (ty == 'E') exp_e<X, DK ? n : 16 * ((n >> 2) & 3) + 4 * (n >> 4) + (n & 3)>(c);
  hook(std::integral_constant<int, 100 + M>{});
  GAP();
}
template <int X, bool ZERO1, bool DK, int PF, typename H, int... Ms> DI void phaseB_(W4C& c, H& hook, iseq<Ms...>) {
  GAP();
  (gapB<X, ZERO1, DK, PF, Ms>(c, hook), ...);
  hook(std::integral_constant<int, 72>{});
  GAP();
}
template <int X, int PF> DI void phaseB(W4C& c) { NoHook h; phaseB_<X, false, true, PF>(c, h, make_iseq<NGB>{}); }
template <int X, int PF, typename H> DI void phaseBh(W4C& c, H& hook) { phaseB_<X, false, true, PF>(c, hook, make_iseq<NGB>{}); }

constexpr unsigned QP_STG = 0xC000u, QP_W8 = 0x24000u, QP_CS = 0x18000u;
constexpr int PR0 = 1, PR1 = 8;
constexpr int PJ = 224;
constexpr int NJ = 200, JK2 = 79, JR0 = 103;
constexpr int JT[] = {1000,20004,20005,32000,1001,20006,20007,32000,31000,1002,22000,32000,1003,21000,21001,32000,1016,34000,21002,21003,32000,1017,34000,21004,21005,32000,1018,34000,21006,21007,30000,32000,16000,1019,
  34000,32000,16001,30001,1004,34000,20008,20009,32000,16002,1005,34000,20010,20011,32000,16003,1006,34000,20012,20013,32000,16004,1007,34000,20014,20015,32000,16005,1008,34000,32000,16006,1020,34000,32000,16007,1021,
  34000,32000,10000,10001,10002,10003,10004,10005,1022,34000,32000,10006,10007,10008,10009,10010,10011,1023,34000,32000,10012,10013,10014,10015,10016,10017,1024,34000,32000,10018,10019,10020,10021,10022,10023,1032,34000,
  32000,10024,10025,10026,1033,34000,32000,10027,10028,10029,10030,10031,1034,34000,32000,10032,10033,10034,10035,10036,10037,1035,34000,32000,10038,10039,10040,10041,10042,10043,30002,1036,34000,32000,10044,10045,10046,
  10047,10048,10049,1037,34000,32000,10050,10051,10052,10053,10054,10055,1038,34000,32000,10056,10057,10058,10059,10060,1039,34000,32000,10061,10062,10063,1040,34000,32000,10064,10065,10066,10067,10068,10069,1048,34000,
  20016,32000,10070,10071,1049,34000,20017,32000,10072,10073,10074,10075,10076,10077,1050,34000,20018,32000,10078,10079,10080,10081,10082,10083,1051,34000,20019,32000,10084,10085,10086,10087,10088,10089,1052,34000,20020,
  32000,10090,10091,10092,10093,10094,10095,1053,34000,20021,32000,10096,10097,10098,1054,34000,20022,32000,10099,10100,10101,10102,10103,1055,34000,20023,32000,10104,10105,10106,10107,1056,34000,32000,1064,34000,32000,
  1065,34000,32000,10108,10109,10110,10111,10112,10113,1066,34000,32000,10114,10115,10116,10117,10118,10119,1067,34000,32000,10120,10121,10122,10123,10124,10125,30003,1068,34000,32000,10126,10127,10128,10129,10130,10131,
  1069,34000,32000,10132,10133,10134,1070,34000,32000,10135,10136,10137,10138,10139,1071,34000,32000,10140,10141,10142,10143,1072,34000,20024,32000,1080,34000,20025,32000,1081,34000,20026,32000,10144,10145,10146,10147,
  10148,10149,1082,34000,20027,32000,10150,10151,10152,10153,10154,10155,1083,34000,20028,32000,10156,10157,10158,10159,10160,10161,1084,34000,20029,32000,10162,10163,10164,10165,10166,10167,1085,34000,20030,32000,10168,
  10169,10170,1086,34000,20031,32000,10171,10172,10173,10174,10175,1087,34000,32000,10176,10177,10178,10179,1088,34000,32000,1096,34000,32000,1097,34000,32000,10180,10181,10182,10183,10184,10185,31001,1098,34000,32000,
  10186,10187,10188,10189,10190,10191,1099,34000,32000,10192,10193,10194,10195,30004,1100,34000,23000,23001,32000,10196,10197,10198,10199,10200,1101,34000,23002,23003,32000,10201,10202,10203,10204,1102,34000,23004,23005,
  24000,32000,1103,34000,23006,23007,32000,10205,10206,1104,34000,23008,32000,10207,10208,10209,10210,10211,1112,34000,23009,32000,10212,10213,10214,10215,1113,34000,23010,32000,10216,10217,10218,10219,10220,10221,1114,
  34000,23011,32000,10222,10223,10224,10225,10226,10227,1115,34000,23012,32000,10228,10229,10230,10231,1116,34000,23013,32000,10232,10233,10234,10235,10236,10237,1117,34000,23014,32000,10238,10239,10240,10241,1118,34000,
  23015,32000,10242,10243,1119,34000,32000,10244,10245,10246,10247,10248,10249,1120,34000,32000,10250,10251,30005,2128,34000,24001,24002,32000,2132,34000,32000,10252,10253,2136,34000,32000,10254,10255,2140,34000,23100,
  32000,10256,2001,34000,32000,10257,10258,2005,34000,32000,10259,10260,31002,2009,34000,32000,10261,10262,2013,32000,2002,34000,23101,32000,10263,2003,33000,32000,2006,34000,32000,10264,10265,2007,33001,32000,2010,
  34000,32000,11000,11001,2014,34000,32000,11002,11016,11017,11003,11004,11005,11018,23102,11006,11007,11008,11009,11010,11011,11012,2080,34000,32000,11013,11014,2017,34000,32000,11019,2018,34000,32000,11020,11021,2019,
  34000,32000,11022,2096,34000,32000,11023,11024,2033,34000,32000,2034,34000,32000,11025,11026,2035,34000,32000,11027,11028,2112,34000,32000,11029,11030,2049,34000,32000,10266,10267,2050,34000,32000,10268,10269,2051,
  34000,32000,2087,34000,32000,10270,10271,2022,34000,32000,10272,10273,2021,34000,32000,10274,10275,2020,34000,32000,10276,2103,34000,32000,10277,2038,32000,2037,34000,32000,10278,2036,34000,32000,10279,2119,34000,
  32000,10280,10281,2054,34000,32000,10282,10283,2053,34000,32000,10284,10285,2052,32000,10286,10287,24003,24004,2011,32000,13000,2015,34000,23103,24005,32000,13001,13002,11032,11033,11034,13003,11048,11035,11036,11037,
  11049,11050,11038,11039,11040,11041,11042,11043,11044,2088,34000,32000,11045,11046,2025,34000,32000,11051,2026,34000,32000,11052,11053,2027,34000,32000,11054,2104,34000,32000,11055,11056,2041,34000,32000,11057,11058,
  2042,34000,32000,11059,11060,2043,34000,32000,11061,11062,2120,32000,13016,2057,34000,24006,32000,15000,2058,32000,13017,2059,32000,13018,2095,34000,24007,32000,15001,2030,32000,13019,2029,32000,13032,2028,34000,23104,
  32000,15008,2111,34000,23105,32000,15009,2046,34000,23106,23107,32000,2045,34000,24008,24009,32000,2044,34000,23108,23109,32000,2127,34000,23110,23111,32000,2062,34000,24010,24011,32000,2061,32000,13033,2060,34000,
  23112,32000,15016,30105,3064,34000,23113,23114,32000,3001,34000,23115,24012,32000,3002,32000,13034,3003,34000,24013,24014,32000,3071,32000,13035,3006,32000,13048,3005,34000,24015,32000,15017,3004,32000,13049,3072,
  32000,13050,3009,34000,24016,32000,15024,3010,32000,13051,3011,32000,13004,3079,34000,24017,32000,15025,3014,32000,13005,3013,32000,13006,3012,34000,24018,32000,15002,3080,32000,13007,3017,32000,13020,3018,34000,24019,
  32000,15003,3019,32000,13021,3087,32000,13022,3022,34000,24020,32000,15010,3021,32000,13023,3020,32000,13036,3088,34000,24021,32000,15011,3025,32000,13037,3026,32000,13038,3027,34000,24022,32000,15018,3095,32000,13039,
  3030,32000,13052,3029,34000,24023,32000,15019,3028,32000,13053,3096,32000,13054,3033,34000,24024,32000,15026,30102,3034,32000,13055,3035,32000,13008,3103,34000,24025,32000,15027,3038,32000,13009,3037,32000,13010,3036,
  34000,24026,32000,15004,3104,32000,13011,3041,32000,13024,3042,34000,24027,32000,15005,3043,32000,13025,3111,32000,13026,3046,34000,24028,32000,15012,3045,32000,13027,3044,32000,13040,3112,34000,24029,32000,15013,3049,
  32000,13041,3050,32000,13042,3051,34000,24030,32000,15020,3119,32000,13043,3054,34000,24031,32000,15021,3053,32000,13056,3052,32000,13057,3120,32000,13058,3057,32000,13059,3058,32000,13012,3059,32000,13013,3127,34000,
  32000,15028,15029,3062,32000,13014,3061,32000,13015,3060,34000,32000,15006,13028,13029,13030,15007,13031,13044,15014,13045,13046,15015,13047,13060,15022,13061,13062,15023,13063,15030,15031,};
constexpr short JS[] = {0,4,8,12,16,21,26,33,37,44,50,56,62,66,70,79,88,97,106,112,120,129,138,148,157,165,171,180,186,196,206,216,226,233,242,250,253,256,265,274,283,293,299,307,314,318,322,332,342,352,362,369,378,385,
  388,391,400,410,417,428,437,443,450,459,467,477,487,495,505,513,519,528,533,539,544,549,554,559,564,570,572,577,580,585,588,593,611,616,620,625,629,634,637,642,647,652,657,662,665,670,675,680,684,688,690,694,698,703,
  708,713,719,722,746,751,755,760,764,769,774,779,784,787,792,795,798,803,806,809,814,819,824,829,834,839,844,847,852,858,863,866,871,874,877,882,885,888,893,896,899,904,907,910,915,918,921,926,929,932,937,940,943,948,
  951,954,959,962,965,970,973,976,981,985,988,993,996,999,1004,1007,1010,1015,1018,1021,1026,1029,1032,1037,1040,1043,1048,1051,1056,1059,1062,1065,1068,1071,1074,1079,1082,1085,1108,};
enum : int { VK_CW, VK_GEN, VK_S, VK_W8, VK_A, VK_WR, VK_STS, VK_GAT, VK_XNS, VK_KV, VK_KEY, VK_V1, VK_K2, VK_PF };
struct VOp { int kind, st, q, pos; };
struct VTab { VOp v[192]; int n; };
constexpr VTab mk_vtab() {
  VTab t{}; int n = 0;
  auto add = [&](int kind, int st, int q, int pos) { t.v[n].kind = kind; t.v[n].st = st; t.v[n].q = q; t.v[n].pos = pos; ++n; };
  const int PS[12] = {7, 10, 12, 15, 18, 20, 23, 26, 28, 31, 33, 36}, P3[12] = {20, 22, 24, 26, 28, 30, 32, 34, 36, 38, 40, 42}, P4[12] = {12, 14, 16, 19, 21, 23, 26, 28, 30, 32, 34, 36};
  for (int q = 0; q < 12; ++q) add(VK_S, 0, q, -19);
  for (int q = 0; q < 3; ++q) add(VK_A, 0, q, -20);
  add(VK_GEN, 0, 0, -20); add(VK_CW, 0, 0, -19);
  for (int q = 0; q < 12; ++q) add(VK_S, 1, q, q < 8 ? q - 10 : q - 8);
  for (int q = 0; q < 12; ++q) add(VK_S, 2, q, 4 + q);
  add(VK_STS, 0, 0, -1);
  for (int q = 0; q < 3; ++q) add(VK_WR, 0, q, 16 + q);
  for (int s = 3; s < 9; ++s) for (int q = 0; q < 12; ++q) add(s == 8 && q < 4 ? VK_W8 : VK_S, s, q, 32 * (s - 3) + (s == 3 ? P3[q] : s == 4 ? P4[q] : PS[q]));
  add(VK_GAT, 0, 0, 128 + 9); add(VK_GAT, 0, 1, 128 + 14);
  add(VK_XNS, 0, 0, 160 + 22); add(VK_XNS, 0, 1, 160 + 25); add(VK_XNS, 0, 2, 160 + 30);
  for (int q = 0; q < 12; ++q) add(VK_KV, 0, q, 192 + PS[q]);
  add(VK_KEY, 0, 0, PJ + 57);
  for (int q = 0; q < 4; ++q) add(VK_V1, 0, q, PJ + 60 + 3 * q);
  for (int q = 0; q < 4; ++q) add(VK_K2, 0, q, PJ + JK2 + 6 * q);
  add(VK_PF, 0, 0, PJ + JR0); add(VK_PF, 0, 1, PJ + JR0 + 6);
  for (int i = 1; i < n; ++i) { const VOp x = t.v[i]; int j = i; while (j > 0 && t.v[j - 1].pos > x.pos) { t.v[j] = t.v[j - 1]; --j; } t.v[j] = x; }
  t.n = n;
  return t;
}
constexpr VTab VT = mk_vtab();
constexpr int vq_first(int pos) { int i = 0; while (i < VT.n && VT.v[i].pos < pos) ++i; return i; }
constexpr int vq_wait(int kind, int st, int pos) {
  int last = -1;
  for (int i = 0; i < VT.n; ++i) if (VT.v[i].kind == kind && VT.v[i].st == st) last = i;
  return vq_first(pos) - 1 - last;
}
constexpr int imin(int a, int b) { return a < b ? a : b; }
constexpr int vq_grp(int i) {
  const VOp o = VT.v[i];
  return o.kind == VK_S ? 3 * o.st + (o.q >> 2) : o.kind == VK_W8 ? 40 : o.kind == VK_KV ? 41 + (o.q >> 2) : o.kind == VK_V1 ? 44 : o.kind == VK_K2 ? 45 : o.kind == VK_PF ? 50 + o.q : -1;
}
constexpr bool vq_newm0(int i) { int j = i - 1; while (j >= 0 && vq_grp(j) < 0) --j; return j < 0 || vq_grp(j) != vq_grp(i); }
template <bool NEWM0, int OFF> DI void lds_dma(const unsigned m0, const unsigned vo, const __amdgpu_buffer_rsrc_t srd, const unsigned so) {
  if constexpr (NEWM0) asm volatile("s_mov_b32 m0, %0\n\ts_nop 0\n\tbuffer_load_dwordx4 %1, %2, %3 offen offset:%c4 lds" :: "s"(m0), "v"(vo), "s"(srd), "s"(so), "i"(OFF) : "m0", "memory");
  else                 asm volatile("buffer_load_dwordx4 %0, %1, %2 offen offset:%c3 lds" :: "v"(vo), "s"(srd), "s"(so), "i"(OFF) : "memory");
}
template <int I0, typename F, int... Is> DI void vq_run(F& vm, iseq<Is...>) { (vm(std::integral_constant<int, I0 + Is>{}), ...); }
template <int POS, typename F> DI void vq_issue(F& vm) { vq_run<vq_first(POS)>(vm, make_iseq<vq_first(POS + 1) - vq_first(POS)>{}); }

constexpr int AW_(int T, int db, int ks) { return ((T & 1) ? 192 : 0) + 16 * db + 4 * ks; }
constexpr int AX_(int T, int qb, int ks) { return ((T & 1) ? 96 : 64) + 16 * qb + 4 * ks; }
template <int T, int DB, int QB, int KS> DI void qp_mfma(f32x16& d) {
  constexpr int aw = AW_(T, DB, KS), ax = AX_(T, QB, KS);
  if constexpr (T == 0 && KS == 0) asm volatile("mf32 %0, a[%c1:%c2], a[%c3:%c4], 0" : "=&v"(d) : "i"(aw), "i"(aw + 3), "i"(ax), "i"(ax + 3));
  else                             asm volatile("mf32 %0, a[%c1:%c2], a[%c3:%c4], %0" : "+v"(d) : "i"(aw), "i"(aw + 3), "i"(ax), "i"(ax + 3));
}
struct QP {
  f32x16 A[4][2];
  bf16x4 cl[2][4], cu[2][4];
  f32x4 cs; u32x4 sel;
  unsigned vwb, vwc, vw[4], vx[4], vxb[4], m0w, m0x, sofw, sofx, kk, sw, sx;
  __amdgpu_buffer_rsrc_t srdW, srdX;
};
template <int S> DI void qp_next(QP& p) {
  constexpr int d = S < PR0 || S > PR1 ? 0 : ((S - PR0) & 1) ? -1 : 1;
  const unsigned k = (unsigned)S + (d == 0 ? 0u : d > 0 ? p.kk : 0u - p.kk);
  p.sw = p.sofw + (k << 19); p.sx = p.sofx + (k << 7);
  asm volatile("" : "+s"(p.sw), "+s"(p.sx));
}
constexpr unsigned qp_wbase(int T) { return T == 8 ? QP_W8 : QP_STG * (T % 3); }
template <int T, int DB, int KS, int U> DI void qp_tr(QP& p) {
  asm volatile("ds_read_b64_tr_b16 a[%c1:%c2], %0 offset:%c3" :: "v"(p.vwb + qp_wbase(T)), "i"(AW_(T, DB, KS) + 2 * U), "i"(AW_(T, DB, KS) + 2 * U + 1), "i"(4096 * KS + 2048 * U + 512 * DB) : "memory");
}
template <int T, int QB, int KS> DI void qp_xr(QP& p) {
  asm volatile("ds_read_b128 a[%c1:%c2], %0 offset:%c3" :: "v"(p.vxb[KS] + QP_STG * (T % 3)), "i"(AX_(T, QB, KS)), "i"(AX_(T, QB, KS) + 3), "i"(4096 * QB) : "memory");
}
template <int T, int I> DI void qp_csr(QP& p) {
  constexpr int ks = I >> 1;
  if constexpr ((I & 1) == 0) asm volatile("ds_read_b64_tr_b16 %0, %1 offset:%2" : "=v"(p.cl[T == 8][ks]) : "v"(p.vwc + qp_wbase(T)), "i"(4096 * ks) : "memory");
  else                        asm volatile("ds_read_b64_tr_b16 %0, %1 offset:%2" : "=v"(p.cu[T == 8][ks]) : "v"(p.vwc + qp_wbase(T)), "i"(4096 * ks + 2048) : "memory");
}
template <int KS, bool ZERO, int L = 0> DI void qp_csd(QP& p) {
  const bf16x8 w = __builtin_shufflevector(p.cl[L][KS], p.cu[L][KS], 0, 1, 2, 3, 4, 5, 6, 7);
  if constexpr (ZERO) asm volatile("mf16 %0, %1, %2, 0" : "=&v"(p.cs) : "v"(w), "v"(p.sel));
  else                asm volatile("mf16 %0, %1, %2, %0" : "+v"(p.cs) : "v"(w), "v"(p.sel));
}
template <int T, int R> DI void qp_rd(QP& p) {
  if constexpr (R < 8) qp_csr<T, R>(p);
  else {
    constexpr int ks = (R - 8) / 10, u = (R - 8) % 10;
    if constexpr (u == 0) qp_xr<T, 0, ks>(p);
    else if constexpr (u == 3) qp_xr<T, 1, ks>(p);
    else { constexpr int v = u < 3 ? u - 1 : u - 2; qp_tr<T, (v >> 1), ks, (v & 1)>(p); }
  }
}
template <int T, int R0, int... Rs> DI void qp_rds(QP& p, iseq<Rs...>) { (qp_rd<T, R0 + Rs>(p), ...); }
#define QP_CSV(L) "+v"(p.cl[L][0]), "+v"(p.cl[L][1]), "+v"(p.cl[L][2]), "+v"(p.cl[L][3]), "+v"(p.cu[L][0]), "+v"(p.cu[L][1]), "+v"(p.cu[L][2]), "+v"(p.cu[L][3])
constexpr int LN_P0 = 32 + 18, LN_P1 = 160 + 21;
constexpr int ln_slots(int pos) { int n = 0; for (int q = LN_P0; q < pos; ++q) if (vq_first(q + 1) == vq_first(q) && (q & 31) != 5) ++n; return n; }
constexpr int ln_lo(int pos) {
  if (pos < LN_P0) return LN_N0;
  if (pos <= LN_P1) return LN_N0 + (ln_slots(pos) * 97) / ln_slots(LN_P1);
  const int s = LN_XN + 1 + (pos - LN_P1) / 3;
  return s < LN_KEY ? s : LN_KEY;
}
template <int I0, int... Is> DI void ln_steps(LNJ& j, const unsigned row, iseq<Is...>) { (ln_step<I0 + Is>(j, row), ...); }
template <int T, int M, typename FV, typename FH> DI void qp_gap(QP& p, FV& vm, FH& hook, LNJ& lj, const unsigned lrow) {
  constexpr int ks = M >> 3, db = (M >> 1) & 3, qb = M & 1, pos = 32 * T + M, BG = T == 0 ? 12 : 6;
  if constexpr (T == 0 && M == 0) asm volatile("s_waitcnt lgkmcnt(10)" ::: "memory", W4_ACLOB, W4_ACLOB_HI);
  if constexpr (T == 0 && M == 8) asm volatile("s_waitcnt lgkmcnt(0)" : QP_CSV(0) :: "memory");
  if constexpr (T > 0 && M == 0) asm volatile("s_waitcnt lgkmcnt(0)" ::: "memory");
  if constexpr (M == BG) {
    constexpr int w0 = vq_wait(VK_S, T + 1, pos);
    constexpr int w = T == 1 ? imin(w0, vq_wait(VK_WR, 0, pos)) : T == 6 ? imin(w0, vq_wait(VK_W8, 8, pos)) : w0;
    asm volatile("s_waitcnt vmcnt(%0)\n\ts_barrier" :: "i"(w) : "memory");
  }
  qp_mfma<T, db, qb, ks>(p.A[db][qb]);
  if constexpr (T == 0) {
    if constexpr (M < 5) qp_rds<0, 28 + 4 * M>(p, make_iseq<4>{});
    if constexpr (M == 5 || M == 6) qp_rds<0, 4 * (M - 5)>(p, make_iseq<4>{});
    if constexpr (M == 18) asm volatile("s_waitcnt lgkmcnt(10)" : QP_CSV(0) :: "memory");
    if constexpr (M >= 12 && M < 28) qp_rds<1, 3 * (M - 12)>(p, make_iseq<3>{});
  } else {
    if constexpr (M == 13) asm volatile("s_waitcnt lgkmcnt(%8)" : QP_CSV(0) : "i"(T == 6 ? 10 : 6) : "memory");
    if constexpr (T == 6 && M == 17) asm volatile("s_waitcnt lgkmcnt(6)" : QP_CSV(1) :: "memory");
    if constexpr (M >= 6 && M < 30) qp_rds<T + 1, 2 * (M - 6)>(p, make_iseq<2>{});
    if constexpr (T == 6 && M >= 6 && M < 14) qp_csr<8, M - 6>(p);
    if constexpr (T == 6 && M >= 30) { qp_tr<8, 0, M - 30, 0>(p); qp_tr<8, 0, M - 30, 1>(p); }
  }
  vq_issue<pos>(vm);
  if constexpr (pos == 3 || pos == 19 || pos == 32 + 11 || (M == 5 && T >= 2 && T <= 5)) qp_next<(pos == 3 ? 2 : pos == 19 ? 3 : T + 3)>(p);
  GAP();
  hook(std::integral_constant<int, pos>{});
  ln_steps<ln_lo(pos)>(lj, lrow, make_iseq<ln_lo(pos + 1) - ln_lo(pos)>{});
  if constexpr (T == 0 && M >= 8 && M < 12) qp_csd<M - 8, M == 8>(p);
  if constexpr (T == 0 && M >= 18 && M < 28 && M % 3 == 0) qp_csd<(M - 18) / 3, false>(p);
  if constexpr (T > 0 && M >= 13 && M < 23 && M % 3 == 1) qp_csd<(M - 13) / 3, false>(p);
  if constexpr (T == 6 && (M == 17 || M == 21 || M == 24 || M == 27)) qp_csd<(M == 17 ? 0 : (M - 18) / 3), false, 1>(p);
  GAP();
}
template <int T, typename FV, typename FH, int... Ms> DI void qp_gaps(QP& p, FV& vm, FH& hook, LNJ& lj, const unsigned lrow, iseq<Ms...>) { (qp_gap<T, Ms>(p, vm, hook, lj, lrow), ...); }
template <int T, typename FV, typename FH> DI void qp_iter(QP& p, FV& vm, FH& hook, LNJ& lj, const unsigned lrow) { qp_gaps<T>(p, vm, hook, lj, lrow, make_iseq<32>{}); }
template <typename FV, typename FH, int... Ts> DI void qp_iters(QP& p, FV& vm, FH& hook, LNJ& lj, const unsigned lrow, iseq<Ts...>) { (qp_iter<Ts>(p, vm, hook, lj, lrow), ...); }

struct QFT { float a[2], cv[4], r1, m[4], m2[4]; unsigned w[8]; u32x4 ca[4], nb[2]; };
template <int N> DI void q_fin(QP& p, QFT& t) {
  constexpr int tau = N / 36, n = N % 36, h = n / 12, u = n % 12, db = tau >> 1, qb = tau & 1;
  f32x16& d = p.A[db][qb];
  if constexpr (n >= 28) acc_write<AQ_(2 * qb + ((n - 28) >> 2), db) + (n & 3)>(t.w[n - 28]);
  else if constexpr (n >= 24) { GAP(); auto pr = __builtin_amdgcn_permlane16_swap(t.w[u], t.w[4 + u], false, false); t.w[u] = pr[0]; t.w[4 + u] = pr[1]; asm volatile("" :: "v"(t.w[u]), "v"(t.w[4 + u])); GAP(); }
  else if constexpr (u < 8) { float v = d[8 * h + u]; asm volatile("v_mul_f32_e32 %0, %0, %1" : "+v"(v) : "v"(t.a[qb])); d[8 * h + u] = v; }
  else { GAP(); t.w[4 * h + u - 8] = cvtpk(d[8 * h + 2 * (u - 8)], d[8 * h + 2 * (u - 8) + 1]); asm volatile("" :: "v"(t.w[4 * h + u - 8])); GAP(); }
}
template <int Q, int I> DI void ref_step(W4C& c, QFT& t) {
  f32x4& s = c.S[0][0][Q];
  if constexpr (I == 0) asm volatile("v_max3_f32 %0, %1, %2, %3" : "=v"(t.m[Q]) : "v"(s[0]), "v"(s[1]), "v"(s[2]));
  else if constexpr (I == 1) asm volatile("v_max_f32_e32 %0, %0, %1" : "+v"(t.m[Q]) : "v"(s[3]));
  else if constexpr (I == 2 || I == 5) asm volatile("v_mov_b32_e32 %0, %1" : "=v"(t.m2[Q]) : "v"(t.m[Q]));
  else if constexpr (I == 3) asm volatile("v_permlane16_swap_b32_e32 %0, %1" : "+v"(t.m[Q]), "+v"(t.m2[Q]));
  else if constexpr (I == 4) asm volatile("v_max_f32_e32 %0, %0, %1" : "+v"(t.m[Q]) : "v"(t.m2[Q]));
  else if constexpr (I == 6) asm volatile("v_permlane32_swap_b32_e32 %0, %1" : "+v"(t.m[Q]), "+v"(t.m2[Q]));
  else if constexpr (I < 11) { float n; asm volatile("v_min_f32_e64 %0, -%1, -%2" : "=v"(n) : "v"(t.m[Q]), "v"(t.m2[Q])); c.NEGM[Q][I - 7] = n; }
  else { float v = s[I - 11]; add_v(v, c.NEGM[Q][0]); s[I - 11] = v; }
}
template <int J, int CODE, typename FV> DI void qj_op(QP& p, W4C& c, QFT& t, FV& vm, const unsigned csa) {
  constexpr int kind = CODE / 1000, a = CODE % 1000;
  if constexpr (kind == 1) {
    constexpr int tau = a >> 4, u = a & 15, db = tau >> 1, qb = tau & 1;
    if constexpr (u < 8) qp_mfma<7 + (u >> 2), db, qb, (u & 3)>(p.A[db][qb]);
    else asm volatile("mf32 %0, %1, %2, %0" : "+v"(p.A[db][qb]) : "v"(t.ca[db]), "v"(t.nb[qb]));
  } else if constexpr (kind == 2 || kind == 3) { constexpr int kb = (a >> 4) & 3, qb = (a >> 2) & 3; qk16<kb, qb, (a & 3), (a >> 6)>(c.S[kind - 2][kb][qb], c.NEGM[qb]); }
  else if constexpr (kind == 10) q_fin<a>(p, t);
  else if constexpr (kind == 11) ref_step<(a >> 4), (a & 15)>(c, t);
  else if constexpr (kind == 13) exp_e<0, a>(c);
  else if constexpr (kind == 15) pack_e<0, a>(c);
  else if constexpr (kind == 16) {
    constexpr int d = a >> 1;
    if constexpr ((a & 1) == 0) { t.r1 = t.cv[d] - rbf(t.cv[d]); t.ca[d][0] = cvtpk(t.cv[d], t.r1); asm volatile("" : "+v"(t.r1), "+v"(t.ca[d])); }
    else { t.ca[d][1] = cvtpk(t.r1 - bfhi(t.ca[d][0]), 0.f); asm volatile("" : "+v"(t.ca[d])); }
  } else if constexpr (kind == 20) qp_tr<8, (a >> 3), ((a >> 1) & 3), (a & 1)>(p);
  else if constexpr (kind == 21) qp_xr<8, (a >> 2), (a & 3)>(p);
  else if constexpr (kind == 22)
    asm volatile("ds_read_b32 %0, %4\n\tds_read_b32 %1, %4 offset:128\n\tds_read_b32 %2, %4 offset:256\n\tds_read_b32 %3, %4 offset:384"
                 : "=&v"(t.cv[0]), "=&v"(t.cv[1]), "=&v"(t.cv[2]), "=&v"(t.cv[3]) : "v"(csa) : "memory");
  else if constexpr (kind == 23) k_read<a % 100, (a >= 100 ? 0x4000 : 0)>(c);
  else if constexpr (kind == 24) tr_read_<a, 0>(c);
  else if constexpr (kind == 30) {
    if constexpr (a == 0) asm volatile("s_waitcnt lgkmcnt(8)" : "+v"(t.cv[0]), "+v"(t.cv[1]), "+v"(t.cv[2]), "+v"(t.cv[3]) :: "memory");
    else if constexpr (a == 200) asm volatile("s_nop 0");
    else asm volatile("s_waitcnt lgkmcnt(%0)" :: "i"(a >= 100 ? a - 100 : 0) : "memory");
  } else if constexpr (kind == 31) {
    if constexpr (a == 0) asm volatile("s_waitcnt vmcnt(%0) lgkmcnt(6)\n\ts_barrier" :: "i"(vq_wait(VK_S, 8, PJ + J)) : "memory");
    else if constexpr (a == 1) asm volatile("s_waitcnt vmcnt(%0)\n\ts_barrier" :: "i"(vq_wait(VK_KV, 0, PJ + J)) : "memory");
    else asm volatile("s_barrier" ::: "memory");
  } else if constexpr (kind == 32) { vq_issue<PJ + J>(vm); GAP(); }
  else if constexpr (kind == 34) real_op();
  else if constexpr (kind == 33) {
    if constexpr (a == 0) { c.pm0[0] = c.m0k + 0x4000u; c.psf[0] = (KTILEB * 3u + c.sofk) & KMASK; asm volatile("" : "+s"(c.pm0[0]), "+s"(c.psf[0])); }
    else { c.pm0[1] = c.m0v; c.psf[1] = (KTILEB * 2u + c.sofk) & KMASK; asm volatile("" : "+s"(c.pm0[1]), "+s"(c.psf[1])); }
  }
}
template <int J, typename FV, int... Is> DI void qj_gap(QP& p, W4C& c, QFT& t, FV& vm, const unsigned csa, iseq<Is...>) { (qj_op<J, JT[JS[J] + Is]>(p, c, t, vm, csa), ...); GAP(); }
template <typename FV, int... Js> DI void qj_gaps(QP& p, W4C& c, QFT& t, FV& vm, const unsigned csa, iseq<Js...>) { (qj_gap<Js>(p, c, t, vm, csa, make_iseq<JS[Js + 1] - JS[Js]>{}), ...); }

constexpr unsigned XRS_ = DM * 2u;
constexpr int op_vr(int f) { return f < 4 ? f : f < 9 ? f : f < 13 ? f - 9 : f - 4; }
constexpr int op_ar(int f) {
  const int n = f - 35;
  return n < 0 ? 84 + 4 * (f - 26) : n < 4 ? 128 + 4 * n : n < 12 ? 4 * (n - 4) : n < 16 ? 144 + 4 * (n - 12) : n < 24 ? 32 + 4 * (n - 16) : n < 32 ? 160 + 4 * (n - 24) : 64 + 4 * (n - 32);
}
constexpr int OP_LA = 4, OP_SF = 22, OP_SP = 25, OP_SD = 29, OP_FEW = 8, OP_NR = 72, OP_RING = 5, OP_GS = 1, OP_P4W = 16;
constexpr short OPU[] = {1,2,3,0,9,10,11,8,5,6,7,4,13,14,15,12,17,18,19,16,21,22,23,20,25,29,26,30,27,31,24,28,5,6,7,11,9,10,13,14,15,3,1,2,17,18,19,23,21,22,25,29,26,30,27,31,0,8,12,16,20,24,28,4,};
constexpr short OPW[] = {12,13,13,13,12,13,13,13,12,12,12,12,12,12,12,12,12,14,16,17,15,14,12,13,13,13,13,13,13,9,9,5,};
constexpr short TT[] = {0,0,22000,0,0,21000,8000,0,0,0,0,21001,8001,0,0,0,0,0,21002,8002,0,0,0,0,21003,8003,0,0,0,11000,0,11001,0,21000,8000,0,11002,0,11003,0,11004,0,21001,8001,0,11005,0,0,11006,0,11007,0,21002,8002,0,
  11008,0,11009,0,11010,0,21003,8003,0,11011,0,0,13004,13005,0,11012,0,21000,8000,0,11013,0,13006,13007,0,11014,0,21001,8001,0,11015,0,0,11016,0,11017,0,21002,8002,0,11018,0,11019,0,11020,0,21003,8003,0,11021,0,0,11022,
  0,11023,0,21000,8000,0,11024,0,11025,0,11026,0,21001,8001,0,11027,0,0,11028,0,11029,0,21002,8002,0,11030,0,11031,0,11032,0,21003,8003,0,11033,15000,0,23000,0,24000,0,22000,0,21000,8000,0,11034,0,11035,0,11036,0,21001,
  8001,0,11037,0,0,11038,0,11039,0,21002,8002,0,11040,0,11041,0,11042,0,21003,8003,0,11043,0,11044,0,11045,0,11046,0,21000,8000,0,11047,0,11048,0,11049,0,21001,8001,0,11050,0,0,11051,0,11052,0,21002,8002,0,11053,0,11054,
  0,11055,0,21003,8003,0,11056,0,0,11057,0,11058,0,21000,8000,0,11059,0,11060,0,11061,0,21001,8001,0,11062,0,0,11063,0,11064,0,21002,8002,0,11065,0,11066,0,11067,0,21003,8003,0,11068,0,0,11069,0,11070,0,21000,8000,0,
  11071,0,11072,0,11073,0,21001,8001,0,11074,0,0,11075,0,11076,0,21002,8002,0,11077,0,11078,0,11079,0,21003,8003,0,11080,0,0,0,0,21000,8000,0,0,0,0,21001,8001,0,0,0,0,0,21002,8002,0,0,0,0,21003,8003,0,0,0,0,0,21000,8000,
  0,0,0,0,21001,8001,0,0,0,0,0,21002,8002,0,0,0,0,21003,8003,0,0,0,25000,0,25001,0,21000,8000,0,25002,0,25003,0,25004,0,21001,8001,0,25005,0,0,25006,0,25007,0,21002,8002,0,25008,0,0,0,21003,8003,0,0,0,0,0,21000,8000,0,0,
  0,0,21001,8001,0,0,0,0,0,21002,8002,0,0,0,0,21003,8003,0,0,0,0,0,21000,8000,0,0,0,0,21001,8001,0,1000,0,0,0,1001,0,21002,8002,0,0,1004,0,0,21003,8003,0,1002,0,0,0,1003,0,21000,8000,0,0,0,0,21001,8001,0,1005,0,0,0,1006,
  0,21002,8002,0,0,1009,0,0,21003,8003,0,1007,0,0,0,1008,0,21000,8000,0,0,0,0,21001,8001,0,1010,0,0,0,1011,0,21002,8002,0,0,1014,0,0,21003,8003,0,1012,0,0,0,31000,1013,0,21000,8000,0,3000,0,31000,4000,0,31000,4105,0,
  21001,8001,0,4210,0,4301,0,31000,4406,0,0,21002,8002,0,5004,6000,6001,0,31000,6002,6003,0,5103,4011,6100,0,21003,8003,0,6101,6102,0,6103,0,5203,4104,6200,0,6201,6202,0,21000,8000,0,5303,6203,6304,0,7000,4209,6305,0,
  6306,6307,0,21001,8001,0,5403,4314,6404,0,6405,0,6406,6407,0,5003,4402,6004,0,21002,8002,0,6005,6006,0,5103,6007,6116,0,7001,4007,6117,0,21003,8003,0,6118,6119,0,4112,0,5204,6216,6217,0,6218,6219,0,21000,8000,0,5303,
  4203,6316,0,6317,6318,0,5403,6319,6408,0,21001,8001,0,7004,4308,6409,0,6410,0,5003,6411,6008,0,4413,6009,0,21002,8002,0,6010,6011,0,5103,6108,6109,0,6110,6111,0,21003,8003,0,5202,7002,6212,6213,0,6214,0,13000,13001,0,
  5303,6215,6312,0,21000,8000,0,6313,6314,0,13002,13003,0,5404,6315,6412,0,21001,8001,0,6413,6414,0,6415,0,7003,0,0,21002,8002,0,0,0,0,21003,8003,0,0,0,0,0,21000,8000,0,0,0,0,21001,8001,0,0,0,0,0,21002,8002,0,0,0,0,
  21003,8003,0,15000,0,0,0,0,21000,8000,0,0,0,0,21001,8001,0,0,0,0,0,21002,8002,0,0,0,0,21003,8003,0,0,0,0,0,21000,8000,0,0,0,0,21001,8001,0,0,0,0,0,21002,8002,0,0,0,0,21003,8003,0,0,14000,0,20000,0,0,21000,8000,0,0,0,0,
  21001,8001,0,0,0,0,0,21002,8002,0,0,0,0,21003,8003,0,0,0,25015,0,25016,0,21000,8000,0,0,0,0,21001,8001,0,0,0,0,0,21002,8002,0,0,0,0,21003,8003,0,0,0,0,0,21000,8000,0,0,0,0,21001,8001,0,0,0,0,0,21002,8002,0,0,0,0,21003,
  8003,0,0,0,0,0,21000,8000,0,0,0,0,21001,8001,0,1100,0,0,0,1101,0,21002,8002,0,0,1104,0,0,21003,8003,0,1102,0,0,0,1103,0,21000,8000,0,0,0,0,21001,8001,0,1105,0,0,0,1106,0,21002,8002,0,0,1109,0,0,21003,8003,0,1107,0,0,0,
  1108,0,21000,8000,0,26000,0,26001,0,26002,0,21001,8001,0,1110,0,0,0,1111,0,21002,8002,0,0,1114,0,0,21003,8003,0,1112,0,0,0,31000,1113,0,0,3000,0,31000,4900,0,31000,4025,0,31000,4130,0,4901,0,4226,0,31000,4331,0,31000,
  4904,0,31000,4429,0,5006,12000,6020,6021,0,31000,6022,6023,0,5105,4034,6120,0,31000,4902,6121,0,6122,6123,0,4127,0,31000,19000,0,31000,13004,13005,0,5208,12001,6224,6225,0,19001,0,31000,13006,13007,0,31000,6226,6227,0,
  31000,19004,0,5309,4232,6324,0,4903,0,31000,19002,0,31000,6325,6326,0,5409,12004,6327,6436,0,19003,0,31000,4328,6437,0,31000,6438,6439,0,5009,4433,6036,0,6037,6038,0,6039,0,5108,12002,6128,6129,0,31000,6130,6131,0,
  5203,6228,6229,0,6230,6231,0,5301,12003,6332,6333,0,31000,6334,6335,0,5400,6432,6433,0,6434,6435,0,0,31000,16000,0,31000,16001,0,31000,16002,0,16003,0,31000,16004,0,31000,16005,0,31000,16006,0,16007,15004,0,0,31000,
  16008,0,31000,16009,0,31000,16010,0,16011,0,31000,16012,0,31000,16013,0,31000,16014,0,16015,0,0,31000,16016,0,31000,16017,0,17000,18000,18001,0,18002,18003,0,31000,18004,18005,0,31000,18006,18007,0,31000,18016,18017,0,
  18018,18019,18008,18009,18010,18011,18012,18013,18014,18015,};
constexpr short TS[] = {0,1,3,4,7,8,9,10,13,14,15,16,17,20,21,22,23,26,27,28,30,32,35,37,39,41,44,46,47,49,51,54,56,58,60,63,65,66,69,71,74,76,79,81,84,86,87,89,91,94,96,98,100,103,105,106,108,110,113,115,117,119,122,
  124,125,127,129,132,134,136,138,141,143,146,148,150,153,155,157,159,162,164,165,167,169,172,174,176,178,181,183,185,187,189,192,194,196,198,201,203,204,206,208,211,213,215,217,220,222,223,225,227,230,232,234,236,239,
  241,242,244,246,249,251,253,255,258,260,261,263,265,268,270,272,274,277,279,280,282,284,287,289,291,293,296,298,299,300,301,304,305,306,307,310,311,312,313,314,317,318,319,320,323,324,325,326,327,330,331,332,333,336,
  337,338,339,340,343,344,345,346,349,350,351,353,355,358,360,362,364,367,369,370,372,374,377,379,380,381,384,385,386,387,388,391,392,393,394,397,398,399,400,401,404,405,406,407,410,411,412,413,414,417,418,419,420,423,
  425,426,427,429,432,433,435,436,439,441,442,443,445,448,449,450,451,454,456,457,458,460,463,464,466,467,470,472,473,474,476,479,480,481,482,485,487,488,489,491,494,495,497,498,501,503,504,505,508,511,512,516,519,522,
  524,526,529,530,533,537,541,545,548,551,553,557,560,563,567,571,574,577,581,583,586,590,593,596,600,604,607,610,612,616,619,622,626,629,633,636,640,642,646,649,652,655,659,662,665,670,672,675,679,682,685,688,692,695,
  698,700,702,703,706,707,708,709,712,713,714,715,716,719,720,721,722,725,726,727,728,729,732,733,734,735,738,739,741,742,743,746,747,748,749,752,753,754,755,756,759,760,761,762,765,766,767,768,769,772,773,774,775,778,
  779,780,781,782,785,786,787,788,791,792,793,796,797,800,801,802,803,806,807,808,809,810,813,814,815,816,819,820,821,823,825,828,829,830,831,834,835,836,837,838,841,842,843,844,847,848,849,850,851,854,855,856,857,860,
  861,862,863,864,867,868,869,870,873,874,875,876,877,880,881,882,883,886,888,889,890,892,895,896,898,899,902,904,905,906,908,911,912,913,914,917,919,920,921,923,926,927,929,930,933,935,936,937,939,942,944,946,948,951,
  953,954,955,957,960,961,963,964,967,969,970,971,974,975,976,980,983,986,988,990,993,996,999,1004,1008,1012,1016,1019,1021,1024,1028,1033,1035,1039,1043,1046,1050,1052,1055,1059,1064,1066,1070,1074,1078,1081,1083,1088,
  1092,1096,1099,1104,1108,1112,1115,1116,1119,1122,1125,1127,1130,1133,1136,1138,1140,1143,1146,1149,1151,1154,1157,1160,1162,1163,1166,1169,1173,1176,1180,1184,1188,1191,1199,};
constexpr bool op_first(int u) { for (int x = u & 32; x < u; ++x) if ((OPU[x] & 3) == (OPU[u] & 3)) return false; return true; }
struct OPS {
  u32x4 fa[OP_LA + 1][4], fb[22]; f32x4 D[2][5], T[5], m[OP_RING], pv[5]; float g0, g1, t0, t1, gx[36];
  u32x4 gv[2][2]; unsigned gs[2];
  unsigned ra[3], sw[2], tg, so; __amdgpu_buffer_rsrc_t srdE, srdI, srdG;
  unsigned va, idx, sa, lb5, lb1, lb2, sn;
  u32x4 srdA, srdL; unsigned fo, sl, sf, tag, bad; unsigned long long seen;
  __amdgpu_buffer_rsrc_t srdB;
};
constexpr unsigned OB = SEQ * HQ * DH * 2;
DI void op_poll(OPS& o) { asm volatile("buffer_load_dword %0, %1, %2, %3 offen sc1" : "=v"(o.fo) : "v"(o.va), "s"(o.srdL), "s"(o.sl) : "memory"); }
DI void op_there(OPS& o) {
  unsigned n;
  asm volatile("s_and_b32 %0, %1, 15\n\ts_cmp_eq_u32 %0, 15\n\ts_cselect_b32 %0, %2, 0" : "=&s"(n) : "s"((unsigned)o.seen), "i"(OB) : "scc");
  o.srdA[2] = n;
}
template <int F> DI void op_reqB(OPS& o) {
  asm volatile("buffer_load_dwordx4 a[%c3:%c4], %0, %1, %2 offen offset:%c5 sc1" :: "v"(o.va), "s"(o.srdB), "s"(1024u * (F & ~3)), "i"(op_ar(F)), "i"(op_ar(F) + 3), "i"(1024 * (F & 3)) : "memory");
}
constexpr unsigned op_off(int st, int i) { const int un = OPU[2 * st + (i >> 1)]; return (131072u * (un & 3) + 1024u * (2 * (un >> 2) + (i & 1))) ^ (st >> 4) * (524288u + 1024u); }
template <int ST, int I> DI void op_offA(OPS& o) {
  GAP(); o.so = o.sa ^ op_off(ST, I); asm volatile("" :: "s"(o.so)); GAP();
}
template <int ST, int I> DI void op_reqA1(OPS& o) {
  asm volatile("buffer_load_dwordx4 %0, %1, %2, %3 offen sc1" : "=v"(o.fa[ST % (OP_LA + 1)][I]) : "v"(o.va), "s"(o.srdA), "s"(o.so) : "memory");
  if constexpr (ST < OP_LA) asm volatile("ps");
}
template <int... Qs> DI void op_reqA0(OPS& o, iseq<Qs...>) { ((op_offA<Qs / 4, Qs % 4>(o), op_reqA1<Qs / 4, Qs % 4>(o)), ...); }
#define OP_F4(s) "+v"(o.fa[(s) % (OP_LA + 1)][0]), "+v"(o.fa[(s) % (OP_LA + 1)][1]), "+v"(o.fa[(s) % (OP_LA + 1)][2]), "+v"(o.fa[(s) % (OP_LA + 1)][3])
#define OP_I4(s) "i"(op_off(s, 0)), "i"(op_off(s, 1)), "i"(op_off(s, 2)), "i"(op_off(s, 3))
template <int S0> DI void op_again(OPS& o) {
  unsigned t, n; const unsigned was = o.srdA[2];
  o.srdA[2] = OB;
  asm volatile("s_cmp_lg_u32 %24, 0\n\ts_cbranch_scc1 1f\n\ts_mov_b32 %18, 0x100000\n\t"
               "2:\n\tv_cmp_eq_u32_e32 vcc, %25, %16\n\ts_and_b32 %17, vcc_lo, %26\n\ts_cmp_eq_u32 %17, 15\n\ts_cbranch_scc1 3f\n\t"
               "s_sleep 1\n\tbuffer_load_dword %16, %19, %21, %22 offen sc1\n\ts_sub_u32 %18, %18, 1\n\ts_waitcnt vmcnt(0)\n\ts_cbranch_scc0 2b\n\t3:\n\t"
               "s_xor_b32 %17, %23, %c27\n\tbuffer_load_dwordx4 %0, %19, %20, %17 offen sc1\n\t" "s_xor_b32 %17, %23, %c28\n\tbuffer_load_dwordx4 %1, %19, %20, %17 offen sc1\n\t"
               "s_xor_b32 %17, %23, %c29\n\tbuffer_load_dwordx4 %2, %19, %20, %17 offen sc1\n\t" "s_xor_b32 %17, %23, %c30\n\tbuffer_load_dwordx4 %3, %19, %20, %17 offen sc1\n\t"
               "s_xor_b32 %17, %23, %c31\n\tbuffer_load_dwordx4 %4, %19, %20, %17 offen sc1\n\t" "s_xor_b32 %17, %23, %c32\n\tbuffer_load_dwordx4 %5, %19, %20, %17 offen sc1\n\t"
               "s_xor_b32 %17, %23, %c33\n\tbuffer_load_dwordx4 %6, %19, %20, %17 offen sc1\n\t" "s_xor_b32 %17, %23, %c34\n\tbuffer_load_dwordx4 %7, %19, %20, %17 offen sc1\n\t"
               "s_xor_b32 %17, %23, %c35\n\tbuffer_load_dwordx4 %8, %19, %20, %17 offen sc1\n\t" "s_xor_b32 %17, %23, %c36\n\tbuffer_load_dwordx4 %9, %19, %20, %17 offen sc1\n\t"
               "s_xor_b32 %17, %23, %c37\n\tbuffer_load_dwordx4 %10, %19, %20, %17 offen sc1\n\t" "s_xor_b32 %17, %23, %c38\n\tbuffer_load_dwordx4 %11, %19, %20, %17 offen sc1\n\t"
               "s_xor_b32 %17, %23, %c39\n\tbuffer_load_dwordx4 %12, %19, %20, %17 offen sc1\n\t" "s_xor_b32 %17, %23, %c40\n\tbuffer_load_dwordx4 %13, %19, %20, %17 offen sc1\n\t"
               "s_xor_b32 %17, %23, %c41\n\tbuffer_load_dwordx4 %14, %19, %20, %17 offen sc1\n\t" "s_xor_b32 %17, %23, %c42\n\tbuffer_load_dwordx4 %15, %19, %20, %17 offen sc1\n\t"
               "1:"
               : OP_F4(S0), OP_F4(S0 + 1), OP_F4(S0 + 2), OP_F4(S0 + 3), "+v"(o.fo), "=&s"(t), "=&s"(n)
               : "v"(o.va), "s"(o.srdA), "s"(o.srdL), "s"(o.sl), "s"(o.sa), "s"(was), "s"(o.tag), "i"(15), OP_I4(S0), OP_I4(S0 + 1), OP_I4(S0 + 2), OP_I4(S0 + 3) : "vcc", "scc", "memory");
}
template <int F> DI void op_ldsB(OPS& o) {
  if constexpr (F < 26) asm volatile("ds_read_b128 %0, %1 offset:%2" : "=v"(o.fb[op_vr(F)]) : "v"(F < 23 ? o.lb5 : o.lb1), "i"(1024 * (F < 23 ? F : F - 23)) : "memory");
  else                  asm volatile("ds_read_b128 a[%c1:%c2], %0 offset:%c3" :: "v"(F < 27 ? o.lb1 : o.lb2), "i"(op_ar(F)), "i"(op_ar(F) + 3), "i"(1024 * (F < 27 ? F - 23 : F - 27)) : "memory");
}
template <int F0, int... Is> DI void op_ldsBs(OPS& o, iseq<Is...>) { (op_ldsB<F0 + Is>(o), ...); }
constexpr int op_ac(int pass, int i, int j) { return j < 4 ? 176 + 16 * i + 4 * j : pass == 0 ? 236 + 4 * i : i == 1 ? 252 : 112 + 4 * i; }
template <int ST, int Q> DI void op_mfma(OPS& o) {
  constexpr int A = ST % (OP_LA + 1), P = ST >> 4;
  if constexpr (Q == 0 || Q == 9) {
    constexpr int u = Q == 9, un = OPU[2 * ST + u], i = un & 3, f = 9 * (un >> 2) + 8, ac = op_ac(P, i, 4), ar = op_ar(f);
    const u32x8 B = __builtin_shufflevector(o.fa[A][2 * u], o.fa[A][2 * u + 1], 0, 1, 2, 3, 4, 5, 6, 7);
    if constexpr (i == 0 && f < 26) asm volatile("sm64 %0, %1, %2, %3" : "+v"(o.D[P][4]) : "v"(o.fb[op_vr(f) % 22]), "v"(B), "v"(o.idx));
    else if constexpr (i == 0)      asm volatile("sm64 %0, a[%c3:%c4], %1, %2" : "+v"(o.D[P][4]) : "v"(B), "v"(o.idx), "i"(ar), "i"(ar + 3));
    else if constexpr (f < 26)      asm volatile("sm64 a[%c3:%c4], %0, %1, %2" :: "v"(o.fb[op_vr(f) % 22]), "v"(B), "v"(o.idx), "i"(ac), "i"(ac + 3));
    else                            asm volatile("sm64 a[%c2:%c3], a[%c4:%c5], %0, %1" :: "v"(B), "v"(o.idx), "i"(ac), "i"(ac + 3), "i"(ar), "i"(ar + 3));
  } else {
    constexpr int d = Q - 1 - (Q > 9), J = d >> 2, u = (d >> 1) & 1, kk = d & 1, un = OPU[2 * ST + u], i = un & 3, f = 9 * (un >> 2) + 4 * kk + J, ac = op_ac(P, i, J), ar = op_ar(f);
    constexpr bool z = kk == 0 && op_first(2 * ST + u);
    static_assert(!z || f < 26, "");
    const u32x4& B = o.fa[A][2 * u + kk];
    if constexpr (i == 0 && z)           asm volatile("mf16 %0, %1, %2, 0" : "=&v"(o.D[P][J]) : "v"(o.fb[op_vr(f) % 22]), "v"(B));
    else if constexpr (i == 0 && f < 26) asm volatile("mf16 %0, %1, %2, %0" : "+v"(o.D[P][J]) : "v"(o.fb[op_vr(f) % 22]), "v"(B));
    else if constexpr (i == 0)           asm volatile("mf16 %0, a[%c2:%c3], %1, %0" : "+v"(o.D[P][J]) : "v"(B), "i"(ar), "i"(ar + 3));
    else if constexpr (z)                asm volatile("mf16 a[%c2:%c3], %0, %1, 0" :: "v"(o.fb[op_vr(f) % 22]), "v"(B), "i"(ac), "i"(ac + 3));
    else if constexpr (f < 26)           asm volatile("mf16 a[%c2:%c3], %0, %1, a[%c2:%c3]" :: "v"(o.fb[op_vr(f) % 22]), "v"(B), "i"(ac), "i"(ac + 3));
    else                                 asm volatile("mf16 a[%c1:%c2], a[%c3:%c4], %0, a[%c1:%c2]" :: "v"(B), "i"(ac), "i"(ac + 3), "i"(ar), "i"(ar + 3));
  }
}
template <int I> DI unsigned op_ax(const unsigned va) { const unsigned r = ((va >> 4) & 15u) * XRS_; return I ? r + 128u + ((va >> 5) & 8u) + ((va >> 7) & 4u) : r + ((va >> 4) & 48u); }
template <int X, int WD> DI unsigned op_word(const OPS& o) { if constexpr (WD < 8) return o.gv[X][WD >> 2][WD & 3]; else return o.gs[X]; }
template <int U> DI void op_gate(OPS& o) {
  constexpr int wd = U / 9, pt = U % 9;
  if constexpr (pt == 0)      asm volatile("v_lshlrev_b32_e32 %0, 16, %2\n\tv_and_b32_e32 %1, 0xffff0000, %2" : "=&v"(o.g0), "=&v"(o.g1) : "v"(op_word<0, wd>(o)));
  else if constexpr (pt == 1) asm volatile("v_mul_f32_e32 %0, 0xbfb8aa3b, %2\n\tv_mul_f32_e32 %1, 0xbfb8aa3b, %3" : "=&v"(o.t0), "=&v"(o.t1) : "v"(o.g0), "v"(o.g1));
  else if constexpr (pt == 2) asm volatile("v_exp_f32_e32 %0, %0" : "+v"(o.t0));
  else if constexpr (pt == 3) asm volatile("v_exp_f32_e32 %0, %0" : "+v"(o.t1));
  else if constexpr (pt == 4) asm volatile("v_add_f32_e32 %0, 1.0, %0\n\tv_add_f32_e32 %1, 1.0, %1" : "+v"(o.t0), "+v"(o.t1));
  else if constexpr (pt == 5) asm volatile("v_rcp_f32_e32 %0, %0" : "+v"(o.t0));
  else if constexpr (pt == 6) asm volatile("v_rcp_f32_e32 %0, %0" : "+v"(o.t1));
  else if constexpr (pt == 7) asm volatile("v_mul_f32_e32 %0, %0, %2\n\tv_mul_f32_e32 %1, %1, %3" : "+v"(o.t0), "+v"(o.t1) : "v"(o.g0), "v"(o.g1));
  else { unsigned r; asm volatile("v_cvt_pk_bf16_f32 %0, %1, %2" : "=v"(r) : "v"(o.t0), "v"(o.t1)); if constexpr (wd < 8) o.gv[0][wd >> 2][wd & 3] = r; else o.gs[0] = r; }
}
template <int ST, int Q, int CODE, typename H> DI void op_t(OPS& o, H& hook) {
  constexpr int kind = CODE / 1000, a = CODE % 1000;
  if constexpr (kind == 0) op_mfma<ST, Q>(o);
  else if constexpr (kind == 1) {
    constexpr int n = a % 100, h = a / 200, ac = op_ac((a / 100) & 1, 1 + n / 5, n % 5), off = n < 10 ? 13312 + 1024 * n : 1024 * (n - 10);
    if constexpr (h == 0) asm volatile("ds_write_b128 %0, a[%c1:%c2] offset:%c3" :: "v"(n < 10 ? o.lb5 : o.lb2), "i"(ac), "i"(ac + 3), "i"(off) : "memory");
    else                  asm volatile("ds_write_b64 %0, a[%c1:%c2] offset:%c3" :: "v"(n < 10 ? o.lb5 : o.lb2), "i"(ac + 2 * h - 2), "i"(ac + 2 * h - 1), "i"(off + 8 * h - 8) : "memory");
  } else if constexpr (kind == 2) asm volatile("s_waitcnt lgkmcnt(%0)" :: "i"(a) : "memory");
  else if constexpr (kind == 3) asm volatile("s_waitcnt lgkmcnt(0)\n\ts_barrier" ::: "memory");
  else if constexpr (kind == 4) {
    constexpr int sl = a / 100, n = a % 20;
    asm volatile("ds_read_b128 %0, %1 offset:%2" : "=v"(sl == 9 ? o.T[n % 5] : o.m[sl % OP_RING]) : "v"(o.ra[n / 5]), "i"(n < 10 ? 13312 + 1024 * n : 1024 * (n - 10)) : "memory");
  } else if constexpr (kind == 5) asm volatile("s_waitcnt lgkmcnt(%1)" : "+v"(o.m[a / 100]) : "i"(a % 100) : "memory");
  else if constexpr (kind == 6 || kind == 18) {
    constexpr int j = (a % 20) >> 2, e = a & 3;
    f32x4& s = kind == 6 && a % 100 < 20 ? o.D[0][j] : o.T[j];
    float v = s[e]; add_v(v, kind == 6 ? o.m[a / 100][e] : o.pv[j][e]); s[e] = v;
  } else if constexpr (kind == 7)
    asm volatile("buffer_store_dwordx4 %0, %1, %2, %3 offen offset:%c4 sc1" :: "v"(o.D[0][a]), "v"(o.va), "s"(o.srdE), "s"(o.sw[a >> 2]), "i"(1024 * (a & 3)) : "memory");
  else if constexpr (kind == 8) op_reqA1<ST + OP_LA, a>(o);
  else if constexpr (kind == 9) op_reqB<a>(o);
  else if constexpr (kind == 19) asm volatile("buffer_load_dwordx4 %0, %1, %2, %3 offen offset:%c4 sc1" : "=v"(o.pv[a]) : "v"(o.va), "s"(o.srdI), "s"(o.sw[a >> 2]), "i"(1024 * (a & 3)) : "memory");
  else if constexpr (kind == 20) asm volatile("buffer_store_dword %0, %1, %2, 0 offen sc1" :: "v"(o.tg), "v"(o.va), "s"(o.srdG) : "memory");
  else if constexpr (kind == 25) { GAP(); hook(IC(a)); GAP(); }
  else if constexpr (kind == 26) {
    if constexpr (a < 2) asm volatile("ds_read_b128 %0, %1 offset:%2" : "=v"(o.gv[1][a]) : "v"(o.lb5), "i"(4096 + 1024 * a) : "memory");
    else                 asm volatile("ds_read_b32 %0, %1 offset:6144" : "=v"(o.gs[1]) : "v"(o.lb5) : "memory");
  } else if constexpr (kind == 22) op_poll(o);
  else if constexpr (kind == 23) asm volatile("v_cmp_eq_u32_e64 %0, %1, %2" : "=s"(o.seen) : "s"(o.tag), "v"(o.fo));
  else if constexpr (kind == 24) op_there(o);
  else if constexpr (kind == 21) op_offA<ST + OP_LA, a>(o);
  else if constexpr (kind == 31) real_op();
  else if constexpr (kind == 11) op_gate<a>(o);
  else if constexpr (kind == 12) asm volatile("" : "+v"(o.T[a]));
  else if constexpr (kind == 13) op_ldsB<9 * (a >> 2) + (a & 3)>(o);
  else if constexpr (kind == 14) asm volatile("s_barrier" ::: "memory");
  else if constexpr (kind == 15) asm volatile("s_waitcnt lgkmcnt(%4)" : "+v"(o.fb[0]), "+v"(o.fb[1]), "+v"(o.fb[2]), "+v"(o.fb[3]) : "i"(a) : "memory");
  else if constexpr (kind == 16) asm volatile("v_lshlrev_b32_e32 %0, 16, %2\n\tv_and_b32_e32 %1, 0xffff0000, %2" : "=&v"(o.gx[2 * a]), "=v"(o.gx[2 * a + 1]) : "v"(op_word<a / 9, a % 9>(o)));
  else if constexpr (kind == 17) asm volatile("s_waitcnt vmcnt(0)" : "+v"(o.pv[0]), "+v"(o.pv[1]), "+v"(o.pv[2]), "+v"(o.pv[3]), "+v"(o.pv[4]) :: "memory");
}
template <int ST, int Q, typename H, int... Is> DI void op_ts(OPS& o, H& hook, iseq<Is...>) { (op_t<ST, Q, TT[TS[18 * ST + Q] + Is]>(o, hook), ...); }
template <int ST, typename H, int... Qs> DI void op_qs(OPS& o, H& hook, iseq<Qs...>) { (op_ts<ST, Qs>(o, hook, make_iseq<TS[18 * ST + Qs + 1] - TS[18 * ST + Qs]>{}), ...); }

template <typename F, int... Is> DI void for_seq(F& f, iseq<Is...>) { (f(std::integral_constant<int, Is>{}), ...); }
DI void wait_flag(const unsigned* p, const unsigned tag) {
#pragma nounroll
  for (unsigned spin = 0; spin < (1u << 20); ++spin) {
    unsigned v;
    asm volatile("global_load_dword %0, %1, off sc1\n\ts_waitcnt vmcnt(0)" : "=v"(v) : "v"(p) : "memory");
    if (__all(v == tag)) break;
    __builtin_amdgcn_s_sleep(1);
  }
}
DI unsigned op_k0(const unsigned blk, const unsigned w) { return ((blk >> 3) & 7u) * 128u + (~blk & 4u) * 16u + 16u * w; }

constexpr int NFIN = 200, NBL = 45, FVW = 44, FVS = 0, FNR = 32;
constexpr short FT[] = {5064,31000,23000,23001,23002,5001,31000,23003,23004,23005,5002,31000,23006,23007,23008,5003,31000,23009,23010,23011,5083,31000,23012,23013,23014,5018,31000,23015,16000,16001,5017,16008,16009,
  16016,5016,16017,16024,5096,16025,16002,16003,5033,31000,23016,23017,5034,31000,23018,23019,23020,5035,31000,23021,23022,5115,31000,25003,23023,5050,31000,23024,23025,5049,31000,25002,23026,5048,31000,23027,23028,
  20015,1000,31000,25001,23029,22000,1001,31000,23030,23031,20031,1003,31000,25000,16010,1002,16011,16018,20047,1004,16019,16026,16027,1005,13000,20063,1007,13001,13002,1006,15000,16004,29010,20076,1008,27000,13003,1009,
  31000,26000,20088,1011,15001,13016,1010,31000,26001,20100,1012,13017,13018,1013,31000,26002,20112,1015,15008,16005,1014,31000,26003,27001,3000,13019,1016,31000,26004,15009,1017,13032,13033,1019,31000,26005,1018,15016,
  16012,1020,31000,26006,24000,1021,31000,24001,24002,24003,1023,31000,26007,1022,31000,24004,24005,24006,1024,31000,24007,24008,1025,31000,24009,24010,24011,1027,13034,1026,13035,13048,1028,15017,16013,1029,13049,13050,
  1031,15024,16020,1030,13051,16021,3001,15025,14057,24012,4000,31000,24013,24014,24015,30012,2000,31000,24016,24017,24018,24019,2001,31000,12000,16028,30028,2003,31000,24020,24021,24022,24023,2002,31000,24024,24025,
  24026,24027,30047,2004,31000,24028,24029,24030,24031,2005,31000,17000,17001,17002,17003,30063,2007,31000,25004,17004,17005,2006,31000,17006,17007,18000,18001,30076,2008,31000,25005,18002,18003,2009,31000,18004,18005,
  18006,18007,30088,2011,19000,19001,19002,19003,2010,31000,28000,17008,17009,30100,2012,31000,17010,17011,17012,17013,2013,31000,25006,17014,17015,30112,2015,31000,18008,18009,18010,18011,2014,31000,18012,18013,18014,
  18015,5068,31000,25007,19004,19005,5005,19006,19007,14058,5006,31000,28001,17016,17017,5007,31000,17018,17019,17020,5087,31000,25008,17021,17022,5022,31000,17023,18016,18017,5021,31000,18018,18019,18020,18021,5020,
  31000,18022,18023,19008,5100,19009,19010,19011,14059,5037,31000,28002,17024,5038,31000,17025,17026,17027,5039,31000,17028,17029,17030,5119,31000,25015,17031,18024,5054,31000,18025,18026,18027,5053,31000,18028,18029,
  18030,18031,5052,31000,25014,19012,1032,19013,19014,19015,16029,13004,13005,22001,1033,31000,28003,15002,1035,13006,13007,1034,31000,25013,15003,1036,13020,13021,1037,15010,13022,1039,31000,25009,13023,1038,15011,
  13036,1040,13037,13038,1041,31000,25012,15018,1043,13039,13052,1042,15019,13053,1044,31000,25010,1045,15026,13054,1047,13055,1046,15027,14012,3002,31000,25011,4001,14013,2016,16006,14014,2017,31000,12001,2019,14015,
  14028,2018,31000,17032,17033,17034,17035,2020,31000,25016,16007,14029,2021,31000,17036,17037,17038,17039,2023,31000,18032,18033,18034,18035,2022,31000,25017,18036,18037,2024,31000,18038,18039,19016,19017,2025,19018,
  19019,16014,14030,2027,31000,28004,17040,17041,2026,31000,17042,17043,17044,17045,2028,31000,25018,17046,17047,2029,31000,18040,18041,18042,18043,2031,31000,18044,18045,18046,18047,2030,31000,25019,19020,19021,5072,
  19022,19023,14031,5009,31000,28005,17048,17049,5010,31000,17050,17051,17052,17053,5011,31000,25020,17054,17055,5091,31000,18048,18049,18050,18051,5026,31000,18052,18053,18054,18055,5025,19024,19025,19026,19027,5024,
  31000,28006,17056,17057,5104,31000,17058,17059,17060,17061,5041,31000,17062,17063,18056,18057,5042,31000,18058,18059,18060,18061,5043,31000,18062,18063,19028,19029,5123,31000,25027,19030,5058,19031,13008,13009,5057,
  31000,28007,15004,5056,13010,13011,5076,31000,25026,15005,22002,5013,13024,13025,5014,15012,13026,5015,31000,25025,13027,5095,15013,13040,5030,13041,13042,5029,31000,25021,15020,5028,13043,13056,5108,15021,13057,5045,
  31000,25024,15028,5046,13058,13059,5047,15029,16015,14044,5127,31000,25022,14045,5062,16022,14046,14047,5061,16023,14060,14061,14062,16030,14063,5060,31000,25023,16031,13012,4002,13013,13014,22003,2032,15006,13015,
  13028,2033,31000,12002,15007,2035,31000,25031,13029,2034,31000,17064,17065,17066,17067,2036,15014,13030,13031,2037,31000,25030,17068,17069,2039,31000,17070,17071,18064,18065,2038,31000,18066,18067,18068,18069,2040,
  31000,25029,18070,18071,2041,19032,19033,19034,19035,2043,31000,28008,17072,17073,2042,31000,17074,17075,17076,17077,2044,31000,25028,17078,17079,2045,31000,18072,18073,18074,18075,2047,31000,18076,18077,18078,18079,
  2046,19036,19037,19038,19039,1048,31000,28009,17080,17081,1049,31000,17082,17083,17084,17085,1051,31000,17086,17087,18080,18081,1050,31000,18082,18083,18084,18085,1052,31000,18086,18087,19040,19041,1053,19042,19043,
  15015,13044,1055,31000,28010,17088,17089,1054,31000,17090,17091,17092,17093,1056,31000,17094,17095,18088,18089,1057,31000,18090,18091,18092,18093,1059,31000,18094,18095,19044,19045,1058,19046,19047,13045,1060,31000,
  28011,15022,13046,1061,13047,13060,1063,15023,13061,13062,1062,15030,13063,3003,15031,30200,30200,4003,31000,2048,31000,2049,31000,12003,2051,31000,2050,31000,17096,17097,17098,17099,2052,31000,2053,31000,17100,17101,
  17102,17103,18096,2055,31000,18097,18098,18099,18100,18101,2054,31000,18102,18103,19048,19049,19050,2056,19051,2057,31000,28012,17104,17105,17106,17107,2059,31000,17108,17109,17110,17111,18104,18105,2058,31000,18106,
  18107,18108,18109,18110,18111,2060,19052,19053,19054,19055,2061,31000,28013,17112,17113,17114,17115,2063,31000,17116,17117,17118,17119,18112,18113,2062,31000,18114,18115,18116,18117,18118,18119,19056,19057,19058,19059,
  28014,17120,17121,17122,17123,17124,17125,17126,17127,18120,18121,18122,18123,18124,18125,18126,18127,19060,19061,19062,19063,28015,};
constexpr short FS[] = {0,5,10,15,20,25,30,34,37,41,45,50,54,58,62,66,70,76,80,85,88,93,95,99,102,107,110,114,117,121,124,128,132,134,138,141,144,147,151,156,159,164,168,173,175,178,181,184,187,190,194,199,206,210,217,
  223,230,236,242,248,254,260,266,271,278,283,290,296,301,305,310,315,320,325,331,336,341,345,350,355,360,365,371,375,383,387,390,394,397,400,404,407,410,414,417,420,423,426,428,431,434,436,439,442,445,451,456,462,468,
  473,479,484,489,495,500,506,512,517,521,526,532,537,543,549,554,559,565,571,577,583,587,591,595,598,603,606,609,613,616,619,623,626,629,633,636,640,644,648,655,660,664,668,672,676,682,686,691,697,703,708,713,718,724,
  729,735,741,746,751,757,763,769,775,780,785,791,797,803,809,813,818,821,825,828,832,834,836,839,841,847,849,856,863,870,872,879,887,895,900,907,915,949,};
struct OST { u32x4 w, P[4][2]; float o[8], il[4], ls[4]; unsigned vo, so[4]; u32x4 srd; bf16x4 lo[8][2], up[8][2]; };
template <int U> DI void o_store(const OST& t) {
  asm volatile("buffer_store_dwordx4 %0, %1, %2, %3 offen sc1" :: "v"(t.w), "v"(t.vo), "s"(t.srd), "s"(t.so[U & 3] + 131072u * (U >> 2)) : "memory");
}
template <int CODE> DI void fin_op(W4C& c, OST& t, OPS& o) {
  constexpr int kind = CODE / 1000, a = CODE % 1000;
  if constexpr (kind == 1) pv16<AO_((a >> 1) & 7, a >> 4), false>(__builtin_shufflevector(c.vlo[(a >> 1) & 7][a & 1], c.vup[(a >> 1) & 7][a & 1], 0, 1, 2, 3, 4, 5, 6, 7), c.Pk[a >> 4][a & 1]);
  else if constexpr (kind == 2) pv16<AO_((a >> 1) & 7, a >> 4), false>(__builtin_shufflevector(t.lo[(a >> 1) & 7][a & 1], t.up[(a >> 1) & 7][a & 1], 0, 1, 2, 3, 4, 5, 6, 7), t.P[a >> 4][a & 1]);
  else if constexpr (kind == 3) sum64(c, a, c.Pk[a]);
  else if constexpr (kind == 4) sum64(c, a, t.P[a]);
  else if constexpr (kind == 5) { constexpr int kb = (a >> 4) & 3, qb = (a >> 2) & 3; qk16<kb, qb, (a & 3), (a >> 6)>(c.S[1][kb][qb], c.NEGM[qb]); }
  else if constexpr (kind == 14) exp_e<0, a>(c);
  else if constexpr (kind == 16) pack_e<0, a>(c);
  else if constexpr (kind == 22) asm volatile("" :: "v"(c.NEGM[a]));
  else if constexpr (kind == 20) { constexpr int d = a >> 4; asm volatile("s_waitcnt lgkmcnt(%4)" : "+v"(c.vlo[d][0]), "+v"(c.vlo[d][1]), "+v"(c.vup[d][0]), "+v"(c.vup[d][1]) : "i"(a & 15) : "memory"); }
  else if constexpr (kind == 23) tr_read_<a, 0>(c);
  else if constexpr (kind == 29) asm volatile("s_waitcnt vmcnt(%0) lgkmcnt(0)\n\ts_barrier" :: "i"(a) : "memory");
  else if constexpr (kind == 12) asm volatile("v_add_f32_e32 %0, %2, %3\n\tv_rcp_f32_e32 %1, %0" : "=&v"(t.ls[a]), "=v"(t.il[a]) : "v"(c.L[a][0]), "v"(c.L[a][1]), "v"(c.L[a]));
  else if constexpr (kind == 13) exp_e<1, a>(c);
  else if constexpr (kind == 15) pack_to<1, a>(c, t.P);
  else if constexpr (kind == 17) t.o[a & 7] = acc_read<a>();
  else if constexpr (kind == 18) {
    asm volatile("v_mul_f32_e32 %0, %0, %1" : "+v"(t.o[a & 7]) : "v"(t.il[a >> 5]));
    if constexpr (a >= 8 && (a & 7) == 0) asm volatile("" :: "v"(t.w));
  } else if constexpr (kind == 19) { GAP(); const unsigned w = cvtpk(t.o[2 * (a & 3)], t.o[2 * (a & 3) + 1]); asm volatile("" :: "v"(w)); t.w[a & 3] = w; GAP(); }
  else if constexpr (kind == 24) tr_rd<a, 0x4000>(c, t.lo, t.up);
  else if constexpr (kind == 25) op_reqB<35 + a>(o);
  else if constexpr (kind == 26) asm volatile("buffer_load_dwordx4 %0, %1, %2 offen offset:%c3 sc1 lds" :: "v"(c.pfv), "s"(c.srdP), "s"(c.pfo + 4096u * (a >> 2)), "i"(1024 * (a & 3)) : "memory");
  else if constexpr (kind == 27) asm volatile("s_mov_b32 m0, %0" :: "s"(c.pfm0 + 4096u * a) : "m0", "memory");
  else if constexpr (kind == 28) o_store<a>(t);
  else if constexpr (kind == 31) real_op();
  else if constexpr (a == 200) asm volatile("s_nop 0");
  else { constexpr int d = a >> 4; asm volatile("s_waitcnt lgkmcnt(%4)" : "+v"(t.lo[d][0]), "+v"(t.lo[d][1]), "+v"(t.up[d][0]), "+v"(t.up[d][1]) : "i"(a & 15) : "memory"); }
}
template <int G, int... Is> DI void fin_gap(W4C& c, OST& t, OPS& o, iseq<Is...>) { (fin_op<FT[FS[G] + Is]>(c, t, o), ...); GAP(); }
template <int... Gs> DI void fin_gaps(W4C& c, OST& t, OPS& o, iseq<Gs...>) { (fin_gap<Gs>(c, t, o, make_iseq<FS[Gs + 1] - FS[Gs]>{}), ...); }

template <int J, int H> DI void redo_q(const __amdgpu_buffer_rsrc_t srdW, const __amdgpu_buffer_rsrc_t srdN, const unsigned vw, const unsigned vx, const unsigned sw, const unsigned sx) {
  f32x4 q[4] = {};
#pragma nounroll
  for (unsigned ks = 0; ks < DM / 32; ++ks) {
    u32x4 w;
#pragma unroll
    for (int e = 0; e < 4; ++e) {
      const unsigned so = sw + (64u * J + 8u * H) + WRS * (32u * ks + 2u * e);
      w[e] = (unsigned)__builtin_amdgcn_raw_buffer_load_b16(srdW, vw, so, 0) | ((unsigned)__builtin_amdgcn_raw_buffer_load_b16(srdW, vw, so + WRS, 0) << 16);
    }
    asm volatile("s_nop 1" : "+v"(w), "+v"(q[0]), "+v"(q[1]), "+v"(q[2]), "+v"(q[3]));
#pragma unroll
    for (int qb = 0; qb < 4; ++qb) {
      u32x4 x;
      asm volatile("buffer_load_dwordx4 %0, %1, %2, %3 offen sc1\n\ts_waitcnt vmcnt(0)" : "=v"(x) : "v"(vx), "s"(srdN), "s"(sx + 64u * ks + 16u * XRS * qb) : "memory");
      sum16<false>(q[qb], w, x);
    }
  }
  asm volatile("s_nop 7" : "+v"(q[0]), "+v"(q[1]), "+v"(q[2]), "+v"(q[3]));
  const float C = 0.12751743082f;
  unsigned r[8];
#pragma unroll
  for (int qb = 0; qb < 4; ++qb) { r[2 * qb] = cvtpk(C * q[qb][0], C * q[qb][1]); r[2 * qb + 1] = cvtpk(C * q[qb][2], C * q[qb][3]); }
  asm volatile("s_nop 1" : "+v"(r[0]), "+v"(r[1]), "+v"(r[2]), "+v"(r[3]), "+v"(r[4]), "+v"(r[5]), "+v"(r[6]), "+v"(r[7]));
  acc_write<AQ_(0, J) + 2 * H>(r[0]); acc_write<AQ_(0, J) + 2 * H + 1>(r[1]); acc_write<AQ_(1, J) + 2 * H>(r[2]); acc_write<AQ_(1, J) + 2 * H + 1>(r[3]);
  acc_write<AQ_(2, J) + 2 * H>(r[4]); acc_write<AQ_(2, J) + 2 * H + 1>(r[5]); acc_write<AQ_(3, J) + 2 * H>(r[6]); acc_write<AQ_(3, J) + 2 * H + 1>(r[7]);
}
template <int I> DI void redo_kload(const unsigned vo, const __amdgpu_buffer_rsrc_t srd, const unsigned so) {
  asm volatile("buffer_load_dwordx4 a[%c3:%c4], %0, %1, %2 offen offset:%c5" :: "v"(vo), "s"(srd), "s"(so + 4096u * (I >> 2)), "i"(192 + 4 * I), "i"(195 + 4 * I), "i"(64 * (I & 3)) : "memory");
}
template <int CM, int... Ms> DI void redo_s(W4C& c, const unsigned vk, const unsigned so, f32x4 (&T)[2][4], iseq<Ms...>) {
  redo_kload<0>(vk, c.srdK, so); redo_kload<1>(vk, c.srdK, so); redo_kload<2>(vk, c.srdK, so); redo_kload<3>(vk, c.srdK, so);
  redo_kload<4>(vk, c.srdK, so); redo_kload<5>(vk, c.srdK, so); redo_kload<6>(vk, c.srdK, so); redo_kload<7>(vk, c.srdK, so);
  asm volatile("s_waitcnt vmcnt(0)" ::: "memory");
  (qk16<(Ms >> 4), ((Ms >> 2) & 3), (Ms & 3), ((Ms & 3) ? 0 : CM)>(T[Ms >> 4][(Ms >> 2) & 3], c.NEGM[(Ms >> 2) & 3]), ...);
  asm volatile("s_nop 7" : "+v"(T[0][0]), "+v"(T[0][1]), "+v"(T[0][2]), "+v"(T[0][3]), "+v"(T[1][0]), "+v"(T[1][1]), "+v"(T[1][2]), "+v"(T[1][3]));
}
template <int DB> DI void redo_pv(const W4C& c, const unsigned vv, const unsigned so, const u32x4 (&P)[4]) {
  u32x4 v;
#pragma unroll
  for (int e = 0; e < 4; ++e) {
    const unsigned s = so + (896u + 32u * DB) + KRS * (16u * (e >> 1) + 2u * (e & 1));
    v[e] = (unsigned)__builtin_amdgcn_raw_buffer_load_b16(c.srdV, vv, s, 0) | ((unsigned)__builtin_amdgcn_raw_buffer_load_b16(c.srdV, vv, s + KRS, 0) << 16);
  }
  asm volatile("s_nop 1" : "+v"(v));
  const bf16x8 vf = __builtin_bit_cast(bf16x8, v);
  pv16<AO_(DB, 0), false>(vf, P[0]); pv16<AO_(DB, 1), false>(vf, P[1]); pv16<AO_(DB, 2), false>(vf, P[2]); pv16<AO_(DB, 3), false>(vf, P[3]);
  GAP();
}
template <int U> DI void redo_unit(OST& t) {
  t.o[0] = acc_read<8 * U>(); t.o[1] = acc_read<8 * U + 1>(); t.o[2] = acc_read<8 * U + 2>(); t.o[3] = acc_read<8 * U + 3>();
  t.o[4] = acc_read<8 * U + 4>(); t.o[5] = acc_read<8 * U + 5>(); t.o[6] = acc_read<8 * U + 6>(); t.o[7] = acc_read<8 * U + 7>();
  const float il = t.il[U >> 2];
  t.w = u32x4{cvtpk(t.o[0] * il, t.o[1] * il), cvtpk(t.o[2] * il, t.o[3] * il), cvtpk(t.o[4] * il, t.o[5] * il), cvtpk(t.o[6] * il, t.o[7] * il)};
  o_store<U>(t);
  asm volatile("s_nop 1" :: "v"(t.w));
}
template <int R0, int... Is> DI void redo_zero(const unsigned z, iseq<Is...>) { (acc_write<R0 + Is>(z), ...); }
template <int... Is> DI void redo_units(OST& t, iseq<Is...>) { (redo_unit<Is>(t), ...); }
template <int F0, int... Is> DI void redo_reqs(OPS& o, iseq<Is...>) { (op_reqB<F0 + Is>(o), ...); }
DI void redo_rows(W4C& c, OST& t, OPS& o, const u16* xn, const u16* wq, const unsigned long long* keys, const unsigned row0, const unsigned h, const unsigned tag) {
  unsigned l;
  asm volatile("s_waitcnt vmcnt(0)\n\tv_mbcnt_lo_u32_b32 %0, -1, 0\n\tv_mbcnt_hi_u32_b32 %0, -1, %0" : "=v"(l) :: "memory");
  const unsigned c16 = l & 15u, g = l >> 4;
  wait_flag((const unsigned*)(keys + row0 + l) + 1, tag);
  {
    const __amdgpu_buffer_rsrc_t srdW = mk_srd(wq, DM * HQ * DH * 2), srdN = mk_srd(xn, SEQ * DM * 2);
    const unsigned vw = WRS * 8u * g + 16u * (c16 >> 2) + 2u * (c16 & 3u), vx = XRS * c16 + 16u * g, sw = 256u * h, sx = XRS * row0;
    redo_q<0, 0>(srdW, srdN, vw, vx, sw, sx); redo_q<0, 1>(srdW, srdN, vw, vx, sw, sx); redo_q<1, 0>(srdW, srdN, vw, vx, sw, sx); redo_q<1, 1>(srdW, srdN, vw, vx, sw, sx);
    redo_q<2, 0>(srdW, srdN, vw, vx, sw, sx); redo_q<2, 1>(srdW, srdN, vw, vx, sw, sx); redo_q<3, 0>(srdW, srdN, vw, vx, sw, sx); redo_q<3, 1>(srdW, srdN, vw, vx, sw, sx);
  }
  const unsigned vk = KRS * c16 + 16u * g, vv = KRS * 4u * g + 2u * c16;
  f32x4 T[2][4];
  float m[4] = {-__builtin_inff(), -__builtin_inff(), -__builtin_inff(), -__builtin_inff()};
#pragma nounroll
  for (unsigned u = 0; u < SEQ / 32; ++u) {
    redo_s<2>(c, vk, 32u * KRS * u, T, make_iseq<32>{});
#pragma unroll
    for (int q = 0; q < 4; ++q)
#pragma unroll
      for (int e = 0; e < 8; ++e) m[q] = max2f(m[q], T[e >> 2][q][e & 3]);
  }
#pragma unroll
  for (int q = 0; q < 4; ++q) {
    asm volatile("s_nop 1" : "+v"(m[q]));
    auto p1 = __builtin_amdgcn_permlane16_swap(__float_as_uint(m[q]), __float_as_uint(m[q]), false, false);
    m[q] = max2f(__uint_as_float(p1[0]), __uint_as_float(p1[1]));
    asm volatile("s_nop 1" : "+v"(m[q]));
    auto p2 = __builtin_amdgcn_permlane32_swap(__float_as_uint(m[q]), __float_as_uint(m[q]), false, false);
    m[q] = -max2f(__uint_as_float(p2[0]), __uint_as_float(p2[1]));
    c.NEGM[q] = f32x4{m[q], m[q], m[q], m[q]};
    c.L[q] = f32x4{0.f, 0.f, 0.f, 0.f};
  }
  redo_zero<0>(0u, make_iseq<128>{});
  asm volatile("s_nop 1" : "+v"(c.NEGM[0]), "+v"(c.NEGM[1]), "+v"(c.NEGM[2]), "+v"(c.NEGM[3]), "+v"(c.L[0]), "+v"(c.L[1]), "+v"(c.L[2]), "+v"(c.L[3]));
#pragma nounroll
  for (unsigned u = 0; u < SEQ / 32; ++u) {
    redo_s<1>(c, vk, 32u * KRS * u, T, make_iseq<32>{});
    u32x4 P[4];
#pragma unroll
    for (int q = 0; q < 4; ++q)
#pragma unroll
      for (int e = 0; e < 4; ++e) P[q][e] = cvtpk(__builtin_amdgcn_exp2f(T[e >> 1][q][2 * (e & 1)]), __builtin_amdgcn_exp2f(T[e >> 1][q][2 * (e & 1) + 1]));
    asm volatile("s_nop 1" : "+v"(P[0]), "+v"(P[1]), "+v"(P[2]), "+v"(P[3]));
#pragma unroll
    for (int q = 0; q < 4; ++q) sum16<false>(c.L[q], c.ONE, P[q]);
    const unsigned so = 32u * KRS * u;
    redo_pv<0>(c, vv, so, P); redo_pv<1>(c, vv, so, P); redo_pv<2>(c, vv, so, P); redo_pv<3>(c, vv, so, P);
    redo_pv<4>(c, vv, so, P); redo_pv<5>(c, vv, so, P); redo_pv<6>(c, vv, so, P); redo_pv<7>(c, vv, so, P);
  }
  asm volatile("s_nop 7" : "+v"(c.L[0]), "+v"(c.L[1]), "+v"(c.L[2]), "+v"(c.L[3]));
#pragma unroll
  for (int q = 0; q < 4; ++q) t.il[q] = __builtin_amdgcn_rcpf(c.L[q][0]);
  redo_units(t, make_iseq<16>{});
  redo_reqs<35>(o, make_iseq<FNR>{});
  asm volatile("s_waitcnt vmcnt(0)" ::: "memory");
}

DI void oproj_phase(OPS& o, char* lds, const u16* __restrict__ xn, unsigned* __restrict__ hand, float* __restrict__ exch, u16* __restrict__ out, const unsigned wid  ) {
  const unsigned ldsb = (unsigned)(size_t)lds, tag = o.tag;
  unsigned fe;
  op_poll(o);
  o.lb5 = ldsb + SD_BASE + BP_W * wid + o.va; o.lb1 = ldsb + 4096u * wid + o.va; o.lb2 = ldsb + 0x4000u + 8192u * wid + o.va;
  asm volatile("s_waitcnt vmcnt(%6)" : "+v"(o.gv[0][0]), "+v"(o.gv[0][1]), "+v"(o.gs[0]), "+v"(o.gv[1][0]), "+v"(o.gv[1][1]), "+v"(o.gs[1]) : "i"(FVW + 1) : "memory");
  static_assert(FVS == 0, "the last request of the final phase is a store");
  op_ldsBs<0>(o, make_iseq<9>{}); op_ldsBs<13>(o, make_iseq<22>{});
  asm volatile("ds_write_b128 %0, %1 offset:4096\n\tds_write_b128 %0, %2 offset:5120\n\tds_write_b32 %0, %3 offset:6144" :: "v"(o.lb5), "v"(o.gv[1][0]), "v"(o.gv[1][1]), "v"(o.gs[1]) : "memory");
  o.idx = (o.va & 128u) ? 0xeeeeeeeeu : 0x44444444u; o.D[0][4] = f32x4{0.f, 0.f, 0.f, 0.f};
  asm volatile("" : "+v"(o.idx), "+v"(o.D[0][4]));
  redo_zero<240>(0u, make_iseq<12>{});
  { unsigned pe; PS_AT(45); PS_PAGE(pe, 45); ps_touch(pe + (wid < 3u ? wid : 0u)); }
  GAP();
  {
    u32x4 sr = o.srdL; sr[2] = o.sf + 4u;
    redo_reqs<35 + FNR>(o, make_iseq<OP_NR - 35 - FNR>{});
    asm volatile("s_waitcnt vmcnt(%7)\n\tbuffer_store_dword %2, %3, %4, %5 offen sc1\n\ts_waitcnt vmcnt(%7)\n\tv_cmp_eq_u32_e64 %0, %6, %1"
                 : "=s"(o.seen), "+v"(o.fo) : "v"(tag), "v"(o.va), "s"(sr), "s"(o.sf), "s"(tag), "i"(OP_NR - 34 - FNR) : "memory");
  }
  op_there(o);
  op_reqA0(o, make_iseq<4 * OP_LA>{});
  GAP();
  unsigned blk = blockIdx.x;
  asm volatile("" : "+s"(blk));
  const unsigned sn = o.sn;
  asm volatile("s_waitcnt lgkmcnt(0)" : "+v"(o.fb[0]), "+v"(o.fb[1]), "+v"(o.fb[2]), "+v"(o.fb[3]), "+v"(o.fb[4]), "+v"(o.fb[5]), "+v"(o.fb[6]), "+v"(o.fb[7]), "+v"(o.fb[8]), "+v"(o.fb[9]), "+v"(o.fb[10]),
               "+v"(o.fb[11]), "+v"(o.fb[12]), "+v"(o.fb[13]), "+v"(o.fb[14]), "+v"(o.fb[15]), "+v"(o.fb[16]), "+v"(o.fb[17]), "+v"(o.fb[18]), "+v"(o.fb[19]), "+v"(o.fb[20]), "+v"(o.fb[21]) :: "memory");
  if (__builtin_expect(o.bad != 0u, 0)) {
    const unsigned lane = o.va >> 4, row0 = (o.sa >> 13 ^ 64u) & ~15u, ax[2] = {op_ax<0>(o.va), op_ax<1>(o.va)};
    unsigned selr, an[2] = {ax[0], ax[1]};
    wait_flag(hand + 2u * (lane & 7u) + 1u, tag);
    wait_flag(hand + H_X + 72u * ((blk >> 3) & 7u) + 4u * (lane < 18u ? lane : 0u), tag);
    asm volatile("buffer_load_dword %0, %1, %2, 0 offen sc1\n\ts_waitcnt vmcnt(0)" : "=v"(selr) : "v"(8u * (lane & 7u)), "s"(o.srdL) : "memory");
#pragma nounroll
    for (unsigned t = 0; t < (unsigned)NSEL; ++t) {
      const unsigned ro = (((unsigned)__builtin_amdgcn_readlane(selr, t) & 0xffffu) - row0) * XRS_;
#pragma unroll
      for (int j = 0; j < 2; ++j) { const unsigned d = ax[j] - ro; an[j] = d < XRS_ ? (SEQ + t - row0) * XRS_ + d : an[j]; }
    }
    asm volatile("buffer_load_dwordx4 %0, %3, %5, %6 offen sc1\n\tbuffer_load_dwordx4 %1, %3, %5, %6 offen offset:64 sc1\n\tbuffer_load_dword %2, %4, %5, %6 offen sc1\n\ts_waitcnt vmcnt(0)"
                 : "=&v"(o.gv[0][0]), "=&v"(o.gv[0][1]), "=&v"(o.gs[0]) : "v"(an[0]), "v"(an[1]), "s"(mk_srd(xn, (SEQ + NSEL) * DM * 2)), "s"(sn) : "memory");
  }
  __amdgpu_buffer_rsrc_t srdO; unsigned ax[2], fes, t;
#define SC(g, x, e) if constexpr (M == (g)) { x = (e); asm volatile("" : "+s"(x)); }
#define VC(g, x, e) if constexpr (M == (g)) { x = (e); asm volatile("" : "+v"(x)); }
  auto prep = [&](auto M_) __attribute__((always_inline)) {
    constexpr int M = decltype(M_)::value;
    if constexpr (M < 6) { acc_write<op_ac(1, 1 + M / 2, 4) + 2 * (M & 1)>(0u); acc_write<op_ac(1, 1 + M / 2, 4) + 2 * (M & 1) + 1>(0u); }
    VC(6, o.ra[0], o.lb5 + BP_W * ((wid ^ 1u) - wid)) VC(7, o.ra[1], o.lb5 + BP_W * ((wid ^ 2u) - wid)) VC(8, o.ra[2], o.lb2 + 8192u * ((wid ^ 3u) - wid))
    SC(9, o.sw[0], 5120u * wid) SC(9, o.sw[1], o.sw[0] + 4096u) SC(9, t, blk * 5120u) SC(10, o.srdE, mk_srd(exch + t, 20480u))
    SC(11, t, (blk ^ 4u) * 5120u) SC(11, fes, 4u * (H_E + 32u * (blk ^ 4u))) SC(12, o.srdI, mk_srd(exch + t, 20480u))
    SC(13, o.srdG, mk_srd(hand + H_E + 32u * blk, ((wid - 1u) >> 31) * 4u)) SC(14, srdO, mk_srd(out, SEQ * DM * 2))
    VC(15, ax[0], op_ax<0>(o.va)) VC(16, ax[1], op_ax<1>(o.va))
  };
  prep(IC(9)); prep(IC(10)); prep(IC(11)); prep(IC(12)); prep(IC(13)); prep(IC(14));
  GAP();
  op_again<0>(o);
  o.sl += 128u; o.srdL[2] += 128u;
  asm volatile("ps");
  auto step = [&](auto ST) __attribute__((always_inline)) {
    constexpr int st = decltype(ST)::value, A = st % (OP_LA + 1);
    if constexpr (st == 16) {
    }
    if constexpr (st == 24) {
    }
    if constexpr (st == 30) {
    }
    if constexpr (st == OP_SD) {
      asm volatile("s_waitcnt vmcnt(%1)" : "+v"(fe) : "i"(OP_FEW) : "memory");
      if (__builtin_expect(!__all(fe == tag), 0)) wait_flag(hand + H_E + 32u * (blk ^ 4u), tag);
    }
    if constexpr (st == 8) { asm volatile("s_waitcnt vmcnt(%0)" :: "i"(OP_P4W) : "memory"); op_again<8>(o); }
    if constexpr (st == 28) { o.D[1][4] = f32x4{0.f, 0.f, 0.f, 0.f}; asm volatile("s_nop 0" : "+v"(o.D[1][4])); }
    asm volatile("s_waitcnt vmcnt(%4)" : "+v"(o.fa[A][0]), "+v"(o.fa[A][1]), "+v"(o.fa[A][2]), "+v"(o.fa[A][3]) : "i"(OPW[st]) : "memory");
    if constexpr (st == 4) asm volatile("" : "+v"(o.fo));
    if constexpr (st == 29) asm volatile("" : "+v"(o.gv[1][0]), "+v"(o.gv[1][1]), "+v"(o.gs[1]));
    GAP();
    op_qs<st>(o, prep, make_iseq<18>{});
    GAP();
    if constexpr (st == OP_SP) { u32x4 sh = o.srdL; sh[2] = 131072u; const unsigned so = fes; asm volatile("buffer_load_dword %0, off, %1, %2 sc1" : "=v"(fe) : "s"(sh), "s"(so) : "memory"); }
    if constexpr (st == OP_SF - 2) { o.tg = tag; asm volatile("" : "+v"(o.tg)); }
    GAP();
  };
  for_seq(step, make_iseq<32>{});
  op_qs<32>(o, prep, make_iseq<1>{});
  auto fin2 = [&](const float s0, const float s1, const int wd) __attribute__((always_inline)) {
    const unsigned sp = cvtpk(s0, s1), pp = cvtpk(bflo(sp) * o.gx[2 * wd], bfhi(sp) * o.gx[2 * wd + 1]);
    return cvtpk(o.gx[18 + 2 * wd] + bflo(pp), o.gx[19 + 2 * wd] + bfhi(pp));
  };
  auto fin = [&](auto J_) __attribute__((always_inline)) {
    constexpr int j = decltype(J_)::value;
    const f32x4 s = o.D[1][2 * j] + o.T[2 * j];
    const __amdgpu_buffer_rsrc_t so = srdO; const unsigned sc = sn, va = ax[j >> 1];
    if constexpr (j < 2) {
      const f32x4 r = o.D[1][2 * j + 1] + o.T[2 * j + 1];
      const u32x4 ov = {fin2(s[0], s[1], 4 * j), fin2(s[2], s[3], 4 * j + 1), fin2(r[0], r[1], 4 * j + 2), fin2(r[2], r[3], 4 * j + 3)};
      asm volatile("buffer_store_dwordx4 %0, %1, %2, %3 offen offset:%c4\n\ts_nop 1" :: "v"(ov), "v"(va), "s"(so), "s"(sc), "i"(64 * j) : "memory");
    } else {
      auto p0 = __builtin_amdgcn_permlane32_swap(__float_as_uint(s[0]), __float_as_uint(s[2]), false, false), p1 = __builtin_amdgcn_permlane32_swap(__float_as_uint(s[1]), __float_as_uint(s[3]), false, false);
      const unsigned ov = fin2(__uint_as_float(p0[0]) + __uint_as_float(p0[1]), __uint_as_float(p1[0]) + __uint_as_float(p1[1]), 8);
      asm volatile("buffer_store_dword %0, %1, %2, %3 offen" :: "v"(ov), "v"(va), "s"(so), "s"(sc) : "memory");
    }
  };
  fin(IC(2)); fin(IC(0));
  GAP();
  fin(IC(1));
}

extern "C" __global__ __launch_bounds__(256, 1) __attribute__((aligned(4096)))
void k_attn(u16* __restrict__ xn, const u16* __restrict__ wq, const u16* __restrict__ Kc, const u16* __restrict__ Vc, u16* __restrict__ O,
            const u16* __restrict__ wo, const u16* __restrict__ wg, const u16* __restrict__ wu, const u16* __restrict__ wd, u16* __restrict__ wot,
            unsigned long long* __restrict__ keys, float* __restrict__ ypart, int* __restrict__ sel, const u16* __restrict__ xres, u16* __restrict__ out,
            const u16* __restrict__ w_r  ) {
  asm volatile(PS_MACROS ::: "memory");
  extern __shared__ __attribute__((aligned(16))) char lds[];
  const unsigned tid = threadIdx.x, lane = tid & 63u, wid = tid >> 6, l32 = lane & 31u, hi = lane >> 5;
  constexpr int T = NT;
  const float C = 0.12751743082f;
  W4C c;
  c.ldsb = (unsigned)(size_t)lds; c.pdum = c.ldsb + PF_DUM;
  asm volatile("" : "+s"(c.pdum));
  c.m0k = rfl(c.ldsb + KRING + 4096u * wid); c.m0v = rfl(c.ldsb + VRING + 4096u * wid);
  c.sofk = rfl(KRS * 16u * wid) + (32u / 1) * KTILEB * ((blockIdx.x >> 3) & 0u);

  const unsigned blk = blockIdx.x, xg = blk & 7u, ii = blk >> 3;
  const unsigned kvh = 2u * xg + (ii >> 4), h = 2u * kvh + ((ii >> 3) & 1u), yb = ii & 7u;
  const unsigned row0 = 256u * yb + 64u * wid;
  const bool moe = blk < (unsigned)MOE_WG;
  const bool ret = !moe && blk < 208u;
  unsigned tag = 0u, gen;
  const unsigned lrow = 8u * blk + 2u * wid + hi;
  QFT qf;
  {
    QP p; LNJ lj; u32x4 cw; u32x2 g0, g1; unsigned pb0, pb1, pb2, pb3, csw; unsigned long long pbase;
    lj.three = l32 < 8u; lj.up = hi != 0u;
    const unsigned vl = 16u * l32 + XRS * hi, vl3 = lj.three ? vl + 1024u : 0x40000000u, srow = XRS * 8u * blk + XRS * 2u * rfl(wid);
    {
      const unsigned r8 = (lane >> 2) & 7u, vws = WRS * ((r8 & 3u) + 8u * (r8 >> 2)) + 64u * hi + 16u * (lane & 3u);
      p.vw[0] = vws + 896u; p.vw[1] = vws; p.vw[2] = vws + (4u * WRS - 2048u + 896u); p.vw[3] = vws + (4u * WRS - 2048u);
#pragma unroll
      for (int q = 0; q < 4; ++q) p.vx[q] = XRS * (lane >> 3) + 16u * ((lane & 7u) ^ (4u * (q & 1) + (lane >> 4))) + (8u * XRS - 1024u) * q;
    }
    p.m0x = rfl(c.ldsb + 0x4000u + 8192u * wid);
    p.sofw = rfl(WRS * 16u * wid + 256u * h);
    p.sofx = rfl(XRS * row0);
    p.kk = (yb ^ (ii >> 3)) & 1u;
    asm volatile("s_nop 4" : "+s"(c.m0k), "+s"(c.m0v), "+s"(c.sofk), "+s"(p.m0x), "+s"(p.sofw), "+s"(p.sofx), "+s"(p.kk));
    asm volatile("" : "+v"(p.vw[0]), "+v"(p.vw[1]), "+v"(p.vw[2]), "+v"(p.vw[3]), "+v"(p.vx[0]), "+v"(p.vx[1]), "+v"(p.vx[2]), "+v"(p.vx[3]));
    GAP();
    p.m0w = c.m0k;
    p.srdW = __builtin_amdgcn_make_buffer_rsrc((void*)((const char*)wq - 896), (short)0, DM * HQ * DH * 2 + 896, SRD_WORD3);
    p.srdX = __builtin_amdgcn_make_buffer_rsrc((void*)xres, (short)0, SEQ * DM * 2, SRD_WORD3);
    auto vm = [&](auto I_) __attribute__((always_inline)) {
      constexpr int I = decltype(I_)::value; constexpr VOp o = VT.v[I]; constexpr bool nm = vq_newm0(I); constexpr int q = o.q & 3;
      if constexpr (o.kind == VK_S) {
        if constexpr (o.q == 0 && o.st < 2) qp_next<o.st>(p);
        if constexpr (o.q < 4) lds_dma<nm, 1024 * q>(p.m0w + QP_STG * (o.st % 3), p.vw[q], p.srdW, p.sw);
        else {
          if constexpr (o.q == 8) { p.sx += 32u * XRS; asm volatile("" : "+s"(p.sx)); }
          lds_dma<nm, 1024 * q>(p.m0x + QP_STG * (o.st % 3) + (o.q >= 8 ? 4096u : 0u), p.vx[q], p.srdX, p.sx);
        }
      } else if constexpr (o.kind == VK_W8) lds_dma<nm, 1024 * q>(p.m0w + QP_W8, p.vw[q], p.srdW, p.sw);
      else if constexpr (o.kind == VK_KV) {
        if constexpr (o.q < 8) lds_dma<nm, 1024 * q>(c.m0k + 0x4000u * (o.q >> 2), (q & 2) ? c.ksx[q & 1] : c.vksrc[q & 1], c.srdK, KTILEB * (o.q >> 2) + c.sofk);
        else lds_dma<true, 0>(c.m0v + 1024u * q, c.vvsrc, c.srdV, c.sofk + (896u + KRS * 8u * (q >> 1) + 128u * (q & 1)));
      } else if constexpr (o.kind == VK_V1) lds_dma<nm, 1024 * q>(c.m0v + 0x4000u, (q & 1) ? c.vvsrc : c.vvsrc0, c.srdV, KTILEB + c.sofk);
      else if constexpr (o.kind == VK_K2) lds_dma<nm, 1024 * q>(c.m0k, (q & 2) ? c.ksx[q & 1] : c.vksrc[q & 1], c.srdK, (KTILEB * 2u + c.sofk) & KMASK);
      else if constexpr (o.kind == VK_PF)
        asm volatile("s_mov_b32 m0, %0\n\ts_add_u32 %1, %1, %4\n\tbuffer_load_dwordx4 %2, %3, %1 offen sc1 lds\n\ts_add_u32 %0, %0, 0x400"
                     : "+s"(c.pfm0), "+s"(c.pfo) : "v"(c.pfv), "s"(c.srdP), "s"(c.pfst) : "m0", "memory", "scc");
      else if constexpr (o.kind == VK_CW) {
        unsigned long long pa, po;
        asm volatile("s_getpc_b64 s[20:21]\n\ts_add_u32 s20, s20, k_attn@rel32@lo+4\n\ts_addc_u32 s21, s21, k_attn@rel32@hi+12\n\ts_mov_b64 %0, s[20:21]" : "=s"(pa) :: "scc", "s20", "s21");
        asm volatile("s_getpc_b64 s[20:21]\n\ts_add_u32 s20, s20, k_attn_end@rel32@lo+4\n\ts_addc_u32 s21, s21, k_attn_end@rel32@hi+12\n\ts_mov_b64 %0, s[20:21]" : "=s"(po) :: "scc", "s20", "s21");
        const __amdgpu_buffer_rsrc_t srdC = __builtin_amdgcn_make_buffer_rsrc((void*)pa, (short)0, (int)(po - pa), SRD_WORD3);
        asm volatile("buffer_load_dwordx4 %0, %1, %2, %3 offen" : "=&v"(cw) : "v"(16u * tid), "s"(srdC), "s"((blk >> 3) * 4096u) : "memory");
      } else if constexpr (o.kind == VK_GEN)
        asm volatile("buffer_load_dword %0, off, %1, 0 offset:%c2 sc1" : "=&v"(gen) : "s"(__builtin_amdgcn_make_buffer_rsrc((void*)sel, (short)0, 8192, SRD_WORD3)), "i"(4 * H_G) : "memory");
      else if constexpr (o.kind == VK_A)
        asm volatile("buffer_load_dwordx4 %0, %1, %2, %3 offen offset:%c4" : "=&v"(lj.a[o.q]) : "v"(o.q == 2 ? vl3 : vl), "s"(p.srdX), "s"(srow), "i"(o.q == 1 ? 512 : 0) : "memory");
      else if constexpr (o.kind == VK_WR)
        asm volatile("buffer_load_dwordx4 %0, %1, %2, 0 offen offset:%c3" : "=&v"(lj.w[o.q]) : "v"(16u * l32), "s"(__builtin_amdgcn_make_buffer_rsrc((void*)w_r, (short)0, DM * 2, SRD_WORD3)), "i"(512 * o.q) : "memory");
      else if constexpr (o.kind == VK_STS || o.kind == VK_KEY) {
        const u32x2 g = {o.kind == VK_STS ? lj.stat : lj.key, tag};
        asm volatile("buffer_store_dwordx2 %0, %1, %2, %3 offen sc1" :: "v"(g), "v"(l32 == 0u ? 8u * lrow : 0x40000000u),
                     "s"(__builtin_amdgcn_make_buffer_rsrc((void*)keys, (short)0, SEQ * 16, SRD_WORD3)), "s"(o.kind == VK_STS ? SEQ * 8u : 0u) : "memory");
      } else if constexpr (o.kind == VK_GAT) {
        asm volatile("buffer_load_dwordx2 %0, %1, %2, %3 offen offset:%c4 sc1" : "=&v"(o.q ? g1 : g0) : "v"(8u * (row0 + l32)),
                     "s"(__builtin_amdgcn_make_buffer_rsrc((void*)keys, (short)0, SEQ * 16, SRD_WORD3)), "s"(SEQ * 8u), "i"(256 * o.q) : "memory");
      } else if constexpr (o.kind == VK_XNS) {
        const __amdgpu_buffer_rsrc_t srdN = __builtin_amdgcn_make_buffer_rsrc((void*)xn, (short)0, SEQ * DM * 2, SRD_WORD3);
        asm volatile("buffer_store_dwordx4 %0, %1, %2, %3 offen offset:%c4 sc1\n\ts_nop 1" :: "v"(lj.a[o.q]), "v"(o.q == 2 ? vl3 : vl), "s"(srdN), "s"(srow), "i"(o.q == 1 ? 512 : 0) : "memory");
      }
    };
    auto hook = [&](auto P_) __attribute__((always_inline)) {
      constexpr int pos = decltype(P_)::value;
      if constexpr (pos == 11) { unsigned w = rfl(wid); asm volatile("" : "+s"(w));  }
      if constexpr (pos == 37) { unsigned w = rfl(wid); asm volatile("" : "+s"(w));  }
      if constexpr (pos == 1) {
      } else if constexpr (pos == 13) { asm volatile("" :: "v"(cw));
      } else if constexpr (pos == 32 + 13) asm volatile("" : "+v"(lj.w[0]), "+v"(lj.w[1]), "+v"(lj.w[2]));
      else if constexpr (pos == -1000 - (32 + 1)) {
        c.vkb[0] = c.ldsb + KRING + 256u * (lane & 15u) + 16u * ((lane >> 4) ^ (lane & 15u));
        asm volatile("" : "+v"(c.vkb[0]));
      } else if constexpr (pos == -1000 - (32 + 3)) {
#pragma unroll
        for (int j = 1; j < 4; ++j) c.vkb[j] = c.vkb[0] ^ (64u * j);
        asm volatile("" : "+v"(c.vkb[1]), "+v"(c.vkb[2]), "+v"(c.vkb[3]));
      } else if constexpr (pos == -1000 - (32 + 5)) {
        c.vvb[0] = c.ldsb + VRING + (((lane & 3u) << 3) | (((lane >> 2) & 3u) << 6) | (((lane >> 4) & 1u) * 288u) | (hi << 11));
        c.vvb[1] = c.vvb[0] ^ 32u;
        asm volatile("" : "+v"(c.vvb[0]), "+v"(c.vvb[1]));
      } else if constexpr (pos == -1000 - (32 + 7)) {
#pragma unroll
        for (int par = 0; par < 2; ++par) c.vksrc[par] = KRS * (lane >> 4) + 16u * ((lane & 15u) ^ (4u * par + (lane >> 4)));
        c.ksx[0] = c.vksrc[0] ^ 128u; c.ksx[1] = c.vksrc[1] ^ 128u;
        asm volatile("" : "+v"(c.vksrc[0]), "+v"(c.vksrc[1]), "+v"(c.ksx[0]), "+v"(c.ksx[1]));
      } else if constexpr (pos == -1000 - (32 + 9)) {
        c.vvsrc = KRS * ((lane >> 2) & 7u) + 64u * hi + 16u * ((lane & 3u) ^ ((lane >> 3) & 2u)); c.vvsrc0 = c.vvsrc + 896u;
        asm volatile("" : "+v"(c.vvsrc), "+v"(c.vvsrc0));
      } else if constexpr (pos == -1000 - (32 + 0)) {
        const unsigned rs = moe ? FF * 2u : ret ? DM * 2u : 64u;
        const unsigned r16 = ret ? ((lane >> 2) & 3u) | (lane & 16u) | ((lane >> 3) & 4u) : lane >> 2;
        c.pfv = rs * r16 + 16u * ((lane & 3u) ^ ((lane >> 4) & 2u));
        asm volatile("" : "+v"(c.pfv));
      } else if constexpr (pos == -1000 - (29)) {
        const unsigned w = rfl(wid), fb = (blk & 7u) * 18u + (blk >> 3);
        pb0 = 64u * fb + 16u * FF * 2u * (w & 1u); pb1 = ((blk - 144u) * 4u + w) * 18432u - 9216u * (w & 1u); pb2 = ((blk - 208u) * 4u + w) * 27648u; pb3 = 18432u * w;
        const u16 *a0 = wg, *a1 = wu;
        asm volatile("s_cmp_lt_u32 %1, 2\n\ts_cselect_b64 %0, %2, %3" : "=s"(pbase) : "s"(w), "s"(a0), "s"(a1) : "scc");
        asm volatile("" : "+s"(pb0), "+s"(pb1), "+s"(pb2), "+s"(pb3));
      } else if constexpr (pos == -1000 - (31)) {
        const u16 *a0 = wo, *a1 = wd; const unsigned b = blk;
        unsigned long long pb; unsigned nb, st, np;
        asm volatile("s_cmp_lt_u32 %8, 208\n\ts_cselect_b64 %0, %9, %10\n\ts_cselect_b32 %5, %5, %6\n\ts_mov_b32 %1, %12\n\ts_cselect_b32 %2, 64, %13\n\ts_cselect_b32 %7, %7, 0\n\ts_cselect_b32 %3, %11, %14\n\t"
                     "s_cmp_lt_u32 %8, 144\n\ts_cselect_b64 %0, %15, %0\n\ts_cselect_b32 %5, %4, %5\n\ts_cselect_b32 %1, %16, %1\n\ts_cselect_b32 %2, %17, %2"
                     : "=&s"(pb), "=&s"(nb), "=&s"(st), "=&s"(np), "+s"(pb0), "+s"(pb1), "+s"(pb2), "+s"(pb3)
                     : "s"(b), "s"(a0), "s"(a1), "s"(18432u), "s"(27648u), "s"(1024u), "s"(20480u), "s"(pbase), "s"(559u * (FF * 2u) + 64u), "s"(FF * 2u * 32u) : "scc");
        pbase = pb; pb0 = nb; pb2 = np; c.pfst = st;
      } else if constexpr (pos == -1000 - (32 + 15)) {
        c.srdP = __builtin_amdgcn_make_buffer_rsrc((void*)(pbase + pb1), (short)0, (int)pb0, SRD_WORD3);
        c.pfo = 0u - c.pfst;
        c.pfm0 = c.ldsb + SD_BASE + pb3; c.pfend = c.pfm0 + pb2;
        asm volatile("" : "+s"(c.srdP), "+s"(c.pfst), "+s"(c.pfo), "+s"(c.pfm0), "+s"(c.pfend));
      } else if constexpr (pos == -1000 - (32 + 17)) {
        unsigned kvo = kvh; asm volatile("" : "+s"(kvo));
        c.srdK = __builtin_amdgcn_make_buffer_rsrc((void*)((const char*)Kc + (size_t)kvo * SEQ * 256), (short)0, SEQ * 256, SRD_WORD3);
        c.srdV = __builtin_amdgcn_make_buffer_rsrc((void*)((const char*)Vc + (size_t)kvo * SEQ * 256 - 896), (short)0, SEQ * 256 + 896, SRD_WORD3);
        asm volatile("" : "+s"(c.srdK), "+s"(c.srdV));
      } else if constexpr (pos == -1000 - (64 + 1)) {
        unsigned kvo = kvh; asm volatile("" : "+s"(kvo));
        const unsigned id = ((blk >> 3) & 15u) * 4u + rfl(wid);
        const u16 *a0 = Kc, *a1 = Vc; unsigned long long pb;
        asm volatile("s_cmp_lt_u32 %1, 32\n\ts_cselect_b64 %0, %2, %3" : "=s"(pb) : "s"(id), "s"(a0), "s"(a1) : "scc");
        c.srdF = __builtin_amdgcn_make_buffer_rsrc((void*)(pb + (size_t)kvo * SEQ * 256), (short)0, SEQ * 256, SRD_WORD3);
        c.sfo = (c.sofk & ~(KTILEB - 1u)) + KTILEB * (LAH + ((id >> 4) & 1u) - (id >> 5)) + 1024u * (id & 15u);
        asm volatile("" : "+s"(c.srdF), "+s"(c.sfo));
      } else if constexpr (pos == 192 + 0) {
        asm volatile("s_waitcnt vmcnt(%2)" : "+v"(g0), "+v"(g1) : "i"(vq_wait(VK_GAT, 0, pos + 1)) : "memory");
        if (__builtin_expect(!__all(g0[1] == tag && g1[1] == tag), 0)) {
          const unsigned long long* stg = keys + SEQ + row0 + l32;
#pragma nounroll
          for (unsigned spin = 0; spin < (1u << 20) && !__all(g0[1] == tag && g1[1] == tag); ++spin) {
            __builtin_amdgcn_s_sleep(6);
            asm volatile("global_load_dwordx2 %0, %2, off sc1\n\tglobal_load_dwordx2 %1, %2, off offset:256 sc1\n\ts_waitcnt vmcnt(0)" : "=&v"(g0), "=&v"(g1) : "v"(stg) : "memory");
          }
        }
      } else if constexpr (pos == 192 + 2 || pos == 192 + 3) {
        constexpr int q = pos == 192 + 3;
        qf.a[q] = C * rcp_nr(bflo((q ? g1 : g0)[0]));
        asm volatile("" : "+v"(qf.a[q]));
      } else if constexpr (pos == 192 + 5 || pos == 192 + 25) {
        constexpr int q = pos == 192 + 25;
        const unsigned n = hi == 0u ? ((q ? g1 : g0)[0] >> 16) ^ 0x8000u : 0u;
        qf.nb[q] = u32x4{n | (n << 16), n, 0u, 0u};
        asm volatile("" : "+v"(qf.nb[q]));
      } else if constexpr (pos == 192 + 30) {
        const f32x4 cs = p.cs;
        asm volatile("ds_write_b128 %0, %1" :: "v"(csw + 0u), "v"(cs) : "memory");
      } else if constexpr (pos == 192 + 22) {
        unsigned a0 = 128u * wid + 64u * (lane & 1u) + 16u * (lane >> 4), a1 = 1024u + 16u * tid;
        asm volatile("" : "+v"(a0), "+v"(a1));
        csw = c.ldsb + QP_CS + ((lane & 14u) == 0u ? a0 : a1);
        asm volatile("" : "+v"(csw));
      }
    };
    asm volatile("" :: "s"(xn), "s"(Kc), "s"(Vc), "s"(O), "s"(wo), "s"(wg), "s"(wu), "s"(wd), "s"(wot), "s"(keys), "s"(ypart), "s"(out), "s"(w_r));
    vq_issue<-20>(vm); vq_issue<-19>(vm);
    asm volatile("s_branch 1f\n.Lps_ret: s_setpc_b64 vcc\n.Lps_tab:\n.irp n," PS_IDS "\ns_branch .Lps_\\n\n.endr\n1:" ::: "memory");
    p.vwb = c.ldsb + ((((lane & 1u) << 5) | ((lane & 2u) << 2)) | (((lane >> 2) & 3u) << 6) | (lane & 16u) | (hi << 8));
#pragma unroll
    for (int ks = 0; ks < 4; ++ks) p.vxb[ks] = c.ldsb + 0x4000u + 8192u * wid + 128u * l32 + 16u * ((2u * ks + hi) ^ ((l32 >> 1) & 7u));
    p.vwc = p.vwb + 512u * wid;
    { const unsigned on = (lane & 15u) == ((lane >> 4) & 1u) ? 0x3f803f80u : 0u; p.sel = u32x4{on, on, on, on}; }
    asm volatile("" : "+v"(p.vwb), "+v"(p.vwc), "+v"(p.vxb[0]), "+v"(p.vxb[1]), "+v"(p.vxb[2]), "+v"(p.vxb[3]), "+v"(p.sel));
    hook(IC(-1000 - (29))); hook(IC(-1000 - (31))); hook(IC(-1000 - (32 + 0))); hook(IC(-1000 - (32 + 1))); hook(IC(-1000 - (32 + 3))); hook(IC(-1000 - (32 + 5))); hook(IC(-1000 - (32 + 7))); hook(IC(-1000 - (32 + 9))); hook(IC(-1000 - (32 + 15))); hook(IC(-1000 - (32 + 17))); hook(IC(-1000 - (64 + 1)));
    GAP();
    asm volatile("s_waitcnt vmcnt(%4)" : "+v"(gen), "+v"(lj.a[0]), "+v"(lj.a[1]), "+v"(lj.a[2]) : "i"(vq_wait(VK_GEN, 0, -10)) : "memory");
    tag = rfl(gen) * 1664525u + 1013904223u;
    asm volatile("" : "+s"(tag));
    {
      auto part = [&](auto Q_) __attribute__((always_inline)) { constexpr int q = decltype(Q_)::value; vq_issue<q - 10>(vm); GAP(); ln_steps<5 * q>(lj, lrow, make_iseq<5>{}); GAP(); };
      for_seq(part, make_iseq<8>{});
    }
    vq_issue<-1>(vm);
    GAP();
    asm volatile("s_waitcnt vmcnt(%0)\n\ts_barrier" :: "i"(vq_wait(VK_S, 0, 0)) : "memory");
    qp_rds<0, 8>(p, make_iseq<20>{});
    qp_iters(p, vm, hook, lj, lrow, make_iseq<7>{});
    asm volatile("s_waitcnt lgkmcnt(5)" ::: "memory");
#pragma unroll
    for (int d = 0; d < 4; ++d) qf.ca[d] = u32x4{0u, 0u, 0u, 0u};
    GAP();
    qj_gaps(p, c, qf, vm, c.ldsb + QP_CS + 4u * l32, make_iseq<NJ>{});
  }
  {
    const unsigned on = (lane & 2u) == 0u ? 0x3f803f80u : 0u; c.ONE = u32x4{on, on, on, on}; c.IDX = (lane & 1u) ? 0xeeeeeeeeu : 0x44444444u;
    c.L[0] = c.L[1] = c.L[2] = c.L[3] = f32x4{0.f, 0.f, 0.f, 0.f};
    asm volatile("" : "+v"(c.ONE), "+v"(c.IDX), "+v"(c.L[0]), "+v"(c.L[1]), "+v"(c.L[2]), "+v"(c.L[3]));
  }
  unsigned rstate = 3u;
  auto route = [&]() __attribute__((always_inline)) {
    unsigned lq;
    asm volatile("v_mbcnt_lo_u32_b32 %0, -1, 0\n\tv_mbcnt_hi_u32_b32 %0, -1, %0" : "=v"(lq));
    const unsigned w = rfl(wid), kb_ = c.ldsb + MO_KEY + 4096u * w;
    bool req = rstate == 0u;
    if (rstate == 1u) {
      u32x4 g[4];
      asm volatile("ds_read_b128 %0, %4\n\tds_read_b128 %1, %4 offset:1024\n\tds_read_b128 %2, %4 offset:2048\n\tds_read_b128 %3, %4 offset:3072\n\ts_waitcnt lgkmcnt(0)"
                   : "=&v"(g[0]), "=&v"(g[1]), "=&v"(g[2]), "=&v"(g[3]) : "v"(kb_ + 16u * lq) : "memory");
      bool ok = true;
#pragma unroll
      for (int j = 0; j < 4; ++j) ok = ok && g[j][1] == tag && g[j][3] == tag;
      if (__all(ok)) {
        unsigned k[8] = {g[0][0], g[0][2], g[1][0], g[1][2], g[2][0], g[2][2], g[3][0], g[3][2]};
        ce(k[0], k[1]); ce(k[2], k[3]); ce(k[4], k[5]); ce(k[6], k[7]);
        ce(k[0], k[2]); ce(k[1], k[3]); ce(k[4], k[6]); ce(k[5], k[7]);
        ce(k[1], k[2]); ce(k[5], k[6]); ce(k[0], k[4]); ce(k[3], k[7]);
        ce(k[1], k[5]); ce(k[2], k[6]);
        ce(k[1], k[4]); ce(k[3], k[6]);
        ce(k[2], k[4]); ce(k[3], k[5]);
        ce(k[3], k[4]);
        unsigned cnd = 0u;
#pragma nounroll
        for (unsigned r = 0; r < (unsigned)NSEL; ++r) {
          unsigned m = k[0], t;
          t = (unsigned)__builtin_amdgcn_update_dpp(0, (int)m, 0xB1, 0xf, 0xf, false); m = t > m ? t : m;
          t = (unsigned)__builtin_amdgcn_update_dpp(0, (int)m, 0x4E, 0xf, 0xf, false); m = t > m ? t : m;
          t = (unsigned)__builtin_amdgcn_update_dpp(0, (int)m, 0x141, 0xf, 0xf, false); m = t > m ? t : m;
          t = (unsigned)__builtin_amdgcn_update_dpp(0, (int)m, 0x140, 0xf, 0xf, false); m = t > m ? t : m;
          cnd = (lq & 15u) == (unsigned)r ? m : cnd;
          const bool pp = k[0] == m;
#pragma unroll
          for (int i = 0; i < 7; ++i) k[i] = pp ? k[i + 1] : k[i];
        }
        lds_st32(c.ldsb + MO_RS + ((lq & 8u) == 0u ? 64u + 128u * w + 2u * (lq & 48u) + 4u * (lq & 7u) : 640u + 4u * lq), cnd);
        asm volatile("s_waitcnt lgkmcnt(0)" ::: "memory");
        lds_st32(c.ldsb + MO_RS + 4u * w, 1u);
        rstate = 2u;
      } else req = true;
    } else if (rstate == 2u) {
      u32x4 fl;
      asm volatile("ds_read_b128 %0, %1\n\ts_waitcnt lgkmcnt(0)" : "=v"(fl) : "v"(c.ldsb + MO_RS) : "memory");
      if (rfl(fl[0] & fl[1] & fl[2] & fl[3]) != 0u) {
        unsigned cv, rank = 0u;
        asm volatile("ds_read_b32 %0, %1" : "=v"(cv) : "v"(c.ldsb + MO_RS + 64u + 128u * w + 4u * (lq & 31u)) : "memory");
#pragma nounroll
        for (unsigned h = 0; h < 4u; ++h) {
          u32x4 c4v[4];
          asm volatile("ds_read_b128 %0, %5\n\tds_read_b128 %1, %5 offset:16\n\tds_read_b128 %2, %5 offset:32\n\tds_read_b128 %3, %5 offset:48\n\ts_waitcnt lgkmcnt(0)"
                       : "=&v"(c4v[0]), "=&v"(c4v[1]), "=&v"(c4v[2]), "=&v"(c4v[3]), "+v"(cv) : "v"(c.ldsb + MO_RS + 64u + 256u * (lq >> 5) + 64u * h) : "memory");
#pragma unroll
          for (int j = 0; j < 4; ++j)
#pragma unroll
            for (int e = 0; e < 4; ++e) rank += c4v[j][e] > cv ? 1u : 0u;
        }
        auto pr = __builtin_amdgcn_permlane32_swap(rank, rank, false, false);
        rank = pr[0] + pr[1];
        {
          const float rs = rbf(__builtin_amdgcn_rcpf(1.f + __builtin_amdgcn_exp2f(key_val(cv) * -1.4426950408889634f)));
          const u32x2 gr = {(__float_as_uint(rs) & 0xffff0000u) | (unsigned)key_row(cv), tag};
          const unsigned vo = lq < 32u && rank < 8u ? 8u * rank : 0x40000000u, vg = (lq | w) == 0u ? 4u * H_G : 0x40000000u;
          asm volatile("s_nop 4\n\tbuffer_store_dwordx2 %0, %1, %3, 0 offen sc1\n\tbuffer_store_dword %4, %2, %3, 0 offen sc1\n\ts_nop 1" :: "v"(gr), "v"(vo), "v"(vg), "s"(mk_srd(sel, 8192u)), "v"(gr[1]) : "memory");
        }
        rstate = 3u;
      }
    }
    if (req) {
      const __amdgpu_buffer_rsrc_t srdK = mk_srd((keys + 512u * w), 4096);
#pragma unroll
      for (int j = 0; j < 4; ++j)
        asm volatile("s_mov_b32 m0, %0\n\ts_nop 4\n\tbuffer_load_dwordx4 %1, %2, %3 offen sc1 lds" :: "s"(kb_ + 1024u * j), "v"(16u * lq), "s"(srdK), "s"(1024u * j) : "m0", "memory");
      rstate = 1u;
    }
  };
  const unsigned wids = rfl(wid);
  auto opq = [&](unsigned v) __attribute__((always_inline)) { asm volatile("" : "+s"(v)); return v; };
  auto lane_id = [&]() __attribute__((always_inline)) { unsigned lq; asm volatile("v_mbcnt_lo_u32_b32 %0, -1, 0\n\tv_mbcnt_hi_u32_b32 %0, -1, %0" : "=v"(lq)); return lq; };
  auto hand_req = [&](const unsigned m0, const unsigned word0, const unsigned nb) __attribute__((always_inline)) {
    const unsigned vo = c.vksrc[0];
    asm volatile("s_mov_b32 m0, %0\n\ts_nop 0\n\tbuffer_load_dwordx4 %1, %2, 0 offen sc1 lds" :: "s"(m0), "v"(vo), "s"(mk_srd(sel + word0, nb)) : "m0", "memory");
  };
  auto flag1 = [&](const unsigned word, const unsigned on) __attribute__((always_inline)) {
    const unsigned vo = c.vvsrc, tg = tag;
    asm volatile("buffer_store_dword %0, %1, %2, 0 offen sc1" :: "v"(tg), "v"(vo), "s"(mk_srd(sel + word, on)) : "memory");
  };
  auto pf1 = [&]() __attribute__((always_inline)) {
    asm volatile("s_mov_b32 m0, %0\n\ts_add_u32 %1, %1, %4\n\tbuffer_load_dwordx4 %2, %3, %1 offen sc1 lds\n\ts_add_u32 %0, %0, 0x400"
                 : "+s"(c.pfm0), "+s"(c.pfo) : "v"(c.pfv), "s"(c.srdP), "s"(c.pfst) : "m0", "memory", "scc");
  };
  auto m0_set = [&](const unsigned m0) __attribute__((always_inline)) { asm volatile("s_mov_b32 m0, %0" :: "s"(m0) : "m0", "memory"); };
  auto dma16 = [&](const __amdgpu_buffer_rsrc_t sr, const unsigned vo) __attribute__((always_inline)) { asm volatile("buffer_load_dwordx4 %0, %1, 0 offen sc1 lds" :: "v"(vo), "s"(sr) : "memory"); };
  auto flag_st = [&](const __amdgpu_buffer_rsrc_t sr, const unsigned tg) __attribute__((always_inline)) { const unsigned vo = c.vvsrc; asm volatile("buffer_store_dword %0, %1, %2, 0 offen sc1" :: "v"(tg), "v"(vo), "s"(sr) : "memory"); };
  struct CW { unsigned long long pa; unsigned so; __amdgpu_buffer_rsrc_t sr; };
  auto warm_step = [&](CW& k, auto I_) __attribute__((always_inline)) {
    constexpr int I = decltype(I_)::value;
    if constexpr (I == 0) asm volatile("s_getpc_b64 s[20:21]\n\ts_add_u32 s20, s20, k_attn@rel32@lo+4\n\ts_addc_u32 s21, s21, k_attn@rel32@hi+12\n\ts_mov_b64 %0, s[20:21]" : "=s"(k.pa) :: "scc", "s20", "s21");
    if constexpr (I == 1) { unsigned nb; asm volatile("s_mov_b32 %0, k_attn_end-k_attn" : "=s"(nb)); k.sr = __builtin_amdgcn_make_buffer_rsrc((void*)k.pa, (short)0, (int)nb, SRD_WORD3); asm volatile("" : "+s"(k.sr)); }
    if constexpr (I == 2) { k.so = (opq(blk) >> 3) * 4096u + (c.m0k - c.ldsb) / 4u; asm volatile("" : "+s"(k.so)); }
    if constexpr (I == 3) m0_set(c.pdum);
    if constexpr (I == 4) { const unsigned vo = c.vksrc[0], so = k.so; const __amdgpu_buffer_rsrc_t sr = k.sr; asm volatile("buffer_load_dwordx4 %0, %1, %2 offen lds" :: "v"(vo), "s"(sr), "s"(so) : "memory"); }
  };
  auto warm_hook = [&](CW& k, auto M_) __attribute__((always_inline)) {
    constexpr int M = decltype(M_)::value, i = M == FB(0) ? 0 : M == FB(1) ? 1 : M == FB(2) ? 2 : M == 39 ? 3 : M == 40 ? 4 : -1;
    if constexpr (i >= 0) warm_step(k, std::integral_constant<int, i>{});
  };
  { unsigned w = rfl(wid); asm volatile("" : "+s"(w));  }
  GAP();
  phaseA_end<2>(c);
  {
    CW k;
    auto hook = [&](auto M_) __attribute__((always_inline)) { warm_hook(k, M_); };
    phaseB_<1, true, true, 2>(c, hook, make_iseq<NGB>{});
  }
  GAP();
  if (__builtin_expect(opq(blk) == RTB, 0)) {
    PS_AT(44);
    {
      const unsigned w = opq(wids), kb_ = c.ldsb + MO_KEY + 4096u * w;
      RTS r; __amdgpu_buffer_rsrc_t sk;
      auto hookA2 = [&](auto M_) __attribute__((always_inline)) {
        constexpr int M = decltype(M_)::value, n = fa_of(M);
        if constexpr (n == 0) { r.lq = lane_id(); asm volatile("" : "+v"(r.lq)); }
        if constexpr (n == 1) { sk = mk_srd(keys + 512u * w, 4096); asm volatile("" : "+s"(sk)); }
        if constexpr (n == 2) { r.va = 16u * r.lq; m0_set(kb_); asm volatile("" : "+v"(r.va)); }
        if constexpr (n == 4 || n == 5 || n == 7 || n == 9) {
          constexpr int j = n == 4 ? 0 : n == 5 ? 1 : n == 7 ? 2 : 3;
          const unsigned v = r.va; const __amdgpu_buffer_rsrc_t sr = sk;
          asm volatile("buffer_load_dwordx4 %0, %1, 0 offen offset:%c2 sc1 lds" :: "v"(v), "s"(sr), "i"(1024 * j) : "memory");
        }
      };
      phaseAh<0, 7>(c, 2, hookA2);
      phaseB<0, 2>(c);
      auto hookA3 = [&](auto M_) __attribute__((always_inline)) { if constexpr (decltype(M_)::value == FA(0)) { r.va += kb_; asm volatile("" : "+v"(r.va)); } };
      phaseAh<1, 2>(c, 3, hookA3);
      auto steps = [&](auto I0_, auto G0_, auto M_) __attribute__((always_inline)) {
        constexpr int M = decltype(M_)::value, I = decltype(I0_)::value + M - 100 - decltype(G0_)::value;
        if constexpr (M >= 100 + decltype(G0_)::value && I < RT_END) rt_step<I>(r, tag);
      };
      auto hookB3 = [&](auto M_) __attribute__((always_inline)) {
        constexpr int M = decltype(M_)::value;
        if constexpr (M == 2 || M == 4 || M == 5 || M == 7) {
          constexpr int j = M == 2 ? 0 : M == 7 ? 3 : M - 3;
          u32x4& d = r.g[j]; const unsigned v = r.va;
          asm volatile("ds_read_b128 %0, %1 offset:%2" : "=v"(d) : "v"(v), "i"(1024 * j) : "memory");
        }
        if constexpr (M == FB(0)) { u32x4 (&g)[4] = r.g; asm volatile("s_waitcnt lgkmcnt(%4)" : "+v"(g[0]), "+v"(g[1]), "+v"(g[2]), "+v"(g[3]) : "i"(kyo(7, FB(0))) : "memory"); }
        steps(IC(0), IC(FB(0)), M_);
      };
      phaseBh<1, 2>(c, hookB3);
      auto hookA4 = [&](auto M_) __attribute__((always_inline)) {
        constexpr int M = decltype(M_)::value;
        steps(IC(NGB - FB(0)), IC(0), M_);
        if constexpr (M == FA(6)) { r.va = (r.lq & 8u) == 0u ? 64u + 128u * w + 2u * (r.lq & 48u) + 4u * (r.lq & 7u) : 640u + 4u * r.lq; asm volatile("" : "+v"(r.va)); }
        if constexpr (M == FA(8)) lds_st32(c.ldsb + MO_RS + r.va, r.cnd);
        if constexpr (M == FA(9)) lds_st32(c.ldsb + MO_RS + 4u * w, __all(r.bad == 0u) ? 1u : 0u);
        if constexpr (M == FA(10)) { r.va = c.ldsb + MO_RS + 64u + 256u * (r.lq >> 5); r.m = c.ldsb + MO_RS + 64u + 128u * w + 4u * (r.lq & 31u); r.rank = 0u; asm volatile("" : "+v"(r.va), "+v"(r.m), "+v"(r.rank)); }
      };
      static_assert(NGB - FB(0) + FA(6) >= RT_END, "");
      phaseAh<0, 3>(c, 4, hookA4);
      auto hookB4 = [&](auto M_) __attribute__((always_inline)) {
        constexpr int M = decltype(M_)::value;
        if constexpr (M == 0) { unsigned& d = r.cv; u32x4& f = r.g[3]; const unsigned v = r.m, v2 = c.ldsb + MO_RS; asm volatile("ds_read_b32 %0, %2\n\tds_read_b128 %1, %3" : "=&v"(d), "=&v"(f) : "v"(v), "v"(v2) : "memory"); }
        if constexpr (M < 60 && M % 4 == 0) { u32x4& d = r.g[(M >> 2) % 3]; const unsigned v = r.va; asm volatile("ds_read_b128 %0, %1 offset:%2" : "=v"(d) : "v"(v), "i"(4 * M) : "memory"); }
        if constexpr (M >= 5 && M < 65 && M % 4 == 1) { u32x4 &d = r.g[((M - 5) >> 2) % 3], &f = r.g[3]; unsigned& cv = r.cv; asm volatile("s_waitcnt lgkmcnt(%3)" : "+v"(d), "+v"(cv), "+v"(f) : "i"(kyo(M - 5, M) + (M < 61)) : "memory"); }
        if constexpr (M >= 105 && M < 165) { unsigned long long& pm = r.pm[M & 1]; const unsigned a = r.g[((M - 105) >> 2) % 3][(M - 105) & 3], cv = r.cv; asm volatile("v_cmp_gt_u32_e64 %0, %1, %2" : "=s"(pm) : "v"(a), "v"(cv)); }
        if constexpr (M >= 106 && M < 166) cnt_s(r.rank, r.pm[(M - 1) & 1]);
        if constexpr (M == 165) { r.bad = r.g[3][0] & r.g[3][1] & r.g[3][2] & r.g[3][3]; asm volatile("" : "+v"(r.bad)); }
        if constexpr (M == 66) { u32x4& d = r.g[0]; const unsigned v = r.va; asm volatile("ds_read_b128 %0, %1 offset:240" : "=v"(d) : "v"(v) : "memory"); }
      };
      phaseBh<0, 2>(c, hookB4);
      auto hookA5 = [&](auto M_) __attribute__((always_inline)) {
        constexpr int M = decltype(M_)::value;
        if constexpr (M == 4) { u32x4& d = r.g[0]; asm volatile("s_waitcnt lgkmcnt(3)" : "+v"(d) :: "memory"); }
        if constexpr (M >= 104 && M < 108) { r.rank += r.g[0][M - 104] > r.cv ? 1u : 0u; asm volatile("" : "+v"(r.rank)); }
        if constexpr (M == FA(1)) { auto pr = __builtin_amdgcn_permlane32_swap(r.rank, r.rank, false, false); r.rank = pr[0] + pr[1]; asm volatile("" : "+v"(r.rank)); }
        if constexpr (M == FA(2)) { r.rs = key_val(r.cv) * -1.4426950408889634f; asm volatile("" : "+v"(r.rs)); }
        if constexpr (M == FA(3)) { r.rs = 1.f + __builtin_amdgcn_exp2f(r.rs); asm volatile("" : "+v"(r.rs)); }
        if constexpr (M == FA(4)) { r.rs = rbf(__builtin_amdgcn_rcpf(r.rs)); asm volatile("" : "+v"(r.rs)); }
        if constexpr (M == FA(5)) { r.m = (__float_as_uint(r.rs) & 0xffff0000u) | (unsigned)key_row(r.cv); r.bad = (r.bad - 1u) & 0x40000000u; asm volatile("" : "+v"(r.m), "+v"(r.bad)); }
        if constexpr (M == FA(7)) { r.va = (r.lq < 32u && r.rank < 8u ? 8u * r.rank : 0x40000000u) | r.bad; asm volatile("" : "+v"(r.va)); }
        if constexpr (M == 100 + FA(8)) { r.cnd = ((r.lq | w) == 0u ? 4u * H_G : 0x40000000u) | r.bad; sk = mk_srd(sel, 8192u); asm volatile("" : "+v"(r.cnd), "+s"(sk)); }
        if constexpr (M == FA(9)) {
          const u32x2 gr = {r.m, tag}; const unsigned vo = r.va, vg = r.cnd; const __amdgpu_buffer_rsrc_t sr = sk;
          asm volatile("buffer_store_dwordx2 %0, %1, %3, 0 offen sc1\n\tbuffer_store_dword %4, %2, %3, 0 offen sc1" :: "v"(gr), "v"(vo), "v"(vg), "s"(sr), "v"(gr[1]) : "memory");
        }
      };
      phaseAh<1, 4>(c, 5, hookA5);
      rstate = rfl(r.bad) ? 0u : 3u;
      if (rstate == 0u) lds_st32(c.ldsb + MO_RS + 4u * w, 0u);
      phaseB<1, 2>(c);
    }
#pragma nounroll
    for (int t = 6; t < 14; t += 2) {
      phaseA<0, 3>(c, t);     phaseB<0, 2>(c);
      phaseA<1, 2>(c, t + 1); phaseB<1, 2>(c);
      if (rstate != 3u) {
        route();
        if (t == 12)
#pragma nounroll
          for (unsigned spin = 0; spin < (1u << 20) && rstate != 3u; ++spin) {
            asm volatile("s_waitcnt vmcnt(0)" ::: "memory");
            route();
            __builtin_amdgcn_s_sleep(8);
          }
      }
      GAP();
    }
  }
  GAP();
  {
    const unsigned b = opq(blk);
    const int t1 = 12 - 6 * (int)((b - 144u) >> 31) - 4 * (int)(((b - 208u) & ~(b - 144u)) >> 31) - 12 * (int)(((b ^ RTB) - 1u) >> 31);
#pragma nounroll
    for (int t = 0; __builtin_expect(t != t1, 1);) {
      t += 2;
      phaseA<0, 3>(c, t);     phaseB<0, 2>(c);
      phaseA<1, 2>(c, t + 1); phaseB<1, 2>(c);
    }
  }
  GAP();
  constexpr auto SG = [](int i) constexpr { constexpr int g[10] = {14, 20, 26, 31, 36, 41, 46, 52, 57, 61}; return g[i + 1]; };
  constexpr auto sg_of = [](int m) constexpr { constexpr int g[10] = {14, 20, 26, 31, 36, 41, 46, 52, 57, 61}; for (int i = 0; i < 10; ++i) if (g[i] == m) return i - 1; return -2; };
  if (__builtin_expect(opq(blk) < 144u, 1)) {
    PS_AT(40);
    const unsigned w = opq(wids), b = opq(blk), fbb = (b & 7u) * 18u + (b >> 3);
    phaseA<0, 3>(c, 8);
    {
      __amdgpu_buffer_rsrc_t sr; unsigned m0;
      auto hook = [&](auto M_) __attribute__((always_inline)) {
        constexpr int M = decltype(M_)::value;
        if constexpr (M == FB(1)) { sr = mk_srd(sel, 64u); asm volatile("" : "+s"(sr)); }
        if constexpr (M == FB(2)) { m0 = c.ldsb + MO_C; asm volatile("" : "+s"(m0)); }
        if constexpr (M == 39) m0_set(m0);
        if constexpr (M == 40) dma16(sr, c.vksrc[0]);
      };
      phaseBh<0, 2>(c, hook);
    }
    {
      unsigned va, vo; u32x2 sv; __amdgpu_buffer_rsrc_t srdX;
      constexpr auto yo = [](int g0, int g1) constexpr { return 2 * (pcnt(PA0, 'T', g1) - pcnt(PA0, 'T', g0)) + pcnt(PA0, 'X', g1) - pcnt(PA0, 'X', g0); };
      auto hook = [&](auto M_) __attribute__((always_inline)) {
        constexpr int M = decltype(M_)::value;
        if constexpr (M == FA(0)) { va = c.ldsb + MO_C + ((c.vksrc[0] >> 1) & 0x38u); asm volatile("" : "+v"(va)); }
        if constexpr (M == FA(2)) { vo = ((c.vksrc[0] >> 1) & 64u) | ((c.vksrc[0] >> 4) & 0x30u); asm volatile("" : "+v"(vo)); }
        if constexpr (M == FA(4)) { srdX = mk_srd(xn, SEQ * DM * 2); asm volatile("" : "+s"(srdX)); }
        if constexpr (M == FA(5)) asm volatile("s_waitcnt vmcnt(2)\n\tds_read_b64 %0, %1" : "=v"(sv) : "v"(va) : "memory");
        if constexpr (M == 100 + FA(6)) {
          asm volatile("s_waitcnt lgkmcnt(%1)" : "+v"(sv) : "i"(yo(FA(5), FA(6))) : "memory");
          if (__builtin_expect(!__all(sv[1] == tag), 0)) {
#pragma nounroll
            for (unsigned spin = 0; spin < (1u << 20) && !__all(sv[1] == tag); ++spin) {
              __builtin_amdgcn_s_sleep(8);
              hand_req(c.ldsb + MO_C, 0u, 64u);
              asm volatile("s_waitcnt vmcnt(0)\n\tds_read_b64 %0, %1\n\ts_waitcnt lgkmcnt(0)" : "=v"(sv) : "v"(va) : "memory");
            }
          }
        }
        if constexpr (M == FA(7)) { vo += (sv[0] & 0xffffu) * XRS; asm volatile("" : "+v"(vo)); }
        if constexpr (M == 100 + FA(8) || M == FA(9) || M == FA(10)) {
          constexpr unsigned i = M == FA(9) ? 1 : M == FA(10) ? 2 : 0;
          const unsigned v = vo, m0 = c.ldsb + MO_XR + 1024u * w + 4096u * i, so = 128u * w + 512u * i; const __amdgpu_buffer_rsrc_t sx = srdX;
          asm volatile("s_mov_b32 m0, %0\n\ts_nop 0\n\tbuffer_load_dwordx4 %1, %2, %3 offen sc1 lds" :: "s"(m0), "v"(v), "s"(sx), "s"(so) : "m0", "memory");
        }
      };
      phaseAh<1, 5>(c, 9, hook);
    }
    phaseB<1, 0>(c);
    f32x4 gu; unsigned vw, vx;
    auto gate_up = [&](auto H_) __attribute__((always_inline)) {
      constexpr int HH = decltype(H_)::value;
      bf16x4 wl[2], wh[2]; bf16x8 xf[2];
      auto hook = [&](auto M_) __attribute__((always_inline)) {
        constexpr int M = decltype(M_)::value, j = sg_of(M % 100);
        if constexpr (M < 100 && j >= 0) asm volatile("s_waitcnt lgkmcnt(%3)" : "+v"(wl[j & 1]), "+v"(wh[j & 1]), "+v"(xf[j & 1]) : "i"(kyo(SG(j - 1) + 1, M)) : "memory");
        if constexpr (M >= 100 && j >= 0) {
          const bf16x8 wfr = __builtin_shufflevector(wl[j & 1], wh[j & 1], 0, 1, 2, 3, 4, 5, 6, 7);
          if constexpr (HH == 0 && j == 0) asm volatile("mf16 %0, %1, %2, 0" : "=&v"(gu) : "v"(wfr), "v"(xf[j & 1]));
          else                             asm volatile("mf16 %0, %1, %2, %0" : "+v"(gu) : "v"(wfr), "v"(xf[j & 1]));
        }
        if constexpr (M >= 100 && j >= -1 && j < 8) {
          constexpr int n = (j + 1) & 1, i = 9 * HH + j + 1;
          asm volatile("ds_read_b64_tr_b16 %0, %1 offset:%2" : "=v"(wl[n]) : "v"(vw), "i"(1024 * i) : "memory");
          asm volatile("ds_read_b64_tr_b16 %0, %1 offset:%2" : "=v"(wh[n]) : "v"(vw), "i"(1024 * i + 256) : "memory");
          asm volatile("ds_read_b128 %0, %1 offset:%2" : "=v"(xf[n]) : "v"(vx), "i"(1024 * (i >> 1) + 128 * (i & 1)) : "memory");
        }
        if constexpr (HH == 1) {
          if constexpr (M == DB(8) || M == DB(9) || M == FB(6) || M == FB(8)) pf1();
          if constexpr (M == FB(9)) { vx += 1024u * w; asm volatile("" : "+v"(vx)); }
          if constexpr (M == 72) { const f32x4 g4 = gu; asm volatile("ds_write_b128 %0, %1 offset:%2" :: "v"(vx), "v"(g4), "i"(MO_H - MO_XR) : "memory"); }
        }
      };
      phaseBh<HH, 0>(c, hook);
    };
    {
      auto hook = [&](auto M_) __attribute__((always_inline)) {
        constexpr int M = decltype(M_)::value;
        if constexpr (M == FA(0)) { vw = c.vvb[0] & 0xffu; vx = c.vvb[0] & 32u; asm volatile("" : "+v"(vw), "+v"(vx)); }
        if constexpr (M == FA(1)) { vw = (vw + 16u * vx) ^ (32u * (w & 1u)); asm volatile("" : "+v"(vw)); }
        if constexpr (M == FA(2)) { vx = 9u * (c.vvb[0] & 2048u); asm volatile("" : "+v"(vx)); }
        if constexpr (M == FA(3)) { vw += vx + (c.ldsb + SD_BASE + 36864u * (w >> 1)); asm volatile("" : "+v"(vw)); }
        if constexpr (M == FA(4)) { vx = c.vksrc[0] + (c.ldsb + MO_XR); asm volatile("" : "+v"(vx)); }
      };
      phaseAh<0, 1>(c, 10, hook);
    }
    gate_up(std::integral_constant<int, 0>{});
    {
      unsigned lq;
      auto hook = [&](auto M_) __attribute__((always_inline)) {
        constexpr int M = decltype(M_)::value;
        if constexpr (M == FA(0)) { lq = lane_id(); asm volatile("" : "+v"(lq)); }
        if constexpr (M == FA(2)) { c.pfv = 16u * (lq & 3u); asm volatile("" : "+v"(c.pfv)); }
        if constexpr (M == FA(4)) { c.pfv += XRS * ((lq >> 2) & 3u); asm volatile("" : "+v"(c.pfv)); }
        if constexpr (M == FA(5)) { c.pfv += 2u * (lq & 32u); asm volatile("" : "+v"(c.pfv)); }
        if constexpr (M == FA(9)) { c.pfv += XRS * (lq & 16u); asm volatile("" : "+v"(c.pfv)); }
        if constexpr (M == 3) { c.srdP = mk_srd(wd, DM * FF * 2); asm volatile("" : "+s"(c.srdP)); }
        if constexpr (M == FA(1)) { c.pfo = 32u * fbb + 4u * w; asm volatile("" : "+s"(c.pfo)); }
        if constexpr (M == FA(3)) { c.pfst = 128u; c.pfo = XRS * c.pfo - 128u; asm volatile("" : "+s"(c.pfst), "+s"(c.pfo)); }
        if constexpr (M == FA(7)) { c.pfm0 = c.ldsb + SD_BASE + 18432u * w; asm volatile("" : "+s"(c.pfm0)); }
      };
      phaseAh<1, 0>(c, 11, hook);
    }
    gate_up(std::integral_constant<int, 1>{});
    {
      auto hook = [&](auto M_) __attribute__((always_inline)) { constexpr int M = decltype(M_)::value; if constexpr (M == FA(0) || M == FA(2) || M == FA(4) || M == FA(5) || M == FA(9)) pf1(); };
      phaseAh<0, 10>(c, 12, hook);
    }
    {
      unsigned vh; f32x2 g, u, s;
      auto hook = [&](auto M_) __attribute__((always_inline)) {
        constexpr int M = decltype(M_)::value;
        if constexpr (M == 109) { vh = c.vksrc[0] + (c.ldsb + MO_H + 1024u * (w >> 1) + 8u * (w & 1u)); asm volatile("" : "+v"(vh)); }
        if constexpr (M == FB(0)) asm volatile("ds_read_b64 %0, %2\n\tds_read_b64 %1, %2 offset:2048" : "=&v"(g), "=&v"(u) : "v"(vh) : "memory");
        if constexpr (M == 116) { vh = c.vksrc[0] + (c.ldsb + MO_HH + 4u * w); asm volatile("" : "+v"(vh)); }
        if constexpr (M == FB(1)) {
          asm volatile("s_waitcnt lgkmcnt(%2)" : "+v"(g), "+v"(u) : "i"(kyo(FB(0), FB(1))) : "memory");
          const unsigned pg = cvtpk(g[0], g[1]);
          g[0] = bflo(pg); g[1] = bfhi(pg);
          asm volatile("" : "+v"(g));
        }
        if constexpr (M == FB(2)) { const unsigned pu = cvtpk(u[0], u[1]); u[0] = bflo(pu); u[1] = bfhi(pu); asm volatile("" : "+v"(u)); }
        if constexpr (M == FB(3)) { s[0] = g[0] * -1.4426950408889634f; s[1] = g[1] * -1.4426950408889634f; asm volatile("" : "+v"(s)); }
        if constexpr (M == DB(8) || M == FB(4)) { constexpr int e = M == FB(4); s[e] = __builtin_amdgcn_exp2f(s[e]); asm volatile("" : "+v"(s)); }
        if constexpr (M == DB(9)) { float s0 = s[0], s1 = s[1]; add_v(s0, 1.f); add_v(s1, 1.f); s = f32x2{s0, s1}; }
        if constexpr (M == FB(5) || M == FB(6)) { constexpr int e = M == FB(6); s[e] = __builtin_amdgcn_rcpf(s[e]); asm volatile("" : "+v"(s)); }
        if constexpr (M == FB(7)) { const unsigned ps = cvtpk(g[0] * s[0], g[1] * s[1]); s[0] = __uint_as_float(ps); asm volatile("" : "+v"(s)); }
        if constexpr (M == FB(8)) { const unsigned ps = __float_as_uint(s[0]); u[0] *= bflo(ps); u[1] *= bfhi(ps); asm volatile("" : "+v"(u)); }
        if constexpr (M == FB(9)) { const unsigned hv = cvtpk(u[0], u[1]); asm volatile("ds_write_b32 %0, %1" :: "v"(vh), "v"(hv) : "memory"); }
      };
      phaseBh<0, 0>(c, hook);
    }
    {
      unsigned vd, vh, vyo, lq;
      __amdgpu_buffer_rsrc_t srdYs;
      bf16x4 dl[2], dh[2]; f32x4 y[2]; u32x4 hwv;
      auto hookA = [&](auto M_) __attribute__((always_inline)) {
        constexpr int M = decltype(M_)::value;
        if constexpr (M == FA(0)) { vh = c.vksrc[0] + (c.ldsb + MO_HH); vd = c.vksrc[0] & 0x300u; asm volatile("" : "+v"(vh), "+v"(vd)); }
        if constexpr (M == FA(1)) { vd = 72u * vd + (c.vvb[0] & 0xdfu); asm volatile("" : "+v"(vd)); }
        if constexpr (M == FA(2)) { vd += c.ldsb + SD_BASE + 512u * (w >> 1) + 32u * (w & 1u); asm volatile("" : "+v"(vd)); }
        if constexpr (M == FA(3)) { lq = lane_id(); asm volatile("" : "+v"(lq)); }
        if constexpr (M == FA(4)) { vyo = (lq & 48u) << 3; asm volatile("" : "+v"(vyo)); }
        if constexpr (M == FA(5)) { vyo |= (lq & 7u) << 4; asm volatile("" : "+v"(vyo)); }
        if constexpr (M == FA(7)) { vyo |= (lq & 8u) << 27; asm volatile("" : "+v"(vyo)); }
        if constexpr (M == FA(9)) { srdYs = mk_srd((const char*)ypart + fbb * 18432u, 18432u); asm volatile("" : "+s"(srdYs)); }
      };
      phaseAh<1, 0>(c, 13, hookA);
      auto hook = [&](auto M_) __attribute__((always_inline)) {
        constexpr int M = decltype(M_)::value, i = sg_of(M % 100), is = sg_of(M - 3);
        if constexpr (M == FB(0)) asm volatile("ds_read_b128 %0, %1" : "=v"(hwv) : "v"(vh) : "memory");
        if constexpr (M < 100 && i >= 0) asm volatile("s_waitcnt lgkmcnt(%3)" : "+v"(dl[i & 1]), "+v"(dh[i & 1]), "+v"(hwv) : "i"(kyo(SG(i - 1) + 1, M)) : "memory");
        if constexpr (M >= 100 && i >= 0) {
          const bf16x8 d8 = __builtin_shufflevector(dl[i & 1], dh[i & 1], 0, 1, 2, 3, 4, 5, 6, 7);
          asm volatile("mf16 %0, %1, %2, 0" : "=&v"(y[i & 1]) : "v"(d8), "v"(hwv));
        }
        if constexpr (M >= 100 && i >= -1 && i < 8) {
          asm volatile("ds_read_b64_tr_b16 %0, %1 offset:%2" : "=v"(dl[(i + 1) & 1]) : "v"(vd), "i"(1024 * (i + 1)) : "memory");
          asm volatile("ds_read_b64_tr_b16 %0, %1 offset:%2" : "=v"(dh[(i + 1) & 1]) : "v"(vd), "i"(1024 * (i + 1) + 256) : "memory");
        }
        if constexpr (M < 100 && is >= 0) {
          const unsigned vo = vyo; const __amdgpu_buffer_rsrc_t sr = srdYs; const f32x4 yv = y[is & 1]; const unsigned so = 512u * w + 2048u * is;
          asm volatile("buffer_store_dwordx4 %0, %1, %2, %3 offen sc1\n\ts_nop 1" :: "v"(yv), "v"(vo), "s"(sr), "s"(so) : "memory");
        }
      };
      phaseBh<1, 0>(c, hook);
    }
  }
  GAP();
  PS_AT(41);
  if (__builtin_expect(opq(blk) - 144u < 64u, 0)) {
    PS_AT(42);
    const unsigned w = opq(wids), b = opq(blk), job = (b - 144u) * 4u + w;
    constexpr unsigned NTS = 128u * 4608u;
    unsigned vw[2], v0, v1, vs;
    __amdgpu_buffer_rsrc_t srdT;
    bf16x4 wl[2], wh[2];
    auto rd = [&](auto I_) __attribute__((always_inline)) {
      constexpr int i = decltype(I_)::value;
      bf16x4 &l = wl[i & 1], &h = wh[i & 1]; const unsigned v = vw[i & 1];
      asm volatile("ds_read_b64_tr_b16 %0, %1 offset:%2" : "=v"(l) : "v"(v), "i"(1024 * (i >> 1)) : "memory");
      asm volatile("ds_read_b64_tr_b16 %0, %1 offset:%2" : "=v"(h) : "v"(v), "i"(1024 * (i >> 1) + 256) : "memory");
    };
    auto st = [&](auto I_, auto Y_) __attribute__((always_inline)) {
      constexpr int i = decltype(I_)::value, u = 2 * i, r = u % 9;
      constexpr auto at = [](int x) constexpr { return (unsigned)(x / 9) * NTS + 2048u * ((x % 9) >> 2) + 64u * ((x % 9) & 3); };
      asm volatile("s_waitcnt lgkmcnt(%2)" : "+v"(wl[i & 1]), "+v"(wh[i & 1]) : "i"(decltype(Y_)::value) : "memory");
      const u32x4 x = __builtin_bit_cast(u32x4, __builtin_shufflevector(wl[i & 1], wh[i & 1], 0, 1, 2, 3, 4, 5, 6, 7));
      const __amdgpu_buffer_rsrc_t sr = srdT; const unsigned up = v1;
      const unsigned vo = r == 8 ? v0 | ((up ^ 1u) << 30) : r == 7 ? v0 | (up << 30) : v0 + up * ((r & 3) == 3 ? 1856u : 64u);
      asm volatile("buffer_store_dwordx4 %0, %1, %2, %3 offen sc1\n\ts_nop 1" :: "v"(x), "v"(vo), "s"(sr), "s"(at(r == 8 ? u + 1 : u)) : "memory");
      if constexpr (r >= 7) {
        const u32x2 e = {x[0], x[2]}, o2 = {x[1], x[3]}; const unsigned v2 = vs | ((r == 8 ? up : up ^ 1u) << 30);
        asm volatile("buffer_store_dwordx2 %0, %2, %3, %4 offen sc1\n\tbuffer_store_dwordx2 %1, %2, %3, %4 offen offset:128 sc1" :: "v"(e), "v"(o2), "v"(v2), "s"(sr), "s"((unsigned)(u / 9) * NTS) : "memory");
      }
    };
    auto hookB = [&](auto I0_, auto M_) __attribute__((always_inline)) {
      constexpr int M = decltype(M_)::value, i0 = decltype(I0_)::value;
      constexpr auto fr = [](int g) constexpr { return fb_of(g) >= 0 || g == DB(8) || g == DB(9); };
      constexpr auto cnt = [=](int g) constexpr { int n = 0; for (int x = 0; x < g; ++x) n += fr(x); return n; };
      constexpr auto prev = [=](int g) constexpr { int x = g - 1; while (x > 0 && !fr(x)) --x; return x; };
      if constexpr (M < NGB) { if constexpr (fr(M)) {
        if constexpr (cnt(M) % 2 == 0) rd(std::integral_constant<int, i0 + cnt(M) / 2>{});
        else st(std::integral_constant<int, i0 + cnt(M) / 2>{}, std::integral_constant<int, kyo(prev(M), M)>{});
      } }
    };
    {
      auto hook = [&](auto M_) __attribute__((always_inline)) {
        constexpr int M = decltype(M_)::value;
        if constexpr (M == FA(0)) { vw[0] = c.vvb[0] & 0xffu; v0 = c.vvb[0] & 32u; asm volatile("" : "+v"(vw[0]), "+v"(v0)); }
        if constexpr (M == FA(1)) { vw[0] += 16u * v0; v0 = c.vvb[0] & 2048u; asm volatile("" : "+v"(vw[0]), "+v"(v0)); }
        if constexpr (M == FA(2)) { vw[0] += 9u * v0; asm volatile("" : "+v"(vw[0])); }
        if constexpr (M == FA(3)) { vw[0] += c.ldsb + SD_BASE + 36864u * (w >> 1) + 9216u * (w & 1u); asm volatile("" : "+v"(vw[0])); }
        if constexpr (M == FA(4)) { v1 = lane_id(); asm volatile("" : "+v"(v1)); }
        if constexpr (M == FA(5)) { vw[1] = vw[0] ^ 32u; v0 = (v1 & 7u) << 4; asm volatile("" : "+v"(vw[1]), "+v"(v0)); }
        if constexpr (M == FA(7)) { vs = v0 | ((v1 & 32u) << 3) | ((v1 & 16u) >> 1); asm volatile("" : "+v"(vs)); }
        if constexpr (M == FA(9)) { v0 = (((v1 & 51u) << 4) | ((v1 & 4u) << 8)) + 4096u * ((job >> 1) & 1u); asm volatile("" : "+v"(v0)); }
        if constexpr (M == FA(10)) { vs += 8192u + 512u * ((job >> 1) & 1u); v1 = (v1 >> 3) & 1u; asm volatile("" : "+v"(vs), "+v"(v1)); }
        if constexpr (M == 100 + FA(6)) { srdT = mk_srd((const char*)wot + ((job & 1u) * 256u + (((job >> 2) & 51u) | ((job >> 1) & 8u) | ((job >> 3) & 4u))) * 9216u, 3u * NTS + 9216u); asm volatile("" : "+s"(srdT)); }
      };
      phaseAh<0, 3>(c, 10, hook);
    }
    { auto hook = [&](auto M_) __attribute__((always_inline)) { hookB(std::integral_constant<int, 0>{}, M_); }; phaseBh<0, 0>(c, hook); }
    {
      auto hook = [&](auto M_) __attribute__((always_inline)) {
        constexpr int M = decltype(M_)::value;
        if constexpr (M < 64) { if constexpr (fa_of(M) >= 0) {
          constexpr int n = fa_of(M), p = n ? FA(n - 1) : 0, y = 2 * (pcnt(PA0, 'T', M) - pcnt(PA0, 'T', p)) + pcnt(PA0, 'X', M) - pcnt(PA0, 'X', p);
          if constexpr (n % 2 == 0) rd(std::integral_constant<int, 6 + n / 2>{});
          else st(std::integral_constant<int, 6 + n / 2>{}, std::integral_constant<int, y>{});
        } }
      };
      phaseAh<1, 15>(c, 11, hook);
    }
    { auto hook = [&](auto M_) __attribute__((always_inline)) { hookB(std::integral_constant<int, 12>{}, M_); }; phaseBh<1, 0>(c, hook); }
    phaseA<0>(c, 12);
    {
      auto hook = [&](auto M_) __attribute__((always_inline)) { if constexpr (decltype(M_)::value == FB(0)) flag1(H_W + (b - 144u), ((w - 1u) >> 31) * 4u); };
      phaseBh<0, 0>(c, hook);
    }
    phaseA<1>(c, 13); phaseB<1, 0>(c);
  }
  OPS o; OST ot; unsigned xo; unsigned long long tm;
  {
    const unsigned w = opq(wids), b = opq(blk);
    {
      __amdgpu_buffer_rsrc_t sf, sh; unsigned tg, m0, mt, hs, t, u; CW k;
      auto hookA = [&](auto M_) __attribute__((always_inline)) {
        constexpr int M = decltype(M_)::value;
        if constexpr (M == FA(0)) { m0 = (((w - 1u) & (b - 144u)) >> 31) * 4u; asm volatile("" : "+s"(m0)); }
        if constexpr (M == FA(1) || M == FA(2)) { if constexpr (M == FA(2)) { sf = mk_srd(sel + H_Y + b, m0); asm volatile("" : "+s"(sf)); } }
        if constexpr (M == FA(3)) { tg = tag; m0 = c.ldsb + WF_FL; asm volatile("" : "+v"(tg), "+s"(m0)); }
        if constexpr (M == FA(4)) { sh = mk_srd(sel + H_W, 256u); asm volatile("" : "+s"(sh)); }
        SC(FA(5), o.srdL, mk_srd4(sel, 64u))
        SC(0, mt, b & 3u) SC(1, t, b >> 6) SC(2, mt, 4u * mt + t) SC(3, t, (b >> 2) & 1u) SC(4, mt, 2u * mt + t)
        SC(5, u, 4u * mt + w) SC(7, o.sn, u * (16u * XRS)) SC(8, t, (b >> 3) & 7u) SC(9, hs, t * 144u) SC(11, o.sn, o.sn + hs)
        SC(12, hs, 4u * t + w) SC(13, ot.so[0], hs << 19) SC(15, o.sf, hs >> 1) SC(16, o.sf, o.sf << 11)
        SC(17, u, u ^ 4u) SC(19, o.sa, u << 17) SC(20, u, (u >> 2) & 3u) SC(21, u, u << 10) SC(23, o.sa, o.sa | u)
        SC(24, t, ~b & 4u) SC(25, t, t + w) SC(26, u, t << 14) SC(27, o.sa, o.sa | u)
        SC(28, mt, mt >> 1) SC(29, t, 8u * mt + t) SC(31, t, t << 8) SC(32, o.sl, t + 4u * H_O)
        SC(33, hs, b & 6u) SC(34, t, b >> 7) SC(35, hs, hs + t) SC(36, t, b & 1u) SC(38, mt, 2u * hs + t) SC(39, u, (b >> 6) & 1u) SC(40, hs, 2u * mt + u)
        SC(42, t, 4u * hs + w) SC(43, t, t << 10) SC(45, ot.so[0], ot.so[0] + t)
        SC(44, u, 4u * mt + u) SC(46, t, w & 1u) SC(48, t, 2u * u + t) SC(49, t, 16u * t + 4u * H_O) SC(50, o.sf, o.sf + t)
        SC(55, t, b - RED0) SC(56, xo, 4u * t + w) SC(58, xo, 16u * xo + 4u * H_X) SC(59, hs, xo + 4u)
        if constexpr (M == 60) asm volatile("s_cmp_lt_u32 %1, %2\n\ts_cselect_b32 %0, %0, 0" : "+s"(hs) : "s"(t), "i"(RED1 - RED0) : "scc");
      };
      phaseAh<0, 0>(c, 14, hookA);
      auto hook = [&](auto M_) __attribute__((always_inline)) {
        constexpr int M = decltype(M_)::value;
        if constexpr (M == FB(0)) flag_st(sf, tg);
        if constexpr (M == FB(1)) warm_step(k, std::integral_constant<int, 0>{});
        if constexpr (M == FB(2)) warm_step(k, std::integral_constant<int, 1>{});
        if constexpr (M == FB(3)) warm_step(k, std::integral_constant<int, 2>{});
        if constexpr (M == FB(4)) warm_step(k, std::integral_constant<int, 3>{});
        if constexpr (M == DB(9)) warm_step(k, std::integral_constant<int, 4>{});
        if constexpr (M == 39) m0_set(m0);
        if constexpr (M == 40) dma16(sh, c.vksrc[0]);
        if constexpr (M == 41) m0_set(m0 + (SEL_C - WF_FL));
        if constexpr (M == DB(8)) { const unsigned v = c.vksrc[0]; const u32x4 r = o.srdL; asm volatile("buffer_load_dwordx4 %0, %1, 0 offen sc1 lds" :: "v"(v), "s"(r) : "memory"); }
        if constexpr (M == DB(8) + 1) { o.srdL[2] = hs; asm volatile("" : "+s"(o.srdL)); }
      };
      phaseBh<0, 0>(c, hook);
    }
    {
      unsigned va, vt, r0, r1; u32x4 f; u32x2 sv;
      auto hookA = [&](auto M_) __attribute__((always_inline)) {
        constexpr int M = decltype(M_)::value;
        if constexpr (M == FA(0)) { va = c.ldsb + WF_FL + (c.vksrc[0] & 0xf0u); asm volatile("" : "+v"(va)); }
        if constexpr (M == FA(1)) { c.pfv = lane_id(); asm volatile("" : "+v"(c.pfv)); }
        if constexpr (M == FA(2)) { c.pfv *= 16u; c.pfo = op_k0(b, w); asm volatile("" : "+v"(c.pfv), "+s"(c.pfo)); }
        if constexpr (M == FA(3)) { c.pfo *= 4608u; asm volatile("" : "+s"(c.pfo)); }
        if constexpr (M == FA(4)) { c.srdP = mk_srd((const char*)wot + c.pfo, 16 * 4608); asm volatile("" : "+s"(c.srdP)); }
        if constexpr (M == FA(5)) { c.pfst = 1024u; c.pfo = 0u - 1024u; c.pfm0 = c.ldsb + SD_BASE + BP_W * w; asm volatile("" : "+s"(c.pfst), "+s"(c.pfo), "+s"(c.pfm0)); }
        if constexpr (M == FA(9)) { c.pfend = c.pfm0 + BP_W; asm volatile("" : "+s"(c.pfend)); }
        VC(FA(6), vt, (c.pfv >> 4) & 7u) SC(40, r0, o.sa >> 13) SC(42, r0, r0 ^ 64u) SC(43, r0, r0 & ~15u) SC(44, r1, 128u * w) SC(45, r1, r1 + (c.ldsb + SEL_C + 256u))
      };
      phaseAh<1, 1>(c, 15, hookA);
      auto hook = [&](auto M_) __attribute__((always_inline)) {
        constexpr int M = decltype(M_)::value;
        if constexpr (M == FB(0)) asm volatile("ds_read_b128 %0, %1" : "=v"(f) : "v"(va) : "memory");
        if constexpr (M == FB(1)) { asm volatile("s_waitcnt lgkmcnt(%1)" : "+v"(f) : "i"(kyo(FB(0), FB(1))) : "memory"); f[0] = (f[0] ^ f[1]) | (f[2] ^ f[3]) | (f[0] ^ f[2]); asm volatile("" : "+v"(f)); }
        if constexpr (M == FB(2)) {
          if (__builtin_expect(!__all((f[0] | (f[1] ^ tag)) == 0u), 0)) {
#pragma nounroll
            for (unsigned spin = 0; spin < (1u << 20); ++spin) {
              __builtin_amdgcn_s_sleep(8);
              hand_req(c.ldsb + WF_FL, H_W, 256u);
              asm volatile("s_waitcnt vmcnt(0)\n\tds_read_b128 %0, %1\n\ts_waitcnt lgkmcnt(0)" : "=v"(f) : "v"(va) : "memory");
              if (__all(((f[0] ^ tag) | (f[1] ^ tag) | (f[2] ^ tag) | (f[3] ^ tag)) == 0u)) break;
            }
            asm volatile("s_mov_b32 m0, %0" :: "s"(c.pm0[1]) : "m0", "memory");
          }
        }
        VC(135, va, 8u * vt + (c.ldsb + SEL_C))
        if constexpr (M == FB(3)) asm volatile("ds_read_b64 %0, %1" : "=v"(sv) : "v"(va) : "memory");
        if constexpr (M == FB(4)) { asm volatile("s_waitcnt lgkmcnt(0)" : "+v"(sv) :: "memory"); sv[0] &= 0xffffu; va = sv[0] - r0; asm volatile("" : "+v"(sv), "+v"(va)); }
        if constexpr (M == FB(5)) { const unsigned x = va; asm volatile("v_cmp_gt_u32_e64 %0, 16, %1" : "=s"(tm) : "v"(x)); }
        if constexpr (M == 152) { const unsigned x = sv[1], g = tag; asm volatile("v_cmp_ne_u32_e64 %0, %1, %2" : "=s"(o.seen) : "s"(g), "v"(x)); }
        VC(154, vt, vt - sv[0]) VC(158, va, va < 16u ? va : 16u) VC(160, vt, vt * XRS + SEQ * XRS) VC(FB(8), va, 4u * va + r1) SC(64, o.bad, (unsigned)o.seen)
        if constexpr (M == FB(9)) lds_st32(va, vt);
      };
      phaseBh<1, 2>(c, hook);
    }
  }
  GAP();
  if (__builtin_expect(((RED0 - 1u - opq(blk)) & (opq(blk) - RED1)) >> 31, 0)) {
    PS_AT(43);
    const unsigned job = (opq(blk) - RED0) * 4u + opq(wids);
    unsigned va; u32x4 f; u32x2 sv, nv; f32x4 y[4]; float a[4]; __amdgpu_buffer_rsrc_t sr;
    auto req_fl = [&](auto W_) __attribute__((always_inline)) {
      u32x4& f_ = f; u32x2& s_ = sv; const unsigned v0 = c.pfv, v1 = (c.pfv >> 1) & 56u; const __amdgpu_buffer_rsrc_t r0 = mk_srd(sel + H_Y, 4u * MOE_WG), r1 = mk_srd(sel, 64u);
      if constexpr (decltype(W_)::value) asm volatile("buffer_load_dwordx4 %0, %2, %4, 0 offen sc1\n\tbuffer_load_dwordx2 %1, %3, %5, 0 offen sc1\n\ts_waitcnt vmcnt(0)" : "=&v"(f_), "=&v"(s_) : "v"(v0), "v"(v1), "s"(r0), "s"(r1) : "memory");
      else                               asm volatile("buffer_load_dwordx4 %0, %2, %4, 0 offen sc1\n\tbuffer_load_dwordx2 %1, %3, %5, 0 offen sc1" : "=&v"(f_), "=&v"(s_) : "v"(v0), "v"(v1), "s"(r0), "s"(r1) : "memory");
    };
    auto bad = [&]() __attribute__((always_inline)) { return vsel(c.pfv >= 16u * (MOE_WG / 4), 0u, (f[0] ^ tag) | (f[1] ^ tag) | (f[2] ^ tag) | (f[3] ^ tag)) | (sv[1] ^ tag); };
#define PINA asm volatile("" : "+v"(a[0]), "+v"(a[1]), "+v"(a[2]), "+v"(a[3]))
    auto batch = [&](auto B_, auto M_) __attribute__((always_inline)) {
      constexpr int B = decltype(B_)::value, M = decltype(M_)::value, g = M - 100, i = g / 9;
      if constexpr (B > 0 && g >= 2 && g < 36 && (g % 9 == 2 || g % 9 == 5) && 4 * (B - 1) + i < 18) {
        constexpr int e = g % 9 == 2 ? 0 : 2; const f32x4 v = y[i];
        if constexpr (B == 1 && i == 0) { a[e] = v[e]; a[e + 1] = v[e + 1]; PIN2(a[e], a[e + 1]); } else { add_v(a[e], v[e]); add_v(a[e + 1], v[e + 1]); }
      }
      if constexpr (M >= 7 && M < 36 && M % 9 == 7 && 4 * B + M / 9 < 18) {
        f32x4& d = y[M / 9]; const unsigned v = va; const __amdgpu_buffer_rsrc_t r = sr;
        asm volatile("buffer_load_dwordx4 %0, %1, %2, %3 offen sc1" : "=v"(d) : "v"(v), "s"(r), "s"(8u * NSEL * DM * 4u * (4 * B + M / 9)) : "memory");
      }
    };
#define PINY asm volatile("" : "+v"(y[0]), "+v"(y[1]), "+v"(y[2]), "+v"(y[3]))
    phaseA<0, 3>(c, 16); phaseB<0, 2>(c);
    phaseA<1, 2>(c, 17);
    { auto hook = [&](auto M_) __attribute__((always_inline)) { if constexpr (decltype(M_)::value == 40) req_fl(IC(0)); }; phaseBh<1, 2>(c, hook); }
    {
      auto hook = [&](auto M_) __attribute__((always_inline)) {
        constexpr int M = decltype(M_)::value;
        if constexpr (M == FA(0)) { va = (c.pfv >> 7) * (unsigned)(NSEL * DM * 4); asm volatile("" : "+v"(va)); }
        if constexpr (M == FA(2)) { va += c.pfv & 112u; asm volatile("" : "+v"(va)); }
        if constexpr (M == FA(4)) { sr = mk_srd(ypart + 32u * job, MOE_WG * NSEL * DM * 4 - 128u * job); asm volatile("" : "+s"(sr)); }
      };
      phaseAh<0, 3>(c, 18, hook);
    }
    asm volatile("" : "+v"(f), "+v"(sv));
    {
      auto hook = [&](auto M_) __attribute__((always_inline)) {
        constexpr int M = decltype(M_)::value;
        if constexpr (M == 104) {
          if (__builtin_expect(!__all(bad() == 0u), 0)) {
#pragma nounroll
            for (unsigned spin = 0; spin < (1u << 20) && !__all(bad() == 0u); ++spin) { __builtin_amdgcn_s_sleep(8); req_fl(IC(1)); }
          }
        }
        batch(IC(0), M_);
      };
      phaseBh<0, 2>(c, hook);
    }
    phaseA<1, 2>(c, 19);
    PINY; { auto hook = [&](auto M_) __attribute__((always_inline)) { batch(IC(1), M_); }; phaseBh<1, 2>(c, hook); }
    phaseA<0, 3>(c, 20);
    PINY; { auto hook = [&](auto M_) __attribute__((always_inline)) { batch(IC(2), M_); }; phaseBh<0, 2>(c, hook); }
    phaseA<1, 2>(c, 21);
    PINY; { auto hook = [&](auto M_) __attribute__((always_inline)) { batch(IC(3), M_); }; phaseBh<1, 2>(c, hook); }
    phaseA<0, 3>(c, 22);
    PINY;
    {
      auto hook = [&](auto M_) __attribute__((always_inline)) {
        batch(IC(4), M_);
        if constexpr (decltype(M_)::value == 40) {
          u32x2& n_ = nv; const unsigned v = (sv[0] & 0xffffu) * XRS, o2 = 8u * job; const __amdgpu_buffer_rsrc_t r = mk_srd(xn, SEQ * DM * 2);
          asm volatile("buffer_load_dwordx2 %0, %1, %2, %3 offen sc1" : "=v"(n_) : "v"(v), "s"(r), "s"(o2) : "memory");
        }
      };
      phaseBh<0, 2>(c, hook);
    }
    phaseA<1, 2>(c, 23);
    asm volatile("" : "+v"(y[0]), "+v"(y[1]), "+v"(nv));
    {
      auto hook = [&](auto M_) __attribute__((always_inline)) {
        constexpr int M = decltype(M_)::value;
        batch(IC(5), M_);
        if constexpr (M == FB(1)) { _Pragma("unroll") for (int e = 0; e < 4; ++e) a[e] = dpp_add<0x128>(a[e]); PINA; }
        if constexpr (M == FB(2))       { a[0] = red_step<4>(a[0]); a[1] = red_step<4>(a[1]); PINA; }
        if constexpr (M == 100 + FB(3)) { a[2] = red_step<4>(a[2]); a[3] = red_step<4>(a[3]); PINA; }
        if constexpr (M == 100 + FB(4) || M == FB(5)) {
          constexpr int e0 = (M == FB(5)) * 2;
#pragma unroll
          for (int e = e0; e < e0 + 2; ++e) {
            auto pr = __builtin_amdgcn_permlane32_swap(__float_as_uint(a[e]), __float_as_uint(a[e]), false, false);
            a[e] = __uint_as_float(pr[0]) + __uint_as_float(pr[1]);
          }
          PINA;
        }
        if constexpr (M == 153 || M == FB(7) || M == FB(8) || M == FB(9)) {
          constexpr int e = M == 153 ? 0 : fb_of(M) - 6;
          a[e] = ((e & 1) ? bfhi(nv[e >> 1]) : bflo(nv[e >> 1])) + rbf(rbf(a[e]) * bfhi(sv[0])); PINA;
        }
        if constexpr (M == 120) { va = (c.pfv >> 4) * XRS; asm volatile("" : "+v"(va)); }
        if constexpr (M == 72) {
          const u32x2 o2 = {cvtpk(a[0], a[1]), cvtpk(a[2], a[3])};
          const unsigned vo = va; const __amdgpu_buffer_rsrc_t r = mk_srd(xn + SEQ * DM + 4u * job, 7u * XRS + 8u);
          asm volatile("buffer_store_dwordx2 %0, %1, %2, 0 offen sc1" :: "v"(o2), "v"(vo), "s"(r) : "memory");
        }
      };
      phaseBh<1, 2>(c, hook);
    }
  }
  GAP();
  {
    int t = 14 + 8 * (int)(((RED0 - 1u - opq(blk)) & (opq(blk) - RED1)) >> 31);
    unsigned tg = tag;
    asm volatile("" : "+v"(tg));
    auto hook = [&](auto M_) __attribute__((always_inline)) {
      constexpr int M = decltype(M_)::value;
      if constexpr (M == FB(0)) { const unsigned d = tg, v = c.pfv, so = xo; const u32x4 r = o.srdL; asm volatile("buffer_store_dword %0, %1, %2, %3 offen sc1" :: "v"(d), "v"(v), "s"(r), "s"(so) : "memory"); }
      if constexpr (M == FB(0) + 1) { o.srdL[2] = 0u; asm volatile("" : "+s"(o.srdL)); }
    };
#pragma nounroll
    do {
      t += 2;
      phaseA<0, 3>(c, t);     phaseB<0, 2>(c);
      phaseA<1, 2>(c, t + 1); phaseBh<1, 2>(c, hook);
    } while (__builtin_expect(t != T - 4, 1));
  }
  GAP();
  {
    const unsigned w = opq(wids), b = opq(blk);
    __amdgpu_buffer_rsrc_t sh; unsigned ax[2], dl, fx, t;
    auto req = [&](auto X_, auto J_) __attribute__((always_inline)) {
      constexpr int X = decltype(X_)::value, J = decltype(J_)::value;
      const unsigned v = ax[J >> 1], so = o.sn; const __amdgpu_buffer_rsrc_t r = sh;
      if constexpr (J < 2) asm volatile("buffer_load_dwordx4 %0, %1, %2, %3 offen offset:%c4 sc1" : "=v"(o.gv[X][J & 1]) : "v"(v), "s"(r), "s"(so), "i"(64 * J) : "memory");
      else                 asm volatile("buffer_load_dword %0, %1, %2, %3 offen sc1" : "=v"(o.gs[X]) : "v"(v), "s"(r), "s"(so) : "memory");
    };
    auto hookA = [&](auto M_) __attribute__((always_inline)) {
      constexpr int M = decltype(M_)::value;
      SC(0, t, (b >> 3) & 7u) SC(1, t, t * 288u) SC(2, t, t + 4u * H_X)
      if constexpr (M == 3) { o.srdL[2] = t + 288u; asm volatile("" : "+s"(o.srdL)); }
      if constexpr (M == FA(0)) { unsigned& d = fx; const unsigned v = c.pfv, so = t; const u32x4 r = o.srdL; asm volatile("buffer_load_dword %0, %1, %2, %3 offen sc1" : "=v"(d) : "v"(v), "s"(r), "s"(so) : "memory"); }
      if constexpr (M == 8) { o.srdL[2] = o.sl + 64u; asm volatile("" : "+s"(o.srdL)); }
      VC(FA(2), ax[0], op_ax<0>(c.pfv)) VC(FA(4), ax[1], op_ax<1>(c.pfv)) SC(FA(3), sh, mk_srd(xres, SEQ * DM * 2))
      if constexpr (M == FA(5)) req(IC(1), IC(0));
      if constexpr (M == FA(6)) req(IC(1), IC(1));
      if constexpr (M == FA(8)) req(IC(1), IC(2));
      SC(48, t, 128u * w) SC(49, t, t + (c.ldsb + SEL_C + 256u)) VC(FA(9), dl, ((c.pfv >> 2) & 60u) + t)
      if constexpr (M == FA(11)) { unsigned& d = dl; asm volatile("ds_read_b32 %0, %0" : "+v"(d) :: "memory"); }
      SC(FA(10), sh, mk_srd(xn, (SEQ + NSEL) * DM * 2))
      SC(24, ot.so[1], ot.so[0] ^ 1024u) SC(25, ot.so[2], ot.so[0] ^ 2048u) SC(26, ot.so[3], ot.so[0] ^ 3072u) SC(FA(7), ot.srd, mk_srd4(O, OB))
    };
    phaseAh<0, 3>(c, T - 2, hookA);
    asm volatile("" : "+v"(dl), "+v"(fx));
    o.srdB = c.srdP; o.va = c.pfv;
    auto hook = [&](auto M_) __attribute__((always_inline)) {
      constexpr int M = decltype(M_)::value;
      constexpr int f = M < NGB && PB2[M < NGB ? M : 0] == 'F' ? pcnt(PB2, 'F', M) : -1;
      VC(100, ax[0], ax[0] + dl) VC(16, ax[1], ax[1] + dl)
      if constexpr (f >= 0 && f < 3) req(IC(0), IC(f));
      if constexpr (M == 33) { const unsigned x = fx, g = tag; asm volatile("v_cmp_ne_u32_e64 %0, %1, %2" : "=s"(o.seen) : "s"(g), "v"(x)); }
      SC(35, t, (unsigned)o.seen & 0x3ffffu)
      if constexpr (M == 40) { unsigned& x = t; const unsigned long long m = tm; asm volatile("s_cmp_lg_u64 %1, 0\n\ts_cselect_b32 %0, %0, 0" : "+s"(x) : "s"(m) : "scc"); }
      SC(44, o.bad, o.bad | t)
      SC(2, c.pfo, 23u * 1024u) SC(46, c.pfo, 27u * 1024u) SC(48, c.pfm0, 8192u * w) SC(49, c.pfm0, c.pfm0 + (c.ldsb + 0x4000u))
    };
    static_assert(pcnt(PB2, 'E', NGB) == NBL && ppos(PB2, 'W', 0) == DB(4), "");
    phaseB_<0, false, false, 0>(c, hook, make_iseq<NGB>{});
  }
  ot.vo = c.pfv;
  fin_gaps(c, ot, o, make_iseq<NFIN>{});
  asm volatile("s_nop 1" :: "v"(ot.w));
  if (__builtin_expect(!__all((ot.ls[0] + ot.ls[1]) + (ot.ls[2] + ot.ls[3]) <= LIM), 0))
    redo_rows(c, ot, o, xn, wq, keys, 256u * ((opq(blk) >> 3) & 7u) + 64u * opq(wids), 4u * (opq(blk) & 7u) + (opq(blk) >> 6), tag);
  o.srdA = ot.srd; o.tag = tag;
  oproj_phase(o, lds, xn, (unsigned*)sel, ypart + MOE_WG * NSEL * DM, out, opq(wids)  );
}
extern "C" __global__ void k_attn_end() { asm volatile(".irp n," PS_IDS "\n.ifndef .Lps_\\n\n.set .Lps_\\n, .Lps_ret\n.endif\n.endr"); }

extern "C" void kernel_launch(void* const* d_in, const int* in_sizes, int n_in,
                              void* d_out, int out_size, void* d_ws, size_t ws_size,
                              hipStream_t stream) {
  const u16* x   = (const u16*)d_in[0];
  const u16* kc  = (const u16*)d_in[1];
  const u16* vc  = (const u16*)d_in[2];
  const u16* wq  = (const u16*)d_in[3];
  const u16* wo  = (const u16*)d_in[4];
  const u16* w_r = (const u16*)d_in[6];
  const u16* wg  = (const u16*)d_in[7];
  const u16* wu  = (const u16*)d_in[8];
  const u16* wd  = (const u16*)d_in[9];
  char* ws = (char*)d_ws;
  u16* xn = (u16*)(ws + WS_XN); unsigned long long* keys = (unsigned long long*)(ws + WS_KEY); int* sel = (int*)(ws + WS_SEL);
  u16* o = (u16*)(ws + WS_O); u16* wot = (u16*)(ws + WS_WOT); float* yp = (float*)(ws + WS_YP);
  (void)hipFuncSetAttribute((const void*)k_attn, hipFuncAttributeMaxDynamicSharedMemorySize, (int)SHM_ATTN);
  hipLaunchKernelGGL(k_attn, dim3(256), dim3(256), SHM_ATTN, stream, xn, wq, kc, vc, o, wo, wg, wu, wd, wot, keys, yp, sel, x, (u16*)d_out, w_r  );
}
```

```cpp
#include <hip/hip_runtime.h>
#include <hip/hip_bf16.h>
#include <utility>
#include <type_traits>

constexpr int SEQ = 2048, DM = 576, HQ = 32, HKV = 16, DH = 128, FF = 4608, NSEL = 8;
constexpr int NT = SEQ / 64;
constexpr float LIM = 1073741824.0f;

typedef float    f32x16 __attribute__((ext_vector_type(16)));
typedef float    f32x2  __attribute__((ext_vector_type(2)));
typedef float    f32x4  __attribute__((ext_vector_type(4)));
typedef __bf16   bf16x8 __attribute__((ext_vector_type(8)));
typedef __bf16   bf16x4 __attribute__((ext_vector_type(4)));
typedef __bf16   bf16x2 __attribute__((ext_vector_type(2)));
typedef unsigned u32x8  __attribute__((ext_vector_type(8)));
typedef unsigned u32x4  __attribute__((ext_vector_type(4)));
typedef unsigned u32x2  __attribute__((ext_vector_type(2)));
typedef unsigned short u16;
template <int... Is> using iseq = std::integer_sequence<int, Is...>;
template <int N> using make_iseq = std::make_integer_sequence<int, N>;

#define DI __device__ __forceinline__
#define GAP() __builtin_amdgcn_sched_barrier(0)
#define IC(x) std::integral_constant<int, (x)>{}
#define SRD_WORD3 0x00020000

DI unsigned rfl(unsigned x) { return __builtin_amdgcn_readfirstlane(x); }
DI unsigned cvtpk(float lo, float hi) { f32x2 v = {lo, hi}; bf16x2 b = __builtin_convertvector(v, bf16x2); return __builtin_bit_cast(unsigned, b); }
DI __amdgpu_buffer_rsrc_t mk_srd(const void* p, const unsigned bytes) {
  asm volatile("" : "+s"(p));
  return __builtin_amdgcn_make_buffer_rsrc((void*)p, (short)0, (int)bytes, SRD_WORD3);
}
DI u32x4 mk_srd4(const void* p, const unsigned bytes) { asm volatile("" : "+s"(p)); const unsigned long long a = (unsigned long long)p; return u32x4{(unsigned)a, (unsigned)(a >> 32) & 0xffffu, bytes, SRD_WORD3}; }
DI unsigned vsel(const bool c, unsigned a, const unsigned b) { asm volatile("" : "+v"(a)); return c ? a : b; }
DI float bflo(unsigned w) { return __uint_as_float(w << 16); }
DI float bfhi(unsigned w) { return __uint_as_float(w & 0xffff0000u); }
DI float rbf(float x) { return bflo(cvtpk(x, 0.f)); }
DI u16 f2bf(float x) { return (u16)(cvtpk(x, 0.f) & 0xffffu); }

#define PS_MACROS \
  ".macro psl n\n.Lps_\\n: s_cbranch_execz .Lpn_\\n\n.endm\n.set .Lpsl, 0\n" \
  ".macro ps\n.set .Lpso, (. - k_attn) >> 12\n.if .Lpso != .Lpsl\n.set .Lpsl, .Lpso\n.altmacro\npsl %%.Lpso\n.noaltmacro\n.endif\n.endm\n" \
  ".macro mf16 a, b, c, d\nv_mfma_f32_16x16x32_bf16 \\a, \\b, \\c, \\d\nps\n.endm\n" \
  ".macro mf32 a, b, c, d\nv_mfma_f32_32x32x16_bf16 \\a, \\b, \\c, \\d\nps\n.endm\n" \
  ".macro sm64 a, b, c, d\nv_smfmac_f32_16x16x64_bf16 \\a, \\b, \\c, \\d\nps\n.endm\n"
#define PS_IDS "0,1,2,3,4,5,6,7,8,9,10,11,12,13,14,15,16,17,18,19,20,21,22,23,24,25,26,27,28,29,30,31,32,33,34,35,36,37,38,39,40,41,42,43,44,45,46,47"
constexpr int PS_A = 37, PS_B = 11, PS_N = 12;
#define PS_AT(n) asm volatile("psl " #n)
#define PS_PG(n, i) "%%(((.Lps_" #n " - k_attn) >> 12) + 1 + " #i ")"
#define PS_CH(k, n, i) "pse " #k ", " PS_PG(n, i) "\n"
#define PS_NX(n, i, j) "psn " PS_PG(n, i) ", " PS_PG(41, j) "\n"
#define PS_END ".irp n," PS_IDS "\n.ifndef .Lps_\\n\n.set .Lps_\\n, .Lps_ret\n.endif\n.endr\n" \
  ".macro pse k, n\n.set .Lpe_\\k, .Lps_\\n\n.endm\n.macro psn a, b\n.set .Lpn_\\a, .Lps_\\b\n.endm\n.altmacro\n" \
  "pse 0, 2\npse 1, 3\npse 2, 4\npse 3, 5\n" \
  PS_CH(4, 40, 0) PS_CH(5, 40, 1) PS_CH(6, 40, 2) PS_CH(7, 40, 3) PS_NX(40, 0, 0) PS_NX(40, 1, 1) PS_NX(40, 2, 2) PS_NX(40, 3, 3) \
  "pse 8, 42\npse 9, 41\n" PS_CH(10, 42, 0) PS_CH(11, 42, 1) "psn 42, " PS_PG(41, 0) "\npsn 41, " PS_PG(41, 1) "\n" PS_NX(42, 0, 2) PS_NX(42, 1, 3) \
  "pse 12, 43\n" PS_CH(13, 43, 1) PS_CH(14, 43, 2) PS_CH(15, 43, 3) "psn 43, " PS_PG(43, 0) "\npsn " PS_PG(43, 1) ", 41\n" PS_NX(43, 0, 0) PS_NX(43, 2, 2) PS_NX(43, 3, 3) \
  PS_CH(16, 41, 0) "pse 17, 41\n" PS_CH(18, 41, 2) PS_CH(19, 41, 3) PS_CH(20, 45, 0) PS_CH(21, 45, 1) PS_CH(22, 45, 2) PS_CH(23, 45, 0) \
  ".noaltmacro\n.irp n," PS_IDS "\n.ifndef .Lpn_\\n\n.set .Lpn_\\n, .Lps_ret\n.endif\n.endr\n"
DI void ps_touch(const unsigned n) {
  asm volatile("s_getpc_b64 s[20:21]\n\ts_lshl2_add_u32 vcc_lo, %0, .Lps_tab - .\n\ts_add_u32 s20, s20, vcc_lo\n\ts_addc_u32 s21, s21, -1\n\ts_mov_b64 exec, 0\n\ts_swappc_b64 vcc, s[20:21]\n\ts_mov_b64 exec, -1"
               :: "s"(n) : "vcc", "scc", "s20", "s21", "memory");
}

constexpr size_t WS_XN   = 0;
constexpr size_t WS_KEY  = WS_XN + (size_t)(SEQ + NSEL) * DM * 2;
constexpr size_t WS_ST   = WS_KEY + (size_t)SEQ * 8;
constexpr size_t WS_SEL  = WS_ST + (size_t)SEQ * 8;
constexpr unsigned H_G = 32u, H_W = 64u, H_Y = 128u, H_O = 4096u, H_X = 1536u, H_E = 12288u;
constexpr size_t WS_O    = WS_SEL + 131072;
constexpr size_t WS_WOT  = WS_O + (size_t)SEQ * HQ * DH * 2;
constexpr int    MOE_WG  = 144;
constexpr size_t WS_YP   = WS_WOT + (size_t)HQ * DH * DM * 2;
constexpr size_t WS_END  = WS_YP + (size_t)MOE_WG * NSEL * DM * 4 + 256u * 20480u;

DI void ce(unsigned& a, unsigned& b) { const unsigned hi_ = a > b ? a : b, lo_ = a > b ? b : a; a = hi_; b = lo_; }
DI int key_row(unsigned k) { return 0xffff - (int)(k & 0xffffu); }
DI float key_val(unsigned k) { const unsigned mono = k >> 16; const unsigned rb = (mono & 0x8000u) ? (mono & 0x7fffu) : (~mono & 0xffffu); return __uint_as_float(rb << 16); }

struct LNJ { u32x4 a[3], w[3]; float x0, s[3], q[3], mb, den, rden, t0, t1, t2, t3; unsigned stat, key; bool three, up; };
DI float rcp_nr(float d) { const float r = __builtin_amdgcn_rcpf(d); return __builtin_fmaf(__builtin_fmaf(-d, r, 1.f), r, r); }
template <int CTRL> DI float dpp_add(float v) { return v + __int_as_float(__builtin_amdgcn_update_dpp(0, __float_as_int(v), CTRL, 0xf, 0xf, false)); }
template <int K> DI float red_step(float v) {
  if constexpr (K == 0) return dpp_add<0xB1>(v);
  else if constexpr (K == 1) return dpp_add<0x4E>(v);
  else if constexpr (K == 2) return dpp_add<0x141>(v);
  else if constexpr (K == 3) return dpp_add<0x140>(v);
  else { auto pr = __builtin_amdgcn_permlane16_swap(__float_as_uint(v), __float_as_uint(v), false, false); return __uint_as_float(pr[0]) + __uint_as_float(pr[1]); }
}
#define PIN2(a, b) asm volatile("" : "+v"(a), "+v"(b))
constexpr int LN_STAT = 34, LN_N0 = 40, LN_XN = LN_N0 + 96, LN_KEY = LN_XN + 7;
template <int I> DI void ln_step(LNJ& j, const unsigned row) {
  if constexpr (I == 0) {
    const float xa = bflo((unsigned)__builtin_amdgcn_readlane((int)j.a[0][0], 0)), xb = bflo((unsigned)__builtin_amdgcn_readlane((int)j.a[0][0], 32));
    j.x0 = j.up ? xb : xa;
    asm volatile("" : "+v"(j.x0));
  } else if constexpr (I <= 24) {
    constexpr int e = I - 1, c = e >> 3, i = (e >> 1) & 3;
    const float d = ((e & 1) ? bfhi(j.a[c][i]) : bflo(j.a[c][i])) - j.x0;
    if constexpr ((e & 7) == 0) { j.s[c] = d; j.q[c] = d * d; } else { j.s[c] += d; j.q[c] = __builtin_fmaf(d, d, j.q[c]); }
    PIN2(j.s[c], j.q[c]);
  } else if constexpr (I == 25) {
    j.t0 = (j.s[0] + j.s[1]) + (j.three ? j.s[2] : 0.f); j.t1 = (j.q[0] + j.q[1]) + (j.three ? j.q[2] : 0.f);
    PIN2(j.t0, j.t1);
  } else if constexpr (I <= 30) {
    j.t0 = red_step<I - 26>(j.t0); j.t1 = red_step<I - 26>(j.t1);
    PIN2(j.t0, j.t1);
  } else if constexpr (I == 31) {
    j.t0 *= 1.0f / DM; j.t1 = __builtin_fmaxf(__builtin_fmaf(-j.t0, j.t0, j.t1 * (1.0f / DM)), 0.f);
    PIN2(j.t0, j.t1);
  } else if constexpr (I == 32) {
    j.mb = rbf(j.x0 + j.t0); j.t1 = rbf(j.t1) + 1e-5f;
    PIN2(j.mb, j.t1);
  } else if constexpr (I == 33) {
    j.den = rbf(__builtin_amdgcn_sqrtf(rbf(j.t1)));
    j.stat = (__float_as_uint(j.mb) & 0xffff0000u) | (__float_as_uint(j.den) >> 16);
    PIN2(j.den, j.stat);
  } else if constexpr (I < LN_N0) {
    if constexpr (I == 34) { j.rden = rcp_nr(j.den); j.s[0] = 0.f; j.s[1] = 0.f; asm volatile("" : "+v"(j.rden)); }
  } else if constexpr (I < LN_XN) {
    constexpr int e = (I - LN_N0) >> 3, st = (I - LN_N0) & 7, c = e >> 2, i = e & 3;
    if constexpr (st == 0)      { j.t0 = bflo(j.a[c][i]); j.t1 = bfhi(j.a[c][i]); }
    else if constexpr (st == 1) { j.t0 -= j.mb; j.t1 -= j.mb; }
    else if constexpr (st == 2) { const unsigned pk = cvtpk(j.t0, j.t1); j.t0 = bflo(pk); j.t1 = __uint_as_float(pk); }
    else if constexpr (st == 3) { j.t0 *= j.rden; j.t1 = bfhi(__float_as_uint(j.t1)); }
    else if constexpr (st == 4) { unsigned pk = cvtpk(j.t0, j.t1 * j.rden); asm volatile("" : "+v"(pk)); j.a[c][i] = pk; }
    else if constexpr (st == 5) { j.t0 = bflo(j.a[c][i]); j.t1 = bflo(j.w[c][i]); }
    else if constexpr (st == 6) { j.s[0] = __builtin_fmaf(j.t0, j.t1, j.s[0]); j.t0 = bfhi(j.a[c][i]); asm volatile("" : "+v"(j.s[0])); }
    else                        { j.s[1] = __builtin_fmaf(j.t0, bfhi(j.w[c][i]), j.s[1]); asm volatile("" : "+v"(j.s[1])); }
    PIN2(j.t0, j.t1);
  } else if constexpr (I == LN_XN) {
    j.t0 = j.s[0] + j.s[1]; asm volatile("" : "+v"(j.t0));
  } else if constexpr (I < LN_XN + 6) {
    j.t0 = red_step<I - LN_XN - 1>(j.t0); asm volatile("" : "+v"(j.t0));
  } else if constexpr (I == LN_XN + 6) {
    const unsigned rb = f2bf(j.t0);
    const unsigned mono = (rb & 0x8000u) ? (~rb & 0xffffu) : (rb | 0x8000u);
    j.key = (mono << 16) | (0xffffu - row);
    asm volatile("" : "+v"(j.key));
  }
}

struct RTS { unsigned k[8], bad, m, cnd, lq, va, cv, rank; u32x4 g[4]; float rs; unsigned long long pm[2]; };
DI void cnd_s(unsigned& d, const unsigned a, const unsigned long long m) { asm volatile("v_cndmask_b32_e64 %0, %0, %1, %2" : "+v"(d) : "v"(a), "s"(m)); }
DI void cnt_s(unsigned& d, const unsigned long long m) { unsigned long long co; asm volatile("v_addc_co_u32_e64 %0, %1, 0, %0, %2" : "+v"(d), "=s"(co) : "s"(m)); }
constexpr int RT_SORT = 3, RT_POP = RT_SORT + 19, RT_END = RT_POP + 64;
template <int CTRL> DI unsigned dpp_umax(unsigned v) { const unsigned t = (unsigned)__builtin_amdgcn_update_dpp(0, (int)v, CTRL, 0xf, 0xf, false); return t > v ? t : v; }
template <int I> DI void rt_step(RTS& r, const unsigned tag) {
  constexpr int net[19][2] = {{0, 1}, {2, 3}, {4, 5}, {6, 7}, {0, 2}, {1, 3}, {4, 6}, {5, 7}, {1, 2}, {5, 6}, {0, 4}, {3, 7}, {1, 5}, {2, 6}, {1, 4}, {3, 6}, {2, 4}, {3, 5}, {3, 4}};
  if constexpr (I == 0) { r.bad = (r.g[0][1] ^ tag) | (r.g[0][3] ^ tag) | (r.g[1][1] ^ tag); asm volatile("" : "+v"(r.bad)); }
  else if constexpr (I == 1) { r.bad |= (r.g[1][3] ^ tag) | (r.g[2][1] ^ tag) | (r.g[2][3] ^ tag); asm volatile("" : "+v"(r.bad)); }
  else if constexpr (I == 2) {
    r.bad |= (r.g[3][1] ^ tag) | (r.g[3][3] ^ tag); r.cnd = 0u; r.cv = r.lq & 15u;
#pragma unroll
    for (int j = 0; j < 8; ++j) r.k[j] = r.g[j >> 1][2 * (j & 1)];
    asm volatile("" : "+v"(r.bad));
  } else if constexpr (I < RT_POP) { ce(r.k[net[I - RT_SORT][0]], r.k[net[I - RT_SORT][1]]); PIN2(r.k[net[I - RT_SORT][0]], r.k[net[I - RT_SORT][1]]); }
  else if constexpr (I < RT_END) {
    constexpr int n = (I - RT_POP) >> 3, p = (I - RT_POP) & 7;
    if constexpr (p == 0) r.m = dpp_umax<0xB1>(r.k[0]);
    else if constexpr (p == 1) { r.m = dpp_umax<0x4E>(r.m); asm volatile("v_cmp_eq_u32_e64 %0, %1, %2" : "=s"(r.pm[1]) : "i"(n), "v"(r.cv)); }
    else if constexpr (p == 2) r.m = dpp_umax<0x141>(r.m);
    else if constexpr (p == 3) r.m = dpp_umax<0x140>(r.m);
    else if constexpr (p == 4) { asm volatile("v_cmp_eq_u32_e64 %0, %1, %2" : "=s"(r.pm[0]) : "v"(r.m), "v"(r.k[0])); cnd_s(r.cnd, r.m, r.pm[1]); }
    else {
#pragma unroll
      for (int i = p == 5 ? 0 : 2 * p - 9; i < 2 * p - 7 && i < 7 - n; ++i) cnd_s(r.k[i], r.k[i + 1], r.pm[0]);
    }
    asm volatile("" : "+v"(r.m));
  }
}

constexpr unsigned PF_DUM = 0x27C00u;
#define KRING 0x0000u
#define VRING 0x8000u
constexpr unsigned SD_BASE = 0x10000u, MO_XR = 0x22000u, MO_H = 0x25200u, MO_C = 0x26400u;
constexpr unsigned RTB = 254u;
constexpr unsigned RED0 = 208u, RED1 = 244u;
constexpr unsigned MO_HH = 0x26C00u;
constexpr unsigned WF_FL = 0x27000u;
constexpr unsigned MO_RS = 0x26800u, MO_KEY = 0x20000u;
constexpr unsigned BP_W = 23552u;
constexpr size_t SHM_ATTN = 0x28000;
constexpr unsigned KRS = 256u, KTILEB = 64u * KRS;
constexpr unsigned LAH = 3u;
constexpr unsigned KMASK = SEQ * KRS - 1u;
constexpr unsigned WRS = HQ * DH * 2u;
constexpr unsigned XRS = DM * 2u;

#define W4_ACLOB_HI "a128","a129","a130","a131","a132","a133","a134","a135","a136","a137","a138","a139","a140","a141","a142","a143","a144","a145","a146","a147","a148","a149","a150","a151","a152","a153","a154","a155","a156","a157","a158","a159","a160","a161","a162","a163","a164","a165","a166","a167","a168","a169","a170","a171","a172","a173","a174","a175","a176","a177","a178","a179","a180","a181","a182","a183","a184","a185","a186","a187","a188","a189","a190","a191","a192","a193","a194","a195","a196","a197","a198","a199","a200","a201","a202","a203","a204","a205","a206","a207","a208","a209","a210","a211","a212","a213","a214","a215","a216","a217","a218","a219","a220","a221","a222","a223","a224","a225","a226","a227","a228","a229","a230","a231","a232","a233","a234","a235","a236","a237","a238","a239","a240","a241","a242","a243","a244","a245","a246","a247","a248","a249","a250","a251","a252","a253","a254","a255"
#define W4_ACLOB "a0","a1","a2","a3","a4","a5","a6","a7","a8","a9","a10","a11","a12","a13","a14","a15","a16","a17","a18","a19","a20","a21","a22","a23","a24","a25","a26","a27","a28","a29","a30","a31","a32","a33","a34","a35","a36","a37","a38","a39","a40","a41","a42","a43","a44","a45","a46","a47","a48","a49","a50","a51","a52","a53","a54","a55","a56","a57","a58","a59","a60","a61","a62","a63","a64","a65","a66","a67","a68","a69","a70","a71","a72","a73","a74","a75","a76","a77","a78","a79","a80","a81","a82","a83","a84","a85","a86","a87","a88","a89","a90","a91","a92","a93","a94","a95","a96","a97","a98","a99","a100","a101","a102","a103","a104","a105","a106","a107","a108","a109","a110","a111","a112","a113","a114","a115","a116","a117","a118","a119","a120","a121","a122","a123","a124","a125","a126","a127"

DI float max2f(float a, float b) { float r; asm("v_max_f32_e32 %0, %1, %2" : "=v"(r) : "v"(a), "v"(b)); return r; }
DI void lds_st32(unsigned addr, unsigned v) { asm volatile("ds_write_b32 %0, %1" :: "v"(addr), "v"(v) : "memory"); }

struct W4C {
  unsigned ksx[2];
  unsigned vkb[4], vvb[2], vksrc[2], vvsrc, vvsrc0, m0k, m0v, sofk, ldsb;
  __amdgpu_buffer_rsrc_t srdK, srdV, srdF; unsigned sfo;
  f32x4 S[2][4][4];
  f32x4 NEGM[4], L[4];
  u32x4 ONE; unsigned IDX;
  bf16x8 Vf[8][2];
  u32x4 Pk[4][2];
  bf16x4 vlo[8][2], vup[8][2];
  unsigned pm0[2], psf[2];
  __amdgpu_buffer_rsrc_t srdP; unsigned pfo, pfm0, pfv, pfst, pfend, pdum;
};

constexpr unsigned FCT = 0x27400u, SEL_C = 0x27800u;
constexpr int AO_(int db, int qb) { return 32 * qb + 4 * db; }
constexpr int AQ_(int qb, int j) { return 128 + 16 * qb + 4 * j; }
constexpr int AK_(int kb, int j) { return 192 + 16 * kb + 4 * j; }

template <int I> DI float acc_read() { float r; asm volatile("v_accvgpr_read_b32 %0, a[%c1]" : "=v"(r) : "i"(I)); return r; }
template <int I> DI void acc_write(const unsigned v) { asm volatile("v_accvgpr_write_b32 a[%c1], %0" :: "v"(v), "i"(I)); }

template <int I, int VSLOT> DI void tr_rd(const W4C& c, bf16x4 (&lo)[8][2], bf16x4 (&up)[8][2]) {
  constexpr int db = I >> 2, k2 = (I >> 1) & 1, off = VSLOT + 8192 * k2 + 4096 * (I & 1) + 512 * (db >> 1);
  if constexpr ((I & 1) == 0) asm volatile("ds_read_b64_tr_b16 %0, %1 offset:%2" : "=v"(lo[db][k2]) : "v"(c.vvb[db & 1]), "i"(off) : "memory");
  else                        asm volatile("ds_read_b64_tr_b16 %0, %1 offset:%2" : "=v"(up[db][k2]) : "v"(c.vvb[db & 1]), "i"(off) : "memory");
}
template <int I, int VSLOT> DI void tr_read_(W4C& c) { tr_rd<I, VSLOT>(c, c.vlo, c.vup); }
template <int I, int SLOT> DI void k_read(W4C& c) {
  asm volatile("ds_read_b128 a[%c1:%c2], %0 offset:%c3" :: "v"(c.vkb[I & 3]), "i"(192 + 4 * I), "i"(195 + 4 * I), "i"(SLOT + 4096 * (I >> 2)) : "memory");
}
template <int KB, int QB, int J, int CM> DI void qk16(f32x4& s, const f32x4& negm) {
  constexpr int ak = AK_(KB, J), aq = AQ_(QB, J);
  if constexpr (CM == 2)      asm volatile("mf16 %0, a[%c1:%c2], a[%c3:%c4], 0" : "=&v"(s) : "i"(ak), "i"(ak + 3), "i"(aq), "i"(aq + 3));
  else if constexpr (CM == 1) asm volatile("mf16 %0, a[%c2:%c3], a[%c4:%c5], %1" : "=&v"(s) : "v"(negm), "i"(ak), "i"(ak + 3), "i"(aq), "i"(aq + 3));
  else                        asm volatile("mf16 %0, a[%c1:%c2], a[%c3:%c4], %0" : "+v"(s) : "i"(ak), "i"(ak + 3), "i"(aq), "i"(aq + 3));
}
template <int X, int M> DI void qk_mfma(W4C& c) { constexpr int kb = M >> 4, qb = (M >> 2) & 3, j = (qb & 1) ? 3 - (M & 3) : (M & 3); qk16<kb, qb, j, ((M & 3) == 0)>(c.S[X][kb][qb], c.NEGM[qb]); }
template <int AO, bool ZERO> DI void pv16(const bf16x8& A, const u32x4& B) {
  if constexpr (ZERO) asm volatile("mf16 a[%c2:%c3], %0, %1, 0" :: "v"(A), "v"(B), "i"(AO), "i"(AO + 3));
  else                asm volatile("mf16 a[%c2:%c3], %0, %1, a[%c2:%c3]" :: "v"(A), "v"(B), "i"(AO), "i"(AO + 3));
}
template <bool ZERO> DI void sum16(f32x4& l, const u32x4& one, const u32x4& B) {
  if constexpr (ZERO) asm volatile("mf16 %0, %1, %2, 0" : "=&v"(l) : "v"(one), "v"(B));
  else                asm volatile("mf16 %0, %1, %2, %0" : "+v"(l) : "v"(one), "v"(B));
}
DI void sum64(W4C& c, const int qb, const u32x4 (&P)[2]) {
  const u32x8 B = __builtin_shufflevector(P[0], P[1], 0, 1, 2, 3, 4, 5, 6, 7);
  asm volatile("sm64 %0, %1, %2, %3" : "+v"(c.L[qb]) : "v"(c.ONE), "v"(B), "v"(c.IDX));
}
constexpr int NGB = 68;
template <int G, bool ZERO> DI void pv_mfma(W4C& c) {
  constexpr int qb = G / 17, r = G % 17, db = (r >> 1) & 7, k2 = (r ^ db) & 1;
  if constexpr (r < 16) pv16<AO_(db, qb), (ZERO && (r & 1) == 0)>(c.Vf[db][k2], c.Pk[qb][k2]);
  else sum64(c, qb, c.Pk[qb]);
}
DI void real_op() { GAP(); __builtin_amdgcn_s_setprio(0); GAP(); }
DI void add_v(float& acc, float x) { asm volatile("v_add_f32_e32 %0, %0, %1" : "+v"(acc) : "v"(x)); }
template <int X, int E> DI void exp_e(W4C& c) {
  GAP();
  const float e = __builtin_amdgcn_exp2f(c.S[X][E >> 4][(E >> 2) & 3][E & 3]);
  asm volatile("" :: "v"(e));
  c.S[X][E >> 4][(E >> 2) & 3][E & 3] = e;
  GAP();
}
template <int X, int U> DI void pack_to(W4C& c, u32x4 (&P)[4][2]) {
  constexpr int kb = U >> 3, qb = (U >> 1) & 3, h = U & 1;
  GAP();
  const unsigned w = cvtpk(c.S[X][kb][qb][2 * h], c.S[X][kb][qb][2 * h + 1]);
  asm volatile("" :: "v"(w));
  P[qb][kb >> 1][2 * (kb & 1) + h] = w;
  GAP();
}
template <int X, int U> DI void pack_e(W4C& c) { pack_to<X, U>(c, c.Pk); }

constexpr int NBE = 40;
constexpr char PA0[] = "EXETEXFTEXETEXFT" "EXETEXFTEXETEXFT" "EXXEXFEXXEXEXEXF" "ECCECFECCECECECF";
constexpr char PB2[] = "EDEKEEDEKEEDEKFES" "DEKEEWEKFEWEKEEWS" "EKEWEKEEFEEEEEEES" "EEEEEEEEEEEEEEEES";
constexpr char PB0[] = "EDEKEEDEKEEDEKFES" "DEKEEDEKFEDEKEEDS" "EKEDEKEEFDEEEFDES" "EEEEEFEEEEEFEEEES";
constexpr int pcnt(const char* p, char ch, int g) { int n = 0; for (int i = 0; i < g; ++i) n += p[i] == ch; return n; }
constexpr int ppos(const char* p, char ch, int n) { int i = 0; while (p[i] != ch || n-- > 0) ++i; return i; }
constexpr int FA(int n) { constexpr int g[12] = {6, 10, 14, 18, 22, 30, 37, 41, 47, 53, 57, 63}; return g[n]; }
constexpr int FB(int n) { constexpr int g[10] = {14, 25, 33, 42, 47, 50, 56, 59, 62, 67}; return g[n]; }
constexpr int fa_of(int m) { for (int n = 0; n < 12; ++n) if (FA(n) == m) return n; return -1; }
constexpr int fb_of(int m) { for (int n = 0; n < 10; ++n) if (FB(n) == m) return n; return -1; }
constexpr int DB(int n) { return ppos(PB0, 'D', n); }
constexpr int kyo(int g0, int g1) { const int n = 2 * (pcnt(PB0, 'K', g1) - pcnt(PB0, 'K', g0)); return n < 15 ? n : 15; }
static_assert(pcnt(PA0, 'E', 64) == 64 - NBE && pcnt(PB0, 'E', NGB) == NBE && pcnt(PB0, 'D', NGB) == 10 && pcnt(PB0, 'K', NGB) == 8 && FB(3) == DB(8) - 1 && FB(4) == DB(9) - 1 && PA0[FA(6)] == 'F' && PA0[FA(8)] == 'F', "");
template <int X, int M, typename H> DI void gapA(W4C& c, const int t, H& hook) {
  constexpr int Xp = X ^ 1;
  constexpr const char* pa = PA0;
  constexpr char ty = pa[M];
  constexpr int nt = 2 * pcnt(pa, 'T', M) + pcnt(pa, 'X', M), np = pcnt(pa, 'X', M) + 2 * pcnt(pa, 'C', M);
  if constexpr (M == 0) asm volatile("" ::: W4_ACLOB, W4_ACLOB_HI);
  qk_mfma<X, M>(c);
  hook(std::integral_constant<int, M>{});
  if constexpr (ty == 'T' || ty == 'X') tr_read_<nt, Xp * 0x4000>(c);
  if constexpr (ty == 'T') tr_read_<nt + 1, Xp * 0x4000>(c);
  if constexpr (ty == 'X' || ty == 'C') pack_e<Xp, np>(c);
  if constexpr (ty == 'C') pack_e<Xp, np + 1>(c);
  if constexpr (ty == 'E') exp_e<Xp, NBE + pcnt(pa, 'E', M)>(c);
  {
    if constexpr (M == FA(6))      { c.pm0[0] = c.m0k + (unsigned)X * 0x4000u; c.pm0[1] = c.m0v + (unsigned)(X ^ 1) * 0x4000u; asm volatile("" : "+s"(c.pm0[0]), "+s"(c.pm0[1])); }
    else if constexpr (M == FA(8)) asm volatile("s_mov_b32 %1, %0\n\ts_add_u32 %0, %0, %2\n\ts_and_b32 %0, %0, %3" : "+s"(c.psf[0]), "=&s"(c.psf[1]) : "i"(KTILEB), "i"(KMASK) : "scc");
  }
  hook(std::integral_constant<int, 100 + M>{});
  GAP();
}
#define W4_V8(a, k) "+v"(a[0][k]), "+v"(a[1][k]), "+v"(a[2][k]), "+v"(a[3][k]), "+v"(a[4][k]), "+v"(a[5][k]), "+v"(a[6][k]), "+v"(a[7][k])
template <int PFW> DI void phaseA_end(W4C& c) {
  asm volatile("s_waitcnt vmcnt(%16) lgkmcnt(0)" : W4_V8(c.vlo, 0), W4_V8(c.vlo, 1) : "i"(PFW) : "memory");
  asm volatile("" : W4_V8(c.vup, 0), W4_V8(c.vup, 1) : "v"(c.NEGM[0]), "v"(c.NEGM[1]), "v"(c.NEGM[2]), "v"(c.NEGM[3]) : "memory");
  GAP();
  __builtin_amdgcn_s_barrier();
  asm volatile("" ::: "memory");
  GAP();
#pragma unroll
  for (int k2 = 0; k2 < 2; ++k2)
#pragma unroll
    for (int db = 0; db < 8; ++db) c.Vf[db][k2] = __builtin_shufflevector(c.vlo[db][k2], c.vup[db][k2], 0, 1, 2, 3, 4, 5, 6, 7);
}
template <int X, int PFW, typename H, int... Ms> DI void phaseA_(W4C& c, const int t, H& hook, iseq<Ms...>) {
  (gapA<X, Ms>(c, t, hook), ...);
  phaseA_end<PFW>(c);
}
struct NoHook { template <typename T> DI void operator()(T) const {} };
template <int X, int PFW = 0> DI void phaseA(W4C& c, const int t) { NoHook h; phaseA_<X, PFW>(c, t, h, make_iseq<64>{}); }
template <int X, int PFW, typename H> DI void phaseAh(W4C& c, const int t, H& hook) { phaseA_<X, PFW>(c, t, hook, make_iseq<64>{}); }

template <int X, bool ZERO1, bool DK, int PF, int M, typename H> DI void gapB(W4C& c, H& hook) {
  constexpr const char* pb = DK ? PB0 : PB2;
  constexpr char ty = pb[M];
  constexpr int n = pcnt(pb, ty, M) + (!DK && ty == 'D' ? 4 : 0), d8 = DB(8), d9 = DB(9);
  pv_mfma<M, ZERO1>(c);
  if constexpr (M == DB(0) - 1) asm volatile("s_mov_b32 m0, %0" :: "s"(c.pm0[!DK]) : "m0", "memory");
  if constexpr (M == DB(4) - 1) asm volatile("s_mov_b32 m0, %0" :: "s"(c.pm0[DK]) : "m0", "memory");
  if constexpr (M == 57) asm volatile("s_waitcnt lgkmcnt(0)" ::: "memory");
  if constexpr (PF && (M == d8 - 1 || M == d9 - 1))
    asm volatile("s_cmp_lt_u32 %0, %1\n\ts_cselect_b32 m0, %0, %2\n\ts_add_u32 %0, %0, 0x400" : "+s"(c.pfm0) : "s"(c.pfend), "s"(c.pdum) : "m0", "memory", "scc");
  if constexpr (PF && (M == d8 - 2 || M == d9 - 2)) asm volatile("s_add_u32 %0, %0, %1" : "+s"(c.pfo) : "s"(c.pfst) : "scc");
  constexpr int lg = PF ? 56 : 40;
  if constexpr (X == 1 && M == lg - 5) asm volatile("s_add_u32 %0, %0, %1" : "+s"(c.sfo) : "i"(2 * KTILEB) : "scc");
  if constexpr (X == 1 && M == lg - 4) asm volatile("s_and_b32 %0, %0, %1" : "+s"(c.sfo) : "i"(KMASK) : "scc");
  if constexpr (X == 1 && M == lg - 2) asm volatile("s_mov_b32 m0, %0" :: "s"(c.pdum) : "m0", "memory");
  if constexpr (X == 1 && M == lg) asm volatile("buffer_load_dwordx4 %0, %1, %2 offen lds" :: "v"(c.vksrc[0]), "s"(c.srdF), "s"(c.sfo) : "memory");
  hook(std::integral_constant<int, M>{});
  if constexpr (ty == 'K') { k_read<2 * n, (X ^ 1) * 0x4000>(c); k_read<2 * n + 1, (X ^ 1) * 0x4000>(c); }
  if constexpr (ty == 'D') {
    if constexpr (n < 4) { asm volatile("buffer_load_dwordx4 %0, %1, %2 offen offset:%c3 lds" :: "v"((n & 2) ? c.ksx[n & 1] : c.vksrc[n & 1]), "s"(c.srdK),  "s"(c.psf[0]), "i"(1024 * (n & 3)) : "memory"); }
    else if constexpr (n < 8) asm volatile("buffer_load_dwordx4 %0, %1, %2 offen offset:%c3 lds" :: "v"((n & 1) ? c.vvsrc : c.vvsrc0), "s"(c.srdV),  "s"(c.psf[1]), "i"(1024 * (n & 3)) : "memory");
    else if constexpr (PF) asm volatile("buffer_load_dwordx4 %0, %1, %2 offen sc1 lds" :: "v"(c.pfv), "s"(c.srdP), "s"(c.pfo) : "memory");
  }
  if constexpr (ty == 'W') asm volatile("buffer_load_dwordx4 %0, %1, %2 offen offset:%c3 sc1 lds" :: "v"(c.pfv), "s"(c.srdP), "s"(c.pfo), "i"(1024 * n) : "memory");
  if constexpr (ty == 'E') exp_e<X, DK ? n : 16 * ((n >> 2) & 3) + 4 * (n >> 4) + (n & 3)>(c);
  hook(std::integral_constant<int, 100 + M>{});
  GAP();
}
template <int X, bool ZERO1, bool DK, int PF, typename H, int... Ms> DI void phaseB_(W4C& c, H& hook, iseq<Ms...>) {
  GAP();
  (gapB<X, ZERO1, DK, PF, Ms>(c, hook), ...);
  hook(std::integral_constant<int, 72>{});
  GAP();
}
template <int X, int PF> DI void phaseB(W4C& c) { NoHook h; phaseB_<X, false, true, PF>(c, h, make_iseq<NGB>{}); }
template <int X, int PF, typename H> DI void phaseBh(W4C& c, H& hook) { phaseB_<X, false, true, PF>(c, hook, make_iseq<NGB>{}); }

constexpr unsigned QP_STG = 0xC000u, QP_W8 = 0x24000u, QP_CS = 0x18000u;
constexpr int PR0 = 1, PR1 = 8;
constexpr int PJ = 224;
constexpr int NJ = 200, JK2 = 79, JR0 = 103;
constexpr int JT[] = {1000,20004,20005,32000,1001,20006,20007,32000,31000,1002,22000,32000,1003,21000,21001,32000,1016,34000,21002,21003,32000,1017,34000,21004,21005,32000,1018,34000,21006,21007,30000,32000,16000,1019,
  34000,32000,16001,30001,1004,34000,20008,20009,32000,16002,1005,34000,20010,20011,32000,16003,1006,34000,20012,20013,32000,16004,1007,34000,20014,20015,32000,16005,1008,34000,32000,16006,1020,34000,32000,16007,1021,
  34000,32000,10000,10001,10002,10003,10004,10005,1022,34000,32000,10006,10007,10008,10009,10010,10011,1023,34000,32000,10012,10013,10014,10015,10016,10017,1024,34000,32000,10018,10019,10020,10021,10022,10023,1032,34000,
  32000,10024,10025,10026,1033,34000,32000,10027,10028,10029,10030,10031,1034,34000,32000,10032,10033,10034,10035,10036,10037,1035,34000,32000,10038,10039,10040,10041,10042,10043,30002,1036,34000,32000,10044,10045,10046,
  10047,10048,10049,1037,34000,32000,10050,10051,10052,10053,10054,10055,1038,34000,32000,10056,10057,10058,10059,10060,1039,34000,32000,10061,10062,10063,1040,34000,32000,10064,10065,10066,10067,10068,10069,1048,34000,
  20016,32000,10070,10071,1049,34000,20017,32000,10072,10073,10074,10075,10076,10077,1050,34000,20018,32000,10078,10079,10080,10081,10082,10083,1051,34000,20019,32000,10084,10085,10086,10087,10088,10089,1052,34000,20020,
  32000,10090,10091,10092,10093,10094,10095,1053,34000,20021,32000,10096,10097,10098,1054,34000,20022,32000,10099,10100,10101,10102,10103,1055,34000,20023,32000,10104,10105,10106,10107,1056,34000,32000,1064,34000,32000,
  1065,34000,32000,10108,10109,10110,10111,10112,10113,1066,34000,32000,10114,10115,10116,10117,10118,10119,1067,34000,32000,10120,10121,10122,10123,10124,10125,30003,1068,34000,32000,10126,10127,10128,10129,10130,10131,
  1069,34000,32000,10132,10133,10134,1070,34000,32000,10135,10136,10137,10138,10139,1071,34000,32000,10140,10141,10142,10143,1072,34000,20024,32000,1080,34000,20025,32000,1081,34000,20026,32000,10144,10145,10146,10147,
  10148,10149,1082,34000,20027,32000,10150,10151,10152,10153,10154,10155,1083,34000,20028,32000,10156,10157,10158,10159,10160,10161,1084,34000,20029,32000,10162,10163,10164,10165,10166,10167,1085,34000,20030,32000,10168,
  10169,10170,1086,34000,20031,32000,10171,10172,10173,10174,10175,1087,34000,32000,10176,10177,10178,10179,1088,34000,32000,1096,34000,32000,1097,34000,32000,10180,10181,10182,10183,10184,10185,31001,1098,34000,32000,
  10186,10187,10188,10189,10190,10191,1099,34000,32000,10192,10193,10194,10195,30004,1100,34000,23000,23001,32000,10196,10197,10198,10199,10200,1101,34000,23002,23003,32000,10201,10202,10203,10204,1102,34000,23004,23005,
  24000,32000,1103,34000,23006,23007,32000,10205,10206,1104,34000,23008,32000,10207,10208,10209,10210,10211,1112,34000,23009,32000,10212,10213,10214,10215,1113,34000,23010,32000,10216,10217,10218,10219,10220,10221,1114,
  34000,23011,32000,10222,10223,10224,10225,10226,10227,1115,34000,23012,32000,10228,10229,10230,10231,1116,34000,23013,32000,10232,10233,10234,10235,10236,10237,1117,34000,23014,32000,10238,10239,10240,10241,1118,34000,
  23015,32000,10242,10243,1119,34000,32000,10244,10245,10246,10247,10248,10249,1120,34000,32000,10250,10251,30005,2128,34000,24001,24002,32000,2132,34000,32000,10252,10253,2136,34000,32000,10254,10255,2140,34000,23100,
  32000,10256,2001,34000,32000,10257,10258,2005,34000,32000,10259,10260,31002,2009,34000,32000,10261,10262,2013,32000,2002,34000,23101,32000,10263,2003,33000,32000,2006,34000,32000,10264,10265,2007,33001,32000,2010,
  34000,32000,11000,11001,2014,34000,32000,11002,11016,11017,11003,11004,11005,11018,23102,11006,11007,11008,11009,11010,11011,11012,2080,34000,32000,11013,11014,2017,34000,32000,11019,2018,34000,32000,11020,11021,2019,
  34000,32000,11022,2096,34000,32000,11023,11024,2033,34000,32000,2034,34000,32000,11025,11026,2035,34000,32000,11027,11028,2112,34000,32000,11029,11030,2049,34000,32000,10266,10267,2050,34000,32000,10268,10269,2051,
  34000,32000,2087,34000,32000,10270,10271,2022,34000,32000,10272,10273,2021,34000,32000,10274,10275,2020,34000,32000,10276,2103,34000,32000,10277,2038,32000,2037,34000,32000,10278,2036,34000,32000,10279,2119,34000,
  32000,10280,10281,2054,34000,32000,10282,10283,2053,34000,32000,10284,10285,2052,32000,10286,10287,24003,24004,2011,32000,13000,2015,34000,23103,24005,32000,13001,13002,11032,11033,11034,13003,11048,11035,11036,11037,
  11049,11050,11038,11039,11040,11041,11042,11043,11044,2088,34000,32000,11045,11046,2025,34000,32000,11051,2026,34000,32000,11052,11053,2027,34000,32000,11054,2104,34000,32000,11055,11056,2041,34000,32000,11057,11058,
  2042,34000,32000,11059,11060,2043,34000,32000,11061,11062,2120,32000,13016,2057,34000,24006,32000,15000,2058,32000,13017,2059,32000,13018,2095,34000,24007,32000,15001,2030,32000,13019,2029,32000,13032,2028,34000,23104,
  32000,15008,2111,34000,23105,32000,15009,2046,34000,23106,23107,32000,2045,34000,24008,24009,32000,2044,34000,23108,23109,32000,2127,34000,23110,23111,32000,2062,34000,24010,24011,32000,2061,32000,13033,2060,34000,
  23112,32000,15016,30105,3064,34000,23113,23114,32000,3001,34000,23115,24012,32000,3002,32000,13034,3003,34000,24013,24014,32000,3071,32000,13035,3006,32000,13048,3005,34000,24015,32000,15017,3004,32000,13049,3072,
  32000,13050,3009,34000,24016,32000,15024,3010,32000,13051,3011,32000,13004,3079,34000,24017,32000,15025,3014,32000,13005,3013,32000,13006,3012,34000,24018,32000,15002,3080,32000,13007,3017,32000,13020,3018,34000,24019,
  32000,15003,3019,32000,13021,3087,32000,13022,3022,34000,24020,32000,15010,3021,32000,13023,3020,32000,13036,3088,34000,24021,32000,15011,3025,32000,13037,3026,32000,13038,3027,34000,24022,32000,15018,3095,32000,13039,
  3030,32000,13052,3029,34000,24023,32000,15019,3028,32000,13053,3096,32000,13054,3033,34000,24024,32000,15026,30102,3034,32000,13055,3035,32000,13008,3103,34000,24025,32000,15027,3038,32000,13009,3037,32000,13010,3036,
  34000,24026,32000,15004,3104,32000,13011,3041,32000,13024,3042,34000,24027,32000,15005,3043,32000,13025,3111,32000,13026,3046,34000,24028,32000,15012,3045,32000,13027,3044,32000,13040,3112,34000,24029,32000,15013,3049,
  32000,13041,3050,32000,13042,3051,34000,24030,32000,15020,3119,32000,13043,3054,34000,24031,32000,15021,3053,32000,13056,3052,32000,13057,3120,32000,13058,3057,32000,13059,3058,32000,13012,3059,32000,13013,3127,34000,
  32000,15028,15029,3062,32000,13014,3061,32000,13015,3060,34000,32000,15006,13028,13029,13030,15007,13031,13044,15014,13045,13046,15015,13047,13060,15022,13061,13062,15023,13063,15030,15031,};
constexpr short JS[] = {0,4,8,12,16,21,26,33,37,44,50,56,62,66,70,79,88,97,106,112,120,129,138,148,157,165,171,180,186,196,206,216,226,233,242,250,253,256,265,274,283,293,299,307,314,318,322,332,342,352,362,369,378,385,
  388,391,400,410,417,428,437,443,450,459,467,477,487,495,505,513,519,528,533,539,544,549,554,559,564,570,572,577,580,585,588,593,611,616,620,625,629,634,637,642,647,652,657,662,665,670,675,680,684,688,690,694,698,703,
  708,713,719,722,746,751,755,760,764,769,774,779,784,787,792,795,798,803,806,809,814,819,824,829,834,839,844,847,852,858,863,866,871,874,877,882,885,888,893,896,899,904,907,910,915,918,921,926,929,932,937,940,943,948,
  951,954,959,962,965,970,973,976,981,985,988,993,996,999,1004,1007,1010,1015,1018,1021,1026,1029,1032,1037,1040,1043,1048,1051,1056,1059,1062,1065,1068,1071,1074,1079,1082,1085,1108,};
enum : int { VK_CW, VK_GEN, VK_S, VK_W8, VK_A, VK_WR, VK_STS, VK_GAT, VK_XNS, VK_KV, VK_KEY, VK_V1, VK_K2, VK_PF };
struct VOp { int kind, st, q, pos; };
struct VTab { VOp v[192]; int n; };
constexpr VTab mk_vtab() {
  VTab t{}; int n = 0;
  auto add = [&](int kind, int st, int q, int pos) { t.v[n].kind = kind; t.v[n].st = st; t.v[n].q = q; t.v[n].pos = pos; ++n; };
  const int PS[12] = {7, 10, 12, 15, 18, 20, 23, 26, 28, 31, 33, 36}, P3[12] = {20, 22, 24, 26, 28, 30, 32, 34, 36, 38, 40, 42}, P4[12] = {12, 14, 16, 19, 21, 23, 26, 28, 30, 32, 34, 36};
  for (int q = 0; q < 12; ++q) add(VK_S, 0, q, q < PS_N ? -3 : -2);
  for (int q = 0; q < 3; ++q) add(VK_A, 0, q, -2);
  add(VK_GEN, 0, 0, -2); add(VK_CW, 0, 0, -2);
  for (int q = 0; q < 12; ++q) add(VK_S, 1, q, q < 8 ? -1 : q - 8);
  for (int q = 0; q < 12; ++q) add(VK_S, 2, q, 4 + q);
  add(VK_STS, 0, 0, 12);
  for (int q = 0; q < 3; ++q) add(VK_WR, 0, q, 16 + q);
  for (int s = 3; s < 9; ++s) for (int q = 0; q < 12; ++q) add(s == 8 && q < 4 ? VK_W8 : VK_S, s, q, 32 * (s - 3) + (s == 3 ? P3[q] : s == 4 ? P4[q] : PS[q]));
  add(VK_GAT, 0, 0, 128 + 9); add(VK_GAT, 0, 1, 128 + 14);
  add(VK_XNS, 0, 0, 160 + 22); add(VK_XNS, 0, 1, 160 + 25); add(VK_XNS, 0, 2, 160 + 30);
  for (int q = 0; q < 12; ++q) add(VK_KV, 0, q, 192 + PS[q]);
  add(VK_KEY, 0, 0, PJ + 57);
  for (int q = 0; q < 4; ++q) add(VK_V1, 0, q, PJ + 60 + 3 * q);
  for (int q = 0; q < 4; ++q) add(VK_K2, 0, q, PJ + JK2 + 6 * q);
  add(VK_PF, 0, 0, PJ + JR0); add(VK_PF, 0, 1, PJ + JR0 + 6);
  for (int i = 1; i < n; ++i) { const VOp x = t.v[i]; int j = i; while (j > 0 && t.v[j - 1].pos > x.pos) { t.v[j] = t.v[j - 1]; --j; } t.v[j] = x; }
  t.n = n;
  return t;
}
constexpr VTab VT = mk_vtab();
constexpr int vq_first(int pos) { int i = 0; while (i < VT.n && VT.v[i].pos < pos) ++i; return i; }
constexpr int vq_wait(int kind, int st, int pos) {
  int last = -1;
  for (int i = 0; i < VT.n; ++i) if (VT.v[i].kind == kind && VT.v[i].st == st) last = i;
  return vq_first(pos) - 1 - last;
}
constexpr int imin(int a, int b) { return a < b ? a : b; }
constexpr int vq_grp(int i) {
  const VOp o = VT.v[i];
  return o.kind == VK_S ? 3 * o.st + (o.q >> 2) : o.kind == VK_W8 ? 40 : o.kind == VK_KV ? 41 + (o.q >> 2) : o.kind == VK_V1 ? 44 : o.kind == VK_K2 ? 45 : o.kind == VK_PF ? 50 + o.q : -1;
}
constexpr bool vq_newm0(int i) { int j = i - 1; while (j >= 0 && vq_grp(j) < 0) --j; return j < 0 || vq_grp(j) != vq_grp(i); }
template <bool NEWM0, int OFF> DI void lds_dma(const unsigned m0, const unsigned vo, const __amdgpu_buffer_rsrc_t srd, const unsigned so) {
  if constexpr (NEWM0) asm volatile("s_mov_b32 m0, %0\n\ts_nop 0\n\tbuffer_load_dwordx4 %1, %2, %3 offen offset:%c4 lds" :: "s"(m0), "v"(vo), "s"(srd), "s"(so), "i"(OFF) : "m0", "memory");
  else                 asm volatile("buffer_load_dwordx4 %0, %1, %2 offen offset:%c3 lds" :: "v"(vo), "s"(srd), "s"(so), "i"(OFF) : "memory");
}
template <int I0, typename F, int... Is> DI void vq_run(F& vm, iseq<Is...>) { (vm(std::integral_constant<int, I0 + Is>{}), ...); }
template <int POS, typename F> DI void vq_issue(F& vm) { vq_run<vq_first(POS)>(vm, make_iseq<vq_first(POS + 1) - vq_first(POS)>{}); }

constexpr int AW_(int T, int db, int ks) { return ((T & 1) ? 192 : 0) + 16 * db + 4 * ks; }
constexpr int AX_(int T, int qb, int ks) { return ((T & 1) ? 96 : 64) + 16 * qb + 4 * ks; }
template <int T, int DB, int QB, int KS> DI void qp_mfma(f32x16& d) {
  constexpr int aw = AW_(T, DB, KS), ax = AX_(T, QB, KS);
  if constexpr (T == 0 && KS == 0) asm volatile("mf32 %0, a[%c1:%c2], a[%c3:%c4], 0" : "=&v"(d) : "i"(aw), "i"(aw + 3), "i"(ax), "i"(ax + 3));
  else                             asm volatile("mf32 %0, a[%c1:%c2], a[%c3:%c4], %0" : "+v"(d) : "i"(aw), "i"(aw + 3), "i"(ax), "i"(ax + 3));
}
struct QP {
  f32x16 A[4][2];
  bf16x4 cl[2][4], cu[2][4];
  f32x4 cs; u32x4 sel;
  unsigned vwb, vwc, vw[4], vx[4], vxb[4], m0w, m0x, sofw, sofx, kk, sw, sx;
  __amdgpu_buffer_rsrc_t srdW, srdX;
};
template <int S> DI void qp_next(QP& p) {
  constexpr int d = S < PR0 || S > PR1 ? 0 : ((S - PR0) & 1) ? -1 : 1;
  const unsigned k = (unsigned)S + (d == 0 ? 0u : d > 0 ? p.kk : 0u - p.kk);
  p.sw = p.sofw + (k << 19); p.sx = p.sofx + (k << 7);
  asm volatile("" : "+s"(p.sw), "+s"(p.sx));
}
constexpr unsigned qp_wbase(int T) { return T == 8 ? QP_W8 : QP_STG * (T % 3); }
template <int T, int DB, int KS, int U> DI void qp_tr(QP& p) {
  asm volatile("ds_read_b64_tr_b16 a[%c1:%c2], %0 offset:%c3" :: "v"(p.vwb + qp_wbase(T)), "i"(AW_(T, DB, KS) + 2 * U), "i"(AW_(T, DB, KS) + 2 * U + 1), "i"(4096 * KS + 2048 * U + 512 * DB) : "memory");
}
template <int T, int QB, int KS> DI void qp_xr(QP& p) {
  asm volatile("ds_read_b128 a[%c1:%c2], %0 offset:%c3" :: "v"(p.vxb[KS] + QP_STG * (T % 3)), "i"(AX_(T, QB, KS)), "i"(AX_(T, QB, KS) + 3), "i"(4096 * QB) : "memory");
}
template <int T, int I> DI void qp_csr(QP& p) {
  constexpr int ks = I >> 1;
  if constexpr ((I & 1) == 0) asm volatile("ds_read_b64_tr_b16 %0, %1 offset:%2" : "=v"(p.cl[T == 8][ks]) : "v"(p.vwc + qp_wbase(T)), "i"(4096 * ks) : "memory");
  else                        asm volatile("ds_read_b64_tr_b16 %0, %1 offset:%2" : "=v"(p.cu[T == 8][ks]) : "v"(p.vwc + qp_wbase(T)), "i"(4096 * ks + 2048) : "memory");
}
template <int KS, bool ZERO, int L = 0> DI void qp_csd(QP& p) {
  const bf16x8 w = __builtin_shufflevector(p.cl[L][KS], p.cu[L][KS], 0, 1, 2, 3, 4, 5, 6, 7);
  if constexpr (ZERO) asm volatile("mf16 %0, %1, %2, 0" : "=&v"(p.cs) : "v"(w), "v"(p.sel));
  else                asm volatile("mf16 %0, %1, %2, %0" : "+v"(p.cs) : "v"(w), "v"(p.sel));
}
template <int T, int R> DI void qp_rd(QP& p) {
  if constexpr (R < 8) qp_csr<T, R>(p);
  else {
    constexpr int ks = (R - 8) / 10, u = (R - 8) % 10;
    if constexpr (u == 0) qp_xr<T, 0, ks>(p);
    else if constexpr (u == 3) qp_xr<T, 1, ks>(p);
    else { constexpr int v = u < 3 ? u - 1 : u - 2; qp_tr<T, (v >> 1), ks, (v & 1)>(p); }
  }
}
template <int T, int R0, int... Rs> DI void qp_rds(QP& p, iseq<Rs...>) { (qp_rd<T, R0 + Rs>(p), ...); }
#define QP_CSV(L) "+v"(p.cl[L][0]), "+v"(p.cl[L][1]), "+v"(p.cl[L][2]), "+v"(p.cl[L][3]), "+v"(p.cu[L][0]), "+v"(p.cu[L][1]), "+v"(p.cu[L][2]), "+v"(p.cu[L][3])
constexpr int LN_P0 = 32 + 18, LN_P1 = 160 + 21;
constexpr int ln_slots(int pos) { int n = 0; for (int q = LN_P0; q < pos; ++q) if (vq_first(q + 1) == vq_first(q) && (q & 31) != 5) ++n; return n; }
constexpr int ln_lo(int pos) {
  if (pos <= 1) return 0;
  if (pos <= 9) return 3 * (pos - 1);
  if (pos <= 11) return 24 + 4 * (pos - 9);
  if (pos < LN_P0) return LN_N0;
  if (pos <= LN_P1) return LN_N0 + (ln_slots(pos) * 97) / ln_slots(LN_P1);
  const int s = LN_XN + 1 + (pos - LN_P1) / 3;
  return s < LN_KEY ? s : LN_KEY;
}
template <int I0, int... Is> DI void ln_steps(LNJ& j, const unsigned row, iseq<Is...>) { (ln_step<I0 + Is>(j, row), ...); }
template <int T, int M, typename FV, typename FH> DI void qp_gap(QP& p, FV& vm, FH& hook, LNJ& lj, const unsigned lrow) {
  constexpr int ks = M >> 3, db = (M >> 1) & 3, qb = M & 1, pos = 32 * T + M, BG = T == 0 ? 12 : 6;
  if constexpr (T == 0 && M == 0) asm volatile("s_waitcnt lgkmcnt(10)" ::: "memory", W4_ACLOB, W4_ACLOB_HI);
  if constexpr (T == 0 && M == 8) asm volatile("s_waitcnt lgkmcnt(0)" : QP_CSV(0) :: "memory");
  if constexpr (T > 0 && M == 0) asm volatile("s_waitcnt lgkmcnt(0)" ::: "memory");
  if constexpr (M == BG) {
    constexpr int w0 = vq_wait(VK_S, T + 1, pos);
    constexpr int w = T == 1 ? imin(w0, vq_wait(VK_WR, 0, pos)) : T == 6 ? imin(w0, vq_wait(VK_W8, 8, pos)) : w0;
    asm volatile("s_waitcnt vmcnt(%0)\n\ts_barrier" :: "i"(w) : "memory");
  }
  qp_mfma<T, db, qb, ks>(p.A[db][qb]);
  if constexpr (T == 0) {
    if constexpr (M < 5) qp_rds<0, 28 + 4 * M>(p, make_iseq<4>{});
    if constexpr (M == 5 || M == 6) qp_rds<0, 4 * (M - 5)>(p, make_iseq<4>{});
    if constexpr (M == 18) asm volatile("s_waitcnt lgkmcnt(10)" : QP_CSV(0) :: "memory");
    if constexpr (M >= 12 && M < 28) qp_rds<1, 3 * (M - 12)>(p, make_iseq<3>{});
  } else {
    if constexpr (M == 13) asm volatile("s_waitcnt lgkmcnt(%8)" : QP_CSV(0) : "i"(T == 6 ? 10 : 6) : "memory");
    if constexpr (T == 6 && M == 17) asm volatile("s_waitcnt lgkmcnt(6)" : QP_CSV(1) :: "memory");
    if constexpr (M >= 6 && M < 30) qp_rds<T + 1, 2 * (M - 6)>(p, make_iseq<2>{});
    if constexpr (T == 6 && M >= 6 && M < 14) qp_csr<8, M - 6>(p);
    if constexpr (T == 6 && M >= 30) { qp_tr<8, 0, M - 30, 0>(p); qp_tr<8, 0, M - 30, 1>(p); }
  }
  vq_issue<pos>(vm);
  if constexpr (pos == 3 || pos == 19 || pos == 32 + 11 || (M == 5 && T >= 2 && T <= 5)) qp_next<(pos == 3 ? 2 : pos == 19 ? 3 : T + 3)>(p);
  GAP();
  hook(std::integral_constant<int, pos>{});
  ln_steps<ln_lo(pos)>(lj, lrow, make_iseq<ln_lo(pos + 1) - ln_lo(pos)>{});
  if constexpr (T == 0 && M >= 8 && M < 12) qp_csd<M - 8, M == 8>(p);
  if constexpr (T == 0 && M >= 18 && M < 28 && M % 3 == 0) qp_csd<(M - 18) / 3, false>(p);
  if constexpr (T > 0 && M >= 13 && M < 23 && M % 3 == 1) qp_csd<(M - 13) / 3, false>(p);
  if constexpr (T == 6 && (M == 17 || M == 21 || M == 24 || M == 27)) qp_csd<(M == 17 ? 0 : (M - 18) / 3), false, 1>(p);
  GAP();
}
template <int T, typename FV, typename FH, int... Ms> DI void qp_gaps(QP& p, FV& vm, FH& hook, LNJ& lj, const unsigned lrow, iseq<Ms...>) { (qp_gap<T, Ms>(p, vm, hook, lj, lrow), ...); }
template <int T, typename FV, typename FH> DI void qp_iter(QP& p, FV& vm, FH& hook, LNJ& lj, const unsigned lrow) { qp_gaps<T>(p, vm, hook, lj, lrow, make_iseq<32>{}); }
template <typename FV, typename FH, int... Ts> DI void qp_iters(QP& p, FV& vm, FH& hook, LNJ& lj, const unsigned lrow, iseq<Ts...>) { (qp_iter<Ts>(p, vm, hook, lj, lrow), ...); }

struct QFT { float a[2], cv[4], r1, m[4], m2[4]; unsigned w[8]; u32x4 ca[4], nb[2]; };
template <int N> DI void q_fin(QP& p, QFT& t) {
  constexpr int tau = N / 36, n = N % 36, h = n / 12, u = n % 12, db = tau >> 1, qb = tau & 1;
  f32x16& d = p.A[db][qb];
  if constexpr (n >= 28) acc_write<AQ_(2 * qb + ((n - 28) >> 2), db) + (n & 3)>(t.w[n - 28]);
  else if constexpr (n >= 24) { GAP(); auto pr = __builtin_amdgcn_permlane16_swap(t.w[u], t.w[4 + u], false, false); t.w[u] = pr[0]; t.w[4 + u] = pr[1]; asm volatile("" :: "v"(t.w[u]), "v"(t.w[4 + u])); GAP(); }
  else if constexpr (u < 8) { float v = d[8 * h + u]; asm volatile("v_mul_f32_e32 %0, %0, %1" : "+v"(v) : "v"(t.a[qb])); d[8 * h + u] = v; }
  else { GAP(); t.w[4 * h + u - 8] = cvtpk(d[8 * h + 2 * (u - 8)], d[8 * h + 2 * (u - 8) + 1]); asm volatile("" :: "v"(t.w[4 * h + u - 8])); GAP(); }
}
template <int Q, int I> DI void ref_step(W4C& c, QFT& t) {
  f32x4& s = c.S[0][0][Q];
  if constexpr (I == 0) asm volatile("v_max3_f32 %0, %1, %2, %3" : "=v"(t.m[Q]) : "v"(s[0]), "v"(s[1]), "v"(s[2]));
  else if constexpr (I == 1) asm volatile("v_max_f32_e32 %0, %0, %1" : "+v"(t.m[Q]) : "v"(s[3]));
  else if constexpr (I == 2 || I == 5) asm volatile("v_mov_b32_e32 %0, %1" : "=v"(t.m2[Q]) : "v"(t.m[Q]));
  else if constexpr (I == 3) asm volatile("v_permlane16_swap_b32_e32 %0, %1" : "+v"(t.m[Q]), "+v"(t.m2[Q]));
  else if constexpr (I == 4) asm volatile("v_max_f32_e32 %0, %0, %1" : "+v"(t.m[Q]) : "v"(t.m2[Q]));
  else if constexpr (I == 6) asm volatile("v_permlane32_swap_b32_e32 %0, %1" : "+v"(t.m[Q]), "+v"(t.m2[Q]));
  else if constexpr (I < 11) { float n; asm volatile("v_min_f32_e64 %0, -%1, -%2" : "=v"(n) : "v"(t.m[Q]), "v"(t.m2[Q])); c.NEGM[Q][I - 7] = n; }
  else { float v = s[I - 11]; add_v(v, c.NEGM[Q][0]); s[I - 11] = v; }
}
template <int J, int CODE, typename FV> DI void qj_op(QP& p, W4C& c, QFT& t, FV& vm, const unsigned csa) {
  constexpr int kind = CODE / 1000, a = CODE % 1000;
  if constexpr (kind == 1) {
    constexpr int tau = a >> 4, u = a & 15, db = tau >> 1, qb = tau & 1;
    if constexpr (u < 8) qp_mfma<7 + (u >> 2), db, qb, (u & 3)>(p.A[db][qb]);
    else asm volatile("mf32 %0, %1, %2, %0" : "+v"(p.A[db][qb]) : "v"(t.ca[db]), "v"(t.nb[qb]));
  } else if constexpr (kind == 2 || kind == 3) { constexpr int kb = (a >> 4) & 3, qb = (a >> 2) & 3; qk16<kb, qb, (a & 3), (a >> 6)>(c.S[kind - 2][kb][qb], c.NEGM[qb]); }
  else if constexpr (kind == 10) q_fin<a>(p, t);
  else if constexpr (kind == 11) ref_step<(a >> 4), (a & 15)>(c, t);
  else if constexpr (kind == 13) exp_e<0, a>(c);
  else if constexpr (kind == 15) pack_e<0, a>(c);
  else if constexpr (kind == 16) {
    constexpr int d = a >> 1;
    if constexpr ((a & 1) == 0) { t.r1 = t.cv[d] - rbf(t.cv[d]); t.ca[d][0] = cvtpk(t.cv[d], t.r1); asm volatile("" : "+v"(t.r1), "+v"(t.ca[d])); }
    else { t.ca[d][1] = cvtpk(t.r1 - bfhi(t.ca[d][0]), 0.f); asm volatile("" : "+v"(t.ca[d])); }
  } else if constexpr (kind == 20) qp_tr<8, (a >> 3), ((a >> 1) & 3), (a & 1)>(p);
  else if constexpr (kind == 21) qp_xr<8, (a >> 2), (a & 3)>(p);
  else if constexpr (kind == 22)
    asm volatile("ds_read_b32 %0, %4\n\tds_read_b32 %1, %4 offset:128\n\tds_read_b32 %2, %4 offset:256\n\tds_read_b32 %3, %4 offset:384"
                 : "=&v"(t.cv[0]), "=&v"(t.cv[1]), "=&v"(t.cv[2]), "=&v"(t.cv[3]) : "v"(csa) : "memory");
  else if constexpr (kind == 23) k_read<a % 100, (a >= 100 ? 0x4000 : 0)>(c);
  else if constexpr (kind == 24) tr_read_<a, 0>(c);
  else if constexpr (kind == 30) {
    if constexpr (a == 0) asm volatile("s_waitcnt lgkmcnt(8)" : "+v"(t.cv[0]), "+v"(t.cv[1]), "+v"(t.cv[2]), "+v"(t.cv[3]) :: "memory");
    else if constexpr (a == 200) asm volatile("s_nop 0");
    else asm volatile("s_waitcnt lgkmcnt(%0)" :: "i"(a >= 100 ? a - 100 : 0) : "memory");
  } else if constexpr (kind == 31) {
    if constexpr (a == 0) asm volatile("s_waitcnt vmcnt(%0) lgkmcnt(6)\n\ts_barrier" :: "i"(vq_wait(VK_S, 8, PJ + J)) : "memory");
    else if constexpr (a == 1) asm volatile("s_waitcnt vmcnt(%0)\n\ts_barrier" :: "i"(vq_wait(VK_KV, 0, PJ + J)) : "memory");
    else asm volatile("s_barrier" ::: "memory");
  } else if constexpr (kind == 32) { vq_issue<PJ + J>(vm); GAP(); }
  else if constexpr (kind == 34) real_op();
  else if constexpr (kind == 33) {
    if constexpr (a == 0) { c.pm0[0] = c.m0k + 0x4000u; c.psf[0] = (KTILEB * 3u + c.sofk) & KMASK; asm volatile("" : "+s"(c.pm0[0]), "+s"(c.psf[0])); }
    else { c.pm0[1] = c.m0v; c.psf[1] = (KTILEB * 2u + c.sofk) & KMASK; asm volatile("" : "+s"(c.pm0[1]), "+s"(c.psf[1])); }
  }
}
template <int J, typename FV, int... Is> DI void qj_gap(QP& p, W4C& c, QFT& t, FV& vm, const unsigned csa, iseq<Is...>) { (qj_op<J, JT[JS[J] + Is]>(p, c, t, vm, csa), ...); GAP(); }
template <typename FV, int... Js> DI void qj_gaps(QP& p, W4C& c, QFT& t, FV& vm, const unsigned csa, iseq<Js...>) { (qj_gap<Js>(p, c, t, vm, csa, make_iseq<JS[Js + 1] - JS[Js]>{}), ...); }

constexpr unsigned XRS_ = DM * 2u;
constexpr int op_vr(int f) { return f < 4 ? f : f < 9 ? f : f < 13 ? f - 9 : f - 4; }
constexpr int op_ar(int f) {
  const int n = f - 35;
  return n < 0 ? 84 + 4 * (f - 26) : n < 4 ? 128 + 4 * n : n < 12 ? 4 * (n - 4) : n < 16 ? 144 + 4 * (n - 12) : n < 24 ? 32 + 4 * (n - 16) : n < 32 ? 160 + 4 * (n - 24) : 64 + 4 * (n - 32);
}
constexpr int OP_LA = 4, OP_SF = 22, OP_SP = 25, OP_SD = 29, OP_FEW = 8, OP_NR = 72, OP_RING = 5, OP_GS = 1, OP_P4W = 16, OP_NI = 16, OP_DS = 4;
constexpr short OPU[] = {1,2,3,0,9,10,11,8,5,6,7,4,13,14,15,12,17,18,19,16,21,22,23,20,25,29,26,30,27,31,24,28,5,6,7,11,9,10,13,14,15,3,1,2,17,18,19,23,21,22,25,29,26,30,27,31,0,8,12,16,20,24,28,4,};
constexpr short OPW[] = {12,13,13,13,12,13,13,13,12,12,12,12,12,12,12,12,12,14,16,17,15,14,12,13,13,13,13,13,13,9,9,5,};
constexpr short TT[] = {0,0,22000,0,0,21016,8016,0,0,0,0,21017,8017,0,0,0,0,0,21018,8018,0,0,0,0,21019,8019,0,0,0,11000,0,11001,0,21020,8020,0,11002,0,11003,0,11004,0,21021,8021,0,11005,0,0,11006,0,11007,0,21022,8022,0,
  11008,0,11009,0,11010,0,21023,8023,0,11011,0,0,13004,13005,0,11012,0,21024,8024,0,11013,0,13006,13007,0,11014,0,21025,8025,0,11015,0,0,11016,0,11017,0,21026,8026,0,11018,0,11019,0,11020,0,21027,8027,0,11021,0,0,11022,
  0,11023,0,21028,8028,0,11024,0,11025,0,11026,0,21029,8029,0,11027,0,0,11028,0,11029,0,21030,8030,0,11030,0,11031,0,11032,0,21031,8031,0,11033,15000,0,23000,0,24000,0,22000,0,21032,8032,0,11034,0,11035,0,11036,0,21033,
  8033,0,11037,0,0,11038,0,11039,0,21034,8034,0,11040,0,11041,0,11042,0,21035,8035,0,11043,0,11044,0,11045,0,11046,0,21036,8036,0,11047,0,11048,0,11049,0,21037,8037,0,11050,0,0,11051,0,11052,0,21038,8038,0,11053,0,11054,
  0,11055,0,21039,8039,0,11056,0,0,11057,0,11058,0,21040,8040,0,11059,0,11060,0,11061,0,21041,8041,0,11062,0,0,11063,0,11064,0,21042,8042,0,11065,0,11066,0,11067,0,21043,8043,0,11068,0,0,11069,0,11070,0,21044,8044,0,
  11071,0,11072,0,11073,0,21045,8045,0,11074,0,0,11075,0,11076,0,21046,8046,0,11077,0,11078,0,11079,0,21047,8047,0,11080,0,0,0,0,21048,8048,0,0,0,0,21049,8049,0,0,0,0,0,21050,8050,0,0,0,0,21051,8051,0,0,0,0,0,21052,8052,
  0,0,0,0,21053,8053,0,0,0,0,0,21054,8054,0,0,0,0,21055,8055,0,0,0,25000,0,25001,0,21056,8056,0,25002,0,25003,0,25004,0,21057,8057,0,25005,0,0,25006,0,25007,0,21058,8058,0,25008,0,0,0,21059,8059,0,0,0,0,0,21060,8060,0,0,
  0,0,21061,8061,0,0,0,0,0,21062,8062,0,0,0,0,21063,8063,0,0,0,0,0,21064,8064,0,0,0,0,21065,8065,0,1000,0,0,0,1001,0,21066,8066,0,0,1004,0,0,21067,8067,0,1002,0,0,0,1003,0,21068,8068,0,0,0,0,21069,8069,0,1005,0,0,0,1006,
  0,21070,8070,0,0,1009,0,0,21071,8071,0,1007,0,0,0,1008,0,21072,8072,0,0,0,0,21073,8073,0,1010,0,0,0,1011,0,21074,8074,0,0,1014,0,0,21075,8075,0,1012,0,0,0,31000,1013,0,21076,8076,0,3000,0,31000,4000,0,31000,4105,0,
  21077,8077,0,4210,0,4301,0,31000,4406,0,0,21078,8078,0,5004,6000,6001,0,31000,6002,6003,0,5103,4011,6100,0,21079,8079,0,6101,6102,0,6103,0,5203,4104,6200,0,6201,6202,0,21080,8080,0,5303,6203,6304,0,7000,4209,6305,0,
  6306,6307,0,21081,8081,0,5403,4314,6404,0,6405,0,6406,6407,0,5003,4402,6004,0,21082,8082,0,6005,6006,0,5103,6007,6116,0,7001,4007,6117,0,21083,8083,0,6118,6119,0,4112,0,5204,6216,6217,0,6218,6219,0,21084,8084,0,5303,
  4203,6316,0,6317,6318,0,5403,6319,6408,0,21085,8085,0,7004,4308,6409,0,6410,0,5003,6411,6008,0,4413,6009,0,21086,8086,0,6010,6011,0,5103,6108,6109,0,6110,6111,0,21087,8087,0,5202,7002,6212,6213,0,6214,0,13000,13001,0,
  5303,6215,6312,0,21088,8088,0,6313,6314,0,13002,13003,0,5404,6315,6412,0,21089,8089,0,6413,6414,0,6415,0,7003,0,0,21090,8090,0,0,0,0,21091,8091,0,0,0,0,0,21092,8092,0,0,0,0,21093,8093,0,0,0,0,0,21094,8094,0,0,0,0,
  21095,8095,0,15000,0,0,0,0,21096,8096,0,0,0,0,21097,8097,0,0,0,0,0,21098,8098,0,0,0,0,21099,8099,0,0,0,0,0,21100,8100,0,0,0,0,21101,8101,0,0,0,0,0,21102,8102,0,0,0,0,21103,8103,0,0,14000,0,20000,0,0,21104,8104,0,0,0,0,
  21105,8105,0,0,0,0,0,21106,8106,0,0,0,0,21107,8107,0,0,0,25015,0,25016,0,21108,8108,0,0,0,0,21109,8109,0,0,0,0,0,21110,8110,0,0,0,0,21111,8111,0,0,0,0,0,21112,8112,0,0,0,0,21113,8113,0,0,0,0,0,21114,8114,0,0,0,0,21115,
  8115,0,0,0,0,0,21116,8116,0,0,0,0,21117,8117,0,1100,0,0,0,1101,0,21118,8118,0,0,1104,0,0,21119,8119,0,1102,0,0,0,1103,0,21120,8120,0,0,0,0,21121,8121,0,1105,0,0,0,1106,0,21122,8122,0,0,1109,0,0,21123,8123,0,1107,0,0,0,
  1108,0,21124,8124,0,26000,0,26001,0,26002,0,21125,8125,0,1110,0,0,0,1111,0,21126,8126,0,0,1114,0,0,21127,8127,0,1112,0,0,0,31000,1113,0,0,3000,0,31000,4900,0,31000,4025,0,31000,4130,0,4901,0,4226,0,31000,4331,0,31000,
  4904,0,31000,4429,0,5006,12000,6020,6021,0,31000,6022,6023,0,5105,4034,6120,0,31000,4902,6121,0,6122,6123,0,4127,0,31000,19000,0,31000,13004,13005,0,5208,12001,6224,6225,0,19001,0,31000,13006,13007,0,31000,6226,6227,0,
  31000,19004,0,5309,4232,6324,0,4903,0,31000,19002,0,31000,6325,6326,0,5409,12004,6327,6436,0,19003,0,31000,4328,6437,0,31000,6438,6439,0,5009,4433,6036,0,6037,6038,0,6039,0,5108,12002,6128,6129,0,31000,6130,6131,0,
  5203,6228,6229,0,6230,6231,0,5301,12003,6332,6333,0,31000,6334,6335,0,5400,6432,6433,0,6434,6435,0,0,31000,16000,0,31000,16001,0,31000,16002,0,16003,0,31000,16004,0,31000,16005,0,31000,16006,0,16007,15004,0,0,31000,
  16008,0,31000,16009,0,31000,16010,0,16011,0,31000,16012,0,31000,16013,0,31000,16014,0,16015,0,0,31000,16016,0,31000,16017,0,17000,18000,18001,0,18002,18003,0,31000,18004,18005,0,31000,18006,18007,0,31000,18016,18017,0,
  18018,18019,18008,18009,18010,18011,18012,18013,18014,18015,};
constexpr short TS[] = {0,1,3,4,7,8,9,10,13,14,15,16,17,20,21,22,23,26,27,28,30,32,35,37,39,41,44,46,47,49,51,54,56,58,60,63,65,66,69,71,74,76,79,81,84,86,87,89,91,94,96,98,100,103,105,106,108,110,113,115,117,119,122,
  124,125,127,129,132,134,136,138,141,143,146,148,150,153,155,157,159,162,164,165,167,169,172,174,176,178,181,183,185,187,189,192,194,196,198,201,203,204,206,208,211,213,215,217,220,222,223,225,227,230,232,234,236,239,
  241,242,244,246,249,251,253,255,258,260,261,263,265,268,270,272,274,277,279,280,282,284,287,289,291,293,296,298,299,300,301,304,305,306,307,310,311,312,313,314,317,318,319,320,323,324,325,326,327,330,331,332,333,336,
  337,338,339,340,343,344,345,346,349,350,351,353,355,358,360,362,364,367,369,370,372,374,377,379,380,381,384,385,386,387,388,391,392,393,394,397,398,399,400,401,404,405,406,407,410,411,412,413,414,417,418,419,420,423,
  425,426,427,429,432,433,435,436,439,441,442,443,445,448,449,450,451,454,456,457,458,460,463,464,466,467,470,472,473,474,476,479,480,481,482,485,487,488,489,491,494,495,497,498,501,503,504,505,508,511,512,516,519,522,
  524,526,529,530,533,537,541,545,548,551,553,557,560,563,567,571,574,577,581,583,586,590,593,596,600,604,607,610,612,616,619,622,626,629,633,636,640,642,646,649,652,655,659,662,665,670,672,675,679,682,685,688,692,695,
  698,700,702,703,706,707,708,709,712,713,714,715,716,719,720,721,722,725,726,727,728,729,732,733,734,735,738,739,741,742,743,746,747,748,749,752,753,754,755,756,759,760,761,762,765,766,767,768,769,772,773,774,775,778,
  779,780,781,782,785,786,787,788,791,792,793,796,797,800,801,802,803,806,807,808,809,810,813,814,815,816,819,820,821,823,825,828,829,830,831,834,835,836,837,838,841,842,843,844,847,848,849,850,851,854,855,856,857,860,
  861,862,863,864,867,868,869,870,873,874,875,876,877,880,881,882,883,886,888,889,890,892,895,896,898,899,902,904,905,906,908,911,912,913,914,917,919,920,921,923,926,927,929,930,933,935,936,937,939,942,944,946,948,951,
  953,954,955,957,960,961,963,964,967,969,970,971,974,975,976,980,983,986,988,990,993,996,999,1004,1008,1012,1016,1019,1021,1024,1028,1033,1035,1039,1043,1046,1050,1052,1055,1059,1064,1066,1070,1074,1078,1081,1083,1088,
  1092,1096,1099,1104,1108,1112,1115,1116,1119,1122,1125,1127,1130,1133,1136,1138,1140,1143,1146,1149,1151,1154,1157,1160,1162,1163,1166,1169,1173,1176,1180,1184,1188,1191,1199,};
constexpr bool op_first(int u) { for (int x = u & 32; x < u; ++x) if ((OPU[x] & 3) == (OPU[u] & 3)) return false; return true; }
struct OPS {
  u32x4 fa[OP_LA + 1][4], fb[22]; f32x4 D[2][5], T[5], m[OP_RING], pv[5]; float g0, g1, t0, t1, gx[36];
  u32x4 gv[2][2]; unsigned gs[2];
  unsigned ra[3], sw[2], tg, so; __amdgpu_buffer_rsrc_t srdE, srdI, srdG;
  unsigned va, idx, sa, lb5, lb1, lb2, sn;
  u32x4 srdA, srdL; unsigned fo, sl, sf, tag, bad; unsigned long long seen;
  __amdgpu_buffer_rsrc_t srdB;
};
constexpr unsigned OB = SEQ * HQ * DH * 2;
DI void op_poll(OPS& o) { asm volatile("buffer_load_dword %0, %1, %2, %3 offen sc1" : "=v"(o.fo) : "v"(o.va), "s"(o.srdL), "s"(o.sl) : "memory"); }
DI void op_there(OPS& o) {
  unsigned n;
  asm volatile("s_and_b32 %0, %1, 15\n\ts_cmp_eq_u32 %0, 15\n\ts_cselect_b32 %0, %2, 0" : "=&s"(n) : "s"((unsigned)o.seen), "i"(OB) : "scc");
  o.srdA[2] = n;
}
template <int F> DI void op_reqB(OPS& o) {
  asm volatile("buffer_load_dwordx4 a[%c3:%c4], %0, %1, %2 offen offset:%c5 sc1" :: "v"(o.va), "s"(o.srdB), "s"(1024u * (F & ~3)), "i"(op_ar(F)), "i"(op_ar(F) + 3), "i"(1024 * (F & 3)) : "memory");
}
constexpr unsigned op_off(int st, int i) { const int un = OPU[2 * st + (i >> 1)]; return (131072u * (un & 3) + 1024u * (2 * (un >> 2) + (i & 1))) ^ (st >> 4) * (524288u + 1024u); }
template <int ST, int I> DI void op_offA(OPS& o) {
  GAP(); o.so = o.sa ^ op_off(ST, I); asm volatile("" :: "s"(o.so)); GAP();
}
template <int ST, int I> DI void op_reqA1(OPS& o) {
  asm volatile("buffer_load_dwordx4 %0, %1, %2, %3 offen sc1" : "=v"(o.fa[ST % (OP_LA + 1)][I]) : "v"(o.va), "s"(o.srdA), "s"(o.so) : "memory");
  if constexpr (ST < OP_LA) asm volatile("ps");
}
template <int... Qs> DI void op_reqA0(OPS& o, iseq<Qs...>) { ((op_offA<Qs / 4, Qs % 4>(o), op_reqA1<Qs / 4, Qs % 4>(o)), ...); }
#define OP_F4(s) "+v"(o.fa[(s) % (OP_LA + 1)][0]), "+v"(o.fa[(s) % (OP_LA + 1)][1]), "+v"(o.fa[(s) % (OP_LA + 1)][2]), "+v"(o.fa[(s) % (OP_LA + 1)][3])
#define OP_I4(s) "i"(op_off(s, 0)), "i"(op_off(s, 1)), "i"(op_off(s, 2)), "i"(op_off(s, 3))
#define OP_L1(d, i) "s_xor_b32 %1, %[sa], %c" #i "\n\tbuffer_load_dwordx4 %" #d ", %[va], %[sr], %1 offen sc1\n\t"
#define OP_HEAD "s_cmp_lg_u32 %[was], 0\n\ts_cbranch_scc0 4f\n\t.subsection 1\n\t4:\n\ts_mov_b32 %2, 0x100000\n\t" \
                "2:\n\tv_cmp_eq_u32_e32 vcc, %[tg], %0\n\ts_and_b32 %1, vcc_lo, 15\n\ts_cmp_eq_u32 %1, 15\n\ts_cbranch_scc1 3f\n\t" \
                "s_sleep 1\n\tbuffer_load_dword %0, %[va], %[sl], %[so] offen sc1\n\ts_sub_u32 %2, %2, 1\n\ts_waitcnt vmcnt(0)\n\ts_cbranch_scc0 2b\n\t3:\n\t"
#define OP_IN [va] "v"(o.va), [sr] "s"(o.srdA), [sl] "s"(o.srdL), [so] "s"(o.sl), [sa] "s"(o.sa), [was] "s"(was), [tg] "s"(o.tag)
template <int S0, int N> DI void op_again(OPS& o) {
  unsigned t, n; const unsigned was = o.srdA[2];
  o.srdA[2] = OB;
  if constexpr (N == 16)
    asm volatile(OP_HEAD OP_L1(3, 19) OP_L1(4, 20) OP_L1(5, 21) OP_L1(6, 22) OP_L1(7, 23) OP_L1(8, 24) OP_L1(9, 25) OP_L1(10, 26) OP_L1(11, 27) OP_L1(12, 28) OP_L1(13, 29) OP_L1(14, 30)
                 OP_L1(15, 31) OP_L1(16, 32) OP_L1(17, 33) OP_L1(18, 34) "s_branch 1f\n\t.subsection 0\n\t1:"
                 : "+v"(o.fo), "=&s"(t), "=&s"(n), OP_F4(S0), OP_F4(S0 + 1), OP_F4(S0 + 2), OP_F4(S0 + 3) : OP_I4(S0), OP_I4(S0 + 1), OP_I4(S0 + 2), OP_I4(S0 + 3), OP_IN : "vcc", "scc", "memory");
  else if constexpr (N == 12)
    asm volatile(OP_HEAD OP_L1(3, 15) OP_L1(4, 16) OP_L1(5, 17) OP_L1(6, 18) OP_L1(7, 19) OP_L1(8, 20) OP_L1(9, 21) OP_L1(10, 22) OP_L1(11, 23) OP_L1(12, 24) OP_L1(13, 25) OP_L1(14, 26) "s_branch 1f\n\t.subsection 0\n\t1:"
                 : "+v"(o.fo), "=&s"(t), "=&s"(n), OP_F4(S0), OP_F4(S0 + 1), OP_F4(S0 + 2) : OP_I4(S0), OP_I4(S0 + 1), OP_I4(S0 + 2), OP_IN : "vcc", "scc", "memory");
  else
    asm volatile(OP_HEAD OP_L1(3, 11) OP_L1(4, 12) OP_L1(5, 13) OP_L1(6, 14) OP_L1(7, 15) OP_L1(8, 16) OP_L1(9, 17) OP_L1(10, 18) "s_branch 1f\n\t.subsection 0\n\t1:"
                 : "+v"(o.fo), "=&s"(t), "=&s"(n), OP_F4(S0), OP_F4(S0 + 1) : OP_I4(S0), OP_I4(S0 + 1), OP_IN : "vcc", "scc", "memory");
}
template <int F> DI void op_ldsB(OPS& o) {
  if constexpr (F < 26) asm volatile("ds_read_b128 %0, %1 offset:%2" : "=v"(o.fb[op_vr(F)]) : "v"(F < 23 ? o.lb5 : o.lb1), "i"(1024 * (F < 23 ? F : F - 23)) : "memory");
  else                  asm volatile("ds_read_b128 a[%c1:%c2], %0 offset:%c3" :: "v"(F < 27 ? o.lb1 : o.lb2), "i"(op_ar(F)), "i"(op_ar(F) + 3), "i"(1024 * (F < 27 ? F - 23 : F - 27)) : "memory");
}
template <int F0, int... Is> DI void op_ldsBs(OPS& o, iseq<Is...>) { (op_ldsB<F0 + Is>(o), ...); }
constexpr int op_ac(int pass, int i, int j) { return j < 4 ? 176 + 16 * i + 4 * j : pass == 0 ? 236 + 4 * i : i == 1 ? 252 : 112 + 4 * i; }
template <int ST, int Q> DI void op_mfma(OPS& o) {
  constexpr int A = ST % (OP_LA + 1), P = ST >> 4;
  if constexpr (Q == 0 || Q == 9) {
    constexpr int u = Q == 9, un = OPU[2 * ST + u], i = un & 3, f = 9 * (un >> 2) + 8, ac = op_ac(P, i, 4), ar = op_ar(f);
    const u32x8 B = __builtin_shufflevector(o.fa[A][2 * u], o.fa[A][2 * u + 1], 0, 1, 2, 3, 4, 5, 6, 7);
    if constexpr (i == 0 && f < 26) asm volatile("sm64 %0, %1, %2, %3" : "+v"(o.D[P][4]) : "v"(o.fb[op_vr(f) % 22]), "v"(B), "v"(o.idx));
    else if constexpr (i == 0)      asm volatile("sm64 %0, a[%c3:%c4], %1, %2" : "+v"(o.D[P][4]) : "v"(B), "v"(o.idx), "i"(ar), "i"(ar + 3));
    else if constexpr (f < 26)      asm volatile("sm64 a[%c3:%c4], %0, %1, %2" :: "v"(o.fb[op_vr(f) % 22]), "v"(B), "v"(o.idx), "i"(ac), "i"(ac + 3));
    else                            asm volatile("sm64 a[%c2:%c3], a[%c4:%c5], %0, %1" :: "v"(B), "v"(o.idx), "i"(ac), "i"(ac + 3), "i"(ar), "i"(ar + 3));
  } else {
    constexpr int d = Q - 1 - (Q > 9), J = d >> 2, u = (d >> 1) & 1, kk = d & 1, un = OPU[2 * ST + u], i = un & 3, f = 9 * (un >> 2) + 4 * kk + J, ac = op_ac(P, i, J), ar = op_ar(f);
    constexpr bool z = kk == 0 && op_first(2 * ST + u);
    static_assert(!z || f < 26, "");
    const u32x4& B = o.fa[A][2 * u + kk];
    if constexpr (i == 0 && z)           asm volatile("mf16 %0, %1, %2, 0" : "=&v"(o.D[P][J]) : "v"(o.fb[op_vr(f) % 22]), "v"(B));
    else if constexpr (i == 0 && f < 26) asm volatile("mf16 %0, %1, %2, %0" : "+v"(o.D[P][J]) : "v"(o.fb[op_vr(f) % 22]), "v"(B));
    else if constexpr (i == 0)           asm volatile("mf16 %0, a[%c2:%c3], %1, %0" : "+v"(o.D[P][J]) : "v"(B), "i"(ar), "i"(ar + 3));
    else if constexpr (z)                asm volatile("mf16 a[%c2:%c3], %0, %1, 0" :: "v"(o.fb[op_vr(f) % 22]), "v"(B), "i"(ac), "i"(ac + 3));
    else if constexpr (f < 26)           asm volatile("mf16 a[%c2:%c3], %0, %1, a[%c2:%c3]" :: "v"(o.fb[op_vr(f) % 22]), "v"(B), "i"(ac), "i"(ac + 3));
    else                                 asm volatile("mf16 a[%c1:%c2], a[%c3:%c4], %0, a[%c1:%c2]" :: "v"(B), "i"(ac), "i"(ac + 3), "i"(ar), "i"(ar + 3));
  }
}
template <int I> DI unsigned op_ax(const unsigned va) { const unsigned r = ((va >> 4) & 15u) * XRS_; return I ? r + 128u + ((va >> 5) & 8u) + ((va >> 7) & 4u) : r + ((va >> 4) & 48u); }
template <int X, int WD> DI unsigned op_word(const OPS& o) { if constexpr (WD < 8) return o.gv[X][WD >> 2][WD & 3]; else return o.gs[X]; }
template <int U> DI void op_gate(OPS& o) {
  constexpr int wd = U / 9, pt = U % 9;
  if constexpr (pt == 0)      asm volatile("v_lshlrev_b32_e32 %0, 16, %2\n\tv_and_b32_e32 %1, 0xffff0000, %2" : "=&v"(o.g0), "=&v"(o.g1) : "v"(op_word<0, wd>(o)));
  else if constexpr (pt == 1) asm volatile("v_mul_f32_e32 %0, 0xbfb8aa3b, %2\n\tv_mul_f32_e32 %1, 0xbfb8aa3b, %3" : "=&v"(o.t0), "=&v"(o.t1) : "v"(o.g0), "v"(o.g1));
  else if constexpr (pt == 2) asm volatile("v_exp_f32_e32 %0, %0" : "+v"(o.t0));
  else if constexpr (pt == 3) asm volatile("v_exp_f32_e32 %0, %0" : "+v"(o.t1));
  else if constexpr (pt == 4) asm volatile("v_add_f32_e32 %0, 1.0, %0\n\tv_add_f32_e32 %1, 1.0, %1" : "+v"(o.t0), "+v"(o.t1));
  else if constexpr (pt == 5) asm volatile("v_rcp_f32_e32 %0, %0" : "+v"(o.t0));
  else if constexpr (pt == 6) asm volatile("v_rcp_f32_e32 %0, %0" : "+v"(o.t1));
  else if constexpr (pt == 7) asm volatile("v_mul_f32_e32 %0, %0, %2\n\tv_mul_f32_e32 %1, %1, %3" : "+v"(o.t0), "+v"(o.t1) : "v"(o.g0), "v"(o.g1));
  else { unsigned r; asm volatile("v_cvt_pk_bf16_f32 %0, %1, %2" : "=v"(r) : "v"(o.t0), "v"(o.t1)); if constexpr (wd < 8) o.gv[0][wd >> 2][wd & 3] = r; else o.gs[0] = r; }
}
template <int ST, int Q, int CODE, typename H> DI void op_t(OPS& o, H& hook) {
  constexpr int kind = CODE / 1000, a = CODE % 1000;
  if constexpr (kind == 0) op_mfma<ST, Q>(o);
  else if constexpr (kind == 1) {
    constexpr int n = a % 100, h = a / 200, ac = op_ac((a / 100) & 1, 1 + n / 5, n % 5), off = n < 10 ? 13312 + 1024 * n : 1024 * (n - 10);
    if constexpr (h == 0) asm volatile("ds_write_b128 %0, a[%c1:%c2] offset:%c3" :: "v"(n < 10 ? o.lb5 : o.lb2), "i"(ac), "i"(ac + 3), "i"(off) : "memory");
    else                  asm volatile("ds_write_b64 %0, a[%c1:%c2] offset:%c3" :: "v"(n < 10 ? o.lb5 : o.lb2), "i"(ac + 2 * h - 2), "i"(ac + 2 * h - 1), "i"(off + 8 * h - 8) : "memory");
  } else if constexpr (kind == 2) asm volatile("s_waitcnt lgkmcnt(%0)" :: "i"(a) : "memory");
  else if constexpr (kind == 3) asm volatile("s_waitcnt lgkmcnt(0)\n\ts_barrier" ::: "memory");
  else if constexpr (kind == 4) {
    constexpr int sl = a / 100, n = a % 20;
    asm volatile("ds_read_b128 %0, %1 offset:%2" : "=v"(sl == 9 ? o.T[n % 5] : o.m[sl % OP_RING]) : "v"(o.ra[n / 5]), "i"(n < 10 ? 13312 + 1024 * n : 1024 * (n - 10)) : "memory");
  } else if constexpr (kind == 5) asm volatile("s_waitcnt lgkmcnt(%1)" : "+v"(o.m[a / 100]) : "i"(a % 100) : "memory");
  else if constexpr (kind == 6 || kind == 18) {
    constexpr int j = (a % 20) >> 2, e = a & 3;
    f32x4& s = kind == 6 && a % 100 < 20 ? o.D[0][j] : o.T[j];
    float v = s[e]; add_v(v, kind == 6 ? o.m[a / 100][e] : o.pv[j][e]); s[e] = v;
  } else if constexpr (kind == 7)
    asm volatile("buffer_store_dwordx4 %0, %1, %2, %3 offen offset:%c4 sc1" :: "v"(o.D[0][a]), "v"(o.va), "s"(o.srdE), "s"(o.sw[a >> 2]), "i"(1024 * (a & 3)) : "memory");
  else if constexpr (kind == 8) op_reqA1<(a >> 2), (a & 3)>(o);
  else if constexpr (kind == 9) op_reqB<a>(o);
  else if constexpr (kind == 19) asm volatile("buffer_load_dwordx4 %0, %1, %2, %3 offen offset:%c4 sc1" : "=v"(o.pv[a]) : "v"(o.va), "s"(o.srdI), "s"(o.sw[a >> 2]), "i"(1024 * (a & 3)) : "memory");
  else if constexpr (kind == 20) asm volatile("buffer_store_dword %0, %1, %2, 0 offen sc1" :: "v"(o.tg), "v"(o.va), "s"(o.srdG) : "memory");
  else if constexpr (kind == 25) { GAP(); hook(IC(a)); GAP(); }
  else if constexpr (kind == 26) {
    if constexpr (a < 2) asm volatile("ds_read_b128 %0, %1 offset:%2" : "=v"(o.gv[1][a]) : "v"(o.lb5), "i"(4096 + 1024 * a) : "memory");
    else                 asm volatile("ds_read_b32 %0, %1 offset:6144" : "=v"(o.gs[1]) : "v"(o.lb5) : "memory");
  } else if constexpr (kind == 22) op_poll(o);
  else if constexpr (kind == 23) asm volatile("v_cmp_eq_u32_e64 %0, %1, %2" : "=s"(o.seen) : "s"(o.tag), "v"(o.fo));
  else if constexpr (kind == 24) op_there(o);
  else if constexpr (kind == 21) op_offA<(a >> 2), (a & 3)>(o);
  else if constexpr (kind == 31) real_op();
  else if constexpr (kind == 11) op_gate<a>(o);
  else if constexpr (kind == 12) asm volatile("" : "+v"(o.T[a]));
  else if constexpr (kind == 13) op_ldsB<9 * (a >> 2) + (a & 3)>(o);
  else if constexpr (kind == 14) asm volatile("s_barrier" ::: "memory");
  else if constexpr (kind == 15) asm volatile("s_waitcnt lgkmcnt(%4)" : "+v"(o.fb[0]), "+v"(o.fb[1]), "+v"(o.fb[2]), "+v"(o.fb[3]) : "i"(a) : "memory");
  else if constexpr (kind == 16) asm volatile("v_lshlrev_b32_e32 %0, 16, %2\n\tv_and_b32_e32 %1, 0xffff0000, %2" : "=&v"(o.gx[2 * a]), "=v"(o.gx[2 * a + 1]) : "v"(op_word<a / 9, a % 9>(o)));
  else if constexpr (kind == 17) asm volatile("s_waitcnt vmcnt(0)" : "+v"(o.pv[0]), "+v"(o.pv[1]), "+v"(o.pv[2]), "+v"(o.pv[3]), "+v"(o.pv[4]) :: "memory");
}
template <int ST, int Q, typename H, int... Is> DI void op_ts(OPS& o, H& hook, iseq<Is...>) { (op_t<ST, Q, TT[TS[18 * ST + Q] + Is]>(o, hook), ...); }
template <int ST, typename H, int... Qs> DI void op_qs(OPS& o, H& hook, iseq<Qs...>) { (op_ts<ST, Qs>(o, hook, make_iseq<TS[18 * ST + Qs + 1] - TS[18 * ST + Qs]>{}), ...); }

template <typename F, int... Is> DI void for_seq(F& f, iseq<Is...>) { (f(std::integral_constant<int, Is>{}), ...); }
DI void wait_flag(const unsigned* p, const unsigned tag) {
#pragma nounroll
  for (unsigned spin = 0; spin < (1u << 20); ++spin) {
    unsigned v;
    asm volatile("global_load_dword %0, %1, off sc1\n\ts_waitcnt vmcnt(0)" : "=v"(v) : "v"(p) : "memory");
    if (__all(v == tag)) break;
    __builtin_amdgcn_s_sleep(1);
  }
}
DI unsigned op_k0(const unsigned blk, const unsigned w) { return ((blk >> 3) & 7u) * 128u + (~blk & 4u) * 16u + 16u * w; }

constexpr int NFIN = 200, NBL = 45, FVW = 44, FVS = 0, FNR = 32;
constexpr short FT[] = {5064,31000,23000,23001,23002,5001,31000,23003,23004,23005,5002,31000,23006,23007,23008,5003,31000,23009,23010,23011,5083,31000,23012,23013,23014,5018,31000,23015,16000,16001,5017,16008,16009,
  16016,5016,16017,16024,5096,16025,16002,16003,5033,31000,23016,23017,5034,31000,23018,23019,23020,5035,31000,23021,23022,5115,31000,25003,23023,5050,31000,23024,23025,5049,31000,25002,23026,5048,31000,23027,23028,
  20015,1000,31000,25001,23029,22000,1001,31000,23030,23031,20031,1003,31000,25000,16010,1002,16011,16018,20047,1004,16019,16026,16027,1005,13000,20063,1007,13001,13002,1006,15000,16004,29010,20076,1008,27000,13003,1009,
  31000,26000,20088,1011,15001,13016,1010,31000,26001,20100,1012,13017,13018,1013,31000,26002,20112,1015,15008,16005,1014,31000,26003,27001,3000,13019,1016,31000,26004,15009,1017,13032,13033,1019,31000,26005,1018,15016,
  16012,1020,31000,26006,24000,1021,31000,24001,24002,24003,1023,31000,26007,1022,31000,24004,24005,24006,1024,31000,24007,24008,1025,31000,24009,24010,24011,1027,13034,1026,13035,13048,1028,15017,16013,1029,13049,13050,
  1031,15024,16020,1030,13051,16021,3001,15025,14057,24012,4000,31000,24013,24014,24015,30012,2000,31000,24016,24017,24018,24019,2001,31000,12000,16028,30028,2003,31000,24020,24021,24022,24023,2002,31000,24024,24025,
  24026,24027,30047,2004,31000,24028,24029,24030,24031,2005,31000,17000,17001,17002,17003,30063,2007,31000,25004,17004,17005,2006,31000,17006,17007,18000,18001,30076,2008,31000,25005,18002,18003,2009,31000,18004,18005,
  18006,18007,30088,2011,19000,19001,19002,19003,2010,31000,28000,17008,17009,30100,2012,31000,17010,17011,17012,17013,2013,31000,25006,17014,17015,30112,2015,31000,18008,18009,18010,18011,2014,31000,18012,18013,18014,
  18015,5068,31000,25007,19004,19005,5005,19006,19007,14058,5006,31000,28001,17016,17017,5007,31000,17018,17019,17020,5087,31000,25008,17021,17022,5022,31000,17023,18016,18017,5021,31000,18018,18019,18020,18021,5020,
  31000,18022,18023,19008,5100,19009,19010,19011,14059,5037,31000,28002,17024,5038,31000,17025,17026,17027,5039,31000,17028,17029,17030,5119,31000,25015,17031,18024,5054,31000,18025,18026,18027,5053,31000,18028,18029,
  18030,18031,5052,31000,25014,19012,1032,19013,19014,19015,16029,13004,13005,22001,1033,31000,28003,15002,1035,13006,13007,1034,31000,25013,15003,1036,13020,13021,1037,15010,13022,1039,31000,25009,13023,1038,15011,
  13036,1040,13037,13038,1041,31000,25012,15018,1043,13039,13052,1042,15019,13053,1044,31000,25010,1045,15026,13054,1047,13055,1046,15027,14012,3002,31000,25011,4001,14013,2016,16006,14014,2017,31000,12001,2019,14015,
  14028,2018,31000,17032,17033,17034,17035,2020,31000,25016,16007,14029,2021,31000,17036,17037,17038,17039,2023,31000,18032,18033,18034,18035,2022,31000,25017,18036,18037,2024,31000,18038,18039,19016,19017,2025,19018,
  19019,16014,14030,2027,31000,28004,17040,17041,2026,31000,17042,17043,17044,17045,2028,31000,25018,17046,17047,2029,31000,18040,18041,18042,18043,2031,31000,18044,18045,18046,18047,2030,31000,25019,19020,19021,5072,
  19022,19023,14031,5009,31000,28005,17048,17049,5010,31000,17050,17051,17052,17053,5011,31000,25020,17054,17055,5091,31000,18048,18049,18050,18051,5026,31000,18052,18053,18054,18055,5025,19024,19025,19026,19027,5024,
  31000,28006,17056,17057,5104,31000,17058,17059,17060,17061,5041,31000,17062,17063,18056,18057,5042,31000,18058,18059,18060,18061,5043,31000,18062,18063,19028,19029,5123,31000,25027,19030,5058,19031,13008,13009,5057,
  31000,28007,15004,5056,13010,13011,5076,31000,25026,15005,22002,5013,13024,13025,5014,15012,13026,5015,31000,25025,13027,5095,15013,13040,5030,13041,13042,5029,31000,25021,15020,5028,13043,13056,5108,15021,13057,5045,
  31000,25024,15028,5046,13058,13059,5047,15029,16015,14044,5127,31000,25022,14045,5062,16022,14046,14047,5061,16023,14060,14061,14062,16030,14063,5060,31000,25023,16031,13012,4002,13013,13014,22003,2032,15006,13015,
  13028,2033,31000,12002,15007,2035,31000,25031,13029,2034,31000,17064,17065,17066,17067,2036,15014,13030,13031,2037,31000,25030,17068,17069,2039,31000,17070,17071,18064,18065,2038,31000,18066,18067,18068,18069,2040,
  31000,25029,18070,18071,2041,19032,19033,19034,19035,2043,31000,28008,17072,17073,2042,31000,17074,17075,17076,17077,2044,31000,25028,17078,17079,2045,31000,18072,18073,18074,18075,2047,31000,18076,18077,18078,18079,
  2046,19036,19037,19038,19039,1048,31000,28009,17080,17081,1049,31000,17082,17083,17084,17085,1051,31000,17086,17087,18080,18081,1050,31000,18082,18083,18084,18085,1052,31000,18086,18087,19040,19041,1053,19042,19043,
  15015,13044,1055,31000,28010,17088,17089,1054,31000,17090,17091,17092,17093,1056,31000,17094,17095,18088,18089,1057,31000,18090,18091,18092,18093,1059,31000,18094,18095,19044,19045,1058,19046,19047,13045,1060,31000,
  28011,15022,13046,1061,13047,13060,1063,15023,13061,13062,1062,15030,13063,3003,15031,30200,30200,4003,31000,2048,31000,2049,31000,12003,2051,31000,2050,31000,17096,17097,17098,17099,2052,31000,2053,31000,17100,17101,
  17102,17103,18096,2055,31000,18097,18098,18099,18100,18101,2054,31000,18102,18103,19048,19049,19050,2056,19051,2057,31000,28012,17104,17105,17106,17107,2059,31000,17108,17109,17110,17111,18104,18105,2058,31000,18106,
  18107,18108,18109,18110,18111,2060,19052,19053,19054,19055,2061,31000,28013,17112,17113,17114,17115,2063,31000,17116,17117,17118,17119,18112,18113,2062,31000,18114,18115,18116,18117,18118,18119,19056,19057,19058,19059,
  28014,17120,17121,17122,17123,17124,17125,17126,17127,18120,18121,18122,18123,18124,18125,18126,18127,19060,19061,19062,19063,28015,};
constexpr short FS[] = {0,5,10,15,20,25,30,34,37,41,45,50,54,58,62,66,70,76,80,85,88,93,95,99,102,107,110,114,117,121,124,128,132,134,138,141,144,147,151,156,159,164,168,173,175,178,181,184,187,190,194,199,206,210,217,
  223,230,236,242,248,254,260,266,271,278,283,290,296,301,305,310,315,320,325,331,336,341,345,350,355,360,365,371,375,383,387,390,394,397,400,404,407,410,414,417,420,423,426,428,431,434,436,439,442,445,451,456,462,468,
  473,479,484,489,495,500,506,512,517,521,526,532,537,543,549,554,559,565,571,577,583,587,591,595,598,603,606,609,613,616,619,623,626,629,633,636,640,644,648,655,660,664,668,672,676,682,686,691,697,703,708,713,718,724,
  729,735,741,746,751,757,763,769,775,780,785,791,797,803,809,813,818,821,825,828,832,834,836,839,841,847,849,856,863,870,872,879,887,895,900,907,915,949,};
struct OST { u32x4 w, P[4][2]; float o[8], il[4], ls[4]; unsigned vo, so[4]; u32x4 srd; bf16x4 lo[8][2], up[8][2]; };
template <int U> DI void o_store(const OST& t) {
  asm volatile("buffer_store_dwordx4 %0, %1, %2, %3 offen sc1" :: "v"(t.w), "v"(t.vo), "s"(t.srd), "s"(t.so[U & 3] + 131072u * (U >> 2)) : "memory");
}
template <int CODE> DI void fin_op(W4C& c, OST& t, OPS& o) {
  constexpr int kind = CODE / 1000, a = CODE % 1000;
  if constexpr (kind == 1) pv16<AO_((a >> 1) & 7, a >> 4), false>(__builtin_shufflevector(c.vlo[(a >> 1) & 7][a & 1], c.vup[(a >> 1) & 7][a & 1], 0, 1, 2, 3, 4, 5, 6, 7), c.Pk[a >> 4][a & 1]);
  else if constexpr (kind == 2) pv16<AO_((a >> 1) & 7, a >> 4), false>(__builtin_shufflevector(t.lo[(a >> 1) & 7][a & 1], t.up[(a >> 1) & 7][a & 1], 0, 1, 2, 3, 4, 5, 6, 7), t.P[a >> 4][a & 1]);
  else if constexpr (kind == 3) sum64(c, a, c.Pk[a]);
  else if constexpr (kind == 4) sum64(c, a, t.P[a]);
  else if constexpr (kind == 5) { constexpr int kb = (a >> 4) & 3, qb = (a >> 2) & 3; qk16<kb, qb, (a & 3), (a >> 6)>(c.S[1][kb][qb], c.NEGM[qb]); }
  else if constexpr (kind == 14) exp_e<0, a>(c);
  else if constexpr (kind == 16) pack_e<0, a>(c);
  else if constexpr (kind == 22) asm volatile("" :: "v"(c.NEGM[a]));
  else if constexpr (kind == 20) { constexpr int d = a >> 4; asm volatile("s_waitcnt lgkmcnt(%4)" : "+v"(c.vlo[d][0]), "+v"(c.vlo[d][1]), "+v"(c.vup[d][0]), "+v"(c.vup[d][1]) : "i"(a & 15) : "memory"); }
  else if constexpr (kind == 23) tr_read_<a, 0>(c);
  else if constexpr (kind == 29) asm volatile("s_waitcnt vmcnt(%0) lgkmcnt(0)\n\ts_barrier" :: "i"(a) : "memory");
  else if constexpr (kind == 12) asm volatile("v_add_f32_e32 %0, %2, %3\n\tv_rcp_f32_e32 %1, %0" : "=&v"(t.ls[a]), "=v"(t.il[a]) : "v"(c.L[a][0]), "v"(c.L[a][1]), "v"(c.L[a]));
  else if constexpr (kind == 13) exp_e<1, a>(c);
  else if constexpr (kind == 15) pack_to<1, a>(c, t.P);
  else if constexpr (kind == 17) t.o[a & 7] = acc_read<a>();
  else if constexpr (kind == 18) {
    asm volatile("v_mul_f32_e32 %0, %0, %1" : "+v"(t.o[a & 7]) : "v"(t.il[a >> 5]));
    if constexpr (a >= 8 && (a & 7) == 0) asm volatile("" :: "v"(t.w));
  } else if constexpr (kind == 19) { GAP(); const unsigned w = cvtpk(t.o[2 * (a & 3)], t.o[2 * (a & 3) + 1]); asm volatile("" :: "v"(w)); t.w[a & 3] = w; GAP(); }
  else if constexpr (kind == 24) tr_rd<a, 0x4000>(c, t.lo, t.up);
  else if constexpr (kind == 25) op_reqB<35 + a>(o);
  else if constexpr (kind == 26) asm volatile("buffer_load_dwordx4 %0, %1, %2 offen offset:%c3 sc1 lds" :: "v"(c.pfv), "s"(c.srdP), "s"(c.pfo + 4096u * (a >> 2)), "i"(1024 * (a & 3)) : "memory");
  else if constexpr (kind == 27) asm volatile("s_mov_b32 m0, %0" :: "s"(c.pfm0 + 4096u * a) : "m0", "memory");
  else if constexpr (kind == 28) o_store<a>(t);
  else if constexpr (kind == 31) real_op();
  else if constexpr (a == 200) asm volatile("s_nop 0");
  else { constexpr int d = a >> 4; asm volatile("s_waitcnt lgkmcnt(%4)" : "+v"(t.lo[d][0]), "+v"(t.lo[d][1]), "+v"(t.up[d][0]), "+v"(t.up[d][1]) : "i"(a & 15) : "memory"); }
}
template <int G, int... Is> DI void fin_gap(W4C& c, OST& t, OPS& o, iseq<Is...>) { (fin_op<FT[FS[G] + Is]>(c, t, o), ...); GAP(); }
template <int... Gs> DI void fin_gaps(W4C& c, OST& t, OPS& o, iseq<Gs...>) { (fin_gap<Gs>(c, t, o, make_iseq<FS[Gs + 1] - FS[Gs]>{}), ...); }

template <int J, int H> DI void redo_q(const __amdgpu_buffer_rsrc_t srdW, const __amdgpu_buffer_rsrc_t srdN, const unsigned vw, const unsigned vx, const unsigned sw, const unsigned sx) {
  f32x4 q[4] = {};
#pragma nounroll
  for (unsigned ks = 0; ks < DM / 32; ++ks) {
    u32x4 w;
#pragma unroll
    for (int e = 0; e < 4; ++e) {
      const unsigned so = sw + (64u * J + 8u * H) + WRS * (32u * ks + 2u * e);
      w[e] = (unsigned)__builtin_amdgcn_raw_buffer_load_b16(srdW, vw, so, 0) | ((unsigned)__builtin_amdgcn_raw_buffer_load_b16(srdW, vw, so + WRS, 0) << 16);
    }
    asm volatile("s_nop 1" : "+v"(w), "+v"(q[0]), "+v"(q[1]), "+v"(q[2]), "+v"(q[3]));
#pragma unroll
    for (int qb = 0; qb < 4; ++qb) {
      u32x4 x;
      asm volatile("buffer_load_dwordx4 %0, %1, %2, %3 offen sc1\n\ts_waitcnt vmcnt(0)" : "=v"(x) : "v"(vx), "s"(srdN), "s"(sx + 64u * ks + 16u * XRS * qb) : "memory");
      sum16<false>(q[qb], w, x);
    }
  }
  asm volatile("s_nop 7" : "+v"(q[0]), "+v"(q[1]), "+v"(q[2]), "+v"(q[3]));
  const float C = 0.12751743082f;
  unsigned r[8];
#pragma unroll
  for (int qb = 0; qb < 4; ++qb) { r[2 * qb] = cvtpk(C * q[qb][0], C * q[qb][1]); r[2 * qb + 1] = cvtpk(C * q[qb][2], C * q[qb][3]); }
  asm volatile("s_nop 1" : "+v"(r[0]), "+v"(r[1]), "+v"(r[2]), "+v"(r[3]), "+v"(r[4]), "+v"(r[5]), "+v"(r[6]), "+v"(r[7]));
  acc_write<AQ_(0, J) + 2 * H>(r[0]); acc_write<AQ_(0, J) + 2 * H + 1>(r[1]); acc_write<AQ_(1, J) + 2 * H>(r[2]); acc_write<AQ_(1, J) + 2 * H + 1>(r[3]);
  acc_write<AQ_(2, J) + 2 * H>(r[4]); acc_write<AQ_(2, J) + 2 * H + 1>(r[5]); acc_write<AQ_(3, J) + 2 * H>(r[6]); acc_write<AQ_(3, J) + 2 * H + 1>(r[7]);
}
template <int I> DI void redo_kload(const unsigned vo, const __amdgpu_buffer_rsrc_t srd, const unsigned so) {
  asm volatile("buffer_load_dwordx4 a[%c3:%c4], %0, %1, %2 offen offset:%c5" :: "v"(vo), "s"(srd), "s"(so + 4096u * (I >> 2)), "i"(192 + 4 * I), "i"(195 + 4 * I), "i"(64 * (I & 3)) : "memory");
}
template <int CM, int... Ms> DI void redo_s(W4C& c, const unsigned vk, const unsigned so, f32x4 (&T)[2][4], iseq<Ms...>) {
  redo_kload<0>(vk, c.srdK, so); redo_kload<1>(vk, c.srdK, so); redo_kload<2>(vk, c.srdK, so); redo_kload<3>(vk, c.srdK, so);
  redo_kload<4>(vk, c.srdK, so); redo_kload<5>(vk, c.srdK, so); redo_kload<6>(vk, c.srdK, so); redo_kload<7>(vk, c.srdK, so);
  asm volatile("s_waitcnt vmcnt(0)" ::: "memory");
  (qk16<(Ms >> 4), ((Ms >> 2) & 3), (Ms & 3), ((Ms & 3) ? 0 : CM)>(T[Ms >> 4][(Ms >> 2) & 3], c.NEGM[(Ms >> 2) & 3]), ...);
  asm volatile("s_nop 7" : "+v"(T[0][0]), "+v"(T[0][1]), "+v"(T[0][2]), "+v"(T[0][3]), "+v"(T[1][0]), "+v"(T[1][1]), "+v"(T[1][2]), "+v"(T[1][3]));
}
template <int DB> DI void redo_pv(const W4C& c, const unsigned vv, const unsigned so, const u32x4 (&P)[4]) {
  u32x4 v;
#pragma unroll
  for (int e = 0; e < 4; ++e) {
    const unsigned s = so + (896u + 32u * DB) + KRS * (16u * (e >> 1) + 2u * (e & 1));
    v[e] = (unsigned)__builtin_amdgcn_raw_buffer_load_b16(c.srdV, vv, s, 0) | ((unsigned)__builtin_amdgcn_raw_buffer_load_b16(c.srdV, vv, s + KRS, 0) << 16);
  }
  asm volatile("s_nop 1" : "+v"(v));
  const bf16x8 vf = __builtin_bit_cast(bf16x8, v);
  pv16<AO_(DB, 0), false>(vf, P[0]); pv16<AO_(DB, 1), false>(vf, P[1]); pv16<AO_(DB, 2), false>(vf, P[2]); pv16<AO_(DB, 3), false>(vf, P[3]);
  GAP();
}
template <int U> DI void redo_unit(OST& t) {
  t.o[0] = acc_read<8 * U>(); t.o[1] = acc_read<8 * U + 1>(); t.o[2] = acc_read<8 * U + 2>(); t.o[3] = acc_read<8 * U + 3>();
  t.o[4] = acc_read<8 * U + 4>(); t.o[5] = acc_read<8 * U + 5>(); t.o[6] = acc_read<8 * U + 6>(); t.o[7] = acc_read<8 * U + 7>();
  const float il = t.il[U >> 2];
  t.w = u32x4{cvtpk(t.o[0] * il, t.o[1] * il), cvtpk(t.o[2] * il, t.o[3] * il), cvtpk(t.o[4] * il, t.o[5] * il), cvtpk(t.o[6] * il, t.o[7] * il)};
  o_store<U>(t);
  asm volatile("s_nop 1" :: "v"(t.w));
}
template <int R0, int... Is> DI void redo_zero(const unsigned z, iseq<Is...>) { (acc_write<R0 + Is>(z), ...); }
template <int... Is> DI void redo_units(OST& t, iseq<Is...>) { (redo_unit<Is>(t), ...); }
template <int F0, int... Is> DI void redo_reqs(OPS& o, iseq<Is...>) { (op_reqB<F0 + Is>(o), ...); }
DI void redo_rows(W4C& c, OST& t, OPS& o, const u16* xn, const u16* wq, const unsigned long long* keys, const unsigned row0, const unsigned h, const unsigned tag) {
  unsigned l;
  asm volatile("s_waitcnt vmcnt(0)\n\tv_mbcnt_lo_u32_b32 %0, -1, 0\n\tv_mbcnt_hi_u32_b32 %0, -1, %0" : "=v"(l) :: "memory");
  const unsigned c16 = l & 15u, g = l >> 4;
  wait_flag((const unsigned*)(keys + row0 + l) + 1, tag);
  {
    const __amdgpu_buffer_rsrc_t srdW = mk_srd(wq, DM * HQ * DH * 2), srdN = mk_srd(xn, SEQ * DM * 2);
    const unsigned vw = WRS * 8u * g + 16u * (c16 >> 2) + 2u * (c16 & 3u), vx = XRS * c16 + 16u * g, sw = 256u * h, sx = XRS * row0;
    redo_q<0, 0>(srdW, srdN, vw, vx, sw, sx); redo_q<0, 1>(srdW, srdN, vw, vx, sw, sx); redo_q<1, 0>(srdW, srdN, vw, vx, sw, sx); redo_q<1, 1>(srdW, srdN, vw, vx, sw, sx);
    redo_q<2, 0>(srdW, srdN, vw, vx, sw, sx); redo_q<2, 1>(srdW, srdN, vw, vx, sw, sx); redo_q<3, 0>(srdW, srdN, vw, vx, sw, sx); redo_q<3, 1>(srdW, srdN, vw, vx, sw, sx);
  }
  const unsigned vk = KRS * c16 + 16u * g, vv = KRS * 4u * g + 2u * c16;
  f32x4 T[2][4];
  float m[4] = {-__builtin_inff(), -__builtin_inff(), -__builtin_inff(), -__builtin_inff()};
#pragma nounroll
  for (unsigned u = 0; u < SEQ / 32; ++u) {
    redo_s<2>(c, vk, 32u * KRS * u, T, make_iseq<32>{});
#pragma unroll
    for (int q = 0; q < 4; ++q)
#pragma unroll
      for (int e = 0; e < 8; ++e) m[q] = max2f(m[q], T[e >> 2][q][e & 3]);
  }
#pragma unroll
  for (int q = 0; q < 4; ++q) {
    asm volatile("s_nop 1" : "+v"(m[q]));
    auto p1 = __builtin_amdgcn_permlane16_swap(__float_as_uint(m[q]), __float_as_uint(m[q]), false, false);
    m[q] = max2f(__uint_as_float(p1[0]), __uint_as_float(p1[1]));
    asm volatile("s_nop 1" : "+v"(m[q]));
    auto p2 = __builtin_amdgcn_permlane32_swap(__float_as_uint(m[q]), __float_as_uint(m[q]), false, false);
    m[q] = -max2f(__uint_as_float(p2[0]), __uint_as_float(p2[1]));
    c.NEGM[q] = f32x4{m[q], m[q], m[q], m[q]};
    c.L[q] = f32x4{0.f, 0.f, 0.f, 0.f};
  }
  redo_zero<0>(0u, make_iseq<128>{});
  asm volatile("s_nop 1" : "+v"(c.NEGM[0]), "+v"(c.NEGM[1]), "+v"(c.NEGM[2]), "+v"(c.NEGM[3]), "+v"(c.L[0]), "+v"(c.L[1]), "+v"(c.L[2]), "+v"(c.L[3]));
#pragma nounroll
  for (unsigned u = 0; u < SEQ / 32; ++u) {
    redo_s<1>(c, vk, 32u * KRS * u, T, make_iseq<32>{});
    u32x4 P[4];
#pragma unroll
    for (int q = 0; q < 4; ++q)
#pragma unroll
      for (int e = 0; e < 4; ++e) P[q][e] = cvtpk(__builtin_amdgcn_exp2f(T[e >> 1][q][2 * (e & 1)]), __builtin_amdgcn_exp2f(T[e >> 1][q][2 * (e & 1) + 1]));
    asm volatile("s_nop 1" : "+v"(P[0]), "+v"(P[1]), "+v"(P[2]), "+v"(P[3]));
#pragma unroll
    for (int q = 0; q < 4; ++q) sum16<false>(c.L[q], c.ONE, P[q]);
    const unsigned so = 32u * KRS * u;
    redo_pv<0>(c, vv, so, P); redo_pv<1>(c, vv, so, P); redo_pv<2>(c, vv, so, P); redo_pv<3>(c, vv, so, P);
    redo_pv<4>(c, vv, so, P); redo_pv<5>(c, vv, so, P); redo_pv<6>(c, vv, so, P); redo_pv<7>(c, vv, so, P);
  }
  asm volatile("s_nop 7" : "+v"(c.L[0]), "+v"(c.L[1]), "+v"(c.L[2]), "+v"(c.L[3]));
#pragma unroll
  for (int q = 0; q < 4; ++q) t.il[q] = __builtin_amdgcn_rcpf(c.L[q][0]);
  redo_units(t, make_iseq<16>{});
  redo_reqs<35>(o, make_iseq<FNR>{});
  asm volatile("s_waitcnt vmcnt(0)" ::: "memory");
}

DI void oproj_phase(OPS& o, char* lds, const u16* __restrict__ xn, unsigned* __restrict__ hand, float* __restrict__ exch, u16* __restrict__ out, const unsigned wid  ) {
  const unsigned ldsb = (unsigned)(size_t)lds, tag = o.tag;
  unsigned fe;
  op_poll(o);
  o.lb5 = ldsb + SD_BASE + BP_W * wid + o.va; o.lb1 = ldsb + 4096u * wid + o.va; o.lb2 = ldsb + 0x4000u + 8192u * wid + o.va;
  asm volatile("s_waitcnt vmcnt(%6)" : "+v"(o.gv[0][0]), "+v"(o.gv[0][1]), "+v"(o.gs[0]), "+v"(o.gv[1][0]), "+v"(o.gv[1][1]), "+v"(o.gs[1]) : "i"(FVW + 1) : "memory");
  static_assert(FVS == 0, "the last request of the final phase is a store");
  op_ldsBs<0>(o, make_iseq<9>{}); op_ldsBs<13>(o, make_iseq<22>{});
  asm volatile("ds_write_b128 %0, %1 offset:4096\n\tds_write_b128 %0, %2 offset:5120\n\tds_write_b32 %0, %3 offset:6144" :: "v"(o.lb5), "v"(o.gv[1][0]), "v"(o.gv[1][1]), "v"(o.gs[1]) : "memory");
  o.idx = (o.va & 128u) ? 0xeeeeeeeeu : 0x44444444u; o.D[0][4] = f32x4{0.f, 0.f, 0.f, 0.f};
  asm volatile("" : "+v"(o.idx), "+v"(o.D[0][4]));
  redo_zero<240>(0u, make_iseq<12>{});
  PS_AT(45); ps_touch(20u + wid);
  GAP();
  {
    u32x4 sr = o.srdL; sr[2] = o.sf + 4u;
    redo_reqs<35 + FNR>(o, make_iseq<OP_NR - 35 - FNR>{});
    asm volatile("s_waitcnt vmcnt(%7)\n\tbuffer_store_dword %2, %3, %4, %5 offen sc1\n\ts_waitcnt vmcnt(%7)\n\tv_cmp_eq_u32_e64 %0, %6, %1"
                 : "=s"(o.seen), "+v"(o.fo) : "v"(tag), "v"(o.va), "s"(sr), "s"(o.sf), "s"(tag), "i"(OP_NR - 34 - FNR) : "memory");
  }
  op_there(o);
  op_reqA0(o, make_iseq<OP_NI>{});
  GAP();
  unsigned blk = blockIdx.x;
  asm volatile("" : "+s"(blk));
  const unsigned sn = o.sn;
  asm volatile("s_waitcnt lgkmcnt(0)" : "+v"(o.fb[0]), "+v"(o.fb[1]), "+v"(o.fb[2]), "+v"(o.fb[3]), "+v"(o.fb[4]), "+v"(o.fb[5]), "+v"(o.fb[6]), "+v"(o.fb[7]), "+v"(o.fb[8]), "+v"(o.fb[9]), "+v"(o.fb[10]),
               "+v"(o.fb[11]), "+v"(o.fb[12]), "+v"(o.fb[13]), "+v"(o.fb[14]), "+v"(o.fb[15]), "+v"(o.fb[16]), "+v"(o.fb[17]), "+v"(o.fb[18]), "+v"(o.fb[19]), "+v"(o.fb[20]), "+v"(o.fb[21]) :: "memory");
  if (__builtin_expect(o.bad != 0u, 0)) {
    const unsigned lane = o.va >> 4, row0 = (o.sa >> 13 ^ 64u) & ~15u, ax[2] = {op_ax<0>(o.va), op_ax<1>(o.va)};
    unsigned selr, an[2] = {ax[0], ax[1]};
    wait_flag(hand + 2u * (lane & 7u) + 1u, tag);
    wait_flag(hand + H_X + 72u * ((blk >> 3) & 7u) + 4u * (lane < 18u ? lane : 0u), tag);
    asm volatile("buffer_load_dword %0, %1, %2, 0 offen sc1\n\ts_waitcnt vmcnt(0)" : "=v"(selr) : "v"(8u * (lane & 7u)), "s"(o.srdL) : "memory");
#pragma nounroll
    for (unsigned t = 0; t < (unsigned)NSEL; ++t) {
      const unsigned ro = (((unsigned)__builtin_amdgcn_readlane(selr, t) & 0xffffu) - row0) * XRS_;
#pragma unroll
      for (int j = 0; j < 2; ++j) { const unsigned d = ax[j] - ro; an[j] = d < XRS_ ? (SEQ + t - row0) * XRS_ + d : an[j]; }
    }
    asm volatile("buffer_load_dwordx4 %0, %3, %5, %6 offen sc1\n\tbuffer_load_dwordx4 %1, %3, %5, %6 offen offset:64 sc1\n\tbuffer_load_dword %2, %4, %5, %6 offen sc1\n\ts_waitcnt vmcnt(0)"
                 : "=&v"(o.gv[0][0]), "=&v"(o.gv[0][1]), "=&v"(o.gs[0]) : "v"(an[0]), "v"(an[1]), "s"(mk_srd(xn, (SEQ + NSEL) * DM * 2)), "s"(sn) : "memory");
  }
  __amdgpu_buffer_rsrc_t srdO; unsigned ax[2], fes, t;
#define SC(g, x, e) if constexpr (M == (g)) { x = (e); asm volatile("" : "+s"(x)); }
#define VC(g, x, e) if constexpr (M == (g)) { x = (e); asm volatile("" : "+v"(x)); }
  auto prep = [&](auto M_) __attribute__((always_inline)) {
    constexpr int M = decltype(M_)::value;
    if constexpr (M < 6) { acc_write<op_ac(1, 1 + M / 2, 4) + 2 * (M & 1)>(0u); acc_write<op_ac(1, 1 + M / 2, 4) + 2 * (M & 1) + 1>(0u); }
    VC(6, o.ra[0], o.lb5 + BP_W * ((wid ^ 1u) - wid)) VC(7, o.ra[1], o.lb5 + BP_W * ((wid ^ 2u) - wid)) VC(8, o.ra[2], o.lb2 + 8192u * ((wid ^ 3u) - wid))
    SC(9, o.sw[0], 5120u * wid) SC(9, o.sw[1], o.sw[0] + 4096u) SC(9, t, blk * 5120u) SC(10, o.srdE, mk_srd(exch + t, 20480u))
    SC(11, t, (blk ^ 4u) * 5120u) SC(11, fes, 4u * (H_E + 32u * (blk ^ 4u))) SC(12, o.srdI, mk_srd(exch + t, 20480u))
    SC(13, o.srdG, mk_srd(hand + H_E + 32u * blk, ((wid - 1u) >> 31) * 4u)) SC(14, srdO, mk_srd(out, SEQ * DM * 2))
    VC(15, ax[0], op_ax<0>(o.va)) VC(16, ax[1], op_ax<1>(o.va))
  };
  asm volatile("ps"); prep(IC(9)); asm volatile("ps"); prep(IC(10)); asm volatile("ps"); prep(IC(11)); asm volatile("ps"); prep(IC(12)); asm volatile("ps"); prep(IC(13)); asm volatile("ps"); prep(IC(14));
  GAP();
  op_again<0, OP_NI>(o);
  o.sl += 128u; o.srdL[2] += 128u;
  asm volatile("ps");
  auto step = [&](auto ST) __attribute__((always_inline)) {
    constexpr int st = decltype(ST)::value, A = st % (OP_LA + 1);
    if constexpr (st == 16) {
    }
    if constexpr (st == 24) {
    }
    if constexpr (st == 30) {
    }
    if constexpr (st == OP_SD) {
      asm volatile("s_waitcnt vmcnt(%1)" : "+v"(fe) : "i"(OP_FEW) : "memory");
      if (__builtin_expect(!__all(fe == tag), 0)) wait_flag(hand + H_E + 32u * (blk ^ 4u), tag);
    }
    if constexpr (st == 8) { asm volatile("s_waitcnt vmcnt(%0)" :: "i"(OP_P4W) : "memory"); op_again<8, 4 * (8 - OP_DS)>(o); }
    if constexpr (st == 28) { o.D[1][4] = f32x4{0.f, 0.f, 0.f, 0.f}; asm volatile("s_nop 0" : "+v"(o.D[1][4])); }
    asm volatile("s_waitcnt vmcnt(%4)" : "+v"(o.fa[A][0]), "+v"(o.fa[A][1]), "+v"(o.fa[A][2]), "+v"(o.fa[A][3]) : "i"(OPW[st]) : "memory");
    if constexpr (st == OP_DS) asm volatile("" : "+v"(o.fo));
    if constexpr (st == 29) asm volatile("" : "+v"(o.gv[1][0]), "+v"(o.gv[1][1]), "+v"(o.gs[1]));
    GAP();
    op_qs<st>(o, prep, make_iseq<18>{});
    GAP();
    if constexpr (st == OP_SP) { u32x4 sh = o.srdL; sh[2] = 131072u; const unsigned so = fes; asm volatile("buffer_load_dword %0, off, %1, %2 sc1" : "=v"(fe) : "s"(sh), "s"(so) : "memory"); }
    if constexpr (st == OP_SF - 2) { o.tg = tag; asm volatile("" : "+v"(o.tg)); }
    GAP();
  };
  for_seq(step, make_iseq<32>{});
  op_qs<32>(o, prep, make_iseq<1>{});
  auto fin2 = [&](const float s0, const float s1, const int wd) __attribute__((always_inline)) {
    const unsigned sp = cvtpk(s0, s1), pp = cvtpk(bflo(sp) * o.gx[2 * wd], bfhi(sp) * o.gx[2 * wd + 1]);
    return cvtpk(o.gx[18 + 2 * wd] + bflo(pp), o.gx[19 + 2 * wd] + bfhi(pp));
  };
  auto fin = [&](auto J_) __attribute__((always_inline)) {
    constexpr int j = decltype(J_)::value;
    const f32x4 s = o.D[1][2 * j] + o.T[2 * j];
    const __amdgpu_buffer_rsrc_t so = srdO; const unsigned sc = sn, va = ax[j >> 1];
    if constexpr (j < 2) {
      const f32x4 r = o.D[1][2 * j + 1] + o.T[2 * j + 1];
      const u32x4 ov = {fin2(s[0], s[1], 4 * j), fin2(s[2], s[3], 4 * j + 1), fin2(r[0], r[1], 4 * j + 2), fin2(r[2], r[3], 4 * j + 3)};
      asm volatile("buffer_store_dwordx4 %0, %1, %2, %3 offen offset:%c4\n\ts_nop 1" :: "v"(ov), "v"(va), "s"(so), "s"(sc), "i"(64 * j) : "memory");
    } else {
      auto p0 = __builtin_amdgcn_permlane32_swap(__float_as_uint(s[0]), __float_as_uint(s[2]), false, false), p1 = __builtin_amdgcn_permlane32_swap(__float_as_uint(s[1]), __float_as_uint(s[3]), false, false);
      const unsigned ov = fin2(__uint_as_float(p0[0]) + __uint_as_float(p0[1]), __uint_as_float(p1[0]) + __uint_as_float(p1[1]), 8);
      asm volatile("buffer_store_dword %0, %1, %2, %3 offen" :: "v"(ov), "v"(va), "s"(so), "s"(sc) : "memory");
    }
  };
  fin(IC(2)); fin(IC(0));
  GAP();
  fin(IC(1));
}

extern "C" __global__ __launch_bounds__(256, 1) __attribute__((aligned(4096)))
void k_attn(u16* __restrict__ xn, const u16* __restrict__ wq, const u16* __restrict__ Kc, const u16* __restrict__ Vc, u16* __restrict__ O,
            const u16* __restrict__ wo, const u16* __restrict__ wg, const u16* __restrict__ wu, const u16* __restrict__ wd, u16* __restrict__ wot,
            unsigned long long* __restrict__ keys, float* __restrict__ ypart, int* __restrict__ sel, const u16* __restrict__ xres, u16* __restrict__ out,
            const u16* __restrict__ w_r  ) {
  asm volatile(PS_MACROS ::: "memory");
  extern __shared__ __attribute__((aligned(16))) char lds[];
  const unsigned tid = threadIdx.x, lane = tid & 63u, wid = tid >> 6, l32 = lane & 31u, hi = lane >> 5;
  constexpr int T = NT;
  const float C = 0.12751743082f;
  W4C c;
  c.ldsb = (unsigned)(size_t)lds; c.pdum = c.ldsb + PF_DUM;
  asm volatile("" : "+s"(c.pdum));
  c.m0k = rfl(c.ldsb + KRING + 4096u * wid); c.m0v = rfl(c.ldsb + VRING + 4096u * wid);
  c.sofk = rfl(KRS * 16u * wid) + (32u / 1) * KTILEB * ((blockIdx.x >> 3) & 0u);

  const unsigned blk = blockIdx.x, xg = blk & 7u, ii = blk >> 3;
  const unsigned kvh = 2u * xg + (ii >> 4), h = 2u * kvh + ((ii >> 3) & 1u), yb = ii & 7u;
  const unsigned row0 = 256u * yb + 64u * wid;
  const bool moe = blk < (unsigned)MOE_WG;
  const bool ret = !moe && blk < 208u;
  unsigned tag = 0u, gen;
  const unsigned lrow = 8u * blk + 2u * wid + hi;
  QFT qf; unsigned psc;
  {
    QP p; LNJ lj; u32x4 cw; u32x2 g0, g1; unsigned pb0, pb1, pb2, pb3, csw; unsigned long long pbase;
    lj.three = l32 < 8u; lj.up = hi != 0u;
    const unsigned vl = 16u * l32 + XRS * hi, vl3 = lj.three ? vl + 1024u : 0x40000000u, srow = XRS * 8u * blk + XRS * 2u * rfl(wid);
    {
      const unsigned r8 = (lane >> 2) & 7u, vws = WRS * ((r8 & 3u) + 8u * (r8 >> 2)) + 64u * hi + 16u * (lane & 3u);
      p.vw[0] = vws + 896u; p.vw[1] = vws; p.vw[2] = vws + (4u * WRS - 2048u + 896u); p.vw[3] = vws + (4u * WRS - 2048u);
#pragma unroll
      for (int q = 0; q < 4; ++q) p.vx[q] = XRS * (lane >> 3) + 16u * ((lane & 7u) ^ (4u * (q & 1) + (lane >> 4))) + (8u * XRS - 1024u) * q;
    }
    p.m0x = rfl(c.ldsb + 0x4000u + 8192u * wid);
    p.sofw = rfl(WRS * 16u * wid + 256u * h);
    p.sofx = rfl(XRS * row0);
    p.kk = (yb ^ (ii >> 3)) & 1u;
    asm volatile("s_nop 4" : "+s"(c.m0k), "+s"(c.m0v), "+s"(c.sofk), "+s"(p.m0x), "+s"(p.sofw), "+s"(p.sofx), "+s"(p.kk));
    asm volatile("" : "+v"(p.vw[0]), "+v"(p.vw[1]), "+v"(p.vw[2]), "+v"(p.vw[3]), "+v"(p.vx[0]), "+v"(p.vx[1]), "+v"(p.vx[2]), "+v"(p.vx[3]));
    GAP();
    p.m0w = c.m0k;
    p.srdW = __builtin_amdgcn_make_buffer_rsrc((void*)((const char*)wq - 896), (short)0, DM * HQ * DH * 2 + 896, SRD_WORD3);
    p.srdX = __builtin_amdgcn_make_buffer_rsrc((void*)xres, (short)0, SEQ * DM * 2, SRD_WORD3);
    auto vm = [&](auto I_) __attribute__((always_inline)) {
      constexpr int I = decltype(I_)::value; constexpr VOp o = VT.v[I]; constexpr bool nm = vq_newm0(I); constexpr int q = o.q & 3;
      if constexpr (o.kind == VK_S) {
        if constexpr (o.q == 0 && o.st < 2) qp_next<o.st>(p);
        if constexpr (o.q < 4) lds_dma<nm, 1024 * q>(p.m0w + QP_STG * (o.st % 3), p.vw[q], p.srdW, p.sw);
        else {
          if constexpr (o.q == 8) { p.sx += 32u * XRS; asm volatile("" : "+s"(p.sx)); }
          lds_dma<nm, 1024 * q>(p.m0x + QP_STG * (o.st % 3) + (o.q >= 8 ? 4096u : 0u), p.vx[q], p.srdX, p.sx);
        }
      } else if constexpr (o.kind == VK_W8) lds_dma<nm, 1024 * q>(p.m0w + QP_W8, p.vw[q], p.srdW, p.sw);
      else if constexpr (o.kind == VK_KV) {
        if constexpr (o.q < 8) lds_dma<nm, 1024 * q>(c.m0k + 0x4000u * (o.q >> 2), (q & 2) ? c.ksx[q & 1] : c.vksrc[q & 1], c.srdK, KTILEB * (o.q >> 2) + c.sofk);
        else lds_dma<true, 0>(c.m0v + 1024u * q, c.vvsrc, c.srdV, c.sofk + (896u + KRS * 8u * (q >> 1) + 128u * (q & 1)));
      } else if constexpr (o.kind == VK_V1) lds_dma<nm, 1024 * q>(c.m0v + 0x4000u, (q & 1) ? c.vvsrc : c.vvsrc0, c.srdV, KTILEB + c.sofk);
      else if constexpr (o.kind == VK_K2) lds_dma<nm, 1024 * q>(c.m0k, (q & 2) ? c.ksx[q & 1] : c.vksrc[q & 1], c.srdK, (KTILEB * 2u + c.sofk) & KMASK);
      else if constexpr (o.kind == VK_PF)
        asm volatile("s_mov_b32 m0, %0\n\ts_add_u32 %1, %1, %4\n\tbuffer_load_dwordx4 %2, %3, %1 offen sc1 lds\n\ts_add_u32 %0, %0, 0x400"
                     : "+s"(c.pfm0), "+s"(c.pfo) : "v"(c.pfv), "s"(c.srdP), "s"(c.pfst) : "m0", "memory", "scc");
      else if constexpr (o.kind == VK_CW) {
        unsigned long long pa; unsigned po;
        asm volatile("s_getpc_b64 s[20:21]\n\ts_add_u32 s20, s20, k_attn@rel32@lo+4\n\ts_addc_u32 s21, s21, k_attn@rel32@hi+12\n\ts_mov_b64 %0, s[20:21]" : "=s"(pa) :: "scc", "s20", "s21");
        asm volatile("s_mov_b32 %0, .Lk_end-k_attn" : "=s"(po));
        const __amdgpu_buffer_rsrc_t srdC = __builtin_amdgcn_make_buffer_rsrc((void*)pa, (short)0, (int)po, SRD_WORD3);
        asm volatile("buffer_load_dwordx4 %0, %1, %2, %3 offen" : "=&v"(cw) : "v"(16u * tid), "s"(srdC), "s"((blk >> 3) * 4096u) : "memory");
      } else if constexpr (o.kind == VK_GEN)
        asm volatile("buffer_load_dword %0, off, %1, 0 offset:%c2 sc1" : "=&v"(gen) : "s"(__builtin_amdgcn_make_buffer_rsrc((void*)sel, (short)0, 8192, SRD_WORD3)), "i"(4 * H_G) : "memory");
      else if constexpr (o.kind == VK_A)
        asm volatile("buffer_load_dwordx4 %0, %1, %2, %3 offen offset:%c4" : "=&v"(lj.a[o.q]) : "v"(o.q == 2 ? vl3 : vl), "s"(p.srdX), "s"(srow), "i"(o.q == 1 ? 512 : 0) : "memory");
      else if constexpr (o.kind == VK_WR)
        asm volatile("buffer_load_dwordx4 %0, %1, %2, 0 offen offset:%c3" : "=&v"(lj.w[o.q]) : "v"(16u * l32), "s"(__builtin_amdgcn_make_buffer_rsrc((void*)w_r, (short)0, DM * 2, SRD_WORD3)), "i"(512 * o.q) : "memory");
      else if constexpr (o.kind == VK_STS || o.kind == VK_KEY) {
        const u32x2 g = {o.kind == VK_STS ? lj.stat : lj.key, tag};
        asm volatile("buffer_store_dwordx2 %0, %1, %2, %3 offen sc1" :: "v"(g), "v"(l32 == 0u ? 8u * lrow : 0x40000000u),
                     "s"(__builtin_amdgcn_make_buffer_rsrc((void*)keys, (short)0, SEQ * 16, SRD_WORD3)), "s"(o.kind == VK_STS ? SEQ * 8u : 0u) : "memory");
      } else if constexpr (o.kind == VK_GAT) {
        asm volatile("buffer_load_dwordx2 %0, %1, %2, %3 offen offset:%c4 sc1" : "=&v"(o.q ? g1 : g0) : "v"(8u * (row0 + l32)),
                     "s"(__builtin_amdgcn_make_buffer_rsrc((void*)keys, (short)0, SEQ * 16, SRD_WORD3)), "s"(SEQ * 8u), "i"(256 * o.q) : "memory");
      } else if constexpr (o.kind == VK_XNS) {
        const __amdgpu_buffer_rsrc_t srdN = __builtin_amdgcn_make_buffer_rsrc((void*)xn, (short)0, SEQ * DM * 2, SRD_WORD3);
        asm volatile("buffer_store_dwordx4 %0, %1, %2, %3 offen offset:%c4 sc1\n\ts_nop 1" :: "v"(lj.a[o.q]), "v"(o.q == 2 ? vl3 : vl), "s"(srdN), "s"(srow), "i"(o.q == 1 ? 512 : 0) : "memory");
      }
    };
    auto hook = [&](auto P_) __attribute__((always_inline)) {
      constexpr int pos = decltype(P_)::value;
      if constexpr (pos == PS_B) ps_touch(psc);
      if constexpr (pos == PS_A) ps_touch(psc & 3u);
      if constexpr (pos == 1) {
        asm volatile("s_waitcnt vmcnt(%5)" : "+v"(gen), "+v"(lj.a[0]), "+v"(lj.a[1]), "+v"(lj.a[2]), "+v"(cw) : "i"(vq_wait(VK_CW, 0, pos + 1)) : "memory");
        tag = rfl(gen) * 1664525u + 1013904223u;
        asm volatile("" : "+s"(tag));
      } else if constexpr (pos == 32 + 13) asm volatile("" : "+v"(lj.w[0]), "+v"(lj.w[1]), "+v"(lj.w[2]));
      else if constexpr (pos == 32 + 1) {
        c.vkb[0] = c.ldsb + KRING + 256u * (lane & 15u) + 16u * ((lane >> 4) ^ (lane & 15u));
        asm volatile("" : "+v"(c.vkb[0]));
      } else if constexpr (pos == 32 + 3) {
#pragma unroll
        for (int j = 1; j < 4; ++j) c.vkb[j] = c.vkb[0] ^ (64u * j);
        asm volatile("" : "+v"(c.vkb[1]), "+v"(c.vkb[2]), "+v"(c.vkb[3]));
      } else if constexpr (pos == 32 + 5) {
        c.vvb[0] = c.ldsb + VRING + (((lane & 3u) << 3) | (((lane >> 2) & 3u) << 6) | (((lane >> 4) & 1u) * 288u) | (hi << 11));
        c.vvb[1] = c.vvb[0] ^ 32u;
        asm volatile("" : "+v"(c.vvb[0]), "+v"(c.vvb[1]));
      } else if constexpr (pos == 32 + 7) {
#pragma unroll
        for (int par = 0; par < 2; ++par) c.vksrc[par] = KRS * (lane >> 4) + 16u * ((lane & 15u) ^ (4u * par + (lane >> 4)));
        c.ksx[0] = c.vksrc[0] ^ 128u; c.ksx[1] = c.vksrc[1] ^ 128u;
        asm volatile("" : "+v"(c.vksrc[0]), "+v"(c.vksrc[1]), "+v"(c.ksx[0]), "+v"(c.ksx[1]));
      } else if constexpr (pos == 32 + 9) {
        c.vvsrc = KRS * ((lane >> 2) & 7u) + 64u * hi + 16u * ((lane & 3u) ^ ((lane >> 3) & 2u)); c.vvsrc0 = c.vvsrc + 896u;
        asm volatile("" : "+v"(c.vvsrc), "+v"(c.vvsrc0));
      } else if constexpr (pos == 32 + 0) {
        const unsigned rs = moe ? FF * 2u : ret ? DM * 2u : 64u;
        const unsigned r16 = ret ? ((lane >> 2) & 3u) | (lane & 16u) | ((lane >> 3) & 4u) : lane >> 2;
        c.pfv = rs * r16 + 16u * ((lane & 3u) ^ ((lane >> 4) & 2u));
        asm volatile("" : "+v"(c.pfv));
      } else if constexpr (pos == 29) {
        const unsigned w = rfl(wid), fb = (blk & 7u) * 18u + (blk >> 3);
        pb0 = 64u * fb + 16u * FF * 2u * (w & 1u); pb1 = ((blk - 144u) * 4u + w) * 18432u - 9216u * (w & 1u); pb2 = ((blk - 208u) * 4u + w) * 27648u; pb3 = 18432u * w;
        const u16 *a0 = wg, *a1 = wu;
        asm volatile("s_cmp_lt_u32 %1, 2\n\ts_cselect_b64 %0, %2, %3" : "=s"(pbase) : "s"(w), "s"(a0), "s"(a1) : "scc");
        asm volatile("" : "+s"(pb0), "+s"(pb1), "+s"(pb2), "+s"(pb3));
      } else if constexpr (pos == 31) {
        const u16 *a0 = wo, *a1 = wd; const unsigned b = blk;
        unsigned long long pb; unsigned nb, st, np;
        asm volatile("s_cmp_lt_u32 %8, 208\n\ts_cselect_b64 %0, %9, %10\n\ts_cselect_b32 %5, %5, %6\n\ts_mov_b32 %1, %12\n\ts_cselect_b32 %2, 64, %13\n\ts_cselect_b32 %7, %7, 0\n\ts_cselect_b32 %3, %11, %14\n\t"
                     "s_cmp_lt_u32 %8, 144\n\ts_cselect_b64 %0, %15, %0\n\ts_cselect_b32 %5, %4, %5\n\ts_cselect_b32 %1, %16, %1\n\ts_cselect_b32 %2, %17, %2"
                     : "=&s"(pb), "=&s"(nb), "=&s"(st), "=&s"(np), "+s"(pb0), "+s"(pb1), "+s"(pb2), "+s"(pb3)
                     : "s"(b), "s"(a0), "s"(a1), "s"(18432u), "s"(27648u), "s"(1024u), "s"(20480u), "s"(pbase), "s"(559u * (FF * 2u) + 64u), "s"(FF * 2u * 32u) : "scc");
        pbase = pb; pb0 = nb; pb2 = np; c.pfst = st;
      } else if constexpr (pos == 32 + 15) {
        c.srdP = __builtin_amdgcn_make_buffer_rsrc((void*)(pbase + pb1), (short)0, (int)pb0, SRD_WORD3);
        c.pfo = 0u - c.pfst;
        c.pfm0 = c.ldsb + SD_BASE + pb3; c.pfend = c.pfm0 + pb2;
        asm volatile("" : "+s"(c.srdP), "+s"(c.pfst), "+s"(c.pfo), "+s"(c.pfm0), "+s"(c.pfend));
      } else if constexpr (pos == 32 + 17) {
        unsigned kvo = kvh; asm volatile("" : "+s"(kvo));
        c.srdK = __builtin_amdgcn_make_buffer_rsrc((void*)((const char*)Kc + (size_t)kvo * SEQ * 256), (short)0, SEQ * 256, SRD_WORD3);
        c.srdV = __builtin_amdgcn_make_buffer_rsrc((void*)((const char*)Vc + (size_t)kvo * SEQ * 256 - 896), (short)0, SEQ * 256 + 896, SRD_WORD3);
        asm volatile("" : "+s"(c.srdK), "+s"(c.srdV));
      } else if constexpr (pos == 64 + 1) {
        unsigned kvo = kvh; asm volatile("" : "+s"(kvo));
        const unsigned id = ((blk >> 3) & 15u) * 4u + rfl(wid);
        const u16 *a0 = Kc, *a1 = Vc; unsigned long long pb;
        asm volatile("s_cmp_lt_u32 %1, 32\n\ts_cselect_b64 %0, %2, %3" : "=s"(pb) : "s"(id), "s"(a0), "s"(a1) : "scc");
        c.srdF = __builtin_amdgcn_make_buffer_rsrc((void*)(pb + (size_t)kvo * SEQ * 256), (short)0, SEQ * 256, SRD_WORD3);
        c.sfo = (c.sofk & ~(KTILEB - 1u)) + KTILEB * (LAH + ((id >> 4) & 1u) - (id >> 5)) + 1024u * (id & 15u);
        asm volatile("" : "+s"(c.srdF), "+s"(c.sfo));
      } else if constexpr (pos == 192 + 0) {
        asm volatile("s_waitcnt vmcnt(%2)" : "+v"(g0), "+v"(g1) : "i"(vq_wait(VK_GAT, 0, pos + 1)) : "memory");
        if (__builtin_expect(!__all(g0[1] == tag && g1[1] == tag), 0)) {
          const unsigned long long* stg = keys + SEQ + row0 + l32;
#pragma nounroll
          for (unsigned spin = 0; spin < (1u << 20) && !__all(g0[1] == tag && g1[1] == tag); ++spin) {
            __builtin_amdgcn_s_sleep(6);
            asm volatile("global_load_dwordx2 %0, %2, off sc1\n\tglobal_load_dwordx2 %1, %2, off offset:256 sc1\n\ts_waitcnt vmcnt(0)" : "=&v"(g0), "=&v"(g1) : "v"(stg) : "memory");
          }
        }
      } else if constexpr (pos == 192 + 2 || pos == 192 + 3) {
        constexpr int q = pos == 192 + 3;
        qf.a[q] = C * rcp_nr(bflo((q ? g1 : g0)[0]));
        asm volatile("" : "+v"(qf.a[q]));
      } else if constexpr (pos == 192 + 5 || pos == 192 + 25) {
        constexpr int q = pos == 192 + 25;
        const unsigned n = hi == 0u ? ((q ? g1 : g0)[0] >> 16) ^ 0x8000u : 0u;
        qf.nb[q] = u32x4{n | (n << 16), n, 0u, 0u};
        asm volatile("" : "+v"(qf.nb[q]));
      } else if constexpr (pos == 192 + 30) {
        const f32x4 cs = p.cs;
        asm volatile("ds_write_b128 %0, %1" :: "v"(csw + 0u), "v"(cs) : "memory");
      } else if constexpr (pos == 192 + 22) {
        unsigned a0 = 128u * wid + 64u * (lane & 1u) + 16u * (lane >> 4), a1 = 1024u + 16u * tid;
        asm volatile("" : "+v"(a0), "+v"(a1));
        csw = c.ldsb + QP_CS + ((lane & 14u) == 0u ? a0 : a1);
        asm volatile("" : "+v"(csw));
      }
    };
    asm volatile("" :: "s"(xn), "s"(Kc), "s"(Vc), "s"(O), "s"(wo), "s"(wg), "s"(wu), "s"(wd), "s"(wot), "s"(keys), "s"(ypart), "s"(out), "s"(w_r));
    vq_issue<-3>(vm);
    asm volatile("s_branch 1f\n.Lps_ret: s_setpc_b64 vcc\n.Lps_tab:\n.irp n,0,1,2,3,4,5,6,7,8,9,10,11,12,13,14,15,16,17,18,19,20,21,22,23\ns_branch .Lpe_\\n\n.endr\n1:" ::: "memory");
    psc = rfl(wid) + 16u - 4u * (((blk - 144u) >> 31) + ((blk - 208u) >> 31) + ((blk - RED1) >> 31));
    asm volatile("" : "+s"(psc));
    hook(IC(-5));
    vq_issue<-2>(vm);
    GAP();
    vq_issue<-1>(vm);
    p.vwb = c.ldsb + ((((lane & 1u) << 5) | ((lane & 2u) << 2)) | (((lane >> 2) & 3u) << 6) | (lane & 16u) | (hi << 8));
#pragma unroll
    for (int ks = 0; ks < 4; ++ks) p.vxb[ks] = c.ldsb + 0x4000u + 8192u * wid + 128u * l32 + 16u * ((2u * ks + hi) ^ ((l32 >> 1) & 7u));
    p.vwc = p.vwb + 512u * wid;
    { const unsigned on = (lane & 15u) == ((lane >> 4) & 1u) ? 0x3f803f80u : 0u; p.sel = u32x4{on, on, on, on}; }
    GAP();
    asm volatile("s_waitcnt vmcnt(%0)\n\ts_barrier" :: "i"(vq_wait(VK_S, 0, 0)) : "memory");
    qp_rds<0, 8>(p, make_iseq<20>{});
    qp_iters(p, vm, hook, lj, lrow, make_iseq<7>{});
    asm volatile("s_waitcnt lgkmcnt(5)" ::: "memory");
#pragma unroll
    for (int d = 0; d < 4; ++d) qf.ca[d] = u32x4{0u, 0u, 0u, 0u};
    GAP();
    qj_gaps(p, c, qf, vm, c.ldsb + QP_CS + 4u * l32, make_iseq<NJ>{});
  }
  {
    const unsigned on = (lane & 2u) == 0u ? 0x3f803f80u : 0u; c.ONE = u32x4{on, on, on, on}; c.IDX = (lane & 1u) ? 0xeeeeeeeeu : 0x44444444u;
    c.L[0] = c.L[1] = c.L[2] = c.L[3] = f32x4{0.f, 0.f, 0.f, 0.f};
    asm volatile("" : "+v"(c.ONE), "+v"(c.IDX), "+v"(c.L[0]), "+v"(c.L[1]), "+v"(c.L[2]), "+v"(c.L[3]));
  }
  unsigned rstate = 3u;
  auto route = [&]() __attribute__((always_inline)) {
    unsigned lq;
    asm volatile("v_mbcnt_lo_u32_b32 %0, -1, 0\n\tv_mbcnt_hi_u32_b32 %0, -1, %0" : "=v"(lq));
    const unsigned w = rfl(wid), kb_ = c.ldsb + MO_KEY + 4096u * w;
    bool req = rstate == 0u;
    if (rstate == 1u) {
      u32x4 g[4];
      asm volatile("ds_read_b128 %0, %4\n\tds_read_b128 %1, %4 offset:1024\n\tds_read_b128 %2, %4 offset:2048\n\tds_read_b128 %3, %4 offset:3072\n\ts_waitcnt lgkmcnt(0)"
                   : "=&v"(g[0]), "=&v"(g[1]), "=&v"(g[2]), "=&v"(g[3]) : "v"(kb_ + 16u * lq) : "memory");
      bool ok = true;
#pragma unroll
      for (int j = 0; j < 4; ++j) ok = ok && g[j][1] == tag && g[j][3] == tag;
      if (__all(ok)) {
        unsigned k[8] = {g[0][0], g[0][2], g[1][0], g[1][2], g[2][0], g[2][2], g[3][0], g[3][2]};
        ce(k[0], k[1]); ce(k[2], k[3]); ce(k[4], k[5]); ce(k[6], k[7]);
        ce(k[0], k[2]); ce(k[1], k[3]); ce(k[4], k[6]); ce(k[5], k[7]);
        ce(k[1], k[2]); ce(k[5], k[6]); ce(k[0], k[4]); ce(k[3], k[7]);
        ce(k[1], k[5]); ce(k[2], k[6]);
        ce(k[1], k[4]); ce(k[3], k[6]);
        ce(k[2], k[4]); ce(k[3], k[5]);
        ce(k[3], k[4]);
        unsigned cnd = 0u;
#pragma nounroll
        for (unsigned r = 0; r < (unsigned)NSEL; ++r) {
          unsigned m = k[0], t;
          t = (unsigned)__builtin_amdgcn_update_dpp(0, (int)m, 0xB1, 0xf, 0xf, false); m = t > m ? t : m;
          t = (unsigned)__builtin_amdgcn_update_dpp(0, (int)m, 0x4E, 0xf, 0xf, false); m = t > m ? t : m;
          t = (unsigned)__builtin_amdgcn_update_dpp(0, (int)m, 0x141, 0xf, 0xf, false); m = t > m ? t : m;
          t = (unsigned)__builtin_amdgcn_update_dpp(0, (int)m, 0x140, 0xf, 0xf, false); m = t > m ? t : m;
          cnd = (lq & 15u) == (unsigned)r ? m : cnd;
          const bool pp = k[0] == m;
#pragma unroll
          for (int i = 0; i < 7; ++i) k[i] = pp ? k[i + 1] : k[i];
        }
        lds_st32(c.ldsb + MO_RS + ((lq & 8u) == 0u ? 64u + 128u * w + 2u * (lq & 48u) + 4u * (lq & 7u) : 640u + 4u * lq), cnd);
        asm volatile("s_waitcnt lgkmcnt(0)" ::: "memory");
        lds_st32(c.ldsb + MO_RS + 4u * w, 1u);
        rstate = 2u;
      } else req = true;
    } else if (rstate == 2u) {
      u32x4 fl;
      asm volatile("ds_read_b128 %0, %1\n\ts_waitcnt lgkmcnt(0)" : "=v"(fl) : "v"(c.ldsb + MO_RS) : "memory");
      if (rfl(fl[0] & fl[1] & fl[2] & fl[3]) != 0u) {
        unsigned cv, rank = 0u;
        asm volatile("ds_read_b32 %0, %1" : "=v"(cv) : "v"(c.ldsb + MO_RS + 64u + 128u * w + 4u * (lq & 31u)) : "memory");
#pragma nounroll
        for (unsigned h = 0; h < 4u; ++h) {
          u32x4 c4v[4];
          asm volatile("ds_read_b128 %0, %5\n\tds_read_b128 %1, %5 offset:16\n\tds_read_b128 %2, %5 offset:32\n\tds_read_b128 %3, %5 offset:48\n\ts_waitcnt lgkmcnt(0)"
                       : "=&v"(c4v[0]), "=&v"(c4v[1]), "=&v"(c4v[2]), "=&v"(c4v[3]), "+v"(cv) : "v"(c.ldsb + MO_RS + 64u + 256u * (lq >> 5) + 64u * h) : "memory");
#pragma unroll
          for (int j = 0; j < 4; ++j)
#pragma unroll
            for (int e = 0; e < 4; ++e) rank += c4v[j][e] > cv ? 1u : 0u;
        }
        auto pr = __builtin_amdgcn_permlane32_swap(rank, rank, false, false);
        rank = pr[0] + pr[1];
        {
          const float rs = rbf(__builtin_amdgcn_rcpf(1.f + __builtin_amdgcn_exp2f(key_val(cv) * -1.4426950408889634f)));
          const u32x2 gr = {(__float_as_uint(rs) & 0xffff0000u) | (unsigned)key_row(cv), tag};
          const unsigned vo = lq < 32u && rank < 8u ? 8u * rank : 0x40000000u, vg = (lq | w) == 0u ? 4u * H_G : 0x40000000u;
          asm volatile("s_nop 4\n\tbuffer_store_dwordx2 %0, %1, %3, 0 offen sc1\n\tbuffer_store_dword %4, %2, %3, 0 offen sc1\n\ts_nop 1" :: "v"(gr), "v"(vo), "v"(vg), "s"(mk_srd(sel, 8192u)), "v"(gr[1]) : "memory");
        }
        rstate = 3u;
      }
    }
    if (req) {
      const __amdgpu_buffer_rsrc_t srdK = mk_srd((keys + 512u * w), 4096);
#pragma unroll
      for (int j = 0; j < 4; ++j)
        asm volatile("s_mov_b32 m0, %0\n\ts_nop 4\n\tbuffer_load_dwordx4 %1, %2, %3 offen sc1 lds" :: "s"(kb_ + 1024u * j), "v"(16u * lq), "s"(srdK), "s"(1024u * j) : "m0", "memory");
      rstate = 1u;
    }
  };
  const unsigned wids = rfl(wid);
  auto opq = [&](unsigned v) __attribute__((always_inline)) { asm volatile("" : "+s"(v)); return v; };
  auto lane_id = [&]() __attribute__((always_inline)) { unsigned lq; asm volatile("v_mbcnt_lo_u32_b32 %0, -1, 0\n\tv_mbcnt_hi_u32_b32 %0, -1, %0" : "=v"(lq)); return lq; };
  auto hand_req = [&](const unsigned m0, const unsigned word0, const unsigned nb) __attribute__((always_inline)) {
    const unsigned vo = c.vksrc[0];
    asm volatile("s_mov_b32 m0, %0\n\ts_nop 0\n\tbuffer_load_dwordx4 %1, %2, 0 offen sc1 lds" :: "s"(m0), "v"(vo), "s"(mk_srd(sel + word0, nb)) : "m0", "memory");
  };
  auto flag1 = [&](const unsigned word, const unsigned on) __attribute__((always_inline)) {
    const unsigned vo = c.vvsrc, tg = tag;
    asm volatile("buffer_store_dword %0, %1, %2, 0 offen sc1" :: "v"(tg), "v"(vo), "s"(mk_srd(sel + word, on)) : "memory");
  };
  auto pf1 = [&]() __attribute__((always_inline)) {
    asm volatile("s_mov_b32 m0, %0\n\ts_add_u32 %1, %1, %4\n\tbuffer_load_dwordx4 %2, %3, %1 offen sc1 lds\n\ts_add_u32 %0, %0, 0x400"
                 : "+s"(c.pfm0), "+s"(c.pfo) : "v"(c.pfv), "s"(c.srdP), "s"(c.pfst) : "m0", "memory", "scc");
  };
  auto m0_set = [&](const unsigned m0) __attribute__((always_inline)) { asm volatile("s_mov_b32 m0, %0" :: "s"(m0) : "m0", "memory"); };
  auto dma16 = [&](const __amdgpu_buffer_rsrc_t sr, const unsigned vo) __attribute__((always_inline)) { asm volatile("buffer_load_dwordx4 %0, %1, 0 offen sc1 lds" :: "v"(vo), "s"(sr) : "memory"); };
  auto flag_st = [&](const __amdgpu_buffer_rsrc_t sr, const unsigned tg) __attribute__((always_inline)) { const unsigned vo = c.vvsrc; asm volatile("buffer_store_dword %0, %1, %2, 0 offen sc1" :: "v"(tg), "v"(vo), "s"(sr) : "memory"); };
  struct CW { unsigned long long pa; unsigned so; __amdgpu_buffer_rsrc_t sr; };
  auto warm_step = [&](CW& k, auto I_) __attribute__((always_inline)) {
    constexpr int I = decltype(I_)::value;
    if constexpr (I == 0) asm volatile("s_getpc_b64 s[20:21]\n\ts_add_u32 s20, s20, k_attn@rel32@lo+4\n\ts_addc_u32 s21, s21, k_attn@rel32@hi+12\n\ts_mov_b64 %0, s[20:21]" : "=s"(k.pa) :: "scc", "s20", "s21");
    if constexpr (I == 1) { unsigned nb; asm volatile("s_mov_b32 %0, .Lk_end-k_attn" : "=s"(nb)); k.sr = __builtin_amdgcn_make_buffer_rsrc((void*)k.pa, (short)0, (int)nb, SRD_WORD3); asm volatile("" : "+s"(k.sr)); }
    if constexpr (I == 2) { k.so = (opq(blk) >> 3) * 4096u + (c.m0k - c.ldsb) / 4u; asm volatile("" : "+s"(k.so)); }
    if constexpr (I == 3) m0_set(c.pdum);
    if constexpr (I == 4) { const unsigned vo = c.vksrc[0], so = k.so; const __amdgpu_buffer_rsrc_t sr = k.sr; asm volatile("buffer_load_dwordx4 %0, %1, %2 offen lds" :: "v"(vo), "s"(sr), "s"(so) : "memory"); }
  };
  auto warm_hook = [&](CW& k, auto M_) __attribute__((always_inline)) {
    constexpr int M = decltype(M_)::value, i = M == FB(0) ? 0 : M == FB(1) ? 1 : M == FB(2) ? 2 : M == 39 ? 3 : M == 40 ? 4 : -1;
    if constexpr (i >= 0) warm_step(k, std::integral_constant<int, i>{});
  };
  if constexpr (PS_B == 999) ps_touch(psc);
  GAP();
  phaseA_end<2>(c);
  {
    CW k;
    auto hook = [&](auto M_) __attribute__((always_inline)) { warm_hook(k, M_); };
    phaseB_<1, true, true, 2>(c, hook, make_iseq<NGB>{});
  }
  GAP();
  if (__builtin_expect(opq(blk) == RTB, 0)) {
    {
      const unsigned w = opq(wids), kb_ = c.ldsb + MO_KEY + 4096u * w;
      RTS r; __amdgpu_buffer_rsrc_t sk;
      auto hookA2 = [&](auto M_) __attribute__((always_inline)) {
        constexpr int M = decltype(M_)::value, n = fa_of(M);
        if constexpr (n == 0) { r.lq = lane_id(); asm volatile("" : "+v"(r.lq)); }
        if constexpr (n == 1) { sk = mk_srd(keys + 512u * w, 4096); asm volatile("" : "+s"(sk)); }
        if constexpr (n == 2) { r.va = 16u * r.lq; m0_set(kb_); asm volatile("" : "+v"(r.va)); }
        if constexpr (n == 4 || n == 5 || n == 7 || n == 9) {
          constexpr int j = n == 4 ? 0 : n == 5 ? 1 : n == 7 ? 2 : 3;
          const unsigned v = r.va; const __amdgpu_buffer_rsrc_t sr = sk;
          asm volatile("buffer_load_dwordx4 %0, %1, 0 offen offset:%c2 sc1 lds" :: "v"(v), "s"(sr), "i"(1024 * j) : "memory");
        }
      };
      phaseAh<0, 7>(c, 2, hookA2);
      phaseB<0, 2>(c);
      auto hookA3 = [&](auto M_) __attribute__((always_inline)) { if constexpr (decltype(M_)::value == FA(0)) { r.va += kb_; asm volatile("" : "+v"(r.va)); } };
      phaseAh<1, 2>(c, 3, hookA3);
      auto steps = [&](auto I0_, auto G0_, auto M_) __attribute__((always_inline)) {
        constexpr int M = decltype(M_)::value, I = decltype(I0_)::value + M - 100 - decltype(G0_)::value;
        if constexpr (M >= 100 + decltype(G0_)::value && I < RT_END) rt_step<I>(r, tag);
      };
      auto hookB3 = [&](auto M_) __attribute__((always_inline)) {
        constexpr int M = decltype(M_)::value;
        if constexpr (M == 2 || M == 4 || M == 5 || M == 7) {
          constexpr int j = M == 2 ? 0 : M == 7 ? 3 : M - 3;
          u32x4& d = r.g[j]; const unsigned v = r.va;
          asm volatile("ds_read_b128 %0, %1 offset:%2" : "=v"(d) : "v"(v), "i"(1024 * j) : "memory");
        }
        if constexpr (M == FB(0)) { u32x4 (&g)[4] = r.g; asm volatile("s_waitcnt lgkmcnt(%4)" : "+v"(g[0]), "+v"(g[1]), "+v"(g[2]), "+v"(g[3]) : "i"(kyo(7, FB(0))) : "memory"); }
        steps(IC(0), IC(FB(0)), M_);
      };
      phaseBh<1, 2>(c, hookB3);
      auto hookA4 = [&](auto M_) __attribute__((always_inline)) {
        constexpr int M = decltype(M_)::value;
        steps(IC(NGB - FB(0)), IC(0), M_);
        if constexpr (M == FA(6)) { r.va = (r.lq & 8u) == 0u ? 64u + 128u * w + 2u * (r.lq & 48u) + 4u * (r.lq & 7u) : 640u + 4u * r.lq; asm volatile("" : "+v"(r.va)); }
        if constexpr (M == FA(8)) lds_st32(c.ldsb + MO_RS + r.va, r.cnd);
        if constexpr (M == FA(9)) lds_st32(c.ldsb + MO_RS + 4u * w, __all(r.bad == 0u) ? 1u : 0u);
        if constexpr (M == FA(10)) { r.va = c.ldsb + MO_RS + 64u + 256u * (r.lq >> 5); r.m = c.ldsb + MO_RS + 64u + 128u * w + 4u * (r.lq & 31u); r.rank = 0u; asm volatile("" : "+v"(r.va), "+v"(r.m), "+v"(r.rank)); }
      };
      static_assert(NGB - FB(0) + FA(6) >= RT_END, "");
      phaseAh<0, 3>(c, 4, hookA4);
      auto hookB4 = [&](auto M_) __attribute__((always_inline)) {
        constexpr int M = decltype(M_)::value;
        if constexpr (M == 0) { unsigned& d = r.cv; u32x4& f = r.g[3]; const unsigned v = r.m, v2 = c.ldsb + MO_RS; asm volatile("ds_read_b32 %0, %2\n\tds_read_b128 %1, %3" : "=&v"(d), "=&v"(f) : "v"(v), "v"(v2) : "memory"); }
        if constexpr (M < 60 && M % 4 == 0) { u32x4& d = r.g[(M >> 2) % 3]; const unsigned v = r.va; asm volatile("ds_read_b128 %0, %1 offset:%2" : "=v"(d) : "v"(v), "i"(4 * M) : "memory"); }
        if constexpr (M >= 5 && M < 65 && M % 4 == 1) { u32x4 &d = r.g[((M - 5) >> 2) % 3], &f = r.g[3]; unsigned& cv = r.cv; asm volatile("s_waitcnt lgkmcnt(%3)" : "+v"(d), "+v"(cv), "+v"(f) : "i"(kyo(M - 5, M) + (M < 61)) : "memory"); }
        if constexpr (M >= 105 && M < 165) { unsigned long long& pm = r.pm[M & 1]; const unsigned a = r.g[((M - 105) >> 2) % 3][(M - 105) & 3], cv = r.cv; asm volatile("v_cmp_gt_u32_e64 %0, %1, %2" : "=s"(pm) : "v"(a), "v"(cv)); }
        if constexpr (M >= 106 && M < 166) cnt_s(r.rank, r.pm[(M - 1) & 1]);
        if constexpr (M == 165) { r.bad = r.g[3][0] & r.g[3][1] & r.g[3][2] & r.g[3][3]; asm volatile("" : "+v"(r.bad)); }
        if constexpr (M == 66) { u32x4& d = r.g[0]; const unsigned v = r.va; asm volatile("ds_read_b128 %0, %1 offset:240" : "=v"(d) : "v"(v) : "memory"); }
      };
      phaseBh<0, 2>(c, hookB4);
      auto hookA5 = [&](auto M_) __attribute__((always_inline)) {
        constexpr int M = decltype(M_)::value;
        if constexpr (M == 4) { u32x4& d = r.g[0]; asm volatile("s_waitcnt lgkmcnt(3)" : "+v"(d) :: "memory"); }
        if constexpr (M >= 104 && M < 108) { r.rank += r.g[0][M - 104] > r.cv ? 1u : 0u; asm volatile("" : "+v"(r.rank)); }
        if constexpr (M == FA(1)) { auto pr = __builtin_amdgcn_permlane32_swap(r.rank, r.rank, false, false); r.rank = pr[0] + pr[1]; asm volatile("" : "+v"(r.rank)); }
        if constexpr (M == FA(2)) { r.rs = key_val(r.cv) * -1.4426950408889634f; asm volatile("" : "+v"(r.rs)); }
        if constexpr (M == FA(3)) { r.rs = 1.f + __builtin_amdgcn_exp2f(r.rs); asm volatile("" : "+v"(r.rs)); }
        if constexpr (M == FA(4)) { r.rs = rbf(__builtin_amdgcn_rcpf(r.rs)); asm volatile("" : "+v"(r.rs)); }
        if constexpr (M == FA(5)) { r.m = (__float_as_uint(r.rs) & 0xffff0000u) | (unsigned)key_row(r.cv); r.bad = (r.bad - 1u) & 0x40000000u; asm volatile("" : "+v"(r.m), "+v"(r.bad)); }
        if constexpr (M == FA(7)) { r.va = (r.lq < 32u && r.rank < 8u ? 8u * r.rank : 0x40000000u) | r.bad; asm volatile("" : "+v"(r.va)); }
        if constexpr (M == 100 + FA(8)) { r.cnd = ((r.lq | w) == 0u ? 4u * H_G : 0x40000000u) | r.bad; sk = mk_srd(sel, 8192u); asm volatile("" : "+v"(r.cnd), "+s"(sk)); }
        if constexpr (M == FA(9)) {
          const u32x2 gr = {r.m, tag}; const unsigned vo = r.va, vg = r.cnd; const __amdgpu_buffer_rsrc_t sr = sk;
          asm volatile("buffer_store_dwordx2 %0, %1, %3, 0 offen sc1\n\tbuffer_store_dword %4, %2, %3, 0 offen sc1" :: "v"(gr), "v"(vo), "v"(vg), "s"(sr), "v"(gr[1]) : "memory");
        }
      };
      phaseAh<1, 4>(c, 5, hookA5);
      rstate = rfl(r.bad) ? 0u : 3u;
      if (rstate == 0u) lds_st32(c.ldsb + MO_RS + 4u * w, 0u);
      phaseB<1, 2>(c);
    }
#pragma nounroll
    for (int t = 6; t < 14; t += 2) {
      phaseA<0, 3>(c, t);     phaseB<0, 2>(c);
      phaseA<1, 2>(c, t + 1); phaseB<1, 2>(c);
      if (rstate != 3u) {
        route();
        if (t == 12)
#pragma nounroll
          for (unsigned spin = 0; spin < (1u << 20) && rstate != 3u; ++spin) {
            asm volatile("s_waitcnt vmcnt(0)" ::: "memory");
            route();
            __builtin_amdgcn_s_sleep(8);
          }
      }
      GAP();
    }
  }
  GAP();
  {
    const unsigned b = opq(blk);
    const int t1 = 12 - 6 * (int)((b - 144u) >> 31) - 4 * (int)(((b - 208u) & ~(b - 144u)) >> 31) - 12 * (int)(((b ^ RTB) - 1u) >> 31);
#pragma nounroll
    for (int t = 0; __builtin_expect(t != t1, 1);) {
      t += 2;
      phaseA<0, 3>(c, t);     phaseB<0, 2>(c);
      phaseA<1, 2>(c, t + 1); phaseB<1, 2>(c);
    }
  }
  GAP();
  constexpr auto SG = [](int i) constexpr { constexpr int g[10] = {14, 20, 26, 31, 36, 41, 46, 52, 57, 61}; return g[i + 1]; };
  constexpr auto sg_of = [](int m) constexpr { constexpr int g[10] = {14, 20, 26, 31, 36, 41, 46, 52, 57, 61}; for (int i = 0; i < 10; ++i) if (g[i] == m) return i - 1; return -2; };
  if (__builtin_expect(opq(blk) < 144u, 1)) {
    PS_AT(40);
    const unsigned w = opq(wids), b = opq(blk), fbb = (b & 7u) * 18u + (b >> 3);
    phaseA<0, 3>(c, 8);
    {
      __amdgpu_buffer_rsrc_t sr; unsigned m0;
      auto hook = [&](auto M_) __attribute__((always_inline)) {
        constexpr int M = decltype(M_)::value;
        if constexpr (M == FB(1)) { sr = mk_srd(sel, 64u); asm volatile("" : "+s"(sr)); }
        if constexpr (M == FB(2)) { m0 = c.ldsb + MO_C; asm volatile("" : "+s"(m0)); }
        if constexpr (M == 39) m0_set(m0);
        if constexpr (M == 40) dma16(sr, c.vksrc[0]);
      };
      phaseBh<0, 2>(c, hook);
    }
    {
      unsigned va, vo; u32x2 sv; __amdgpu_buffer_rsrc_t srdX;
      constexpr auto yo = [](int g0, int g1) constexpr { return 2 * (pcnt(PA0, 'T', g1) - pcnt(PA0, 'T', g0)) + pcnt(PA0, 'X', g1) - pcnt(PA0, 'X', g0); };
      auto hook = [&](auto M_) __attribute__((always_inline)) {
        constexpr int M = decltype(M_)::value;
        if constexpr (M == FA(0)) { va = c.ldsb + MO_C + ((c.vksrc[0] >> 1) & 0x38u); asm volatile("" : "+v"(va)); }
        if constexpr (M == FA(2)) { vo = ((c.vksrc[0] >> 1) & 64u) | ((c.vksrc[0] >> 4) & 0x30u); asm volatile("" : "+v"(vo)); }
        if constexpr (M == FA(4)) { srdX = mk_srd(xn, SEQ * DM * 2); asm volatile("" : "+s"(srdX)); }
        if constexpr (M == FA(5)) asm volatile("s_waitcnt vmcnt(2)\n\tds_read_b64 %0, %1" : "=v"(sv) : "v"(va) : "memory");
        if constexpr (M == 100 + FA(6)) {
          asm volatile("s_waitcnt lgkmcnt(%1)" : "+v"(sv) : "i"(yo(FA(5), FA(6))) : "memory");
          if (__builtin_expect(!__all(sv[1] == tag), 0)) {
#pragma nounroll
            for (unsigned spin = 0; spin < (1u << 20) && !__all(sv[1] == tag); ++spin) {
              __builtin_amdgcn_s_sleep(8);
              hand_req(c.ldsb + MO_C, 0u, 64u);
              asm volatile("s_waitcnt vmcnt(0)\n\tds_read_b64 %0, %1\n\ts_waitcnt lgkmcnt(0)" : "=v"(sv) : "v"(va) : "memory");
            }
          }
        }
        if constexpr (M == FA(7)) { vo += (sv[0] & 0xffffu) * XRS; asm volatile("" : "+v"(vo)); }
        if constexpr (M == 100 + FA(8) || M == FA(9) || M == FA(10)) {
          constexpr unsigned i = M == FA(9) ? 1 : M == FA(10) ? 2 : 0;
          const unsigned v = vo, m0 = c.ldsb + MO_XR + 1024u * w + 4096u * i, so = 128u * w + 512u * i; const __amdgpu_buffer_rsrc_t sx = srdX;
          asm volatile("s_mov_b32 m0, %0\n\ts_nop 0\n\tbuffer_load_dwordx4 %1, %2, %3 offen sc1 lds" :: "s"(m0), "v"(v), "s"(sx), "s"(so) : "m0", "memory");
        }
      };
      phaseAh<1, 5>(c, 9, hook);
    }
    phaseB<1, 0>(c);
    f32x4 gu; unsigned vw, vx;
    auto gate_up = [&](auto H_) __attribute__((always_inline)) {
      constexpr int HH = decltype(H_)::value;
      bf16x4 wl[2], wh[2]; bf16x8 xf[2];
      auto hook = [&](auto M_) __attribute__((always_inline)) {
        constexpr int M = decltype(M_)::value, j = sg_of(M % 100);
        if constexpr (M < 100 && j >= 0) asm volatile("s_waitcnt lgkmcnt(%3)" : "+v"(wl[j & 1]), "+v"(wh[j & 1]), "+v"(xf[j & 1]) : "i"(kyo(SG(j - 1) + 1, M)) : "memory");
        if constexpr (M >= 100 && j >= 0) {
          const bf16x8 wfr = __builtin_shufflevector(wl[j & 1], wh[j & 1], 0, 1, 2, 3, 4, 5, 6, 7);
          if constexpr (HH == 0 && j == 0) asm volatile("mf16 %0, %1, %2, 0" : "=&v"(gu) : "v"(wfr), "v"(xf[j & 1]));
          else                             asm volatile("mf16 %0, %1, %2, %0" : "+v"(gu) : "v"(wfr), "v"(xf[j & 1]));
        }
        if constexpr (M >= 100 && j >= -1 && j < 8) {
          constexpr int n = (j + 1) & 1, i = 9 * HH + j + 1;
          asm volatile("ds_read_b64_tr_b16 %0, %1 offset:%2" : "=v"(wl[n]) : "v"(vw), "i"(1024 * i) : "memory");
          asm volatile("ds_read_b64_tr_b16 %0, %1 offset:%2" : "=v"(wh[n]) : "v"(vw), "i"(1024 * i + 256) : "memory");
          asm volatile("ds_read_b128 %0, %1 offset:%2" : "=v"(xf[n]) : "v"(vx), "i"(1024 * (i >> 1) + 128 * (i & 1)) : "memory");
        }
        if constexpr (HH == 1) {
          if constexpr (M == DB(8) || M == DB(9) || M == FB(6) || M == FB(8)) pf1();
          if constexpr (M == FB(9)) { vx += 1024u * w; asm volatile("" : "+v"(vx)); }
          if constexpr (M == 72) { const f32x4 g4 = gu; asm volatile("ds_write_b128 %0, %1 offset:%2" :: "v"(vx), "v"(g4), "i"(MO_H - MO_XR) : "memory"); }
        }
      };
      phaseBh<HH, 0>(c, hook);
    };
    {
      auto hook = [&](auto M_) __attribute__((always_inline)) {
        constexpr int M = decltype(M_)::value;
        if constexpr (M == FA(0)) { vw = c.vvb[0] & 0xffu; vx = c.vvb[0] & 32u; asm volatile("" : "+v"(vw), "+v"(vx)); }
        if constexpr (M == FA(1)) { vw = (vw + 16u * vx) ^ (32u * (w & 1u)); asm volatile("" : "+v"(vw)); }
        if constexpr (M == FA(2)) { vx = 9u * (c.vvb[0] & 2048u); asm volatile("" : "+v"(vx)); }
        if constexpr (M == FA(3)) { vw += vx + (c.ldsb + SD_BASE + 36864u * (w >> 1)); asm volatile("" : "+v"(vw)); }
        if constexpr (M == FA(4)) { vx = c.vksrc[0] + (c.ldsb + MO_XR); asm volatile("" : "+v"(vx)); }
      };
      phaseAh<0, 1>(c, 10, hook);
    }
    gate_up(std::integral_constant<int, 0>{});
    {
      unsigned lq;
      auto hook = [&](auto M_) __attribute__((always_inline)) {
        constexpr int M = decltype(M_)::value;
        if constexpr (M == FA(0)) { lq = lane_id(); asm volatile("" : "+v"(lq)); }
        if constexpr (M == FA(2)) { c.pfv = 16u * (lq & 3u); asm volatile("" : "+v"(c.pfv)); }
        if constexpr (M == FA(4)) { c.pfv += XRS * ((lq >> 2) & 3u); asm volatile("" : "+v"(c.pfv)); }
        if constexpr (M == FA(5)) { c.pfv += 2u * (lq & 32u); asm volatile("" : "+v"(c.pfv)); }
        if constexpr (M == FA(9)) { c.pfv += XRS * (lq & 16u); asm volatile("" : "+v"(c.pfv)); }
        if constexpr (M == 3) { c.srdP = mk_srd(wd, DM * FF * 2); asm volatile("" : "+s"(c.srdP)); }
        if constexpr (M == FA(1)) { c.pfo = 32u * fbb + 4u * w; asm volatile("" : "+s"(c.pfo)); }
        if constexpr (M == FA(3)) { c.pfst = 128u; c.pfo = XRS * c.pfo - 128u; asm volatile("" : "+s"(c.pfst), "+s"(c.pfo)); }
        if constexpr (M == FA(7)) { c.pfm0 = c.ldsb + SD_BASE + 18432u * w; asm volatile("" : "+s"(c.pfm0)); }
      };
      phaseAh<1, 0>(c, 11, hook);
    }
    gate_up(std::integral_constant<int, 1>{});
    {
      auto hook = [&](auto M_) __attribute__((always_inline)) { constexpr int M = decltype(M_)::value; if constexpr (M == FA(0) || M == FA(2) || M == FA(4) || M == FA(5) || M == FA(9)) pf1(); };
      phaseAh<0, 10>(c, 12, hook);
    }
    {
      unsigned vh; f32x2 g, u, s;
      auto hook = [&](auto M_) __attribute__((always_inline)) {
        constexpr int M = decltype(M_)::value;
        if constexpr (M == 109) { vh = c.vksrc[0] + (c.ldsb + MO_H + 1024u * (w >> 1) + 8u * (w & 1u)); asm volatile("" : "+v"(vh)); }
        if constexpr (M == FB(0)) asm volatile("ds_read_b64 %0, %2\n\tds_read_b64 %1, %2 offset:2048" : "=&v"(g), "=&v"(u) : "v"(vh) : "memory");
        if constexpr (M == 116) { vh = c.vksrc[0] + (c.ldsb + MO_HH + 4u * w); asm volatile("" : "+v"(vh)); }
        if constexpr (M == FB(1)) {
          asm volatile("s_waitcnt lgkmcnt(%2)" : "+v"(g), "+v"(u) : "i"(kyo(FB(0), FB(1))) : "memory");
          const unsigned pg = cvtpk(g[0], g[1]);
          g[0] = bflo(pg); g[1] = bfhi(pg);
          asm volatile("" : "+v"(g));
        }
        if constexpr (M == FB(2)) { const unsigned pu = cvtpk(u[0], u[1]); u[0] = bflo(pu); u[1] = bfhi(pu); asm volatile("" : "+v"(u)); }
        if constexpr (M == FB(3)) { s[0] = g[0] * -1.4426950408889634f; s[1] = g[1] * -1.4426950408889634f; asm volatile("" : "+v"(s)); }
        if constexpr (M == DB(8) || M == FB(4)) { constexpr int e = M == FB(4); s[e] = __builtin_amdgcn_exp2f(s[e]); asm volatile("" : "+v"(s)); }
        if constexpr (M == DB(9)) { float s0 = s[0], s1 = s[1]; add_v(s0, 1.f); add_v(s1, 1.f); s = f32x2{s0, s1}; }
        if constexpr (M == FB(5) || M == FB(6)) { constexpr int e = M == FB(6); s[e] = __builtin_amdgcn_rcpf(s[e]); asm volatile("" : "+v"(s)); }
        if constexpr (M == FB(7)) { const unsigned ps = cvtpk(g[0] * s[0], g[1] * s[1]); s[0] = __uint_as_float(ps); asm volatile("" : "+v"(s)); }
        if constexpr (M == FB(8)) { const unsigned ps = __float_as_uint(s[0]); u[0] *= bflo(ps); u[1] *= bfhi(ps); asm volatile("" : "+v"(u)); }
        if constexpr (M == FB(9)) { const unsigned hv = cvtpk(u[0], u[1]); asm volatile("ds_write_b32 %0, %1" :: "v"(vh), "v"(hv) : "memory"); }
      };
      phaseBh<0, 0>(c, hook);
    }
    {
      unsigned vd, vh, vyo, lq;
      __amdgpu_buffer_rsrc_t srdYs;
      bf16x4 dl[2], dh[2]; f32x4 y[2]; u32x4 hwv;
      auto hookA = [&](auto M_) __attribute__((always_inline)) {
        constexpr int M = decltype(M_)::value;
        if constexpr (M == FA(0)) { vh = c.vksrc[0] + (c.ldsb + MO_HH); vd = c.vksrc[0] & 0x300u; asm volatile("" : "+v"(vh), "+v"(vd)); }
        if constexpr (M == FA(1)) { vd = 72u * vd + (c.vvb[0] & 0xdfu); asm volatile("" : "+v"(vd)); }
        if constexpr (M == FA(2)) { vd += c.ldsb + SD_BASE + 512u * (w >> 1) + 32u * (w & 1u); asm volatile("" : "+v"(vd)); }
        if constexpr (M == FA(3)) { lq = lane_id(); asm volatile("" : "+v"(lq)); }
        if constexpr (M == FA(4)) { vyo = (lq & 48u) << 3; asm volatile("" : "+v"(vyo)); }
        if constexpr (M == FA(5)) { vyo |= (lq & 7u) << 4; asm volatile("" : "+v"(vyo)); }
        if constexpr (M == FA(7)) { vyo |= (lq & 8u) << 27; asm volatile("" : "+v"(vyo)); }
        if constexpr (M == FA(9)) { srdYs = mk_srd((const char*)ypart + fbb * 18432u, 18432u); asm volatile("" : "+s"(srdYs)); }
      };
      phaseAh<1, 0>(c, 13, hookA);
      auto hook = [&](auto M_) __attribute__((always_inline)) {
        constexpr int M = decltype(M_)::value, i = sg_of(M % 100), is = sg_of(M - 3);
        if constexpr (M == FB(0)) asm volatile("ds_read_b128 %0, %1" : "=v"(hwv) : "v"(vh) : "memory");
        if constexpr (M < 100 && i >= 0) asm volatile("s_waitcnt lgkmcnt(%3)" : "+v"(dl[i & 1]), "+v"(dh[i & 1]), "+v"(hwv) : "i"(kyo(SG(i - 1) + 1, M)) : "memory");
        if constexpr (M >= 100 && i >= 0) {
          const bf16x8 d8 = __builtin_shufflevector(dl[i & 1], dh[i & 1], 0, 1, 2, 3, 4, 5, 6, 7);
          asm volatile("mf16 %0, %1, %2, 0" : "=&v"(y[i & 1]) : "v"(d8), "v"(hwv));
        }
        if constexpr (M >= 100 && i >= -1 && i < 8) {
          asm volatile("ds_read_b64_tr_b16 %0, %1 offset:%2" : "=v"(dl[(i + 1) & 1]) : "v"(vd), "i"(1024 * (i + 1)) : "memory");
          asm volatile("ds_read_b64_tr_b16 %0, %1 offset:%2" : "=v"(dh[(i + 1) & 1]) : "v"(vd), "i"(1024 * (i + 1) + 256) : "memory");
        }
        if constexpr (M < 100 && is >= 0) {
          const unsigned vo = vyo; const __amdgpu_buffer_rsrc_t sr = srdYs; const f32x4 yv = y[is & 1]; const unsigned so = 512u * w + 2048u * is;
          asm volatile("buffer_store_dwordx4 %0, %1, %2, %3 offen sc1\n\ts_nop 1" :: "v"(yv), "v"(vo), "s"(sr), "s"(so) : "memory");
        }
      };
      phaseBh<1, 0>(c, hook);
    }
  }
  GAP();
  PS_AT(41);
  if (__builtin_expect(opq(blk) - 144u < 64u, 0)) {
    PS_AT(42);
    const unsigned w = opq(wids), b = opq(blk), job = (b - 144u) * 4u + w;
    constexpr unsigned NTS = 128u * 4608u;
    unsigned vw[2], v0, v1, vs;
    __amdgpu_buffer_rsrc_t srdT;
    bf16x4 wl[2], wh[2];
    auto rd = [&](auto I_) __attribute__((always_inline)) {
      constexpr int i = decltype(I_)::value;
      bf16x4 &l = wl[i & 1], &h = wh[i & 1]; const unsigned v = vw[i & 1];
      asm volatile("ds_read_b64_tr_b16 %0, %1 offset:%2" : "=v"(l) : "v"(v), "i"(1024 * (i >> 1)) : "memory");
      asm volatile("ds_read_b64_tr_b16 %0, %1 offset:%2" : "=v"(h) : "v"(v), "i"(1024 * (i >> 1) + 256) : "memory");
    };
    auto st = [&](auto I_, auto Y_) __attribute__((always_inline)) {
      constexpr int i = decltype(I_)::value, u = 2 * i, r = u % 9;
      constexpr auto at = [](int x) constexpr { return (unsigned)(x / 9) * NTS + 2048u * ((x % 9) >> 2) + 64u * ((x % 9) & 3); };
      asm volatile("s_waitcnt lgkmcnt(%2)" : "+v"(wl[i & 1]), "+v"(wh[i & 1]) : "i"(decltype(Y_)::value) : "memory");
      const u32x4 x = __builtin_bit_cast(u32x4, __builtin_shufflevector(wl[i & 1], wh[i & 1], 0, 1, 2, 3, 4, 5, 6, 7));
      const __amdgpu_buffer_rsrc_t sr = srdT; const unsigned up = v1;
      const unsigned vo = r == 8 ? v0 | ((up ^ 1u) << 30) : r == 7 ? v0 | (up << 30) : v0 + up * ((r & 3) == 3 ? 1856u : 64u);
      asm volatile("buffer_store_dwordx4 %0, %1, %2, %3 offen sc1\n\ts_nop 1" :: "v"(x), "v"(vo), "s"(sr), "s"(at(r == 8 ? u + 1 : u)) : "memory");
      if constexpr (r >= 7) {
        const u32x2 e = {x[0], x[2]}, o2 = {x[1], x[3]}; const unsigned v2 = vs | ((r == 8 ? up : up ^ 1u) << 30);
        asm volatile("buffer_store_dwordx2 %0, %2, %3, %4 offen sc1\n\tbuffer_store_dwordx2 %1, %2, %3, %4 offen offset:128 sc1" :: "v"(e), "v"(o2), "v"(v2), "s"(sr), "s"((unsigned)(u / 9) * NTS) : "memory");
      }
    };
    auto hookB = [&](auto I0_, auto M_) __attribute__((always_inline)) {
      constexpr int M = decltype(M_)::value, i0 = decltype(I0_)::value;
      constexpr auto fr = [](int g) constexpr { return fb_of(g) >= 0 || g == DB(8) || g == DB(9); };
      constexpr auto cnt = [=](int g) constexpr { int n = 0; for (int x = 0; x < g; ++x) n += fr(x); return n; };
      constexpr auto prev = [=](int g) constexpr { int x = g - 1; while (x > 0 && !fr(x)) --x; return x; };
      if constexpr (M < NGB) { if constexpr (fr(M)) {
        if constexpr (cnt(M) % 2 == 0) rd(std::integral_constant<int, i0 + cnt(M) / 2>{});
        else st(std::integral_constant<int, i0 + cnt(M) / 2>{}, std::integral_constant<int, kyo(prev(M), M)>{});
      } }
    };
    {
      auto hook = [&](auto M_) __attribute__((always_inline)) {
        constexpr int M = decltype(M_)::value;
        if constexpr (M == FA(0)) { vw[0] = c.vvb[0] & 0xffu; v0 = c.vvb[0] & 32u; asm volatile("" : "+v"(vw[0]), "+v"(v0)); }
        if constexpr (M == FA(1)) { vw[0] += 16u * v0; v0 = c.vvb[0] & 2048u; asm volatile("" : "+v"(vw[0]), "+v"(v0)); }
        if constexpr (M == FA(2)) { vw[0] += 9u * v0; asm volatile("" : "+v"(vw[0])); }
        if constexpr (M == FA(3)) { vw[0] += c.ldsb + SD_BASE + 36864u * (w >> 1) + 9216u * (w & 1u); asm volatile("" : "+v"(vw[0])); }
        if constexpr (M == FA(4)) { v1 = lane_id(); asm volatile("" : "+v"(v1)); }
        if constexpr (M == FA(5)) { vw[1] = vw[0] ^ 32u; v0 = (v1 & 7u) << 4; asm volatile("" : "+v"(vw[1]), "+v"(v0)); }
        if constexpr (M == FA(7)) { vs = v0 | ((v1 & 32u) << 3) | ((v1 & 16u) >> 1); asm volatile("" : "+v"(vs)); }
        if constexpr (M == FA(9)) { v0 = (((v1 & 51u) << 4) | ((v1 & 4u) << 8)) + 4096u * ((job >> 1) & 1u); asm volatile("" : "+v"(v0)); }
        if constexpr (M == FA(10)) { vs += 8192u + 512u * ((job >> 1) & 1u); v1 = (v1 >> 3) & 1u; asm volatile("" : "+v"(vs), "+v"(v1)); }
        if constexpr (M == 100 + FA(6)) { srdT = mk_srd((const char*)wot + ((job & 1u) * 256u + (((job >> 2) & 51u) | ((job >> 1) & 8u) | ((job >> 3) & 4u))) * 9216u, 3u * NTS + 9216u); asm volatile("" : "+s"(srdT)); }
      };
      phaseAh<0, 3>(c, 10, hook);
    }
    { auto hook = [&](auto M_) __attribute__((always_inline)) { hookB(std::integral_constant<int, 0>{}, M_); }; phaseBh<0, 0>(c, hook); }
    {
      auto hook = [&](auto M_) __attribute__((always_inline)) {
        constexpr int M = decltype(M_)::value;
        if constexpr (M < 64) { if constexpr (fa_of(M) >= 0) {
          constexpr int n = fa_of(M), p = n ? FA(n - 1) : 0, y = 2 * (pcnt(PA0, 'T', M) - pcnt(PA0, 'T', p)) + pcnt(PA0, 'X', M) - pcnt(PA0, 'X', p);
          if constexpr (n % 2 == 0) rd(std::integral_constant<int, 6 + n / 2>{});
          else st(std::integral_constant<int, 6 + n / 2>{}, std::integral_constant<int, y>{});
        } }
      };
      phaseAh<1, 15>(c, 11, hook);
    }
    { auto hook = [&](auto M_) __attribute__((always_inline)) { hookB(std::integral_constant<int, 12>{}, M_); }; phaseBh<1, 0>(c, hook); }
    phaseA<0>(c, 12);
    {
      auto hook = [&](auto M_) __attribute__((always_inline)) { if constexpr (decltype(M_)::value == FB(0)) flag1(H_W + (b - 144u), ((w - 1u) >> 31) * 4u); };
      phaseBh<0, 0>(c, hook);
    }
    phaseA<1>(c, 13); phaseB<1, 0>(c);
  }
  OPS o; OST ot; unsigned xo; unsigned long long tm;
  {
    const unsigned w = opq(wids), b = opq(blk);
    {
      __amdgpu_buffer_rsrc_t sf, sh; unsigned tg, m0, mt, hs, t, u; CW k;
      auto hookA = [&](auto M_) __attribute__((always_inline)) {
        constexpr int M = decltype(M_)::value;
        if constexpr (M == FA(0)) { m0 = (((w - 1u) & (b - 144u)) >> 31) * 4u; asm volatile("" : "+s"(m0)); }
        if constexpr (M == FA(1) || M == FA(2)) { if constexpr (M == FA(2)) { sf = mk_srd(sel + H_Y + b, m0); asm volatile("" : "+s"(sf)); } }
        if constexpr (M == FA(3)) { tg = tag; m0 = c.ldsb + WF_FL; asm volatile("" : "+v"(tg), "+s"(m0)); }
        if constexpr (M == FA(4)) { sh = mk_srd(sel + H_W, 256u); asm volatile("" : "+s"(sh)); }
        SC(FA(5), o.srdL, mk_srd4(sel, 64u))
        SC(0, mt, b & 3u) SC(1, t, b >> 6) SC(2, mt, 4u * mt + t) SC(3, t, (b >> 2) & 1u) SC(4, mt, 2u * mt + t)
        SC(5, u, 4u * mt + w) SC(7, o.sn, u * (16u * XRS)) SC(8, t, (b >> 3) & 7u) SC(9, hs, t * 144u) SC(11, o.sn, o.sn + hs)
        SC(12, hs, 4u * t + w) SC(13, ot.so[0], hs << 19) SC(15, o.sf, hs >> 1) SC(16, o.sf, o.sf << 11)
        SC(17, u, u ^ 4u) SC(19, o.sa, u << 17) SC(20, u, (u >> 2) & 3u) SC(21, u, u << 10) SC(23, o.sa, o.sa | u)
        SC(24, t, ~b & 4u) SC(25, t, t + w) SC(26, u, t << 14) SC(27, o.sa, o.sa | u)
        SC(28, mt, mt >> 1) SC(29, t, 8u * mt + t) SC(31, t, t << 8) SC(32, o.sl, t + 4u * H_O)
        SC(33, hs, b & 6u) SC(34, t, b >> 7) SC(35, hs, hs + t) SC(36, t, b & 1u) SC(38, mt, 2u * hs + t) SC(39, u, (b >> 6) & 1u) SC(40, hs, 2u * mt + u)
        SC(42, t, 4u * hs + w) SC(43, t, t << 10) SC(45, ot.so[0], ot.so[0] + t)
        SC(44, u, 4u * mt + u) SC(46, t, w & 1u) SC(48, t, 2u * u + t) SC(49, t, 16u * t + 4u * H_O) SC(50, o.sf, o.sf + t)
        SC(55, t, b - RED0) SC(56, xo, 4u * t + w) SC(58, xo, 16u * xo + 4u * H_X) SC(59, hs, xo + 4u)
        if constexpr (M == 60) asm volatile("s_cmp_lt_u32 %1, %2\n\ts_cselect_b32 %0, %0, 0" : "+s"(hs) : "s"(t), "i"(RED1 - RED0) : "scc");
      };
      phaseAh<0, 0>(c, 14, hookA);
      auto hook = [&](auto M_) __attribute__((always_inline)) {
        constexpr int M = decltype(M_)::value;
        if constexpr (M == FB(0)) flag_st(sf, tg);
        if constexpr (M == FB(1)) warm_step(k, std::integral_constant<int, 0>{});
        if constexpr (M == FB(2)) warm_step(k, std::integral_constant<int, 1>{});
        if constexpr (M == FB(3)) warm_step(k, std::integral_constant<int, 2>{});
        if constexpr (M == FB(4)) warm_step(k, std::integral_constant<int, 3>{});
        if constexpr (M == DB(9)) warm_step(k, std::integral_constant<int, 4>{});
        if constexpr (M == 39) m0_set(m0);
        if constexpr (M == 40) dma16(sh, c.vksrc[0]);
        if constexpr (M == 41) m0_set(m0 + (SEL_C - WF_FL));
        if constexpr (M == DB(8)) { const unsigned v = c.vksrc[0]; const u32x4 r = o.srdL; asm volatile("buffer_load_dwordx4 %0, %1, 0 offen sc1 lds" :: "v"(v), "s"(r) : "memory"); }
        if constexpr (M == DB(8) + 1) { o.srdL[2] = hs; asm volatile("" : "+s"(o.srdL)); }
      };
      phaseBh<0, 0>(c, hook);
    }
    {
      unsigned va, vt, r0, r1; u32x4 f; u32x2 sv;
      auto hookA = [&](auto M_) __attribute__((always_inline)) {
        constexpr int M = decltype(M_)::value;
        if constexpr (M == FA(0)) { va = c.ldsb + WF_FL + (c.vksrc[0] & 0xf0u); asm volatile("" : "+v"(va)); }
        if constexpr (M == FA(1)) { c.pfv = lane_id(); asm volatile("" : "+v"(c.pfv)); }
        if constexpr (M == FA(2)) { c.pfv *= 16u; c.pfo = op_k0(b, w); asm volatile("" : "+v"(c.pfv), "+s"(c.pfo)); }
        if constexpr (M == FA(3)) { c.pfo *= 4608u; asm volatile("" : "+s"(c.pfo)); }
        if constexpr (M == FA(4)) { c.srdP = mk_srd((const char*)wot + c.pfo, 16 * 4608); asm volatile("" : "+s"(c.srdP)); }
        if constexpr (M == FA(5)) { c.pfst = 1024u; c.pfo = 0u - 1024u; c.pfm0 = c.ldsb + SD_BASE + BP_W * w; asm volatile("" : "+s"(c.pfst), "+s"(c.pfo), "+s"(c.pfm0)); }
        if constexpr (M == FA(9)) { c.pfend = c.pfm0 + BP_W; asm volatile("" : "+s"(c.pfend)); }
        VC(FA(6), vt, (c.pfv >> 4) & 7u) SC(40, r0, o.sa >> 13) SC(42, r0, r0 ^ 64u) SC(43, r0, r0 & ~15u) SC(44, r1, 128u * w) SC(45, r1, r1 + (c.ldsb + SEL_C + 256u))
      };
      phaseAh<1, 1>(c, 15, hookA);
      auto hook = [&](auto M_) __attribute__((always_inline)) {
        constexpr int M = decltype(M_)::value;
        if constexpr (M == FB(0)) asm volatile("ds_read_b128 %0, %1" : "=v"(f) : "v"(va) : "memory");
        if constexpr (M == FB(1)) { asm volatile("s_waitcnt lgkmcnt(%1)" : "+v"(f) : "i"(kyo(FB(0), FB(1))) : "memory"); f[0] = (f[0] ^ f[1]) | (f[2] ^ f[3]) | (f[0] ^ f[2]); asm volatile("" : "+v"(f)); }
        if constexpr (M == FB(2)) {
          if (__builtin_expect(!__all((f[0] | (f[1] ^ tag)) == 0u), 0)) {
#pragma nounroll
            for (unsigned spin = 0; spin < (1u << 20); ++spin) {
              __builtin_amdgcn_s_sleep(8);
              hand_req(c.ldsb + WF_FL, H_W, 256u);
              asm volatile("s_waitcnt vmcnt(0)\n\tds_read_b128 %0, %1\n\ts_waitcnt lgkmcnt(0)" : "=v"(f) : "v"(va) : "memory");
              if (__all(((f[0] ^ tag) | (f[1] ^ tag) | (f[2] ^ tag) | (f[3] ^ tag)) == 0u)) break;
            }
            asm volatile("s_mov_b32 m0, %0" :: "s"(c.pm0[1]) : "m0", "memory");
          }
        }
        VC(135, va, 8u * vt + (c.ldsb + SEL_C))
        if constexpr (M == FB(3)) asm volatile("ds_read_b64 %0, %1" : "=v"(sv) : "v"(va) : "memory");
        if constexpr (M == FB(4)) { asm volatile("s_waitcnt lgkmcnt(0)" : "+v"(sv) :: "memory"); sv[0] &= 0xffffu; va = sv[0] - r0; asm volatile("" : "+v"(sv), "+v"(va)); }
        if constexpr (M == FB(5)) { const unsigned x = va; asm volatile("v_cmp_gt_u32_e64 %0, 16, %1" : "=s"(tm) : "v"(x)); }
        if constexpr (M == 152) { const unsigned x = sv[1], g = tag; asm volatile("v_cmp_ne_u32_e64 %0, %1, %2" : "=s"(o.seen) : "s"(g), "v"(x)); }
        VC(154, vt, vt - sv[0]) VC(158, va, va < 16u ? va : 16u) VC(160, vt, vt * XRS + SEQ * XRS) VC(FB(8), va, 4u * va + r1) SC(64, o.bad, (unsigned)o.seen)
        if constexpr (M == FB(9)) lds_st32(va, vt);
      };
      phaseBh<1, 2>(c, hook);
    }
  }
  GAP();
  if (__builtin_expect(((RED0 - 1u - opq(blk)) & (opq(blk) - RED1)) >> 31, 0)) {
    PS_AT(43);
    const unsigned job = (opq(blk) - RED0) * 4u + opq(wids);
    unsigned va; u32x4 f; u32x2 sv, nv; f32x4 y[4]; float a[4]; __amdgpu_buffer_rsrc_t sr;
    auto req_fl = [&](auto W_) __attribute__((always_inline)) {
      u32x4& f_ = f; u32x2& s_ = sv; const unsigned v0 = c.pfv, v1 = (c.pfv >> 1) & 56u; const __amdgpu_buffer_rsrc_t r0 = mk_srd(sel + H_Y, 4u * MOE_WG), r1 = mk_srd(sel, 64u);
      if constexpr (decltype(W_)::value) asm volatile("buffer_load_dwordx4 %0, %2, %4, 0 offen sc1\n\tbuffer_load_dwordx2 %1, %3, %5, 0 offen sc1\n\ts_waitcnt vmcnt(0)" : "=&v"(f_), "=&v"(s_) : "v"(v0), "v"(v1), "s"(r0), "s"(r1) : "memory");
      else                               asm volatile("buffer_load_dwordx4 %0, %2, %4, 0 offen sc1\n\tbuffer_load_dwordx2 %1, %3, %5, 0 offen sc1" : "=&v"(f_), "=&v"(s_) : "v"(v0), "v"(v1), "s"(r0), "s"(r1) : "memory");
    };
    auto bad = [&]() __attribute__((always_inline)) { return vsel(c.pfv >= 16u * (MOE_WG / 4), 0u, (f[0] ^ tag) | (f[1] ^ tag) | (f[2] ^ tag) | (f[3] ^ tag)) | (sv[1] ^ tag); };
#define PINA asm volatile("" : "+v"(a[0]), "+v"(a[1]), "+v"(a[2]), "+v"(a[3]))
    auto batch = [&](auto B_, auto M_) __attribute__((always_inline)) {
      constexpr int B = decltype(B_)::value, M = decltype(M_)::value, g = M - 100, i = g / 9;
      if constexpr (B > 0 && g >= 2 && g < 36 && (g % 9 == 2 || g % 9 == 5) && 4 * (B - 1) + i < 18) {
        constexpr int e = g % 9 == 2 ? 0 : 2; const f32x4 v = y[i];
        if constexpr (B == 1 && i == 0) { a[e] = v[e]; a[e + 1] = v[e + 1]; PIN2(a[e], a[e + 1]); } else { add_v(a[e], v[e]); add_v(a[e + 1], v[e + 1]); }
      }
      if constexpr (M >= 7 && M < 36 && M % 9 == 7 && 4 * B + M / 9 < 18) {
        f32x4& d = y[M / 9]; const unsigned v = va; const __amdgpu_buffer_rsrc_t r = sr;
        asm volatile("buffer_load_dwordx4 %0, %1, %2, %3 offen sc1" : "=v"(d) : "v"(v), "s"(r), "s"(8u * NSEL * DM * 4u * (4 * B + M / 9)) : "memory");
      }
    };
#define PINY asm volatile("" : "+v"(y[0]), "+v"(y[1]), "+v"(y[2]), "+v"(y[3]))
    phaseA<0, 3>(c, 16); phaseB<0, 2>(c);
    phaseA<1, 2>(c, 17);
    { auto hook = [&](auto M_) __attribute__((always_inline)) { if constexpr (decltype(M_)::value == 40) req_fl(IC(0)); }; phaseBh<1, 2>(c, hook); }
    {
      auto hook = [&](auto M_) __attribute__((always_inline)) {
        constexpr int M = decltype(M_)::value;
        if constexpr (M == FA(0)) { va = (c.pfv >> 7) * (unsigned)(NSEL * DM * 4); asm volatile("" : "+v"(va)); }
        if constexpr (M == FA(2)) { va += c.pfv & 112u; asm volatile("" : "+v"(va)); }
        if constexpr (M == FA(4)) { sr = mk_srd(ypart + 32u * job, MOE_WG * NSEL * DM * 4 - 128u * job); asm volatile("" : "+s"(sr)); }
      };
      phaseAh<0, 3>(c, 18, hook);
    }
    asm volatile("" : "+v"(f), "+v"(sv));
    {
      auto hook = [&](auto M_) __attribute__((always_inline)) {
        constexpr int M = decltype(M_)::value;
        if constexpr (M == 104) {
          if (__builtin_expect(!__all(bad() == 0u), 0)) {
#pragma nounroll
            for (unsigned spin = 0; spin < (1u << 20) && !__all(bad() == 0u); ++spin) { __builtin_amdgcn_s_sleep(8); req_fl(IC(1)); }
          }
        }
        batch(IC(0), M_);
      };
      phaseBh<0, 2>(c, hook);
    }
    phaseA<1, 2>(c, 19);
    PINY; { auto hook = [&](auto M_) __attribute__((always_inline)) { batch(IC(1), M_); }; phaseBh<1, 2>(c, hook); }
    phaseA<0, 3>(c, 20);
    PINY; { auto hook = [&](auto M_) __attribute__((always_inline)) { batch(IC(2), M_); }; phaseBh<0, 2>(c, hook); }
    phaseA<1, 2>(c, 21);
    PINY; { auto hook = [&](auto M_) __attribute__((always_inline)) { batch(IC(3), M_); }; phaseBh<1, 2>(c, hook); }
    phaseA<0, 3>(c, 22);
    PINY;
    {
      auto hook = [&](auto M_) __attribute__((always_inline)) {
        batch(IC(4), M_);
        if constexpr (decltype(M_)::value == 40) {
          u32x2& n_ = nv; const unsigned v = (sv[0] & 0xffffu) * XRS, o2 = 8u * job; const __amdgpu_buffer_rsrc_t r = mk_srd(xn, SEQ * DM * 2);
          asm volatile("buffer_load_dwordx2 %0, %1, %2, %3 offen sc1" : "=v"(n_) : "v"(v), "s"(r), "s"(o2) : "memory");
        }
      };
      phaseBh<0, 2>(c, hook);
    }
    phaseA<1, 2>(c, 23);
    asm volatile("" : "+v"(y[0]), "+v"(y[1]), "+v"(nv));
    {
      auto hook = [&](auto M_) __attribute__((always_inline)) {
        constexpr int M = decltype(M_)::value;
        batch(IC(5), M_);
        if constexpr (M == FB(1)) { _Pragma("unroll") for (int e = 0; e < 4; ++e) a[e] = dpp_add<0x128>(a[e]); PINA; }
        if constexpr (M == FB(2))       { a[0] = red_step<4>(a[0]); a[1] = red_step<4>(a[1]); PINA; }
        if constexpr (M == 100 + FB(3)) { a[2] = red_step<4>(a[2]); a[3] = red_step<4>(a[3]); PINA; }
        if constexpr (M == 100 + FB(4) || M == FB(5)) {
          constexpr int e0 = (M == FB(5)) * 2;
#pragma unroll
          for (int e = e0; e < e0 + 2; ++e) {
            auto pr = __builtin_amdgcn_permlane32_swap(__float_as_uint(a[e]), __float_as_uint(a[e]), false, false);
            a[e] = __uint_as_float(pr[0]) + __uint_as_float(pr[1]);
          }
          PINA;
        }
        if constexpr (M == 153 || M == FB(7) || M == FB(8) || M == FB(9)) {
          constexpr int e = M == 153 ? 0 : fb_of(M) - 6;
          a[e] = ((e & 1) ? bfhi(nv[e >> 1]) : bflo(nv[e >> 1])) + rbf(rbf(a[e]) * bfhi(sv[0])); PINA;
        }
        if constexpr (M == 120) { va = (c.pfv >> 4) * XRS; asm volatile("" : "+v"(va)); }
        if constexpr (M == 72) {
          const u32x2 o2 = {cvtpk(a[0], a[1]), cvtpk(a[2], a[3])};
          const unsigned vo = va; const __amdgpu_buffer_rsrc_t r = mk_srd(xn + SEQ * DM + 4u * job, 7u * XRS + 8u);
          asm volatile("buffer_store_dwordx2 %0, %1, %2, 0 offen sc1" :: "v"(o2), "v"(vo), "s"(r) : "memory");
        }
      };
      phaseBh<1, 2>(c, hook);
    }
  }
  GAP();
  {
    int t = 14 + 8 * (int)(((RED0 - 1u - opq(blk)) & (opq(blk) - RED1)) >> 31);
    unsigned tg = tag;
    asm volatile("" : "+v"(tg));
    auto hook = [&](auto M_) __attribute__((always_inline)) {
      constexpr int M = decltype(M_)::value;
      if constexpr (M == FB(0)) { const unsigned d = tg, v = c.pfv, so = xo; const u32x4 r = o.srdL; asm volatile("buffer_store_dword %0, %1, %2, %3 offen sc1" :: "v"(d), "v"(v), "s"(r), "s"(so) : "memory"); }
      if constexpr (M == FB(0) + 1) { o.srdL[2] = 0u; asm volatile("" : "+s"(o.srdL)); }
    };
#pragma nounroll
    do {
      t += 2;
      phaseA<0, 3>(c, t);     phaseB<0, 2>(c);
      phaseA<1, 2>(c, t + 1); phaseBh<1, 2>(c, hook);
    } while (__builtin_expect(t != T - 4, 1));
  }
  GAP();
  {
    const unsigned w = opq(wids), b = opq(blk);
    __amdgpu_buffer_rsrc_t sh; unsigned ax[2], dl, fx, t;
    auto req = [&](auto X_, auto J_) __attribute__((always_inline)) {
      constexpr int X = decltype(X_)::value, J = decltype(J_)::value;
      const unsigned v = ax[J >> 1], so = o.sn; const __amdgpu_buffer_rsrc_t r = sh;
      if constexpr (J < 2) asm volatile("buffer_load_dwordx4 %0, %1, %2, %3 offen offset:%c4 sc1" : "=v"(o.gv[X][J & 1]) : "v"(v), "s"(r), "s"(so), "i"(64 * J) : "memory");
      else                 asm volatile("buffer_load_dword %0, %1, %2, %3 offen sc1" : "=v"(o.gs[X]) : "v"(v), "s"(r), "s"(so) : "memory");
    };
    auto hookA = [&](auto M_) __attribute__((always_inline)) {
      constexpr int M = decltype(M_)::value;
      SC(0, t, (b >> 3) & 7u) SC(1, t, t * 288u) SC(2, t, t + 4u * H_X)
      if constexpr (M == 3) { o.srdL[2] = t + 288u; asm volatile("" : "+s"(o.srdL)); }
      if constexpr (M == FA(0)) { unsigned& d = fx; const unsigned v = c.pfv, so = t; const u32x4 r = o.srdL; asm volatile("buffer_load_dword %0, %1, %2, %3 offen sc1" : "=v"(d) : "v"(v), "s"(r), "s"(so) : "memory"); }
      if constexpr (M == 8) { o.srdL[2] = o.sl + 64u; asm volatile("" : "+s"(o.srdL)); }
      VC(FA(2), ax[0], op_ax<0>(c.pfv)) VC(FA(4), ax[1], op_ax<1>(c.pfv)) SC(FA(3), sh, mk_srd(xres, SEQ * DM * 2))
      if constexpr (M == FA(5)) req(IC(1), IC(0));
      if constexpr (M == FA(6)) req(IC(1), IC(1));
      if constexpr (M == FA(8)) req(IC(1), IC(2));
      SC(48, t, 128u * w) SC(49, t, t + (c.ldsb + SEL_C + 256u)) VC(FA(9), dl, ((c.pfv >> 2) & 60u) + t)
      if constexpr (M == FA(11)) { unsigned& d = dl; asm volatile("ds_read_b32 %0, %0" : "+v"(d) :: "memory"); }
      SC(FA(10), sh, mk_srd(xn, (SEQ + NSEL) * DM * 2))
      SC(24, ot.so[1], ot.so[0] ^ 1024u) SC(25, ot.so[2], ot.so[0] ^ 2048u) SC(26, ot.so[3], ot.so[0] ^ 3072u) SC(FA(7), ot.srd, mk_srd4(O, OB))
    };
    phaseAh<0, 3>(c, T - 2, hookA);
    asm volatile("" : "+v"(dl), "+v"(fx));
    o.srdB = c.srdP; o.va = c.pfv;
    auto hook = [&](auto M_) __attribute__((always_inline)) {
      constexpr int M = decltype(M_)::value;
      constexpr int f = M < NGB && PB2[M < NGB ? M : 0] == 'F' ? pcnt(PB2, 'F', M) : -1;
      VC(100, ax[0], ax[0] + dl) VC(16, ax[1], ax[1] + dl)
      if constexpr (f >= 0 && f < 3) req(IC(0), IC(f));
      if constexpr (M == 33) { const unsigned x = fx, g = tag; asm volatile("v_cmp_ne_u32_e64 %0, %1, %2" : "=s"(o.seen) : "s"(g), "v"(x)); }
      SC(35, t, (unsigned)o.seen & 0x3ffffu)
      if constexpr (M == 40) { unsigned& x = t; const unsigned long long m = tm; asm volatile("s_cmp_lg_u64 %1, 0\n\ts_cselect_b32 %0, %0, 0" : "+s"(x) : "s"(m) : "scc"); }
      SC(44, o.bad, o.bad | t)
      SC(2, c.pfo, 23u * 1024u) SC(46, c.pfo, 27u * 1024u) SC(48, c.pfm0, 8192u * w) SC(49, c.pfm0, c.pfm0 + (c.ldsb + 0x4000u))
    };
    static_assert(pcnt(PB2, 'E', NGB) == NBL && ppos(PB2, 'W', 0) == DB(4), "");
    phaseB_<0, false, false, 0>(c, hook, make_iseq<NGB>{});
  }
  ot.vo = c.pfv;
  fin_gaps(c, ot, o, make_iseq<NFIN>{});
  asm volatile("s_nop 1" :: "v"(ot.w));
  if (__builtin_expect(!__all((ot.ls[0] + ot.ls[1]) + (ot.ls[2] + ot.ls[3]) <= LIM), 0))
    redo_rows(c, ot, o, xn, wq, keys, 256u * ((opq(blk) >> 3) & 7u) + 64u * opq(wids), 4u * (opq(blk) & 7u) + (opq(blk) >> 6), tag);
  o.srdA = ot.srd; o.tag = tag;
  oproj_phase(o, lds, xn, (unsigned*)sel, ypart + MOE_WG * NSEL * DM, out, opq(wids)  );
}
extern "C" __global__ void k_attn_end() { asm volatile(PS_END ".subsection 1\n.Lk_end:\n.fill 64, 4, 0xBF800000\n.subsection 0" ::: "memory"); }

extern "C" void kernel_launch(void* const* d_in, const int* in_sizes, int n_in,
                              void* d_out, int out_size, void* d_ws, size_t ws_size,
                              hipStream_t stream) {
  const u16* x   = (const u16*)d_in[0];
  const u16* kc  = (const u16*)d_in[1];
  const u16* vc  = (const u16*)d_in[2];
  const u16* wq  = (const u16*)d_in[3];
  const u16* wo  = (const u16*)d_in[4];
  const u16* w_r = (const u16*)d_in[6];
  const u16* wg  = (const u16*)d_in[7];
  const u16* wu  = (const u16*)d_in[8];
  const u16* wd  = (const u16*)d_in[9];
  char* ws = (char*)d_ws;
  u16* xn = (u16*)(ws + WS_XN); unsigned long long* keys = (unsigned long long*)(ws + WS_KEY); int* sel = (int*)(ws + WS_SEL);
  u16* o = (u16*)(ws + WS_O); u16* wot = (u16*)(ws + WS_WOT); float* yp = (float*)(ws + WS_YP);
  (void)hipFuncSetAttribute((const void*)k_attn, hipFuncAttributeMaxDynamicSharedMemorySize, (int)SHM_ATTN);
  hipLaunchKernelGGL(k_attn, dim3(256), dim3(256), SHM_ATTN, stream, xn, wq, kc, vc, o, wo, wg, wu, wd, wot, keys, yp, sel, x, (u16*)d_out, w_r  );
}
```

```cpp
#include <hip/hip_runtime.h>
#include <hip/hip_bf16.h>
#include <utility>
#include <type_traits>

constexpr int SEQ = 2048, DM = 576, HQ = 32, HKV = 16, DH = 128, FF = 4608, NSEL = 8;
constexpr int NT = SEQ / 64;
constexpr float LIM = 1073741824.0f;

typedef float    f32x16 __attribute__((ext_vector_type(16)));
typedef float    f32x2  __attribute__((ext_vector_type(2)));
typedef float    f32x4  __attribute__((ext_vector_type(4)));
typedef __bf16   bf16x8 __attribute__((ext_vector_type(8)));
typedef __bf16   bf16x4 __attribute__((ext_vector_type(4)));
typedef __bf16   bf16x2 __attribute__((ext_vector_type(2)));
typedef unsigned u32x8  __attribute__((ext_vector_type(8)));
typedef unsigned u32x4  __attribute__((ext_vector_type(4)));
typedef unsigned u32x2  __attribute__((ext_vector_type(2)));
typedef unsigned short u16;
template <int... Is> using iseq = std::integer_sequence<int, Is...>;
template <int N> using make_iseq = std::make_integer_sequence<int, N>;

#define DI __device__ __forceinline__
#define GAP() __builtin_amdgcn_sched_barrier(0)
#define IC(x) std::integral_constant<int, (x)>{}
#define SRD_WORD3 0x00020000

DI unsigned rfl(unsigned x) { return __builtin_amdgcn_readfirstlane(x); }
DI unsigned cvtpk(float lo, float hi) { f32x2 v = {lo, hi}; bf16x2 b = __builtin_convertvector(v, bf16x2); return __builtin_bit_cast(unsigned, b); }
DI __amdgpu_buffer_rsrc_t mk_srd(const void* p, const unsigned bytes) {
  asm volatile("" : "+s"(p));
  return __builtin_amdgcn_make_buffer_rsrc((void*)p, (short)0, (int)bytes, SRD_WORD3);
}
DI u32x4 mk_srd4(const void* p, const unsigned bytes) { asm volatile("" : "+s"(p)); const unsigned long long a = (unsigned long long)p; return u32x4{(unsigned)a, (unsigned)(a >> 32) & 0xffffu, bytes, SRD_WORD3}; }
template <int N> DI unsigned sha(const unsigned a, const unsigned b) { unsigned r; asm volatile("s_lshl%c3_add_u32 %0, %1, %2" : "=s"(r) : "s"(a), "s"(b), "i"(N) : "scc"); return r; }
template <int N, unsigned B> DI unsigned shi(const unsigned a) { unsigned r; asm volatile("s_lshl%c3_add_u32 %0, %1, %2" : "=s"(r) : "s"(a), "i"(B), "i"(N) : "scc"); return r; }
DI unsigned vsel(const bool c, unsigned a, const unsigned b) { asm volatile("" : "+v"(a)); return c ? a : b; }
DI float bflo(unsigned w) { return __uint_as_float(w << 16); }
DI float bfhi(unsigned w) { return __uint_as_float(w & 0xffff0000u); }
DI float rbf(float x) { return bflo(cvtpk(x, 0.f)); }
DI u16 f2bf(float x) { return (u16)(cvtpk(x, 0.f) & 0xffffu); }

#define PS_MACROS \
  ".macro psl n\n.Lps_\\n: s_cbranch_execz .Lpn_\\n\n.endm\n.set .Lpsl, 0\n" \
  ".macro ps\n.set .Lpso, (. - k_attn) >> 12\n.if .Lpso != .Lpsl\n.set .Lpsl, .Lpso\n.altmacro\npsl %%.Lpso\n.noaltmacro\n.endif\n.endm\n" \
  ".macro mf16 a, b, c, d\nv_mfma_f32_16x16x32_bf16 \\a, \\b, \\c, \\d\nps\n.endm\n" \
  ".macro mf32 a, b, c, d\nv_mfma_f32_32x32x16_bf16 \\a, \\b, \\c, \\d\nps\n.endm\n" \
  ".macro sm64 a, b, c, d\nv_smfmac_f32_16x16x64_bf16 \\a, \\b, \\c, \\d\nps\n.endm\n"
#define PS_IDS "0,1,2,3,4,5,6,7,8,9,10,11,12,13,14,15,16,17,18,19,20,21,22,23,24,25,26,27,28,29,30,31,32,33,34,35,36,37,38,39,40,41,42,43,44,45,46,47"
constexpr int PS_A = 37, PS_B = 11, PS_N = 12;
#define PS_AT(n) asm volatile("psl " #n)
#define PS_PG(n, i) "%%(((.Lps_" #n " - k_attn) >> 12) + 1 + " #i ")"
#define PS_CH(k, n, i) "pse " #k ", " PS_PG(n, i) "\n"
#define PS_NX(n, i, j) "psn " PS_PG(n, i) ", " PS_PG(41, j) "\n"
#define PS_END ".irp n," PS_IDS "\n.ifndef .Lps_\\n\n.set .Lps_\\n, .Lps_ret\n.endif\n.endr\n" \
  ".macro pse k, n\n.set .Lpe_\\k, .Lps_\\n\n.endm\n.macro psn a, b\n.set .Lpn_\\a, .Lps_\\b\n.endm\n.altmacro\n" \
  "pse 0, 2\npse 1, 3\npse 2, 4\npse 3, 5\n" \
  PS_CH(4, 40, 0) PS_CH(5, 40, 1) PS_CH(6, 40, 2) PS_CH(7, 40, 3) PS_NX(40, 0, 0) PS_NX(40, 1, 1) PS_NX(40, 2, 2) PS_NX(40, 3, 3) \
  "pse 8, 42\npse 9, 41\n" PS_CH(10, 42, 0) PS_CH(11, 42, 1) "psn 42, " PS_PG(41, 0) "\npsn 41, " PS_PG(41, 1) "\n" PS_NX(42, 0, 2) "psn " PS_PG(42, 1) ", " PS_PG(42, 2) "\n" PS_NX(42, 2, 3) \
  "pse 12, 43\n" PS_CH(13, 43, 1) PS_CH(14, 43, 2) PS_CH(15, 43, 3) "psn 43, " PS_PG(43, 0) "\npsn " PS_PG(43, 1) ", 41\n" PS_NX(43, 0, 0) PS_NX(43, 2, 2) PS_NX(43, 3, 3) \
  PS_CH(16, 41, 0) "pse 17, 41\n" PS_CH(18, 41, 2) PS_CH(19, 41, 3) PS_CH(20, 45, 0) PS_CH(21, 45, 1) PS_CH(22, 45, 2) PS_CH(23, 45, 0) \
  ".noaltmacro\n.irp n," PS_IDS "\n.ifndef .Lpn_\\n\n.set .Lpn_\\n, .Lps_ret\n.endif\n.endr\n"
DI void ps_touch(const unsigned n) {
  asm volatile("s_getpc_b64 s[20:21]\n\ts_lshl2_add_u32 vcc_lo, %0, .Lps_tab - .\n\ts_add_u32 s20, s20, vcc_lo\n\ts_addc_u32 s21, s21, -1\n\ts_mov_b64 exec, 0\n\ts_swappc_b64 vcc, s[20:21]\n\ts_mov_b64 exec, -1"
               :: "s"(n) : "vcc", "scc", "s20", "s21", "memory");
}

constexpr unsigned H_G = 32u, H_W = 64u, H_Y = 128u, H_O = 8192u, H_X = 1536u, H_E = 16384u;
constexpr unsigned WS_KEY = 131072u;
constexpr unsigned WS_ST  = WS_KEY + SEQ * 8;
constexpr unsigned WS_XN  = WS_ST + SEQ * 8;
constexpr unsigned WS_WOT = WS_XN + (SEQ + NSEL) * DM * 2;
constexpr int      MOE_WG = 144;
constexpr unsigned WS_YP  = WS_WOT + HQ * DH * DM * 2;
constexpr unsigned WS_EX  = WS_YP + MOE_WG * NSEL * DM * 4;
constexpr unsigned WS_O   = 1u << 24, WS_END = 2u << 24;
static_assert(WS_EX + 256u * 20480u <= WS_O && WS_WOT % 1024u == 0u, "");

DI void ce(unsigned& a, unsigned& b) { const unsigned hi_ = a > b ? a : b, lo_ = a > b ? b : a; a = hi_; b = lo_; }
DI int key_row(unsigned k) { return 0xffff - (int)(k & 0xffffu); }
DI float key_val(unsigned k) { const unsigned mono = k >> 16; const unsigned rb = (mono & 0x8000u) ? (mono & 0x7fffu) : (~mono & 0xffffu); return __uint_as_float(rb << 16); }

struct LNJ { u32x4 a[2], w[2]; unsigned a2, w2; float x0, s[2], q[2], mb, den, rden, t0, t1; unsigned stat, key; bool up; };
template <int E> DI unsigned ln_a(const LNJ& j) { if constexpr (E < 8) return j.a[E >> 2][E & 3]; else return j.a2; }
template <int E> DI unsigned ln_w(const LNJ& j) { if constexpr (E < 8) return j.w[E >> 2][E & 3]; else return j.w2; }
DI float rcp_nr(float d) { const float r = __builtin_amdgcn_rcpf(d); return __builtin_fmaf(__builtin_fmaf(-d, r, 1.f), r, r); }
template <int CTRL> DI float dpp_add(float v) { return v + __int_as_float(__builtin_amdgcn_update_dpp(0, __float_as_int(v), CTRL, 0xf, 0xf, false)); }
template <int K> DI float red_step(float v) {
  if constexpr (K == 0) return dpp_add<0xB1>(v);
  else if constexpr (K == 1) return dpp_add<0x4E>(v);
  else if constexpr (K == 2) return dpp_add<0x141>(v);
  else if constexpr (K == 3) return dpp_add<0x140>(v);
  else { auto pr = __builtin_amdgcn_permlane16_swap(__float_as_uint(v), __float_as_uint(v), false, false); return __uint_as_float(pr[0]) + __uint_as_float(pr[1]); }
}
#define PIN2(a, b) asm volatile("" : "+v"(a), "+v"(b))
constexpr int LN_N0 = 29, LN_XN = LN_N0 + 72, LN_KEY = LN_XN + 7;
template <int I> DI void ln_step(LNJ& j, const unsigned row) {
  if constexpr (I == 0) {
    const float xa = bflo((unsigned)__builtin_amdgcn_readlane((int)j.a[0][0], 0)), xb = bflo((unsigned)__builtin_amdgcn_readlane((int)j.a[0][0], 32));
    j.x0 = j.up ? xb : xa;
    asm volatile("" : "+v"(j.x0));
  } else if constexpr (I <= 18) {
    constexpr int e = I - 1, c = e & 1;
    const float d = (c ? bfhi(ln_a<(e >> 1)>(j)) : bflo(ln_a<(e >> 1)>(j))) - j.x0;
    if constexpr (e < 2) { j.s[c] = d; j.q[c] = d * d; } else { j.s[c] += d; j.q[c] = __builtin_fmaf(d, d, j.q[c]); }
    PIN2(j.s[c], j.q[c]);
  } else if constexpr (I == 19) {
    j.t0 = j.s[0] + j.s[1]; j.t1 = j.q[0] + j.q[1];
    PIN2(j.t0, j.t1);
  } else if constexpr (I <= 24) {
    j.t0 = red_step<I - 20>(j.t0); j.t1 = red_step<I - 20>(j.t1);
    PIN2(j.t0, j.t1);
  } else if constexpr (I == 25) {
    j.t0 *= 1.0f / DM; j.t1 = __builtin_fmaxf(__builtin_fmaf(-j.t0, j.t0, j.t1 * (1.0f / DM)), 0.f);
    PIN2(j.t0, j.t1);
  } else if constexpr (I == 26) {
    j.mb = rbf(j.x0 + j.t0); j.t1 = rbf(j.t1) + 1e-5f;
    PIN2(j.mb, j.t1);
  } else if constexpr (I == 27) {
    j.den = rbf(__builtin_amdgcn_sqrtf(rbf(j.t1)));
    j.stat = (__float_as_uint(j.mb) & 0xffff0000u) | (__float_as_uint(j.den) >> 16);
    PIN2(j.den, j.stat);
  } else if constexpr (I < LN_N0) {
    j.rden = rcp_nr(j.den); j.s[0] = 0.f; j.s[1] = 0.f; asm volatile("" : "+v"(j.rden));
  } else if constexpr (I < LN_XN) {
    constexpr int e = (I - LN_N0) >> 3, st = (I - LN_N0) & 7;
    if constexpr (st == 0)      { j.t0 = bflo(ln_a<e>(j)); j.t1 = bfhi(ln_a<e>(j)); }
    else if constexpr (st == 1) { j.t0 -= j.mb; j.t1 -= j.mb; }
    else if constexpr (st == 2) { const unsigned pk = cvtpk(j.t0, j.t1); j.t0 = bflo(pk); j.t1 = __uint_as_float(pk); }
    else if constexpr (st == 3) { j.t0 *= j.rden; j.t1 = bfhi(__float_as_uint(j.t1)); }
    else if constexpr (st == 4) { unsigned pk = cvtpk(j.t0, j.t1 * j.rden); asm volatile("" : "+v"(pk)); if constexpr (e < 8) j.a[e >> 2][e & 3] = pk; else j.a2 = pk; }
    else if constexpr (st == 5) { j.t0 = bflo(ln_a<e>(j)); j.t1 = bflo(ln_w<e>(j)); }
    else if constexpr (st == 6) { j.s[0] = __builtin_fmaf(j.t0, j.t1, j.s[0]); j.t0 = bfhi(ln_a<e>(j)); asm volatile("" : "+v"(j.s[0])); }
    else                        { j.s[1] = __builtin_fmaf(j.t0, bfhi(ln_w<e>(j)), j.s[1]); asm volatile("" : "+v"(j.s[1])); }
    PIN2(j.t0, j.t1);
  } else if constexpr (I == LN_XN) {
    j.t0 = j.s[0] + j.s[1]; asm volatile("" : "+v"(j.t0));
  } else if constexpr (I < LN_XN + 6) {
    j.t0 = red_step<I - LN_XN - 1>(j.t0); asm volatile("" : "+v"(j.t0));
  } else if constexpr (I == LN_XN + 6) {
    const unsigned rb = f2bf(j.t0);
    const unsigned mono = (rb & 0x8000u) ? (~rb & 0xffffu) : (rb | 0x8000u);
    j.key = (mono << 16) | (0xffffu - row);
    asm volatile("" : "+v"(j.key));
  }
}

struct RTS { unsigned k[8], bad, m, cnd, lq, va, cv, rank; u32x4 g[4]; float rs; unsigned long long pm[2]; };
DI void cnd_s(unsigned& d, const unsigned a, const unsigned long long m) { asm volatile("v_cndmask_b32_e64 %0, %0, %1, %2" : "+v"(d) : "v"(a), "s"(m)); }
DI void cnt_s(unsigned& d, const unsigned long long m) { unsigned long long co; asm volatile("v_addc_co_u32_e64 %0, %1, 0, %0, %2" : "+v"(d), "=s"(co) : "s"(m)); }
constexpr int RT_SORT = 3, RT_POP = RT_SORT + 19, RT_END = RT_POP + 64;
template <int CTRL> DI unsigned dpp_umax(unsigned v) { const unsigned t = (unsigned)__builtin_amdgcn_update_dpp(0, (int)v, CTRL, 0xf, 0xf, false); return t > v ? t : v; }
template <int I> DI void rt_step(RTS& r, const unsigned tag) {
  constexpr int net[19][2] = {{0, 1}, {2, 3}, {4, 5}, {6, 7}, {0, 2}, {1, 3}, {4, 6}, {5, 7}, {1, 2}, {5, 6}, {0, 4}, {3, 7}, {1, 5}, {2, 6}, {1, 4}, {3, 6}, {2, 4}, {3, 5}, {3, 4}};
  if constexpr (I == 0) { r.bad = (r.g[0][1] ^ tag) | (r.g[0][3] ^ tag) | (r.g[1][1] ^ tag); asm volatile("" : "+v"(r.bad)); }
  else if constexpr (I == 1) { r.bad |= (r.g[1][3] ^ tag) | (r.g[2][1] ^ tag) | (r.g[2][3] ^ tag); asm volatile("" : "+v"(r.bad)); }
  else if constexpr (I == 2) {
    r.bad |= (r.g[3][1] ^ tag) | (r.g[3][3] ^ tag); r.cnd = 0u; r.cv = r.lq & 15u;
#pragma unroll
    for (int j = 0; j < 8; ++j) r.k[j] = r.g[j >> 1][2 * (j & 1)];
    asm volatile("" : "+v"(r.bad));
  } else if constexpr (I < RT_POP) { ce(r.k[net[I - RT_SORT][0]], r.k[net[I - RT_SORT][1]]); PIN2(r.k[net[I - RT_SORT][0]], r.k[net[I - RT_SORT][1]]); }
  else if constexpr (I < RT_END) {
    constexpr int n = (I - RT_POP) >> 3, p = (I - RT_POP) & 7;
    if constexpr (p == 0) r.m = dpp_umax<0xB1>(r.k[0]);
    else if constexpr (p == 1) { r.m = dpp_umax<0x4E>(r.m); asm volatile("v_cmp_eq_u32_e64 %0, %1, %2" : "=s"(r.pm[1]) : "i"(n), "v"(r.cv)); }
    else if constexpr (p == 2) r.m = dpp_umax<0x141>(r.m);
    else if constexpr (p == 3) r.m = dpp_umax<0x140>(r.m);
    else if constexpr (p == 4) { asm volatile("v_cmp_eq_u32_e64 %0, %1, %2" : "=s"(r.pm[0]) : "v"(r.m), "v"(r.k[0])); cnd_s(r.cnd, r.m, r.pm[1]); }
    else {
#pragma unroll
      for (int i = p == 5 ? 0 : 2 * p - 9; i < 2 * p - 7 && i < 7 - n; ++i) cnd_s(r.k[i], r.k[i + 1], r.pm[0]);
    }
    asm volatile("" : "+v"(r.m));
  }
}

constexpr unsigned PF_DUM = 0x27C00u;
#define KRING 0x0000u
#define VRING 0x8000u
constexpr unsigned SD_BASE = 0x10000u, MO_XR = 0x22000u, MO_H = 0x25200u, MO_C = 0x26400u;
constexpr unsigned RTB = 254u;
constexpr unsigned RED0 = 208u, RED1 = 244u;
constexpr unsigned MO_HH = 0x26C00u;
constexpr unsigned WF_FL = 0x27000u;
constexpr unsigned MO_RS = 0x26800u, MO_KEY = 0x20000u;
constexpr unsigned BP_W = 23552u;
constexpr size_t SHM_ATTN = 0x28000;
constexpr unsigned KRS = 256u, KTILEB = 64u * KRS;
constexpr unsigned LAH = 3u;
constexpr unsigned KMASK = SEQ * KRS - 1u;
constexpr unsigned WRS = HQ * DH * 2u;
constexpr unsigned XRS = DM * 2u;

#define W4_ACLOB_HI "a128","a129","a130","a131","a132","a133","a134","a135","a136","a137","a138","a139","a140","a141","a142","a143","a144","a145","a146","a147","a148","a149","a150","a151","a152","a153","a154","a155","a156","a157","a158","a159","a160","a161","a162","a163","a164","a165","a166","a167","a168","a169","a170","a171","a172","a173","a174","a175","a176","a177","a178","a179","a180","a181","a182","a183","a184","a185","a186","a187","a188","a189","a190","a191","a192","a193","a194","a195","a196","a197","a198","a199","a200","a201","a202","a203","a204","a205","a206","a207","a208","a209","a210","a211","a212","a213","a214","a215","a216","a217","a218","a219","a220","a221","a222","a223","a224","a225","a226","a227","a228","a229","a230","a231","a232","a233","a234","a235","a236","a237","a238","a239","a240","a241","a242","a243","a244","a245","a246","a247","a248","a249","a250","a251","a252","a253","a254","a255"
#define W4_ACLOB "a0","a1","a2","a3","a4","a5","a6","a7","a8","a9","a10","a11","a12","a13","a14","a15","a16","a17","a18","a19","a20","a21","a22","a23","a24","a25","a26","a27","a28","a29","a30","a31","a32","a33","a34","a35","a36","a37","a38","a39","a40","a41","a42","a43","a44","a45","a46","a47","a48","a49","a50","a51","a52","a53","a54","a55","a56","a57","a58","a59","a60","a61","a62","a63","a64","a65","a66","a67","a68","a69","a70","a71","a72","a73","a74","a75","a76","a77","a78","a79","a80","a81","a82","a83","a84","a85","a86","a87","a88","a89","a90","a91","a92","a93","a94","a95","a96","a97","a98","a99","a100","a101","a102","a103","a104","a105","a106","a107","a108","a109","a110","a111","a112","a113","a114","a115","a116","a117","a118","a119","a120","a121","a122","a123","a124","a125","a126","a127"

DI float max2f(float a, float b) { float r; asm("v_max_f32_e32 %0, %1, %2" : "=v"(r) : "v"(a), "v"(b)); return r; }
DI void lds_st32(unsigned addr, unsigned v) { asm volatile("ds_write_b32 %0, %1" :: "v"(addr), "v"(v) : "memory"); }

struct W4C {
  unsigned ksx[2];
  unsigned vkb[4], vvb[2], vksrc[2], vvsrc, vvsrc0, m0k, m0v, sofk;
  static constexpr unsigned ldsb = 0u;
  u32x4 srdS, srdL;
  __amdgpu_buffer_rsrc_t srdK, srdV, srdF; unsigned sfo;
  f32x4 S[2][4][4];
  f32x4 NEGM[4], L[4];
  u32x4 ONE; unsigned IDX;
  bf16x8 Vf[8][2];
  u32x4 Pk[4][2];
  bf16x4 vlo[8][2], vup[8][2];
  unsigned pm0[2], psf[2];
  u32x4 srdP; unsigned pfo, pfm0, pfv, pfst, pfend, pdum;
};

constexpr unsigned FCT = 0x27400u, SEL_C = 0x27800u;
constexpr int AO_(int db, int qb) { return 32 * qb + 4 * db; }
constexpr int AQ_(int qb, int j) { return 128 + 16 * qb + 4 * j; }
constexpr int AK_(int kb, int j) { return 192 + 16 * kb + 4 * j; }

template <int I> DI float acc_read() { float r; asm volatile("v_accvgpr_read_b32 %0, a[%c1]" : "=v"(r) : "i"(I)); return r; }
template <int I> DI void acc_write(const unsigned v) { asm volatile("v_accvgpr_write_b32 a[%c1], %0" :: "v"(v), "i"(I)); }

template <int I, int VSLOT> DI void tr_rd(const W4C& c, bf16x4 (&lo)[8][2], bf16x4 (&up)[8][2]) {
  constexpr int db = I >> 2, k2 = (I >> 1) & 1, off = VSLOT + 8192 * k2 + 4096 * (I & 1) + 512 * (db >> 1);
  if constexpr ((I & 1) == 0) asm volatile("ds_read_b64_tr_b16 %0, %1 offset:%2" : "=v"(lo[db][k2]) : "v"(c.vvb[db & 1]), "i"(off) : "memory");
  else                        asm volatile("ds_read_b64_tr_b16 %0, %1 offset:%2" : "=v"(up[db][k2]) : "v"(c.vvb[db & 1]), "i"(off) : "memory");
}
template <int I, int VSLOT> DI void tr_read_(W4C& c) { tr_rd<I, VSLOT>(c, c.vlo, c.vup); }
template <int I, int SLOT> DI void k_read(W4C& c) {
  asm volatile("ds_read_b128 a[%c1:%c2], %0 offset:%c3" :: "v"(c.vkb[I & 3]), "i"(192 + 4 * I), "i"(195 + 4 * I), "i"(SLOT + 4096 * (I >> 2)) : "memory");
}
template <int KB, int QB, int J, int CM> DI void qk16(f32x4& s, const f32x4& negm) {
  constexpr int ak = AK_(KB, J), aq = AQ_(QB, J);
  if constexpr (CM == 2)      asm volatile("mf16 %0, a[%c1:%c2], a[%c3:%c4], 0" : "=&v"(s) : "i"(ak), "i"(ak + 3), "i"(aq), "i"(aq + 3));
  else if constexpr (CM == 1) asm volatile("mf16 %0, a[%c2:%c3], a[%c4:%c5], %1" : "=&v"(s) : "v"(negm), "i"(ak), "i"(ak + 3), "i"(aq), "i"(aq + 3));
  else                        asm volatile("mf16 %0, a[%c1:%c2], a[%c3:%c4], %0" : "+v"(s) : "i"(ak), "i"(ak + 3), "i"(aq), "i"(aq + 3));
}
template <int X, int M> DI void qk_mfma(W4C& c) { constexpr int kb = M >> 4, qb = (M >> 2) & 3, j = (qb & 1) ? 3 - (M & 3) : (M & 3); qk16<kb, qb, j, ((M & 3) == 0)>(c.S[X][kb][qb], c.NEGM[qb]); }
template <int AO, bool ZERO> DI void pv16(const bf16x8& A, const u32x4& B) {
  if constexpr (ZERO) asm volatile("mf16 a[%c2:%c3], %0, %1, 0" :: "v"(A), "v"(B), "i"(AO), "i"(AO + 3));
  else                asm volatile("mf16 a[%c2:%c3], %0, %1, a[%c2:%c3]" :: "v"(A), "v"(B), "i"(AO), "i"(AO + 3));
}
template <bool ZERO> DI void sum16(f32x4& l, const u32x4& one, const u32x4& B) {
  if constexpr (ZERO) asm volatile("mf16 %0, %1, %2, 0" : "=&v"(l) : "v"(one), "v"(B));
  else                asm volatile("mf16 %0, %1, %2, %0" : "+v"(l) : "v"(one), "v"(B));
}
DI void sum64(W4C& c, const int qb, const u32x4 (&P)[2]) {
  const u32x8 B = __builtin_shufflevector(P[0], P[1], 0, 1, 2, 3, 4, 5, 6, 7);
  asm volatile("sm64 %0, %1, %2, %3" : "+v"(c.L[qb]) : "v"(c.ONE), "v"(B), "v"(c.IDX));
}
constexpr int NGB = 68;
template <int G, bool ZERO> DI void pv_mfma(W4C& c) {
  constexpr int qb = G / 17, r = G % 17, db = (r >> 1) & 7, k2 = (r ^ db) & 1;
  if constexpr (r < 16) pv16<AO_(db, qb), (ZERO && (r & 1) == 0)>(c.Vf[db][k2], c.Pk[qb][k2]);
  else sum64(c, qb, c.Pk[qb]);
}
DI void real_op() { GAP(); __builtin_amdgcn_s_setprio(0); GAP(); }
DI void add_v(float& acc, float x) { asm volatile("v_add_f32_e32 %0, %0, %1" : "+v"(acc) : "v"(x)); }
DI void mul_v(float& acc, float x) { asm volatile("v_mul_f32_e32 %0, %0, %1" : "+v"(acc) : "v"(x)); }
template <int X, int E> DI void exp_e(W4C& c) {
  GAP();
  const float e = __builtin_amdgcn_exp2f(c.S[X][E >> 4][(E >> 2) & 3][E & 3]);
  asm volatile("" :: "v"(e));
  c.S[X][E >> 4][(E >> 2) & 3][E & 3] = e;
  GAP();
}
template <int X, int U> DI void pack_to(W4C& c, u32x4 (&P)[4][2]) {
  constexpr int kb = U >> 3, qb = (U >> 1) & 3, h = U & 1;
  GAP();
  const unsigned w = cvtpk(c.S[X][kb][qb][2 * h], c.S[X][kb][qb][2 * h + 1]);
  asm volatile("" :: "v"(w));
  P[qb][kb >> 1][2 * (kb & 1) + h] = w;
  GAP();
}
template <int X, int U> DI void pack_e(W4C& c) { pack_to<X, U>(c, c.Pk); }

constexpr int NBE = 40;
constexpr char PA0[] = "EXETEXFTEXETEXFT" "EXETEXFTEXETEXFT" "EXXEXFEXXEXEXEXF" "ECCECFECCECECECF";
constexpr char PB2[] = "EDEKEEDEKEEDEKFES" "DEKEEWEKFEWEKEEWS" "EKEWEKEEFEEEEEEES" "EEEEEEEEEEEEEEEES";
constexpr char PB0[] = "EDEKEEDEKEEDEKFES" "DEKEEDEKFEDEKEEDS" "EKEDEKEEFDEEEFDES" "EEEEEFEEEEEFEEEES";
constexpr int pcnt(const char* p, char ch, int g) { int n = 0; for (int i = 0; i < g; ++i) n += p[i] == ch; return n; }
constexpr int ppos(const char* p, char ch, int n) { int i = 0; while (p[i] != ch || n-- > 0) ++i; return i; }
constexpr int FA(int n) { constexpr int g[12] = {6, 10, 14, 18, 22, 30, 37, 41, 47, 53, 57, 63}; return g[n]; }
constexpr int FB(int n) { constexpr int g[10] = {14, 25, 33, 42, 47, 50, 56, 59, 62, 67}; return g[n]; }
constexpr int fa_of(int m) { for (int n = 0; n < 12; ++n) if (FA(n) == m) return n; return -1; }
constexpr int fb_of(int m) { for (int n = 0; n < 10; ++n) if (FB(n) == m) return n; return -1; }
constexpr int DB(int n) { return ppos(PB0, 'D', n); }
constexpr int kyo(int g0, int g1) { const int n = 2 * (pcnt(PB0, 'K', g1) - pcnt(PB0, 'K', g0)); return n < 15 ? n : 15; }
static_assert(pcnt(PA0, 'E', 64) == 64 - NBE && pcnt(PB0, 'E', NGB) == NBE && pcnt(PB0, 'D', NGB) == 10 && pcnt(PB0, 'K', NGB) == 8 && FB(3) == DB(8) - 1 && FB(4) == DB(9) - 1 && PA0[FA(6)] == 'F' && PA0[FA(8)] == 'F', "");
template <int X, int M, typename H> DI void gapA(W4C& c, const int t, H& hook) {
  constexpr int Xp = X ^ 1;
  constexpr const char* pa = PA0;
  constexpr char ty = pa[M];
  constexpr int nt = 2 * pcnt(pa, 'T', M) + pcnt(pa, 'X', M), np = pcnt(pa, 'X', M) + 2 * pcnt(pa, 'C', M);
  if constexpr (M == 0) asm volatile("" ::: W4_ACLOB, W4_ACLOB_HI);
  qk_mfma<X, M>(c);
  GAP();
  hook(std::integral_constant<int, M>{});
  if constexpr (ty == 'T' || ty == 'X') tr_read_<nt, Xp * 0x4000>(c);
  if constexpr (ty == 'T') tr_read_<nt + 1, Xp * 0x4000>(c);
  if constexpr (ty == 'X' || ty == 'C') pack_e<Xp, np>(c);
  if constexpr (ty == 'C') pack_e<Xp, np + 1>(c);
  if constexpr (ty == 'E') exp_e<Xp, NBE + pcnt(pa, 'E', M)>(c);
  {
    if constexpr (M == FA(6))      { c.pm0[0] = c.m0k + (unsigned)X * 0x4000u; c.pm0[1] = c.m0v + (unsigned)(X ^ 1) * 0x4000u; asm volatile("" : "+s"(c.pm0[0]), "+s"(c.pm0[1])); }
    else if constexpr (M == FA(8)) asm volatile("s_mov_b32 %1, %0\n\ts_add_u32 %0, %0, %2\n\ts_and_b32 %0, %0, %3" : "+s"(c.psf[0]), "=&s"(c.psf[1]) : "i"(KTILEB), "i"(KMASK) : "scc");
  }
  hook(std::integral_constant<int, 100 + M>{});
  GAP();
}
#define W4_V8(a, k) "+v"(a[0][k]), "+v"(a[1][k]), "+v"(a[2][k]), "+v"(a[3][k]), "+v"(a[4][k]), "+v"(a[5][k]), "+v"(a[6][k]), "+v"(a[7][k])
template <int PFW> DI void phaseA_end(W4C& c) {
  asm volatile("s_waitcnt vmcnt(%16) lgkmcnt(0)" : W4_V8(c.vlo, 0), W4_V8(c.vlo, 1) : "i"(PFW) : "memory");
  asm volatile("" : W4_V8(c.vup, 0), W4_V8(c.vup, 1) : "v"(c.NEGM[0]), "v"(c.NEGM[1]), "v"(c.NEGM[2]), "v"(c.NEGM[3]) : "memory");
  GAP();
  __builtin_amdgcn_s_barrier();
  asm volatile("" ::: "memory");
  GAP();
#pragma unroll
  for (int k2 = 0; k2 < 2; ++k2)
#pragma unroll
    for (int db = 0; db < 8; ++db) c.Vf[db][k2] = __builtin_shufflevector(c.vlo[db][k2], c.vup[db][k2], 0, 1, 2, 3, 4, 5, 6, 7);
}
template <int X, int PFW, typename H, int... Ms> DI void phaseA_(W4C& c, const int t, H& hook, iseq<Ms...>) {
  (gapA<X, Ms>(c, t, hook), ...);
  phaseA_end<PFW>(c);
}
struct NoHook { template <typename T> DI void operator()(T) const {} };
template <int X, int PFW = 0> DI void phaseA(W4C& c, const int t) { NoHook h; phaseA_<X, PFW>(c, t, h, make_iseq<64>{}); }
template <int X, int PFW, typename H> DI void phaseAh(W4C& c, const int t, H& hook) { phaseA_<X, PFW>(c, t, hook, make_iseq<64>{}); }

template <int X, bool ZERO1, bool DK, int PF, int M, typename H> DI void gapB(W4C& c, H& hook) {
  constexpr const char* pb = DK ? PB0 : PB2;
  constexpr char ty = pb[M];
  constexpr int n = pcnt(pb, ty, M) + (!DK && ty == 'D' ? 4 : 0), d8 = DB(8), d9 = DB(9);
  pv_mfma<M, ZERO1>(c);
  GAP();
  if constexpr (M == DB(0) - 1) asm volatile("s_mov_b32 m0, %0" :: "s"(c.pm0[!DK]) : "m0", "memory");
  if constexpr (M == DB(4) - 1) asm volatile("s_mov_b32 m0, %0" :: "s"(c.pm0[DK]) : "m0", "memory");
  if constexpr (M == 57) asm volatile("s_waitcnt lgkmcnt(0)" ::: "memory");
  if constexpr (PF && (M == d8 - 1 || M == d9 - 1))
    asm volatile("s_cmp_lt_u32 %0, %1\n\ts_cselect_b32 m0, %0, %2\n\ts_add_u32 %0, %0, 0x400" : "+s"(c.pfm0) : "s"(c.pfend), "s"(c.pdum) : "m0", "memory", "scc");
  if constexpr (PF && (M == d8 - 2 || M == d9 - 2)) asm volatile("s_add_u32 %0, %0, %1" : "+s"(c.pfo) : "s"(c.pfst) : "scc");
  constexpr int lg = PF ? 56 : 40;
  if constexpr (X == 1 && M == lg - 5) asm volatile("s_add_u32 %0, %0, %1" : "+s"(c.sfo) : "i"(2 * KTILEB) : "scc");
  if constexpr (X == 1 && M == lg - 4) asm volatile("s_and_b32 %0, %0, %1" : "+s"(c.sfo) : "i"(KMASK) : "scc");
  if constexpr (X == 1 && M == lg - 2) asm volatile("s_mov_b32 m0, %0" :: "s"(c.pdum) : "m0", "memory");
  if constexpr (X == 1 && M == lg) asm volatile("buffer_load_dwordx4 %0, %1, %2 offen lds" :: "v"(c.vksrc[0]), "s"(c.srdF), "s"(c.sfo) : "memory");
  hook(std::integral_constant<int, M>{});
  if constexpr (ty == 'K') { k_read<2 * n, (X ^ 1) * 0x4000>(c); k_read<2 * n + 1, (X ^ 1) * 0x4000>(c); }
  if constexpr (ty == 'D') {
    if constexpr (n < 4) { asm volatile("buffer_load_dwordx4 %0, %1, %2 offen offset:%c3 lds" :: "v"((n & 2) ? c.ksx[n & 1] : c.vksrc[n & 1]), "s"(c.srdK),  "s"(c.psf[0]), "i"(1024 * (n & 3)) : "memory"); }
    else if constexpr (n < 8) asm volatile("buffer_load_dwordx4 %0, %1, %2 offen offset:%c3 lds" :: "v"((n & 1) ? c.vvsrc : c.vvsrc0), "s"(c.srdV),  "s"(c.psf[1]), "i"(1024 * (n & 3)) : "memory");
    else if constexpr (PF) asm volatile("buffer_load_dwordx4 %0, %1, %2 offen sc1 lds" :: "v"(c.pfv), "s"(c.srdP), "s"(c.pfo) : "memory");
  }
  if constexpr (ty == 'W') asm volatile("buffer_load_dwordx4 %0, %1, %2 offen offset:%c3 sc1 lds" :: "v"(c.pfv), "s"(c.srdP), "s"(c.pfo), "i"(1024 * n) : "memory");
  if constexpr (ty == 'E') exp_e<X, DK ? n : 16 * ((n >> 2) & 3) + 4 * (n >> 4) + (n & 3)>(c);
  hook(std::integral_constant<int, 100 + M>{});
  GAP();
}
template <int X, bool ZERO1, bool DK, int PF, typename H, int... Ms> DI void phaseB_(W4C& c, H& hook, iseq<Ms...>) {
  GAP();
  (gapB<X, ZERO1, DK, PF, Ms>(c, hook), ...);
  hook(std::integral_constant<int, 72>{});
  GAP();
}
template <int X, int PF> DI void phaseB(W4C& c) { NoHook h; phaseB_<X, false, true, PF>(c, h, make_iseq<NGB>{}); }
template <int X, int PF, typename H> DI void phaseBh(W4C& c, H& hook) { phaseB_<X, false, true, PF>(c, hook, make_iseq<NGB>{}); }

constexpr unsigned QP_STG = 0xC000u, QP_W8 = 0x24000u, QP_CS = 0x18000u;
constexpr int PR0 = 1, PR1 = 8;
constexpr int PJ = 224;
constexpr int NJ = 200, JK2 = 79, JR0 = 103;
constexpr int JT[] = {1000,20004,20005,32000,1001,20006,20007,32000,31000,1002,22000,32000,1003,21000,21001,32000,1016,34000,21002,21003,32000,1017,34000,21004,21005,32000,1018,34000,21006,21007,30000,32000,16000,1019,
  34000,32000,16001,30001,1004,34000,20008,20009,32000,16002,1005,34000,20010,20011,32000,16003,1006,34000,20012,20013,32000,16004,1007,34000,20014,20015,32000,16005,1008,34000,32000,16006,1020,34000,32000,16007,1021,
  34000,32000,10000,10001,10002,10003,10004,10005,1022,34000,32000,10006,10007,10008,10009,10010,10011,1023,34000,32000,10012,10013,10014,10015,10016,10017,1024,34000,32000,10018,10019,10020,10021,10022,10023,1032,34000,
  32000,10024,10025,10026,1033,34000,32000,10027,10028,10029,10030,10031,1034,34000,32000,10032,10033,10034,10035,10036,10037,1035,34000,32000,10038,10039,10040,10041,10042,10043,30002,1036,34000,32000,10044,10045,10046,
  10047,10048,10049,1037,34000,32000,10050,10051,10052,10053,10054,10055,1038,34000,32000,10056,10057,10058,10059,10060,1039,34000,32000,10061,10062,10063,1040,34000,32000,10064,10065,10066,10067,10068,10069,1048,34000,
  20016,32000,10070,10071,1049,34000,20017,32000,10072,10073,10074,10075,10076,10077,1050,34000,20018,32000,10078,10079,10080,10081,10082,10083,1051,34000,20019,32000,10084,10085,10086,10087,10088,10089,1052,34000,20020,
  32000,10090,10091,10092,10093,10094,10095,1053,34000,20021,32000,10096,10097,10098,1054,34000,20022,32000,10099,10100,10101,10102,10103,1055,34000,20023,32000,10104,10105,10106,10107,1056,34000,32000,1064,34000,32000,
  1065,34000,32000,10108,10109,10110,10111,10112,10113,1066,34000,32000,10114,10115,10116,10117,10118,10119,1067,34000,32000,10120,10121,10122,10123,10124,10125,30003,1068,34000,32000,10126,10127,10128,10129,10130,10131,
  1069,34000,32000,10132,10133,10134,1070,34000,32000,10135,10136,10137,10138,10139,1071,34000,32000,10140,10141,10142,10143,1072,34000,20024,32000,1080,34000,20025,32000,1081,34000,20026,32000,10144,10145,10146,10147,
  10148,10149,1082,34000,20027,32000,10150,10151,10152,10153,10154,10155,1083,34000,20028,32000,10156,10157,10158,10159,10160,10161,1084,34000,20029,32000,10162,10163,10164,10165,10166,10167,1085,34000,20030,32000,10168,
  10169,10170,1086,34000,20031,32000,10171,10172,10173,10174,10175,1087,34000,32000,10176,10177,10178,10179,1088,34000,32000,1096,34000,32000,1097,34000,32000,10180,10181,10182,10183,10184,10185,31001,1098,34000,32000,
  10186,10187,10188,10189,10190,10191,1099,34000,32000,10192,10193,10194,10195,30004,1100,34000,23000,23001,32000,10196,10197,10198,10199,10200,1101,34000,23002,23003,32000,10201,10202,10203,10204,1102,34000,23004,23005,
  24000,32000,1103,34000,23006,23007,32000,10205,10206,1104,34000,23008,32000,10207,10208,10209,10210,10211,1112,34000,23009,32000,10212,10213,10214,10215,1113,34000,23010,32000,10216,10217,10218,10219,10220,10221,1114,
  34000,23011,32000,10222,10223,10224,10225,10226,10227,1115,34000,23012,32000,10228,10229,10230,10231,1116,34000,23013,32000,10232,10233,10234,10235,10236,10237,1117,34000,23014,32000,10238,10239,10240,10241,1118,34000,
  23015,32000,10242,10243,1119,34000,32000,10244,10245,10246,10247,10248,10249,1120,34000,32000,10250,10251,30005,2128,34000,24001,24002,32000,2132,34000,32000,10252,10253,2136,34000,32000,10254,10255,2140,34000,23100,
  32000,10256,2001,34000,32000,10257,10258,2005,34000,32000,10259,10260,31002,2009,34000,32000,10261,10262,2013,32000,2002,34000,23101,32000,10263,2003,33000,32000,2006,34000,32000,10264,10265,2007,33001,32000,2010,
  34000,32000,11000,11001,2014,34000,32000,11002,11016,11017,11003,11004,11005,11018,23102,11006,11007,11008,11009,11010,11011,11012,2080,34000,32000,11013,11014,2017,34000,32000,11019,2018,34000,32000,11020,11021,2019,
  34000,32000,11022,2096,34000,32000,11023,11024,2033,34000,32000,2034,34000,32000,11025,11026,2035,34000,32000,11027,11028,2112,34000,32000,11029,11030,2049,34000,32000,10266,10267,2050,34000,32000,10268,10269,2051,
  34000,32000,2087,34000,32000,10270,10271,2022,34000,32000,10272,10273,2021,34000,32000,10274,10275,2020,34000,32000,10276,2103,34000,32000,10277,2038,32000,2037,34000,32000,10278,2036,34000,32000,10279,2119,34000,
  32000,10280,10281,2054,34000,32000,10282,10283,2053,34000,32000,10284,10285,2052,32000,10286,10287,24003,24004,2011,32000,13000,2015,34000,23103,24005,32000,13001,13002,11032,11033,11034,13003,11048,11035,11036,11037,
  11049,11050,11038,11039,11040,11041,11042,11043,11044,2088,34000,32000,11045,11046,2025,34000,32000,11051,2026,34000,32000,11052,11053,2027,34000,32000,11054,2104,34000,32000,11055,11056,2041,34000,32000,11057,11058,
  2042,34000,32000,11059,11060,2043,34000,32000,11061,11062,2120,32000,13016,2057,34000,24006,32000,15000,2058,32000,13017,2059,32000,13018,2095,34000,24007,32000,15001,2030,32000,13019,2029,32000,13032,2028,34000,23104,
  32000,15008,2111,34000,23105,32000,15009,2046,34000,23106,23107,32000,2045,34000,24008,24009,32000,2044,34000,23108,23109,32000,2127,34000,23110,23111,32000,2062,34000,24010,24011,32000,2061,32000,13033,2060,34000,
  23112,32000,15016,30105,3064,34000,23113,23114,32000,3001,34000,23115,24012,32000,3002,32000,13034,3003,34000,24013,24014,32000,3071,32000,13035,3006,32000,13048,3005,34000,24015,32000,15017,3004,32000,13049,3072,
  32000,13050,3009,34000,24016,32000,15024,3010,32000,13051,3011,32000,13004,3079,34000,24017,32000,15025,3014,32000,13005,3013,32000,13006,3012,34000,24018,32000,15002,3080,32000,13007,3017,32000,13020,3018,34000,24019,
  32000,15003,3019,32000,13021,3087,32000,13022,3022,34000,24020,32000,15010,3021,32000,13023,3020,32000,13036,3088,34000,24021,32000,15011,3025,32000,13037,3026,32000,13038,3027,34000,24022,32000,15018,3095,32000,13039,
  3030,32000,13052,3029,34000,24023,32000,15019,3028,32000,13053,3096,32000,13054,3033,34000,24024,32000,15026,30102,3034,32000,13055,3035,32000,13008,3103,34000,24025,32000,15027,3038,32000,13009,3037,32000,13010,3036,
  34000,24026,32000,15004,3104,32000,13011,3041,32000,13024,3042,34000,24027,32000,15005,3043,32000,13025,3111,32000,13026,3046,34000,24028,32000,15012,3045,32000,13027,3044,32000,13040,3112,34000,24029,32000,15013,3049,
  32000,13041,3050,32000,13042,3051,34000,24030,32000,15020,3119,32000,13043,3054,34000,24031,32000,15021,3053,32000,13056,3052,32000,13057,3120,32000,13058,3057,32000,13059,3058,32000,13012,3059,32000,13013,3127,34000,
  32000,15028,15029,3062,32000,13014,3061,32000,13015,3060,34000,32000,15006,13028,13029,13030,15007,13031,13044,15014,13045,13046,15015,13047,13060,15022,13061,13062,15023,13063,15030,15031,};
constexpr short JS[] = {0,4,8,12,16,21,26,33,37,44,50,56,62,66,70,79,88,97,106,112,120,129,138,148,157,165,171,180,186,196,206,216,226,233,242,250,253,256,265,274,283,293,299,307,314,318,322,332,342,352,362,369,378,385,
  388,391,400,410,417,428,437,443,450,459,467,477,487,495,505,513,519,528,533,539,544,549,554,559,564,570,572,577,580,585,588,593,611,616,620,625,629,634,637,642,647,652,657,662,665,670,675,680,684,688,690,694,698,703,
  708,713,719,722,746,751,755,760,764,769,774,779,784,787,792,795,798,803,806,809,814,819,824,829,834,839,844,847,852,858,863,866,871,874,877,882,885,888,893,896,899,904,907,910,915,918,921,926,929,932,937,940,943,948,
  951,954,959,962,965,970,973,976,981,985,988,993,996,999,1004,1007,1010,1015,1018,1021,1026,1029,1032,1037,1040,1043,1048,1051,1056,1059,1062,1065,1068,1071,1074,1079,1082,1085,1108,};
enum : int { VK_CW, VK_GEN, VK_S, VK_W8, VK_A, VK_WR, VK_STS, VK_GAT, VK_XNS, VK_KV, VK_KEY, VK_V1, VK_K2, VK_PF };
struct VOp { int kind, st, q, pos; };
struct VTab { VOp v[192]; int n; };
constexpr VTab mk_vtab() {
  VTab t{}; int n = 0;
  auto add = [&](int kind, int st, int q, int pos) { t.v[n].kind = kind; t.v[n].st = st; t.v[n].q = q; t.v[n].pos = pos; ++n; };
  const int PS[12] = {7, 10, 12, 15, 18, 20, 23, 26, 28, 31, 33, 36}, P3[12] = {20, 22, 24, 26, 28, 30, 32, 34, 36, 38, 40, 42}, P4[12] = {12, 14, 16, 19, 21, 23, 26, 28, 30, 32, 34, 36};
  for (int q = 0; q < 12; ++q) add(VK_S, 0, q, q < PS_N ? -3 : -2);
  for (int q = 0; q < 3; ++q) add(VK_A, 0, q, -2);
  add(VK_GEN, 0, 0, -2); add(VK_CW, 0, 0, -2);
  for (int q = 0; q < 12; ++q) add(VK_S, 1, q, q < 8 ? -1 : q - 8);
  for (int q = 0; q < 12; ++q) add(VK_S, 2, q, 4 + q);
  add(VK_STS, 0, 0, 12);
  for (int q = 0; q < 3; ++q) add(VK_WR, 0, q, 16 + q);
  for (int s = 3; s < 9; ++s) for (int q = 0; q < 12; ++q) add(s == 8 && q < 4 ? VK_W8 : VK_S, s, q, 32 * (s - 3) + (s == 3 ? P3[q] : s == 4 ? P4[q] : PS[q]));
  add(VK_GAT, 0, 0, 128 + 9); add(VK_GAT, 0, 1, 128 + 14);
  add(VK_XNS, 0, 0, 160 + 22); add(VK_XNS, 0, 1, 160 + 25); add(VK_XNS, 0, 2, 160 + 30);
  for (int q = 0; q < 12; ++q) add(VK_KV, 0, q, 192 + PS[q]);
  add(VK_KEY, 0, 0, PJ + 57);
  for (int q = 0; q < 4; ++q) add(VK_V1, 0, q, PJ + 60 + 3 * q);
  for (int q = 0; q < 4; ++q) add(VK_K2, 0, q, PJ + JK2 + 6 * q);
  add(VK_PF, 0, 0, PJ + JR0); add(VK_PF, 0, 1, PJ + JR0 + 6);
  for (int i = 1; i < n; ++i) { const VOp x = t.v[i]; int j = i; while (j > 0 && t.v[j - 1].pos > x.pos) { t.v[j] = t.v[j - 1]; --j; } t.v[j] = x; }
  t.n = n;
  return t;
}
constexpr VTab VT = mk_vtab();
constexpr int vq_first(int pos) { int i = 0; while (i < VT.n && VT.v[i].pos < pos) ++i; return i; }
constexpr int vq_wait(int kind, int st, int pos) {
  int last = -1;
  for (int i = 0; i < VT.n; ++i) if (VT.v[i].kind == kind && VT.v[i].st == st) last = i;
  return vq_first(pos) - 1 - last;
}
constexpr int imin(int a, int b) { return a < b ? a : b; }
constexpr int vq_grp(int i) {
  const VOp o = VT.v[i];
  return o.kind == VK_S ? 3 * o.st + (o.q >> 2) : o.kind == VK_W8 ? 40 : o.kind == VK_KV ? 41 + (o.q >> 2) : o.kind == VK_V1 ? 44 : o.kind == VK_K2 ? 45 : o.kind == VK_PF ? 50 + o.q : -1;
}
constexpr bool vq_newm0(int i) { int j = i - 1; while (j >= 0 && vq_grp(j) < 0) --j; return j < 0 || vq_grp(j) != vq_grp(i); }
template <bool NEWM0, int OFF> DI void lds_dma(const unsigned m0, const unsigned vo, const __amdgpu_buffer_rsrc_t srd, const unsigned so) {
  if constexpr (NEWM0) asm volatile("s_mov_b32 m0, %0\n\ts_nop 0\n\tbuffer_load_dwordx4 %1, %2, %3 offen offset:%c4 lds" :: "s"(m0), "v"(vo), "s"(srd), "s"(so), "i"(OFF) : "m0", "memory");
  else                 asm volatile("buffer_load_dwordx4 %0, %1, %2 offen offset:%c3 lds" :: "v"(vo), "s"(srd), "s"(so), "i"(OFF) : "memory");
}
template <int I0, typename F, int... Is> DI void vq_run(F& vm, iseq<Is...>) { (vm(std::integral_constant<int, I0 + Is>{}), ...); }
template <int POS, typename F> DI void vq_issue(F& vm) { vq_run<vq_first(POS)>(vm, make_iseq<vq_first(POS + 1) - vq_first(POS)>{}); }

constexpr int AW_(int T, int db, int ks) { return ((T & 1) ? 192 : 0) + 16 * db + 4 * ks; }
constexpr int AX_(int T, int qb, int ks) { return ((T & 1) ? 96 : 64) + 16 * qb + 4 * ks; }
template <int T, int DB, int QB, int KS> DI void qp_mfma(f32x16& d) {
  constexpr int aw = AW_(T, DB, KS), ax = AX_(T, QB, KS);
  if constexpr (T == 0 && KS == 0) asm volatile("mf32 %0, a[%c1:%c2], a[%c3:%c4], 0" : "=&v"(d) : "i"(aw), "i"(aw + 3), "i"(ax), "i"(ax + 3));
  else                             asm volatile("mf32 %0, a[%c1:%c2], a[%c3:%c4], %0" : "+v"(d) : "i"(aw), "i"(aw + 3), "i"(ax), "i"(ax + 3));
}
struct QP {
  f32x16 A[4][2];
  bf16x4 cl[2][4], cu[2][4];
  f32x4 cs; u32x4 sel;
  unsigned vwb, vwc, vw[4], vx[4], vxb[4], m0w, m0x, sofw, sofx, kk, sw, sx;
  __amdgpu_buffer_rsrc_t srdW, srdX;
};
template <int S> DI void qp_next(QP& p) {
  constexpr int d = S < PR0 || S > PR1 ? 0 : ((S - PR0) & 1) ? -1 : 1;
  const unsigned k = (unsigned)S + (d == 0 ? 0u : d > 0 ? p.kk : 0u - p.kk);
  p.sw = p.sofw + (k << 19); p.sx = p.sofx + (k << 7);
  asm volatile("" : "+s"(p.sw), "+s"(p.sx));
}
constexpr unsigned qp_wbase(int T) { return T == 8 ? QP_W8 : QP_STG * (T % 3); }
template <int T, int DB, int KS, int U> DI void qp_tr(QP& p) {
  asm volatile("ds_read_b64_tr_b16 a[%c1:%c2], %0 offset:%c3" :: "v"(p.vwb + qp_wbase(T)), "i"(AW_(T, DB, KS) + 2 * U), "i"(AW_(T, DB, KS) + 2 * U + 1), "i"(4096 * KS + 2048 * U + 512 * DB) : "memory");
}
template <int T, int QB, int KS> DI void qp_xr(QP& p) {
  asm volatile("ds_read_b128 a[%c1:%c2], %0 offset:%c3" :: "v"(p.vxb[KS] + QP_STG * (T % 3)), "i"(AX_(T, QB, KS)), "i"(AX_(T, QB, KS) + 3), "i"(4096 * QB) : "memory");
}
template <int T, int I> DI void qp_csr(QP& p) {
  constexpr int ks = I >> 1;
  if constexpr ((I & 1) == 0) asm volatile("ds_read_b64_tr_b16 %0, %1 offset:%2" : "=v"(p.cl[T == 8][ks]) : "v"(p.vwc + qp_wbase(T)), "i"(4096 * ks) : "memory");
  else                        asm volatile("ds_read_b64_tr_b16 %0, %1 offset:%2" : "=v"(p.cu[T == 8][ks]) : "v"(p.vwc + qp_wbase(T)), "i"(4096 * ks + 2048) : "memory");
}
template <int KS, bool ZERO, int L = 0> DI void qp_csd(QP& p) {
  const bf16x8 w = __builtin_shufflevector(p.cl[L][KS], p.cu[L][KS], 0, 1, 2, 3, 4, 5, 6, 7);
  if constexpr (ZERO) asm volatile("mf16 %0, %1, %2, 0" : "=&v"(p.cs) : "v"(w), "v"(p.sel));
  else                asm volatile("mf16 %0, %1, %2, %0" : "+v"(p.cs) : "v"(w), "v"(p.sel));
}
template <int T, int R> DI void qp_rd(QP& p) {
  if constexpr (R < 8) qp_csr<T, R>(p);
  else {
    constexpr int ks = (R - 8) / 10, u = (R - 8) % 10;
    if constexpr (u == 0) qp_xr<T, 0, ks>(p);
    else if constexpr (u == 3) qp_xr<T, 1, ks>(p);
    else { constexpr int v = u < 3 ? u - 1 : u - 2; qp_tr<T, (v >> 1), ks, (v & 1)>(p); }
  }
}
template <int T, int R0, int... Rs> DI void qp_rds(QP& p, iseq<Rs...>) { (qp_rd<T, R0 + Rs>(p), ...); }
#define QP_CSV(L) "+v"(p.cl[L][0]), "+v"(p.cl[L][1]), "+v"(p.cl[L][2]), "+v"(p.cl[L][3]), "+v"(p.cu[L][0]), "+v"(p.cu[L][1]), "+v"(p.cu[L][2]), "+v"(p.cu[L][3])
constexpr int LN_P0 = 32 + 18, LN_P1 = 160 + 21;
constexpr int ln_slots(int pos) { int n = 0; for (int q = LN_P0; q < pos; ++q) if (vq_first(q + 1) == vq_first(q) && (q & 31) != 5) ++n; return n; }
constexpr int ln_lo(int pos) {
  if (pos <= 1) return 0;
  if (pos <= 12) return (28 * (pos - 1)) / 11;
  if (pos < 30) return LN_N0 - 1;
  if (pos < LN_P0) return LN_N0;
  if (pos <= LN_P1) return LN_N0 + (ln_slots(pos) * 73) / ln_slots(LN_P1);
  const int s = LN_XN + 1 + (pos - LN_P1) / 3;
  return s < LN_KEY ? s : LN_KEY;
}
template <int I0, int... Is> DI void ln_steps(LNJ& j, const unsigned row, iseq<Is...>) { (ln_step<I0 + Is>(j, row), ...); }
template <int T, int M, typename FV, typename FH> DI void qp_gap(QP& p, FV& vm, FH& hook, LNJ& lj, const unsigned lrow) {
  constexpr int ks = M >> 3, db = (M >> 1) & 3, qb = M & 1, pos = 32 * T + M, BG = T == 0 ? 12 : 6;
  if constexpr (T == 0 && M == 0) asm volatile("s_waitcnt lgkmcnt(10)" ::: "memory", W4_ACLOB, W4_ACLOB_HI);
  if constexpr (T == 0 && M == 8) asm volatile("s_waitcnt lgkmcnt(0)" : QP_CSV(0) :: "memory");
  if constexpr (T > 0 && M == 0) asm volatile("s_waitcnt lgkmcnt(0)" ::: "memory");
  if constexpr (M == BG) {
    constexpr int w0 = vq_wait(VK_S, T + 1, pos);
    constexpr int w = T == 1 ? imin(w0, vq_wait(VK_WR, 0, pos)) : T == 6 ? imin(w0, vq_wait(VK_W8, 8, pos)) : w0;
    asm volatile("s_waitcnt vmcnt(%0)\n\ts_barrier" :: "i"(w) : "memory");
  }
  qp_mfma<T, db, qb, ks>(p.A[db][qb]);
  if constexpr (T == 0) {
    if constexpr (M < 5) qp_rds<0, 28 + 4 * M>(p, make_iseq<4>{});
    if constexpr (M == 5 || M == 6) qp_rds<0, 4 * (M - 5)>(p, make_iseq<4>{});
    if constexpr (M == 18) asm volatile("s_waitcnt lgkmcnt(10)" : QP_CSV(0) :: "memory");
    if constexpr (M >= 12 && M < 28) qp_rds<1, 3 * (M - 12)>(p, make_iseq<3>{});
  } else {
    if constexpr (M == 13) asm volatile("s_waitcnt lgkmcnt(%8)" : QP_CSV(0) : "i"(T == 6 ? 10 : 6) : "memory");
    if constexpr (T == 6 && M == 17) asm volatile("s_waitcnt lgkmcnt(6)" : QP_CSV(1) :: "memory");
    if constexpr (M >= 6 && M < 30) qp_rds<T + 1, 2 * (M - 6)>(p, make_iseq<2>{});
    if constexpr (T == 6 && M >= 6 && M < 14) qp_csr<8, M - 6>(p);
    if constexpr (T == 6 && M >= 30) { qp_tr<8, 0, M - 30, 0>(p); qp_tr<8, 0, M - 30, 1>(p); }
  }
  vq_issue<pos>(vm);
  if constexpr (pos == 3 || pos == 19 || pos == 32 + 11 || (M == 5 && T >= 2 && T <= 5)) qp_next<(pos == 3 ? 2 : pos == 19 ? 3 : T + 3)>(p);
  GAP();
  hook(std::integral_constant<int, pos>{});
  ln_steps<ln_lo(pos)>(lj, lrow, make_iseq<ln_lo(pos + 1) - ln_lo(pos)>{});
  if constexpr (T == 0 && M >= 8 && M < 12) qp_csd<M - 8, M == 8>(p);
  if constexpr (T == 0 && M >= 18 && M < 28 && M % 3 == 0) qp_csd<(M - 18) / 3, false>(p);
  if constexpr (T > 0 && M >= 13 && M < 23 && M % 3 == 1) qp_csd<(M - 13) / 3, false>(p);
  if constexpr (T == 6 && (M == 17 || M == 21 || M == 24 || M == 27)) qp_csd<(M == 17 ? 0 : (M - 18) / 3), false, 1>(p);
  GAP();
}
template <int T, typename FV, typename FH, int... Ms> DI void qp_gaps(QP& p, FV& vm, FH& hook, LNJ& lj, const unsigned lrow, iseq<Ms...>) { (qp_gap<T, Ms>(p, vm, hook, lj, lrow), ...); }
template <int T, typename FV, typename FH> DI void qp_iter(QP& p, FV& vm, FH& hook, LNJ& lj, const unsigned lrow) { qp_gaps<T>(p, vm, hook, lj, lrow, make_iseq<32>{}); }
template <typename FV, typename FH, int... Ts> DI void qp_iters(QP& p, FV& vm, FH& hook, LNJ& lj, const unsigned lrow, iseq<Ts...>) { (qp_iter<Ts>(p, vm, hook, lj, lrow), ...); }

struct QFT { float a[2], cv[4], r1, m[4], m2[4]; unsigned w[8]; u32x4 ca[4], nb[2]; };
template <int N> DI void q_fin(QP& p, QFT& t) {
  constexpr int tau = N / 36, n = N % 36, h = n / 12, u = n % 12, db = tau >> 1, qb = tau & 1;
  f32x16& d = p.A[db][qb];
  if constexpr (n >= 28) acc_write<AQ_(2 * qb + ((n - 28) >> 2), db) + (n & 3)>(t.w[n - 28]);
  else if constexpr (n >= 24) { GAP(); auto pr = __builtin_amdgcn_permlane16_swap(t.w[u], t.w[4 + u], false, false); t.w[u] = pr[0]; t.w[4 + u] = pr[1]; asm volatile("" :: "v"(t.w[u]), "v"(t.w[4 + u])); GAP(); }
  else if constexpr (u < 8) { float v = d[8 * h + u]; asm volatile("v_mul_f32_e32 %0, %0, %1" : "+v"(v) : "v"(t.a[qb])); d[8 * h + u] = v; }
  else { GAP(); t.w[4 * h + u - 8] = cvtpk(d[8 * h + 2 * (u - 8)], d[8 * h + 2 * (u - 8) + 1]); asm volatile("" :: "v"(t.w[4 * h + u - 8])); GAP(); }
}
template <int Q, int I> DI void ref_step(W4C& c, QFT& t) {
  f32x4& s = c.S[0][0][Q];
  if constexpr (I == 0) asm volatile("v_max3_f32 %0, %1, %2, %3" : "=v"(t.m[Q]) : "v"(s[0]), "v"(s[1]), "v"(s[2]));
  else if constexpr (I == 1) asm volatile("v_max_f32_e32 %0, %0, %1" : "+v"(t.m[Q]) : "v"(s[3]));
  else if constexpr (I == 2 || I == 5) asm volatile("v_mov_b32_e32 %0, %1" : "=v"(t.m2[Q]) : "v"(t.m[Q]));
  else if constexpr (I == 3) asm volatile("v_permlane16_swap_b32_e32 %0, %1" : "+v"(t.m[Q]), "+v"(t.m2[Q]));
  else if constexpr (I == 4) asm volatile("v_max_f32_e32 %0, %0, %1" : "+v"(t.m[Q]) : "v"(t.m2[Q]));
  else if constexpr (I == 6) asm volatile("v_permlane32_swap_b32_e32 %0, %1" : "+v"(t.m[Q]), "+v"(t.m2[Q]));
  else if constexpr (I < 11) { float n; asm volatile("v_min_f32_e64 %0, -%1, -%2" : "=v"(n) : "v"(t.m[Q]), "v"(t.m2[Q])); c.NEGM[Q][I - 7] = n; }
  else { float v = s[I - 11]; add_v(v, c.NEGM[Q][0]); s[I - 11] = v; }
}
template <int J, int CODE, typename FV> DI void qj_op(QP& p, W4C& c, QFT& t, FV& vm, const unsigned csa) {
  constexpr int kind = CODE / 1000, a = CODE % 1000;
  if constexpr (kind == 1) {
    constexpr int tau = a >> 4, u = a & 15, db = tau >> 1, qb = tau & 1;
    if constexpr (u < 8) qp_mfma<7 + (u >> 2), db, qb, (u & 3)>(p.A[db][qb]);
    else asm volatile("mf32 %0, %1, %2, %0" : "+v"(p.A[db][qb]) : "v"(t.ca[db]), "v"(t.nb[qb]));
  } else if constexpr (kind == 2 || kind == 3) { constexpr int kb = (a >> 4) & 3, qb = (a >> 2) & 3; qk16<kb, qb, (a & 3), (a >> 6)>(c.S[kind - 2][kb][qb], c.NEGM[qb]); }
  else if constexpr (kind == 10) q_fin<a>(p, t);
  else if constexpr (kind == 11) ref_step<(a >> 4), (a & 15)>(c, t);
  else if constexpr (kind == 13) exp_e<0, a>(c);
  else if constexpr (kind == 15) pack_e<0, a>(c);
  else if constexpr (kind == 16) {
    constexpr int d = a >> 1;
    if constexpr ((a & 1) == 0) { t.r1 = t.cv[d] - rbf(t.cv[d]); t.ca[d][0] = cvtpk(t.cv[d], t.r1); asm volatile("" : "+v"(t.r1), "+v"(t.ca[d])); }
    else { t.ca[d][1] = cvtpk(t.r1 - bfhi(t.ca[d][0]), 0.f); asm volatile("" : "+v"(t.ca[d])); }
  } else if constexpr (kind == 20) qp_tr<8, (a >> 3), ((a >> 1) & 3), (a & 1)>(p);
  else if constexpr (kind == 21) qp_xr<8, (a >> 2), (a & 3)>(p);
  else if constexpr (kind == 22)
    asm volatile("ds_read_b32 %0, %4\n\tds_read_b32 %1, %4 offset:128\n\tds_read_b32 %2, %4 offset:256\n\tds_read_b32 %3, %4 offset:384"
                 : "=&v"(t.cv[0]), "=&v"(t.cv[1]), "=&v"(t.cv[2]), "=&v"(t.cv[3]) : "v"(csa) : "memory");
  else if constexpr (kind == 23) k_read<a % 100, (a >= 100 ? 0x4000 : 0)>(c);
  else if constexpr (kind == 24) tr_read_<a, 0>(c);
  else if constexpr (kind == 30) {
    if constexpr (a == 0) asm volatile("s_waitcnt lgkmcnt(8)" : "+v"(t.cv[0]), "+v"(t.cv[1]), "+v"(t.cv[2]), "+v"(t.cv[3]) :: "memory");
    else if constexpr (a == 200) asm volatile("s_nop 0");
    else asm volatile("s_waitcnt lgkmcnt(%0)" :: "i"(a >= 100 ? a - 100 : 0) : "memory");
  } else if constexpr (kind == 31) {
    if constexpr (a == 0) asm volatile("s_waitcnt vmcnt(%0) lgkmcnt(6)\n\ts_barrier" :: "i"(vq_wait(VK_S, 8, PJ + J)) : "memory");
    else if constexpr (a == 1) asm volatile("s_waitcnt vmcnt(%0)\n\ts_barrier" :: "i"(vq_wait(VK_KV, 0, PJ + J)) : "memory");
    else asm volatile("s_barrier" ::: "memory");
  } else if constexpr (kind == 32) { vq_issue<PJ + J>(vm); GAP(); }
  else if constexpr (kind == 34) real_op();
  else if constexpr (kind == 33) {
    if constexpr (a == 0) { c.pm0[0] = c.m0k + 0x4000u; c.psf[0] = (KTILEB * 3u + c.sofk) & KMASK; asm volatile("" : "+s"(c.pm0[0]), "+s"(c.psf[0])); }
    else { c.pm0[1] = c.m0v; c.psf[1] = (KTILEB * 2u + c.sofk) & KMASK; asm volatile("" : "+s"(c.pm0[1]), "+s"(c.psf[1])); }
  }
}
template <int J, typename FV, int... Is> DI void qj_gap(QP& p, W4C& c, QFT& t, FV& vm, const unsigned csa, iseq<Is...>) { (qj_op<J, JT[JS[J] + Is]>(p, c, t, vm, csa), ...); GAP(); }
template <typename FV, int... Js> DI void qj_gaps(QP& p, W4C& c, QFT& t, FV& vm, const unsigned csa, iseq<Js...>) { (qj_gap<Js>(p, c, t, vm, csa, make_iseq<JS[Js + 1] - JS[Js]>{}), ...); }

constexpr unsigned XRS_ = DM * 2u;
constexpr int op_vr(int f) { return f < 4 ? f : f < 9 ? f : f < 13 ? f - 9 : f - 4; }
constexpr int op_ar(int f) {
  const int n = f - 35;
  return n < 0 ? 84 + 4 * (f - 26) : n < 4 ? 128 + 4 * n : n < 12 ? 4 * (n - 4) : n < 16 ? 144 + 4 * (n - 12) : n < 24 ? 32 + 4 * (n - 16) : n < 32 ? 160 + 4 * (n - 24) : 64 + 4 * (n - 32);
}
constexpr int OP_LA = 4, OP_SF = 22, OP_SP = 25, OP_SD = 29, OP_FEW = 8, OP_NR = 72, OP_RING = 5, OP_GS = 1, OP_P4W = 16, OP_NI = 16, OP_DS = 4;
constexpr short OPU[] = {1,2,3,0,9,10,11,8,5,6,7,4,13,14,15,12,17,18,19,16,21,22,23,20,25,29,26,30,27,31,24,28,5,6,7,11,9,10,13,14,15,3,1,2,17,18,19,23,21,22,25,29,26,30,27,31,0,8,12,16,20,24,28,4,};
constexpr short OPW[] = {12,13,13,13,12,13,13,13,12,12,12,12,12,12,12,12,12,14,16,17,15,14,12,13,13,13,13,13,13,9,9,5,};
constexpr short TT[] = {0,0,22000,0,0,21016,8016,0,0,0,0,21017,8017,0,0,0,0,0,21018,8018,0,0,0,0,21019,8019,0,0,0,11000,0,11001,0,21020,8020,0,11002,0,11003,0,11004,0,21021,8021,0,11005,0,0,11006,0,11007,0,21022,8022,0,
  11008,0,11009,0,11010,0,21023,8023,0,11011,0,0,13004,13005,0,11012,0,21024,8024,0,11013,0,13006,13007,0,11014,0,21025,8025,0,11015,0,0,11016,0,11017,0,21026,8026,0,11018,0,11019,0,11020,0,21027,8027,0,11021,0,0,11022,
  0,11023,0,21028,8028,0,11024,0,11025,0,11026,0,21029,8029,0,11027,0,0,11028,0,11029,0,21030,8030,0,11030,0,11031,0,11032,0,21031,8031,0,11033,15000,0,23000,0,24000,0,22000,0,21032,8032,0,11034,0,11035,0,11036,0,21033,
  8033,0,11037,0,0,11038,0,11039,0,21034,8034,0,11040,0,11041,0,11042,0,21035,8035,0,11043,0,11044,0,11045,0,11046,0,21036,8036,0,11047,0,11048,0,11049,0,21037,8037,0,11050,0,0,11051,0,11052,0,21038,8038,0,11053,0,11054,
  0,11055,0,21039,8039,0,11056,0,0,11057,0,11058,0,21040,8040,0,11059,0,11060,0,11061,0,21041,8041,0,11062,0,0,11063,0,11064,0,21042,8042,0,11065,0,11066,0,11067,0,21043,8043,0,11068,0,0,11069,0,11070,0,21044,8044,0,
  11071,0,11072,0,11073,0,21045,8045,0,11074,0,0,11075,0,11076,0,21046,8046,0,11077,0,11078,0,11079,0,21047,8047,0,11080,0,0,0,0,21048,8048,0,0,0,0,21049,8049,0,0,0,0,0,21050,8050,0,0,0,0,21051,8051,0,0,0,0,0,21052,8052,
  0,0,0,0,21053,8053,0,0,0,0,0,21054,8054,0,0,0,0,21055,8055,0,0,0,25000,0,25001,0,21056,8056,0,25002,0,25003,0,25004,0,21057,8057,0,25005,0,0,25006,0,25007,0,21058,8058,0,25008,0,0,0,21059,8059,0,0,0,0,0,21060,8060,0,0,
  0,0,21061,8061,0,0,0,0,0,21062,8062,0,0,0,0,21063,8063,0,0,0,0,0,21064,8064,0,0,0,0,21065,8065,0,1000,0,0,0,1001,0,21066,8066,0,0,1004,0,0,21067,8067,0,1002,0,0,0,1003,0,21068,8068,0,0,0,0,21069,8069,0,1005,0,0,0,1006,
  0,21070,8070,0,0,1009,0,0,21071,8071,0,1007,0,0,0,1008,0,21072,8072,0,0,0,0,21073,8073,0,1010,0,0,0,1011,0,21074,8074,0,0,1014,0,0,21075,8075,0,1012,0,0,0,31000,1013,0,21076,8076,0,3000,0,31000,4000,0,31000,4105,0,
  21077,8077,0,4210,0,4301,0,31000,4406,0,0,21078,8078,0,5004,6000,6001,0,31000,6002,6003,0,5103,4011,6100,0,21079,8079,0,6101,6102,0,6103,0,5203,4104,6200,0,6201,6202,0,21080,8080,0,5303,6203,6304,0,7000,4209,6305,0,
  6306,6307,0,21081,8081,0,5403,4314,6404,0,6405,0,6406,6407,0,5003,4402,6004,0,21082,8082,0,6005,6006,0,5103,6007,6116,0,7001,4007,6117,0,21083,8083,0,6118,6119,0,4112,0,5204,6216,6217,0,6218,6219,0,21084,8084,0,5303,
  4203,6316,0,6317,6318,0,5403,6319,6408,0,21085,8085,0,7004,4308,6409,0,6410,0,5003,6411,6008,0,4413,6009,0,21086,8086,0,6010,6011,0,5103,6108,6109,0,6110,6111,0,21087,8087,0,5202,7002,6212,6213,0,6214,0,13000,13001,0,
  5303,6215,6312,0,21088,8088,0,6313,6314,0,13002,13003,0,5404,6315,6412,0,21089,8089,0,6413,6414,0,6415,0,7003,0,0,21090,8090,0,0,0,0,21091,8091,0,0,0,0,0,21092,8092,0,0,0,0,21093,8093,0,0,0,0,0,21094,8094,0,0,0,0,
  21095,8095,0,15000,0,0,0,0,21096,8096,0,0,0,0,21097,8097,0,0,0,0,0,21098,8098,0,0,0,0,21099,8099,0,0,0,0,0,21100,8100,0,0,0,0,21101,8101,0,0,0,0,0,21102,8102,0,0,0,0,21103,8103,0,0,14000,0,20000,0,0,21104,8104,0,0,0,0,
  21105,8105,0,0,0,0,0,21106,8106,0,0,0,0,21107,8107,0,0,0,25015,0,25016,0,21108,8108,0,0,0,0,21109,8109,0,0,0,0,0,21110,8110,0,0,0,0,21111,8111,0,0,0,0,0,21112,8112,0,0,0,0,21113,8113,0,0,0,0,0,21114,8114,0,0,0,0,21115,
  8115,0,0,0,0,0,21116,8116,0,0,0,0,21117,8117,0,1100,0,0,0,1101,0,21118,8118,0,0,1104,0,0,21119,8119,0,1102,0,0,0,1103,0,21120,8120,0,0,0,0,21121,8121,0,1105,0,0,0,1106,0,21122,8122,0,0,1109,0,0,21123,8123,0,1107,0,0,0,
  1108,0,21124,8124,0,26000,0,26001,0,26002,0,21125,8125,0,1110,0,0,0,1111,0,21126,8126,0,0,1114,0,0,21127,8127,0,1112,0,0,0,31000,1113,0,0,3000,0,31000,4900,0,31000,4025,0,31000,4130,0,4901,0,4226,0,31000,4331,0,31000,
  4904,0,31000,4429,0,5006,12000,6020,6021,0,31000,6022,6023,0,5105,4034,6120,0,31000,4902,6121,0,6122,6123,0,4127,0,31000,19000,0,31000,13004,13005,0,5208,12001,6224,6225,0,19001,0,31000,13006,13007,0,31000,6226,6227,0,
  31000,19004,0,5309,4232,6324,0,4903,0,31000,19002,0,31000,6325,6326,0,5409,12004,6327,6436,0,19003,0,31000,4328,6437,0,31000,6438,6439,0,5009,4433,6036,0,6037,6038,0,6039,0,5108,12002,6128,6129,0,31000,6130,6131,0,
  5203,6228,6229,0,6230,6231,0,5301,12003,6332,6333,0,31000,6334,6335,0,5400,6432,6433,0,6434,6435,0,0,31000,16000,0,31000,16001,0,31000,16002,0,16003,0,31000,16004,0,31000,16005,0,31000,16006,0,16007,15004,0,0,31000,
  16008,0,31000,16009,0,31000,16010,0,16011,0,31000,16012,0,31000,16013,0,31000,16014,0,16015,0,0,31000,16016,0,31000,16017,0,17000,18000,18001,0,18002,18003,0,31000,18004,18005,0,31000,18006,18007,0,31000,18016,18017,0,
  18018,18019,18008,18009,18010,18011,18012,18013,18014,18015,};
constexpr short TS[] = {0,1,3,4,7,8,9,10,13,14,15,16,17,20,21,22,23,26,27,28,30,32,35,37,39,41,44,46,47,49,51,54,56,58,60,63,65,66,69,71,74,76,79,81,84,86,87,89,91,94,96,98,100,103,105,106,108,110,113,115,117,119,122,
  124,125,127,129,132,134,136,138,141,143,146,148,150,153,155,157,159,162,164,165,167,169,172,174,176,178,181,183,185,187,189,192,194,196,198,201,203,204,206,208,211,213,215,217,220,222,223,225,227,230,232,234,236,239,
  241,242,244,246,249,251,253,255,258,260,261,263,265,268,270,272,274,277,279,280,282,284,287,289,291,293,296,298,299,300,301,304,305,306,307,310,311,312,313,314,317,318,319,320,323,324,325,326,327,330,331,332,333,336,
  337,338,339,340,343,344,345,346,349,350,351,353,355,358,360,362,364,367,369,370,372,374,377,379,380,381,384,385,386,387,388,391,392,393,394,397,398,399,400,401,404,405,406,407,410,411,412,413,414,417,418,419,420,423,
  425,426,427,429,432,433,435,436,439,441,442,443,445,448,449,450,451,454,456,457,458,460,463,464,466,467,470,472,473,474,476,479,480,481,482,485,487,488,489,491,494,495,497,498,501,503,504,505,508,511,512,516,519,522,
  524,526,529,530,533,537,541,545,548,551,553,557,560,563,567,571,574,577,581,583,586,590,593,596,600,604,607,610,612,616,619,622,626,629,633,636,640,642,646,649,652,655,659,662,665,670,672,675,679,682,685,688,692,695,
  698,700,702,703,706,707,708,709,712,713,714,715,716,719,720,721,722,725,726,727,728,729,732,733,734,735,738,739,741,742,743,746,747,748,749,752,753,754,755,756,759,760,761,762,765,766,767,768,769,772,773,774,775,778,
  779,780,781,782,785,786,787,788,791,792,793,796,797,800,801,802,803,806,807,808,809,810,813,814,815,816,819,820,821,823,825,828,829,830,831,834,835,836,837,838,841,842,843,844,847,848,849,850,851,854,855,856,857,860,
  861,862,863,864,867,868,869,870,873,874,875,876,877,880,881,882,883,886,888,889,890,892,895,896,898,899,902,904,905,906,908,911,912,913,914,917,919,920,921,923,926,927,929,930,933,935,936,937,939,942,944,946,948,951,
  953,954,955,957,960,961,963,964,967,969,970,971,974,975,976,980,983,986,988,990,993,996,999,1004,1008,1012,1016,1019,1021,1024,1028,1033,1035,1039,1043,1046,1050,1052,1055,1059,1064,1066,1070,1074,1078,1081,1083,1088,
  1092,1096,1099,1104,1108,1112,1115,1116,1119,1122,1125,1127,1130,1133,1136,1138,1140,1143,1146,1149,1151,1154,1157,1160,1162,1163,1166,1169,1173,1176,1180,1184,1188,1191,1199,};
constexpr bool op_first(int u) { for (int x = u & 32; x < u; ++x) if ((OPU[x] & 3) == (OPU[u] & 3)) return false; return true; }
struct OPS {
  u32x4 fa[OP_LA + 1][4], fb[22]; f32x4 D[2][5], T[5], m[OP_RING], pv[5]; float g0, g1, t0, t1, gx[36];
  u32x4 gv[2][2]; unsigned gs[2];
  unsigned ra[3], sw[2], si[2], sg, tg, so, sb;
  unsigned va, idx, sa, lb5, lb1, lb2, sn;
  u32x4 srdA, srdL, srdS; unsigned fo, sl, sf, tag, bad; unsigned long long seen;
};
constexpr unsigned OB = WS_END;
DI void op_poll(OPS& o) { asm volatile("buffer_load_dword %0, %1, %2, %3 offen sc1" : "=v"(o.fo) : "v"(o.va), "s"(o.srdL), "s"(o.sl) : "memory"); }
DI void op_there(OPS& o) {
  unsigned n;
  asm volatile("s_and_b32 %0, %1, 15\n\ts_cmp_eq_u32 %0, 15\n\ts_cselect_b32 %0, %2, 0" : "=&s"(n) : "s"((unsigned)o.seen), "i"(OB) : "scc");
  o.srdA[2] = n;
}
template <int F> DI void op_reqB(OPS& o) {
  asm volatile("buffer_load_dwordx4 a[%c3:%c4], %0, %1, %2 offen offset:%c5 sc1" :: "v"(o.va), "s"(o.srdS), "s"(o.sb + 1024u * (F & ~3)), "i"(op_ar(F)), "i"(op_ar(F) + 3), "i"(1024 * (F & 3)) : "memory");
}
constexpr unsigned op_off(int st, int i) { const int un = OPU[2 * st + (i >> 1)]; return (131072u * (un & 3) + 1024u * (2 * (un >> 2) + (i & 1))) ^ (st >> 4) * (524288u + 1024u); }
template <int ST, int I> DI void op_offA(OPS& o) {
  GAP(); o.so = o.sa ^ op_off(ST, I); asm volatile("" :: "s"(o.so)); GAP();
}
template <int ST, int I> DI void op_reqA1(OPS& o) {
  asm volatile("buffer_load_dwordx4 %0, %1, %2, %3 offen sc1" : "=v"(o.fa[ST % (OP_LA + 1)][I]) : "v"(o.va), "s"(o.srdA), "s"(o.so) : "memory");
  if constexpr (ST < OP_LA) asm volatile("ps");
}
template <int... Qs> DI void op_reqA0(OPS& o, iseq<Qs...>) { ((op_offA<Qs / 4, Qs % 4>(o), op_reqA1<Qs / 4, Qs % 4>(o)), ...); }
#define OP_F4(s) "+v"(o.fa[(s) % (OP_LA + 1)][0]), "+v"(o.fa[(s) % (OP_LA + 1)][1]), "+v"(o.fa[(s) % (OP_LA + 1)][2]), "+v"(o.fa[(s) % (OP_LA + 1)][3])
#define OP_I4(s) "i"(op_off(s, 0)), "i"(op_off(s, 1)), "i"(op_off(s, 2)), "i"(op_off(s, 3))
#define OP_L1(d, i) "s_xor_b32 %1, %[sa], %c" #i "\n\tbuffer_load_dwordx4 %" #d ", %[va], %[sr], %1 offen sc1\n\t"
#define OP_HEAD "s_cmp_lg_u32 %[was], 0\n\ts_cbranch_scc0 4f\n\t.subsection 1\n\t4:\n\ts_mov_b32 %2, 0x100000\n\t" \
                "2:\n\tv_cmp_eq_u32_e32 vcc, %[tg], %0\n\ts_and_b32 %1, vcc_lo, 15\n\ts_cmp_eq_u32 %1, 15\n\ts_cbranch_scc1 3f\n\t" \
                "s_sleep 1\n\tbuffer_load_dword %0, %[va], %[sl], %[so] offen sc1\n\ts_sub_u32 %2, %2, 1\n\ts_waitcnt vmcnt(0)\n\ts_cbranch_scc0 2b\n\t3:\n\t"
#define OP_IN [va] "v"(o.va), [sr] "s"(o.srdA), [sl] "s"(o.srdL), [so] "s"(o.sl), [sa] "s"(o.sa), [was] "s"(was), [tg] "s"(o.tag)
template <int S0, int N> DI void op_again(OPS& o) {
  unsigned t, n; const unsigned was = o.srdA[2];
  o.srdA[2] = OB;
  if constexpr (N == 16)
    asm volatile(OP_HEAD OP_L1(3, 19) OP_L1(4, 20) OP_L1(5, 21) OP_L1(6, 22) OP_L1(7, 23) OP_L1(8, 24) OP_L1(9, 25) OP_L1(10, 26) OP_L1(11, 27) OP_L1(12, 28) OP_L1(13, 29) OP_L1(14, 30)
                 OP_L1(15, 31) OP_L1(16, 32) OP_L1(17, 33) OP_L1(18, 34) "s_branch 1f\n\t.subsection 0\n\t1:"
                 : "+v"(o.fo), "=&s"(t), "=&s"(n), OP_F4(S0), OP_F4(S0 + 1), OP_F4(S0 + 2), OP_F4(S0 + 3) : OP_I4(S0), OP_I4(S0 + 1), OP_I4(S0 + 2), OP_I4(S0 + 3), OP_IN : "vcc", "scc", "memory");
  else if constexpr (N == 12)
    asm volatile(OP_HEAD OP_L1(3, 15) OP_L1(4, 16) OP_L1(5, 17) OP_L1(6, 18) OP_L1(7, 19) OP_L1(8, 20) OP_L1(9, 21) OP_L1(10, 22) OP_L1(11, 23) OP_L1(12, 24) OP_L1(13, 25) OP_L1(14, 26) "s_branch 1f\n\t.subsection 0\n\t1:"
                 : "+v"(o.fo), "=&s"(t), "=&s"(n), OP_F4(S0), OP_F4(S0 + 1), OP_F4(S0 + 2) : OP_I4(S0), OP_I4(S0 + 1), OP_I4(S0 + 2), OP_IN : "vcc", "scc", "memory");
  else
    asm volatile(OP_HEAD OP_L1(3, 11) OP_L1(4, 12) OP_L1(5, 13) OP_L1(6, 14) OP_L1(7, 15) OP_L1(8, 16) OP_L1(9, 17) OP_L1(10, 18) "s_branch 1f\n\t.subsection 0\n\t1:"
                 : "+v"(o.fo), "=&s"(t), "=&s"(n), OP_F4(S0), OP_F4(S0 + 1) : OP_I4(S0), OP_I4(S0 + 1), OP_IN : "vcc", "scc", "memory");
}
template <int F> DI void op_ldsB(OPS& o) {
  if constexpr (F < 26) asm volatile("ds_read_b128 %0, %1 offset:%2" : "=v"(o.fb[op_vr(F)]) : "v"(F < 23 ? o.lb5 : o.lb1), "i"(1024 * (F < 23 ? F : F - 23)) : "memory");
  else                  asm volatile("ds_read_b128 a[%c1:%c2], %0 offset:%c3" :: "v"(F < 27 ? o.lb1 : o.lb2), "i"(op_ar(F)), "i"(op_ar(F) + 3), "i"(1024 * (F < 27 ? F - 23 : F - 27)) : "memory");
}
template <int F0, int... Is> DI void op_ldsBs(OPS& o, iseq<Is...>) { (op_ldsB<F0 + Is>(o), ...); }
constexpr int op_ac(int pass, int i, int j) { return j < 4 ? 176 + 16 * i + 4 * j : pass == 0 ? 236 + 4 * i : i == 1 ? 252 : 112 + 4 * i; }
template <int ST, int Q> DI void op_mfma(OPS& o) {
  constexpr int A = ST % (OP_LA + 1), P = ST >> 4;
  if constexpr (Q == 0 || Q == 9) {
    constexpr int u = Q == 9, un = OPU[2 * ST + u], i = un & 3, f = 9 * (un >> 2) + 8, ac = op_ac(P, i, 4), ar = op_ar(f);
    const u32x8 B = __builtin_shufflevector(o.fa[A][2 * u], o.fa[A][2 * u + 1], 0, 1, 2, 3, 4, 5, 6, 7);
    if constexpr (i == 0 && f < 26) asm volatile("sm64 %0, %1, %2, %3" : "+v"(o.D[P][4]) : "v"(o.fb[op_vr(f) % 22]), "v"(B), "v"(o.idx));
    else if constexpr (i == 0)      asm volatile("sm64 %0, a[%c3:%c4], %1, %2" : "+v"(o.D[P][4]) : "v"(B), "v"(o.idx), "i"(ar), "i"(ar + 3));
    else if constexpr (f < 26)      asm volatile("sm64 a[%c3:%c4], %0, %1, %2" :: "v"(o.fb[op_vr(f) % 22]), "v"(B), "v"(o.idx), "i"(ac), "i"(ac + 3));
    else                            asm volatile("sm64 a[%c2:%c3], a[%c4:%c5], %0, %1" :: "v"(B), "v"(o.idx), "i"(ac), "i"(ac + 3), "i"(ar), "i"(ar + 3));
  } else {
    constexpr int d = Q - 1 - (Q > 9), J = d >> 2, u = (d >> 1) & 1, kk = d & 1, un = OPU[2 * ST + u], i = un & 3, f = 9 * (un >> 2) + 4 * kk + J, ac = op_ac(P, i, J), ar = op_ar(f);
    constexpr bool z = kk == 0 && op_first(2 * ST + u);
    static_assert(!z || f < 26, "");
    const u32x4& B = o.fa[A][2 * u + kk];
    if constexpr (i == 0 && z)           asm volatile("mf16 %0, %1, %2, 0" : "=&v"(o.D[P][J]) : "v"(o.fb[op_vr(f) % 22]), "v"(B));
    else if constexpr (i == 0 && f < 26) asm volatile("mf16 %0, %1, %2, %0" : "+v"(o.D[P][J]) : "v"(o.fb[op_vr(f) % 22]), "v"(B));
    else if constexpr (i == 0)           asm volatile("mf16 %0, a[%c2:%c3], %1, %0" : "+v"(o.D[P][J]) : "v"(B), "i"(ar), "i"(ar + 3));
    else if constexpr (z)                asm volatile("mf16 a[%c2:%c3], %0, %1, 0" :: "v"(o.fb[op_vr(f) % 22]), "v"(B), "i"(ac), "i"(ac + 3));
    else if constexpr (f < 26)           asm volatile("mf16 a[%c2:%c3], %0, %1, a[%c2:%c3]" :: "v"(o.fb[op_vr(f) % 22]), "v"(B), "i"(ac), "i"(ac + 3));
    else                                 asm volatile("mf16 a[%c1:%c2], a[%c3:%c4], %0, a[%c1:%c2]" :: "v"(B), "i"(ac), "i"(ac + 3), "i"(ar), "i"(ar + 3));
  }
}
template <int I> DI unsigned op_ax(const unsigned va) { const unsigned r = ((va >> 4) & 15u) * XRS_; return I ? r + 128u + ((va >> 5) & 8u) + ((va >> 7) & 4u) : r + ((va >> 4) & 48u); }
template <int X, int WD> DI unsigned op_word(const OPS& o) { if constexpr (WD < 8) return o.gv[X][WD >> 2][WD & 3]; else return o.gs[X]; }
template <int U> DI void op_gate(OPS& o) {
  constexpr int wd = U / 9, pt = U % 9;
  if constexpr (pt == 0)      asm volatile("v_lshlrev_b32_e32 %0, 16, %2\n\tv_and_b32_e32 %1, 0xffff0000, %2" : "=&v"(o.g0), "=&v"(o.g1) : "v"(op_word<0, wd>(o)));
  else if constexpr (pt == 1) asm volatile("v_mul_f32_e32 %0, 0xbfb8aa3b, %2\n\tv_mul_f32_e32 %1, 0xbfb8aa3b, %3" : "=&v"(o.t0), "=&v"(o.t1) : "v"(o.g0), "v"(o.g1));
  else if constexpr (pt == 2) asm volatile("v_exp_f32_e32 %0, %0" : "+v"(o.t0));
  else if constexpr (pt == 3) asm volatile("v_exp_f32_e32 %0, %0" : "+v"(o.t1));
  else if constexpr (pt == 4) asm volatile("v_add_f32_e32 %0, 1.0, %0\n\tv_add_f32_e32 %1, 1.0, %1" : "+v"(o.t0), "+v"(o.t1));
  else if constexpr (pt == 5) asm volatile("v_rcp_f32_e32 %0, %0" : "+v"(o.t0));
  else if constexpr (pt == 6) asm volatile("v_rcp_f32_e32 %0, %0" : "+v"(o.t1));
  else if constexpr (pt == 7) asm volatile("v_mul_f32_e32 %0, %0, %2\n\tv_mul_f32_e32 %1, %1, %3" : "+v"(o.t0), "+v"(o.t1) : "v"(o.g0), "v"(o.g1));
  else { unsigned r; asm volatile("v_cvt_pk_bf16_f32 %0, %1, %2" : "=v"(r) : "v"(o.t0), "v"(o.t1)); if constexpr (wd < 8) o.gv[0][wd >> 2][wd & 3] = r; else o.gs[0] = r; }
}
template <int ST, int Q, int CODE, typename H> DI void op_t(OPS& o, H& hook) {
  constexpr int kind = CODE / 1000, a = CODE % 1000;
  if constexpr (kind == 0) op_mfma<ST, Q>(o);
  else if constexpr (kind == 1) {
    constexpr int n = a % 100, h = a / 200, ac = op_ac((a / 100) & 1, 1 + n / 5, n % 5), off = n < 10 ? 13312 + 1024 * n : 1024 * (n - 10);
    if constexpr (h == 0) asm volatile("ds_write_b128 %0, a[%c1:%c2] offset:%c3" :: "v"(n < 10 ? o.lb5 : o.lb2), "i"(ac), "i"(ac + 3), "i"(off) : "memory");
    else                  asm volatile("ds_write_b64 %0, a[%c1:%c2] offset:%c3" :: "v"(n < 10 ? o.lb5 : o.lb2), "i"(ac + 2 * h - 2), "i"(ac + 2 * h - 1), "i"(off + 8 * h - 8) : "memory");
  } else if constexpr (kind == 2) asm volatile("s_waitcnt lgkmcnt(%0)" :: "i"(a) : "memory");
  else if constexpr (kind == 3) asm volatile("s_waitcnt lgkmcnt(0)\n\ts_barrier" ::: "memory");
  else if constexpr (kind == 4) {
    constexpr int sl = a / 100, n = a % 20;
    asm volatile("ds_read_b128 %0, %1 offset:%2" : "=v"(sl == 9 ? o.T[n % 5] : o.m[sl % OP_RING]) : "v"(o.ra[n / 5]), "i"(n < 10 ? 13312 + 1024 * n : 1024 * (n - 10)) : "memory");
  } else if constexpr (kind == 5) asm volatile("s_waitcnt lgkmcnt(%1)" : "+v"(o.m[a / 100]) : "i"(a % 100) : "memory");
  else if constexpr (kind == 6 || kind == 18) {
    constexpr int j = (a % 20) >> 2, e = a & 3;
    f32x4& s = kind == 6 && a % 100 < 20 ? o.D[0][j] : o.T[j];
    float v = s[e]; add_v(v, kind == 6 ? o.m[a / 100][e] : o.pv[j][e]); s[e] = v;
  } else if constexpr (kind == 7)
    asm volatile("buffer_store_dwordx4 %0, %1, %2, %3 offen offset:%c4 sc1" :: "v"(o.D[0][a]), "v"(o.va), "s"(o.srdS), "s"(o.sw[a >> 2]), "i"(1024 * (a & 3)) : "memory");
  else if constexpr (kind == 8) op_reqA1<(a >> 2), (a & 3)>(o);
  else if constexpr (kind == 9) op_reqB<a>(o);
  else if constexpr (kind == 19) asm volatile("buffer_load_dwordx4 %0, %1, %2, %3 offen offset:%c4 sc1" : "=v"(o.pv[a]) : "v"(o.va), "s"(o.srdS), "s"(o.si[a >> 2]), "i"(1024 * (a & 3)) : "memory");
  else if constexpr (kind == 20) asm volatile("buffer_store_dword %0, %1, %2, %3 offen sc1" :: "v"(o.tg), "v"(o.va), "s"(o.srdL), "s"(o.sg) : "memory");
  else if constexpr (kind == 25) { GAP(); hook(IC(a)); GAP(); }
  else if constexpr (kind == 26) {
    if constexpr (a < 2) asm volatile("ds_read_b128 %0, %1 offset:%2" : "=v"(o.gv[1][a]) : "v"(o.lb5), "i"(4096 + 1024 * a) : "memory");
    else                 asm volatile("ds_read_b32 %0, %1 offset:6144" : "=v"(o.gs[1]) : "v"(o.lb5) : "memory");
  } else if constexpr (kind == 22) op_poll(o);
  else if constexpr (kind == 23) asm volatile("v_cmp_eq_u32_e64 %0, %1, %2" : "=s"(o.seen) : "s"(o.tag), "v"(o.fo));
  else if constexpr (kind == 24) op_there(o);
  else if constexpr (kind == 21) op_offA<(a >> 2), (a & 3)>(o);
  else if constexpr (kind == 31) real_op();
  else if constexpr (kind == 11) op_gate<a>(o);
  else if constexpr (kind == 12) asm volatile("" : "+v"(o.T[a]));
  else if constexpr (kind == 13) op_ldsB<9 * (a >> 2) + (a & 3)>(o);
  else if constexpr (kind == 14) asm volatile("s_barrier" ::: "memory");
  else if constexpr (kind == 15) asm volatile("s_waitcnt lgkmcnt(%4)" : "+v"(o.fb[0]), "+v"(o.fb[1]), "+v"(o.fb[2]), "+v"(o.fb[3]) : "i"(a) : "memory");
  else if constexpr (kind == 16) asm volatile("v_lshlrev_b32_e32 %0, 16, %2\n\tv_and_b32_e32 %1, 0xffff0000, %2" : "=&v"(o.gx[2 * a]), "=v"(o.gx[2 * a + 1]) : "v"(op_word<a / 9, a % 9>(o)));
  else if constexpr (kind == 17) asm volatile("s_waitcnt vmcnt(0)" : "+v"(o.pv[0]), "+v"(o.pv[1]), "+v"(o.pv[2]), "+v"(o.pv[3]), "+v"(o.pv[4]) :: "memory");
}
template <int ST, int Q, typename H, int... Is> DI void op_ts(OPS& o, H& hook, iseq<Is...>) { (op_t<ST, Q, TT[TS[18 * ST + Q] + Is]>(o, hook), ...); }
template <int ST, typename H, int... Qs> DI void op_qs(OPS& o, H& hook, iseq<Qs...>) { (op_ts<ST, Qs>(o, hook, make_iseq<TS[18 * ST + Qs + 1] - TS[18 * ST + Qs]>{}), ...); }

template <typename F, int... Is> DI void for_seq(F& f, iseq<Is...>) { (f(std::integral_constant<int, Is>{}), ...); }
DI void wait_flag(const u32x4 srd, const unsigned p, const unsigned tag) {
#pragma nounroll
  for (unsigned spin = 0; spin < (1u << 20); ++spin) {
    unsigned v;
    asm volatile("buffer_load_dword %0, %1, %2, 0 offen sc1\n\ts_waitcnt vmcnt(0)" : "=v"(v) : "v"(p), "s"(srd) : "memory");
    if (__all(v == tag)) break;
    __builtin_amdgcn_s_sleep(1);
  }
}
DI unsigned op_k0(const unsigned blk, const unsigned w) { return ((blk >> 3) & 7u) * 128u + (~blk & 4u) * 16u + 16u * w; }

constexpr int NFIN = 200, NBL = 45, FVW = 44, FVS = 0, FNR = 32;
constexpr short FT[] = {5064,31000,23000,23001,23002,5001,31000,23003,23004,23005,5002,31000,23006,23007,23008,5003,31000,23009,23010,23011,5083,31000,23012,23013,23014,5018,31000,23015,16000,16001,5017,16008,16009,
  16016,5016,16017,16024,5096,16025,16002,16003,5033,31000,23016,23017,5034,31000,23018,23019,23020,5035,31000,23021,23022,5115,31000,25003,23023,5050,31000,23024,23025,5049,31000,25002,23026,5048,31000,23027,23028,
  20015,1000,31000,25001,23029,22000,1001,31000,23030,23031,20031,1003,31000,25000,16010,1002,16011,16018,20047,1004,16019,16026,16027,1005,13000,20063,1007,13001,13002,1006,15000,16004,29010,20076,1008,27000,13003,1009,
  31000,26000,20088,1011,15001,13016,1010,31000,26001,20100,1012,13017,13018,1013,31000,26002,20112,1015,15008,16005,1014,31000,26003,27001,3000,13019,1016,31000,26004,15009,1017,13032,13033,1019,31000,26005,1018,15016,
  16012,1020,31000,26006,24000,1021,31000,24001,24002,24003,1023,31000,26007,1022,31000,24004,24005,24006,1024,31000,24007,24008,1025,31000,24009,24010,24011,1027,13034,1026,13035,13048,1028,15017,16013,1029,13049,13050,
  1031,15024,16020,1030,13051,16021,3001,15025,14057,24012,4000,31000,24013,24014,24015,30012,2000,31000,24016,24017,24018,24019,2001,31000,12000,16028,30028,2003,31000,24020,24021,24022,24023,2002,31000,24024,24025,
  24026,24027,30047,2004,31000,24028,24029,24030,24031,2005,31000,17000,17001,17002,17003,30063,2007,31000,25004,17004,17005,2006,31000,17006,17007,18000,18001,30076,2008,31000,25005,18002,18003,2009,31000,18004,18005,
  18006,18007,30088,2011,19000,19001,19002,19003,2010,31000,28000,17008,17009,30100,2012,31000,17010,17011,17012,17013,2013,31000,25006,17014,17015,30112,2015,31000,18008,18009,18010,18011,2014,31000,18012,18013,18014,
  18015,5068,31000,25007,19004,19005,5005,19006,19007,14058,5006,31000,28001,17016,17017,5007,31000,17018,17019,17020,5087,31000,25008,17021,17022,5022,31000,17023,18016,18017,5021,31000,18018,18019,18020,18021,5020,
  31000,18022,18023,19008,5100,19009,19010,19011,14059,5037,31000,28002,17024,5038,31000,17025,17026,17027,5039,31000,17028,17029,17030,5119,31000,25015,17031,18024,5054,31000,18025,18026,18027,5053,31000,18028,18029,
  18030,18031,5052,31000,25014,19012,1032,19013,19014,19015,16029,13004,13005,22001,1033,31000,28003,15002,1035,13006,13007,1034,31000,25013,15003,1036,13020,13021,1037,15010,13022,1039,31000,25009,13023,1038,15011,
  13036,1040,13037,13038,1041,31000,25012,15018,1043,13039,13052,1042,15019,13053,1044,31000,25010,1045,15026,13054,1047,13055,1046,15027,14012,3002,31000,25011,4001,14013,2016,16006,14014,2017,31000,12001,2019,14015,
  14028,2018,31000,17032,17033,17034,17035,2020,31000,25016,16007,14029,2021,31000,17036,17037,17038,17039,2023,31000,18032,18033,18034,18035,2022,31000,25017,18036,18037,2024,31000,18038,18039,19016,19017,2025,19018,
  19019,16014,14030,2027,31000,28004,17040,17041,2026,31000,17042,17043,17044,17045,2028,31000,25018,17046,17047,2029,31000,18040,18041,18042,18043,2031,31000,18044,18045,18046,18047,2030,31000,25019,19020,19021,5072,
  19022,19023,14031,5009,31000,28005,17048,17049,5010,31000,17050,17051,17052,17053,5011,31000,25020,17054,17055,5091,31000,18048,18049,18050,18051,5026,31000,18052,18053,18054,18055,5025,19024,19025,19026,19027,5024,
  31000,28006,17056,17057,5104,31000,17058,17059,17060,17061,5041,31000,17062,17063,18056,18057,5042,31000,18058,18059,18060,18061,5043,31000,18062,18063,19028,19029,5123,31000,25027,19030,5058,19031,13008,13009,5057,
  31000,28007,15004,5056,13010,13011,5076,31000,25026,15005,22002,5013,13024,13025,5014,15012,13026,5015,31000,25025,13027,5095,15013,13040,5030,13041,13042,5029,31000,25021,15020,5028,13043,13056,5108,15021,13057,5045,
  31000,25024,15028,5046,13058,13059,5047,15029,16015,14044,5127,31000,25022,14045,5062,16022,14046,14047,5061,16023,14060,14061,14062,16030,14063,5060,31000,25023,16031,13012,4002,13013,13014,22003,2032,15006,13015,
  13028,2033,31000,12002,15007,2035,31000,25031,13029,2034,31000,17064,17065,17066,17067,2036,15014,13030,13031,2037,31000,25030,17068,17069,2039,31000,17070,17071,18064,18065,2038,31000,18066,18067,18068,18069,2040,
  31000,25029,18070,18071,2041,19032,19033,19034,19035,2043,31000,28008,17072,17073,2042,31000,17074,17075,17076,17077,2044,31000,25028,17078,17079,2045,31000,18072,18073,18074,18075,2047,31000,18076,18077,18078,18079,
  2046,19036,19037,19038,19039,1048,31000,28009,17080,17081,1049,31000,17082,17083,17084,17085,1051,31000,17086,17087,18080,18081,1050,31000,18082,18083,18084,18085,1052,31000,18086,18087,19040,19041,1053,19042,19043,
  15015,13044,1055,31000,28010,17088,17089,1054,31000,17090,17091,17092,17093,1056,31000,17094,17095,18088,18089,1057,31000,18090,18091,18092,18093,1059,31000,18094,18095,19044,19045,1058,19046,19047,13045,1060,31000,
  28011,15022,13046,1061,13047,13060,1063,15023,13061,13062,1062,15030,13063,3003,15031,30200,30200,4003,31000,2048,31000,2049,31000,12003,2051,31000,2050,31000,17096,17097,17098,17099,2052,31000,2053,31000,17100,17101,
  17102,17103,18096,2055,31000,18097,18098,18099,18100,18101,2054,31000,18102,18103,19048,19049,19050,2056,19051,2057,31000,28012,17104,17105,17106,17107,2059,31000,17108,17109,17110,17111,18104,18105,2058,31000,18106,
  18107,18108,18109,18110,18111,2060,19052,19053,19054,19055,2061,31000,28013,17112,17113,17114,17115,2063,31000,17116,17117,17118,17119,18112,18113,2062,31000,18114,18115,18116,18117,18118,18119,19056,19057,19058,19059,
  28014,17120,17121,17122,17123,17124,17125,17126,17127,18120,18121,18122,18123,18124,18125,18126,18127,19060,19061,19062,19063,28015,};
constexpr short FS[] = {0,5,10,15,20,25,30,34,37,41,45,50,54,58,62,66,70,76,80,85,88,93,95,99,102,107,110,114,117,121,124,128,132,134,138,141,144,147,151,156,159,164,168,173,175,178,181,184,187,190,194,199,206,210,217,
  223,230,236,242,248,254,260,266,271,278,283,290,296,301,305,310,315,320,325,331,336,341,345,350,355,360,365,371,375,383,387,390,394,397,400,404,407,410,414,417,420,423,426,428,431,434,436,439,442,445,451,456,462,468,
  473,479,484,489,495,500,506,512,517,521,526,532,537,543,549,554,559,565,571,577,583,587,591,595,598,603,606,609,613,616,619,623,626,629,633,636,640,644,648,655,660,664,668,672,676,682,686,691,697,703,708,713,718,724,
  729,735,741,746,751,757,763,769,775,780,785,791,797,803,809,813,818,821,825,828,832,834,836,839,841,847,849,856,863,870,872,879,887,895,900,907,915,949,};
struct OST { u32x4 w, P[4][2]; float o[8], il[4], ls[4]; unsigned vo, so[4]; u32x4 srd; bf16x4 lo[8][2], up[8][2]; };
template <int U> DI void o_store(const OST& t) {
  asm volatile("buffer_store_dwordx4 %0, %1, %2, %3 offen sc1" :: "v"(t.w), "v"(t.vo), "s"(t.srd), "s"(t.so[U & 3] + 131072u * (U >> 2)) : "memory");
}
template <int CODE> DI void fin_op(W4C& c, OST& t, OPS& o) {
  constexpr int kind = CODE / 1000, a = CODE % 1000;
  if constexpr (kind == 1) pv16<AO_((a >> 1) & 7, a >> 4), false>(__builtin_shufflevector(c.vlo[(a >> 1) & 7][a & 1], c.vup[(a >> 1) & 7][a & 1], 0, 1, 2, 3, 4, 5, 6, 7), c.Pk[a >> 4][a & 1]);
  else if constexpr (kind == 2) pv16<AO_((a >> 1) & 7, a >> 4), false>(__builtin_shufflevector(t.lo[(a >> 1) & 7][a & 1], t.up[(a >> 1) & 7][a & 1], 0, 1, 2, 3, 4, 5, 6, 7), t.P[a >> 4][a & 1]);
  else if constexpr (kind == 3) sum64(c, a, c.Pk[a]);
  else if constexpr (kind == 4) sum64(c, a, t.P[a]);
  else if constexpr (kind == 5) { constexpr int kb = (a >> 4) & 3, qb = (a >> 2) & 3; qk16<kb, qb, (a & 3), (a >> 6)>(c.S[1][kb][qb], c.NEGM[qb]); }
  else if constexpr (kind == 14) exp_e<0, a>(c);
  else if constexpr (kind == 16) pack_e<0, a>(c);
  else if constexpr (kind == 22) asm volatile("" :: "v"(c.NEGM[a]));
  else if constexpr (kind == 20) { constexpr int d = a >> 4; asm volatile("s_waitcnt lgkmcnt(%4)" : "+v"(c.vlo[d][0]), "+v"(c.vlo[d][1]), "+v"(c.vup[d][0]), "+v"(c.vup[d][1]) : "i"(a & 15) : "memory"); }
  else if constexpr (kind == 23) tr_read_<a, 0>(c);
  else if constexpr (kind == 29) asm volatile("s_waitcnt vmcnt(%0) lgkmcnt(0)\n\ts_barrier" :: "i"(a) : "memory");
  else if constexpr (kind == 12) asm volatile("v_add_f32_e32 %0, %2, %3\n\tv_rcp_f32_e32 %1, %0" : "=&v"(t.ls[a]), "=v"(t.il[a]) : "v"(c.L[a][0]), "v"(c.L[a][1]), "v"(c.L[a]));
  else if constexpr (kind == 13) exp_e<1, a>(c);
  else if constexpr (kind == 15) pack_to<1, a>(c, t.P);
  else if constexpr (kind == 17) t.o[a & 7] = acc_read<a>();
  else if constexpr (kind == 18) {
    asm volatile("v_mul_f32_e32 %0, %0, %1" : "+v"(t.o[a & 7]) : "v"(t.il[a >> 5]));
    if constexpr (a >= 8 && (a & 7) == 0) asm volatile("" :: "v"(t.w));
  } else if constexpr (kind == 19) { GAP(); const unsigned w = cvtpk(t.o[2 * (a & 3)], t.o[2 * (a & 3) + 1]); asm volatile("" :: "v"(w)); t.w[a & 3] = w; GAP(); }
  else if constexpr (kind == 24) tr_rd<a, 0x4000>(c, t.lo, t.up);
  else if constexpr (kind == 25) op_reqB<35 + a>(o);
  else if constexpr (kind == 26) asm volatile("buffer_load_dwordx4 %0, %1, %2 offen offset:%c3 sc1 lds" :: "v"(c.pfv), "s"(c.srdP), "s"(c.pfo + 4096u * (a >> 2)), "i"(1024 * (a & 3)) : "memory");
  else if constexpr (kind == 27) asm volatile("s_mov_b32 m0, %0" :: "s"(c.pfm0 + 4096u * a) : "m0", "memory");
  else if constexpr (kind == 28) o_store<a>(t);
  else if constexpr (kind == 31) real_op();
  else if constexpr (a == 200) asm volatile("s_nop 0");
  else { constexpr int d = a >> 4; asm volatile("s_waitcnt lgkmcnt(%4)" : "+v"(t.lo[d][0]), "+v"(t.lo[d][1]), "+v"(t.up[d][0]), "+v"(t.up[d][1]) : "i"(a & 15) : "memory"); }
}
template <int G, int... Is> DI void fin_gap(W4C& c, OST& t, OPS& o, iseq<Is...>) { (fin_op<FT[FS[G] + Is]>(c, t, o), ...); GAP(); }
template <int... Gs> DI void fin_gaps(W4C& c, OST& t, OPS& o, iseq<Gs...>) { (fin_gap<Gs>(c, t, o, make_iseq<FS[Gs + 1] - FS[Gs]>{}), ...); }

template <int J, int H> DI void redo_q(const __amdgpu_buffer_rsrc_t srdW, const u32x4 srdN, const unsigned vw, const unsigned vx, const unsigned sw, const unsigned sx) {
  f32x4 q[4] = {};
#pragma nounroll
  for (unsigned ks = 0; ks < DM / 32; ++ks) {
    u32x4 w;
#pragma unroll
    for (int e = 0; e < 4; ++e) {
      const unsigned so = sw + (64u * J + 8u * H) + WRS * (32u * ks + 2u * e);
      w[e] = (unsigned)__builtin_amdgcn_raw_buffer_load_b16(srdW, vw, so, 0) | ((unsigned)__builtin_amdgcn_raw_buffer_load_b16(srdW, vw, so + WRS, 0) << 16);
    }
    asm volatile("s_nop 1" : "+v"(w), "+v"(q[0]), "+v"(q[1]), "+v"(q[2]), "+v"(q[3]));
#pragma unroll
    for (int qb = 0; qb < 4; ++qb) {
      u32x4 x;
      asm volatile("buffer_load_dwordx4 %0, %1, %2, %3 offen sc1\n\ts_waitcnt vmcnt(0)" : "=v"(x) : "v"(vx), "s"(srdN), "s"(sx + 64u * ks + 16u * XRS * qb) : "memory");
      sum16<false>(q[qb], w, x);
    }
  }
  asm volatile("s_nop 7" : "+v"(q[0]), "+v"(q[1]), "+v"(q[2]), "+v"(q[3]));
  const float C = 0.12751743082f;
  unsigned r[8];
#pragma unroll
  for (int qb = 0; qb < 4; ++qb) { r[2 * qb] = cvtpk(C * q[qb][0], C * q[qb][1]); r[2 * qb + 1] = cvtpk(C * q[qb][2], C * q[qb][3]); }
  asm volatile("s_nop 1" : "+v"(r[0]), "+v"(r[1]), "+v"(r[2]), "+v"(r[3]), "+v"(r[4]), "+v"(r[5]), "+v"(r[6]), "+v"(r[7]));
  acc_write<AQ_(0, J) + 2 * H>(r[0]); acc_write<AQ_(0, J) + 2 * H + 1>(r[1]); acc_write<AQ_(1, J) + 2 * H>(r[2]); acc_write<AQ_(1, J) + 2 * H + 1>(r[3]);
  acc_write<AQ_(2, J) + 2 * H>(r[4]); acc_write<AQ_(2, J) + 2 * H + 1>(r[5]); acc_write<AQ_(3, J) + 2 * H>(r[6]); acc_write<AQ_(3, J) + 2 * H + 1>(r[7]);
}
template <int I> DI void redo_kload(const unsigned vo, const __amdgpu_buffer_rsrc_t srd, const unsigned so) {
  asm volatile("buffer_load_dwordx4 a[%c3:%c4], %0, %1, %2 offen offset:%c5" :: "v"(vo), "s"(srd), "s"(so + 4096u * (I >> 2)), "i"(192 + 4 * I), "i"(195 + 4 * I), "i"(64 * (I & 3)) : "memory");
}
template <int CM, int... Ms> DI void redo_s(W4C& c, const unsigned vk, const unsigned so, f32x4 (&T)[2][4], iseq<Ms...>) {
  redo_kload<0>(vk, c.srdK, so); redo_kload<1>(vk, c.srdK, so); redo_kload<2>(vk, c.srdK, so); redo_kload<3>(vk, c.srdK, so);
  redo_kload<4>(vk, c.srdK, so); redo_kload<5>(vk, c.srdK, so); redo_kload<6>(vk, c.srdK, so); redo_kload<7>(vk, c.srdK, so);
  asm volatile("s_waitcnt vmcnt(0)" ::: "memory");
  (qk16<(Ms >> 4), ((Ms >> 2) & 3), (Ms & 3), ((Ms & 3) ? 0 : CM)>(T[Ms >> 4][(Ms >> 2) & 3], c.NEGM[(Ms >> 2) & 3]), ...);
  asm volatile("s_nop 7" : "+v"(T[0][0]), "+v"(T[0][1]), "+v"(T[0][2]), "+v"(T[0][3]), "+v"(T[1][0]), "+v"(T[1][1]), "+v"(T[1][2]), "+v"(T[1][3]));
}
template <int DB> DI void redo_pv(const W4C& c, const unsigned vv, const unsigned so, const u32x4 (&P)[4]) {
  u32x4 v;
#pragma unroll
  for (int e = 0; e < 4; ++e) {
    const unsigned s = so + (896u + 32u * DB) + KRS * (16u * (e >> 1) + 2u * (e & 1));
    v[e] = (unsigned)__builtin_amdgcn_raw_buffer_load_b16(c.srdV, vv, s, 0) | ((unsigned)__builtin_amdgcn_raw_buffer_load_b16(c.srdV, vv, s + KRS, 0) << 16);
  }
  asm volatile("s_nop 1" : "+v"(v));
  const bf16x8 vf = __builtin_bit_cast(bf16x8, v);
  pv16<AO_(DB, 0), false>(vf, P[0]); pv16<AO_(DB, 1), false>(vf, P[1]); pv16<AO_(DB, 2), false>(vf, P[2]); pv16<AO_(DB, 3), false>(vf, P[3]);
  GAP();
}
template <int U> DI void redo_unit(OST& t) {
  t.o[0] = acc_read<8 * U>(); t.o[1] = acc_read<8 * U + 1>(); t.o[2] = acc_read<8 * U + 2>(); t.o[3] = acc_read<8 * U + 3>();
  t.o[4] = acc_read<8 * U + 4>(); t.o[5] = acc_read<8 * U + 5>(); t.o[6] = acc_read<8 * U + 6>(); t.o[7] = acc_read<8 * U + 7>();
  const float il = t.il[U >> 2];
  t.w = u32x4{cvtpk(t.o[0] * il, t.o[1] * il), cvtpk(t.o[2] * il, t.o[3] * il), cvtpk(t.o[4] * il, t.o[5] * il), cvtpk(t.o[6] * il, t.o[7] * il)};
  o_store<U>(t);
  asm volatile("s_nop 1" :: "v"(t.w));
}
template <int R0, int... Is> DI void redo_zero(const unsigned z, iseq<Is...>) { (acc_write<R0 + Is>(z), ...); }
template <int... Is> DI void redo_units(OST& t, iseq<Is...>) { (redo_unit<Is>(t), ...); }
template <int F0, int... Is> DI void redo_reqs(OPS& o, iseq<Is...>) { (op_reqB<F0 + Is>(o), ...); }
DI void redo_rows(W4C& c, OST& t, OPS& o, const u16* wq, const unsigned row0, const unsigned h, const unsigned tag) {
  unsigned l;
  asm volatile("s_waitcnt vmcnt(0)\n\tv_mbcnt_lo_u32_b32 %0, -1, 0\n\tv_mbcnt_hi_u32_b32 %0, -1, %0" : "=v"(l) :: "memory");
  const unsigned c16 = l & 15u, g = l >> 4;
  wait_flag(c.srdS, WS_KEY + 8u * (row0 + l) + 4u, tag);
  {
    const __amdgpu_buffer_rsrc_t srdW = mk_srd(wq, DM * HQ * DH * 2); const u32x4 srdN = c.srdS;
    const unsigned vw = WRS * 8u * g + 16u * (c16 >> 2) + 2u * (c16 & 3u), vx = XRS * c16 + 16u * g, sw = 256u * h, sx = WS_XN + XRS * row0;
    redo_q<0, 0>(srdW, srdN, vw, vx, sw, sx); redo_q<0, 1>(srdW, srdN, vw, vx, sw, sx); redo_q<1, 0>(srdW, srdN, vw, vx, sw, sx); redo_q<1, 1>(srdW, srdN, vw, vx, sw, sx);
    redo_q<2, 0>(srdW, srdN, vw, vx, sw, sx); redo_q<2, 1>(srdW, srdN, vw, vx, sw, sx); redo_q<3, 0>(srdW, srdN, vw, vx, sw, sx); redo_q<3, 1>(srdW, srdN, vw, vx, sw, sx);
  }
  const unsigned vk = KRS * c16 + 16u * g, vv = KRS * 4u * g + 2u * c16;
  f32x4 T[2][4];
  float m[4] = {-__builtin_inff(), -__builtin_inff(), -__builtin_inff(), -__builtin_inff()};
#pragma nounroll
  for (unsigned u = 0; u < SEQ / 32; ++u) {
    redo_s<2>(c, vk, 32u * KRS * u, T, make_iseq<32>{});
#pragma unroll
    for (int q = 0; q < 4; ++q)
#pragma unroll
      for (int e = 0; e < 8; ++e) m[q] = max2f(m[q], T[e >> 2][q][e & 3]);
  }
#pragma unroll
  for (int q = 0; q < 4; ++q) {
    asm volatile("s_nop 1" : "+v"(m[q]));
    auto p1 = __builtin_amdgcn_permlane16_swap(__float_as_uint(m[q]), __float_as_uint(m[q]), false, false);
    m[q] = max2f(__uint_as_float(p1[0]), __uint_as_float(p1[1]));
    asm volatile("s_nop 1" : "+v"(m[q]));
    auto p2 = __builtin_amdgcn_permlane32_swap(__float_as_uint(m[q]), __float_as_uint(m[q]), false, false);
    m[q] = -max2f(__uint_as_float(p2[0]), __uint_as_float(p2[1]));
    c.NEGM[q] = f32x4{m[q], m[q], m[q], m[q]};
    c.L[q] = f32x4{0.f, 0.f, 0.f, 0.f};
  }
  redo_zero<0>(0u, make_iseq<128>{});
  asm volatile("s_nop 1" : "+v"(c.NEGM[0]), "+v"(c.NEGM[1]), "+v"(c.NEGM[2]), "+v"(c.NEGM[3]), "+v"(c.L[0]), "+v"(c.L[1]), "+v"(c.L[2]), "+v"(c.L[3]));
#pragma nounroll
  for (unsigned u = 0; u < SEQ / 32; ++u) {
    redo_s<1>(c, vk, 32u * KRS * u, T, make_iseq<32>{});
    u32x4 P[4];
#pragma unroll
    for (int q = 0; q < 4; ++q)
#pragma unroll
      for (int e = 0; e < 4; ++e) P[q][e] = cvtpk(__builtin_amdgcn_exp2f(T[e >> 1][q][2 * (e & 1)]), __builtin_amdgcn_exp2f(T[e >> 1][q][2 * (e & 1) + 1]));
    asm volatile("s_nop 1" : "+v"(P[0]), "+v"(P[1]), "+v"(P[2]), "+v"(P[3]));
#pragma unroll
    for (int q = 0; q < 4; ++q) sum16<false>(c.L[q], c.ONE, P[q]);
    const unsigned so = 32u * KRS * u;
    redo_pv<0>(c, vv, so, P); redo_pv<1>(c, vv, so, P); redo_pv<2>(c, vv, so, P); redo_pv<3>(c, vv, so, P);
    redo_pv<4>(c, vv, so, P); redo_pv<5>(c, vv, so, P); redo_pv<6>(c, vv, so, P); redo_pv<7>(c, vv, so, P);
  }
  asm volatile("s_nop 7" : "+v"(c.L[0]), "+v"(c.L[1]), "+v"(c.L[2]), "+v"(c.L[3]));
#pragma unroll
  for (int q = 0; q < 4; ++q) t.il[q] = __builtin_amdgcn_rcpf(c.L[q][0]);
  redo_units(t, make_iseq<16>{});
  redo_reqs<35>(o, make_iseq<FNR>{});
  asm volatile("s_waitcnt vmcnt(0)" ::: "memory");
}

DI void oproj_phase(OPS& o, char* lds, u16* __restrict__ out, const unsigned wid  ) {
  const unsigned ldsb = 0u, tag = o.tag;
  unsigned fe;
  op_poll(o);
  o.lb5 = ldsb + SD_BASE + BP_W * wid + o.va; o.lb1 = ldsb + 4096u * wid + o.va; o.lb2 = ldsb + 0x4000u + 8192u * wid + o.va;
  asm volatile("s_waitcnt vmcnt(%6)" : "+v"(o.gv[0][0]), "+v"(o.gv[0][1]), "+v"(o.gs[0]), "+v"(o.gv[1][0]), "+v"(o.gv[1][1]), "+v"(o.gs[1]) : "i"(FVW + 1) : "memory");
  static_assert(FVS == 0, "the last request of the final phase is a store");
  op_ldsBs<0>(o, make_iseq<9>{}); op_ldsBs<13>(o, make_iseq<22>{});
  asm volatile("ds_write_b128 %0, %1 offset:4096\n\tds_write_b128 %0, %2 offset:5120\n\tds_write_b32 %0, %3 offset:6144" :: "v"(o.lb5), "v"(o.gv[1][0]), "v"(o.gv[1][1]), "v"(o.gs[1]) : "memory");
  o.idx = (o.va & 128u) ? 0xeeeeeeeeu : 0x44444444u; o.D[0][4] = f32x4{0.f, 0.f, 0.f, 0.f};
  asm volatile("" : "+v"(o.idx), "+v"(o.D[0][4]));
  redo_zero<240>(0u, make_iseq<12>{});
  PS_AT(45); ps_touch(20u + wid);
  GAP();
  {
    u32x4 sr = o.srdL; sr[2] = o.sf + 4u;
    redo_reqs<35 + FNR>(o, make_iseq<OP_NR - 35 - FNR>{});
    asm volatile("s_waitcnt vmcnt(%7)\n\tbuffer_store_dword %2, %3, %4, %5 offen sc1\n\ts_waitcnt vmcnt(%7)\n\tv_cmp_eq_u32_e64 %0, %6, %1"
                 : "=s"(o.seen), "+v"(o.fo) : "v"(tag), "v"(o.va), "s"(sr), "s"(o.sf), "s"(tag), "i"(OP_NR - 34 - FNR) : "memory");
  }
  op_there(o);
  op_reqA0(o, make_iseq<OP_NI>{});
  GAP();
  unsigned blk = blockIdx.x;
  asm volatile("" : "+s"(blk));
  const unsigned sn = o.sn;
  asm volatile("s_waitcnt lgkmcnt(0)" : "+v"(o.fb[0]), "+v"(o.fb[1]), "+v"(o.fb[2]), "+v"(o.fb[3]), "+v"(o.fb[4]), "+v"(o.fb[5]), "+v"(o.fb[6]), "+v"(o.fb[7]), "+v"(o.fb[8]), "+v"(o.fb[9]), "+v"(o.fb[10]),
               "+v"(o.fb[11]), "+v"(o.fb[12]), "+v"(o.fb[13]), "+v"(o.fb[14]), "+v"(o.fb[15]), "+v"(o.fb[16]), "+v"(o.fb[17]), "+v"(o.fb[18]), "+v"(o.fb[19]), "+v"(o.fb[20]), "+v"(o.fb[21]) :: "memory");
  if (__builtin_expect(o.bad != 0u, 0)) {
    const unsigned lane = o.va >> 4, row0 = (o.sa >> 13 ^ 64u) & 2032u, ax[2] = {op_ax<0>(o.va), op_ax<1>(o.va)};
    unsigned selr, an[2] = {ax[0], ax[1]};
    wait_flag(o.srdS, 8u * (lane & 7u) + 4u, tag);
    wait_flag(o.srdS, 4u * H_X + 288u * ((blk >> 3) & 7u) + 16u * (lane < 18u ? lane : 0u), tag);
    asm volatile("buffer_load_dword %0, %1, %2, 0 offen sc1\n\ts_waitcnt vmcnt(0)" : "=v"(selr) : "v"(8u * (lane & 7u)), "s"(o.srdL) : "memory");
#pragma nounroll
    for (unsigned t = 0; t < (unsigned)NSEL; ++t) {
      const unsigned ro = (((unsigned)__builtin_amdgcn_readlane(selr, t) & 0xffffu) - row0) * XRS_;
#pragma unroll
      for (int j = 0; j < 2; ++j) { const unsigned d = ax[j] - ro; an[j] = d < XRS_ ? (SEQ + t - row0) * XRS_ + d : an[j]; }
    }
    asm volatile("buffer_load_dwordx4 %0, %3, %5, %6 offen sc1\n\tbuffer_load_dwordx4 %1, %3, %5, %6 offen offset:64 sc1\n\tbuffer_load_dword %2, %4, %5, %6 offen sc1\n\ts_waitcnt vmcnt(0)"
                 : "=&v"(o.gv[0][0]), "=&v"(o.gv[0][1]), "=&v"(o.gs[0]) : "v"(an[0]), "v"(an[1]), "s"(o.srdS), "s"(sn + WS_XN) : "memory");
  }
  __amdgpu_buffer_rsrc_t srdO; unsigned ax[2], fes, t;
#define SC(g, x, e) if constexpr (M == (g)) { x = (e); asm volatile("" : "+s"(x)); }
#define SA(g, x, e) if constexpr (M == (g)) x = (e);
#define VC(g, x, e) if constexpr (M == (g)) { x = (e); asm volatile("" : "+v"(x)); }
  auto prep = [&](auto M_) __attribute__((always_inline)) {
    constexpr int M = decltype(M_)::value;
    if constexpr (M < 6) { acc_write<op_ac(1, 1 + M / 2, 4) + 2 * (M & 1)>(0u); acc_write<op_ac(1, 1 + M / 2, 4) + 2 * (M & 1) + 1>(0u); }
    VC(6, o.ra[0], o.lb5 + BP_W * ((wid ^ 1u) - wid)) VC(7, o.ra[1], o.lb5 + BP_W * ((wid ^ 2u) - wid)) VC(8, o.ra[2], o.lb2 + 8192u * ((wid ^ 3u) - wid))
    SC(9, t, 5120u * wid + WS_EX) SC(10, o.sw[0], blk * 20480u + t) SC(10, o.sw[1], o.sw[0] + 4096u) SC(11, o.si[0], (blk ^ 4u) * 20480u + t) SC(11, o.si[1], o.si[0] + 4096u)
    SC(12, o.sg, 128u * blk + 4u * H_E) SC(12, fes, o.sg ^ 512u) SC(13, t, (o.sg + 4u) & ((int)(wid - 1u) >> 31)) SC(14, srdO, mk_srd(out, SEQ * DM * 2))
    VC(15, ax[0], op_ax<0>(o.va)) VC(16, ax[1], op_ax<1>(o.va))
  };
  asm volatile("ps"); prep(IC(9)); asm volatile("ps"); prep(IC(10)); asm volatile("ps"); prep(IC(11)); asm volatile("ps"); prep(IC(12)); asm volatile("ps"); prep(IC(13)); asm volatile("ps"); prep(IC(14));
  GAP();
  op_again<0, OP_NI>(o);
  o.sl += 128u; o.srdL[2] += 128u;
  asm volatile("ps");
  auto step = [&](auto ST) __attribute__((always_inline)) {
    constexpr int st = decltype(ST)::value, A = st % (OP_LA + 1);
    if constexpr (st == 16) {
    }
    if constexpr (st == 24) {
    }
    if constexpr (st == 30) {
    }
    if constexpr (st == OP_SD) {
      asm volatile("s_waitcnt vmcnt(%1)" : "+v"(fe) : "i"(OP_FEW) : "memory");
      if (__builtin_expect(!__all(fe == tag), 0)) wait_flag(o.srdS, fes, tag);
    }
    if constexpr (st == 8) { asm volatile("s_waitcnt vmcnt(%0)" :: "i"(OP_P4W) : "memory"); op_again<8, 4 * (8 - OP_DS)>(o); }
    if constexpr (st == 28) { o.D[1][4] = f32x4{0.f, 0.f, 0.f, 0.f}; asm volatile("s_nop 0" : "+v"(o.D[1][4])); }
    asm volatile("s_waitcnt vmcnt(%4)" : "+v"(o.fa[A][0]), "+v"(o.fa[A][1]), "+v"(o.fa[A][2]), "+v"(o.fa[A][3]) : "i"(OPW[st]) : "memory");
    if constexpr (st == OP_DS) asm volatile("" : "+v"(o.fo));
    if constexpr (st == 29) asm volatile("" : "+v"(o.gv[1][0]), "+v"(o.gv[1][1]), "+v"(o.gs[1]));
    GAP();
    op_qs<st>(o, prep, make_iseq<18>{});
    GAP();
    if constexpr (st == OP_SP) { const u32x4 sh = o.srdS; const unsigned so = fes; asm volatile("buffer_load_dword %0, off, %1, %2 sc1" : "=v"(fe) : "s"(sh), "s"(so) : "memory"); }
    if constexpr (st == OP_SF - 2) { o.tg = tag; o.srdL[2] = t; asm volatile("" : "+v"(o.tg), "+s"(o.srdL)); }
    GAP();
  };
  for_seq(step, make_iseq<32>{});
  op_qs<32>(o, prep, make_iseq<1>{});
  auto fin2 = [&](const float s0, const float s1, const int wd) __attribute__((always_inline)) {
    const unsigned sp = cvtpk(s0, s1), pp = cvtpk(bflo(sp) * o.gx[2 * wd], bfhi(sp) * o.gx[2 * wd + 1]);
    return cvtpk(o.gx[18 + 2 * wd] + bflo(pp), o.gx[19 + 2 * wd] + bfhi(pp));
  };
  auto fin = [&](auto J_) __attribute__((always_inline)) {
    constexpr int j = decltype(J_)::value;
    const f32x4 s = o.D[1][2 * j] + o.T[2 * j];
    const __amdgpu_buffer_rsrc_t so = srdO; const unsigned sc = sn, va = ax[j >> 1];
    if constexpr (j < 2) {
      const f32x4 r = o.D[1][2 * j + 1] + o.T[2 * j + 1];
      const u32x4 ov = {fin2(s[0], s[1], 4 * j), fin2(s[2], s[3], 4 * j + 1), fin2(r[0], r[1], 4 * j + 2), fin2(r[2], r[3], 4 * j + 3)};
      asm volatile("buffer_store_dwordx4 %0, %1, %2, %3 offen offset:%c4\n\ts_nop 1" :: "v"(ov), "v"(va), "s"(so), "s"(sc), "i"(64 * j) : "memory");
    } else {
      auto p0 = __builtin_amdgcn_permlane32_swap(__float_as_uint(s[0]), __float_as_uint(s[2]), false, false), p1 = __builtin_amdgcn_permlane32_swap(__float_as_uint(s[1]), __float_as_uint(s[3]), false, false);
      const unsigned ov = fin2(__uint_as_float(p0[0]) + __uint_as_float(p0[1]), __uint_as_float(p1[0]) + __uint_as_float(p1[1]), 8);
      asm volatile("buffer_store_dword %0, %1, %2, %3 offen" :: "v"(ov), "v"(va), "s"(so), "s"(sc) : "memory");
    }
  };
  fin(IC(2)); fin(IC(0));
  GAP();
  fin(IC(1));
}

extern "C" __global__ __launch_bounds__(256, 1) __attribute__((aligned(4096)))
void k_attn(char* __restrict__ ws, const u16* __restrict__ wq, const u16* __restrict__ xres, const u16* __restrict__ w_r, const u16* __restrict__ Kc, const u16* __restrict__ Vc,
            const u16* __restrict__ wo, const u16* __restrict__ wg, const u16* __restrict__ wu, const u16* __restrict__ wd, u16* __restrict__ out  ) {
  asm volatile(PS_MACROS ::: "memory");
  extern __shared__ __attribute__((aligned(16))) char lds[];
  const unsigned tid = threadIdx.x, lane = tid & 63u, wid = tid >> 6, l32 = lane & 31u, hi = lane >> 5;
  constexpr int T = NT;
  const float C = 0.12751743082f;
  W4C c;
  c.pdum = c.ldsb + PF_DUM;
  asm volatile("" : "+s"(c.pdum) : "s"((unsigned)(size_t)lds));
  c.m0k = rfl(c.ldsb + KRING + 4096u * wid); c.m0v = rfl(c.ldsb + VRING + 4096u * wid);
  c.sofk = rfl(KRS * 16u * wid) + (32u / 1) * KTILEB * ((blockIdx.x >> 3) & 0u);

  const unsigned blk = blockIdx.x, xg = blk & 7u, ii = blk >> 3;
  const unsigned kvh = 2u * xg + (ii >> 4), h = 2u * kvh + ((ii >> 3) & 1u), yb = ii & 7u;
  const unsigned row0 = 256u * yb + 64u * wid;
  const bool moe = blk < (unsigned)MOE_WG;
  const bool ret = !moe && blk < 208u;
  unsigned tag = 0u, gen;
  const unsigned lrow = 8u * blk + 2u * wid + hi;
  QFT qf; unsigned psc, cwo; u32x4 srdC;
  {
    QP p; LNJ lj; u32x4 cw; u32x2 g0, g1; unsigned pb0, pb1, pb2, pb3, csw, vg; unsigned long long pbase;
    lj.up = hi != 0u;
    const unsigned vl = 16u * l32 + XRS * hi, vl3 = 4u * l32 + XRS * hi, srow = XRS * 8u * blk + XRS * 2u * rfl(wid);
    {
      const unsigned r8 = (lane >> 2) & 7u, vws = WRS * ((r8 & 3u) + 8u * (r8 >> 2)) + 64u * hi + 16u * (lane & 3u);
      p.vw[0] = vws + 896u; p.vw[1] = vws; p.vw[2] = vws + (4u * WRS - 2048u + 896u); p.vw[3] = vws + (4u * WRS - 2048u);
#pragma unroll
      for (int q = 0; q < 4; ++q) p.vx[q] = XRS * (lane >> 3) + 16u * ((lane & 7u) ^ (4u * (q & 1) + (lane >> 4))) + (8u * XRS - 1024u) * q;
    }
    p.m0x = rfl(c.ldsb + 0x4000u + 8192u * wid);
    p.sofw = rfl(WRS * 16u * wid + 256u * h);
    p.sofx = rfl(XRS * row0);
    p.kk = (yb ^ (ii >> 3)) & 1u;
    asm volatile("s_nop 4" : "+s"(c.m0k), "+s"(c.m0v), "+s"(c.sofk), "+s"(p.m0x), "+s"(p.sofw), "+s"(p.sofx), "+s"(p.kk));
    asm volatile("" : "+v"(p.vw[0]), "+v"(p.vw[1]), "+v"(p.vw[2]), "+v"(p.vw[3]), "+v"(p.vx[0]), "+v"(p.vx[1]), "+v"(p.vx[2]), "+v"(p.vx[3]));
    GAP();
    p.m0w = c.m0k;
    p.srdW = __builtin_amdgcn_make_buffer_rsrc((void*)((const char*)wq - 896), (short)0, DM * HQ * DH * 2 + 896, SRD_WORD3);
    p.srdX = __builtin_amdgcn_make_buffer_rsrc((void*)xres, (short)0, SEQ * DM * 2, SRD_WORD3);
    auto vm = [&](auto I_) __attribute__((always_inline)) {
      constexpr int I = decltype(I_)::value; constexpr VOp o = VT.v[I]; constexpr bool nm = vq_newm0(I); constexpr int q = o.q & 3;
      if constexpr (o.kind == VK_S) {
        if constexpr (o.q == 0 && o.st < 2) qp_next<o.st>(p);
        if constexpr (o.q < 4) lds_dma<nm, 1024 * q>(p.m0w + QP_STG * (o.st % 3), p.vw[q], p.srdW, p.sw);
        else {
          if constexpr (o.q == 8) { p.sx += 32u * XRS; asm volatile("" : "+s"(p.sx)); }
          lds_dma<nm, 1024 * q>(p.m0x + QP_STG * (o.st % 3) + (o.q >= 8 ? 4096u : 0u), p.vx[q], p.srdX, p.sx);
        }
      } else if constexpr (o.kind == VK_W8) lds_dma<nm, 1024 * q>(p.m0w + QP_W8, p.vw[q], p.srdW, p.sw);
      else if constexpr (o.kind == VK_KV) {
        if constexpr (o.q < 8) lds_dma<nm, 1024 * q>(c.m0k + 0x4000u * (o.q >> 2), (q & 2) ? c.ksx[q & 1] : c.vksrc[q & 1], c.srdK, KTILEB * (o.q >> 2) + c.sofk);
        else lds_dma<true, 0>(c.m0v + 1024u * q, c.vvsrc, c.srdV, c.sofk + (896u + KRS * 8u * (q >> 1) + 128u * (q & 1)));
      } else if constexpr (o.kind == VK_V1) lds_dma<nm, 1024 * q>(c.m0v + 0x4000u, (q & 1) ? c.vvsrc : c.vvsrc0, c.srdV, KTILEB + c.sofk);
      else if constexpr (o.kind == VK_K2) lds_dma<nm, 1024 * q>(c.m0k, (q & 2) ? c.ksx[q & 1] : c.vksrc[q & 1], c.srdK, (KTILEB * 2u + c.sofk) & KMASK);
      else if constexpr (o.kind == VK_PF)
        asm volatile("s_mov_b32 m0, %0\n\ts_add_u32 %1, %1, %4\n\tbuffer_load_dwordx4 %2, %3, %1 offen sc1 lds\n\ts_add_u32 %0, %0, 0x400"
                     : "+s"(c.pfm0), "+s"(c.pfo) : "v"(c.pfv), "s"(c.srdP), "s"(c.pfst) : "m0", "memory", "scc");
      else if constexpr (o.kind == VK_CW) {
        unsigned long long pa; unsigned po;
        asm volatile("s_getpc_b64 s[20:21]\n\ts_add_u32 s20, s20, k_attn@rel32@lo+4\n\ts_addc_u32 s21, s21, k_attn@rel32@hi+12\n\ts_mov_b64 %0, s[20:21]" : "=s"(pa) :: "scc", "s20", "s21");
        asm volatile("s_mov_b32 %0, .Lk_end-k_attn" : "=s"(po));
        srdC = mk_srd4((const void*)pa, po); cwo = (blk >> 3) * 4096u + 1024u * rfl(wid);
        asm volatile("" : "+s"(srdC), "+s"(cwo));
        asm volatile("buffer_load_dwordx4 %0, %1, %2, %3 offen" : "=&v"(cw) : "v"(16u * lane), "s"(srdC), "s"(cwo) : "memory");
      } else if constexpr (o.kind == VK_GEN)
        asm volatile("buffer_load_dword %0, off, %1, 0 offset:%c2 sc1" : "=&v"(gen) : "s"(c.srdS), "i"(4 * H_G) : "memory");
      else if constexpr (o.kind == VK_A && o.q < 2)
        asm volatile("buffer_load_dwordx4 %0, %1, %2, %3 offen offset:%c4" : "=&v"(lj.a[o.q]) : "v"(vl), "s"(p.srdX), "s"(srow), "i"(512 * o.q) : "memory");
      else if constexpr (o.kind == VK_A) asm volatile("buffer_load_dword %0, %1, %2, %3 offen offset:1024" : "=&v"(lj.a2) : "v"(vl3), "s"(p.srdX), "s"(srow) : "memory");
      else if constexpr (o.kind == VK_WR && o.q < 2)
        asm volatile("buffer_load_dwordx4 %0, %1, %2, 0 offen offset:%c3" : "=&v"(lj.w[o.q]) : "v"(16u * l32), "s"(__builtin_amdgcn_make_buffer_rsrc((void*)w_r, (short)0, DM * 2, SRD_WORD3)), "i"(512 * o.q) : "memory");
      else if constexpr (o.kind == VK_WR)
        asm volatile("buffer_load_dword %0, %1, %2, 0 offen offset:1024" : "=&v"(lj.w2) : "v"(4u * l32), "s"(__builtin_amdgcn_make_buffer_rsrc((void*)w_r, (short)0, DM * 2, SRD_WORD3)) : "memory");
      else if constexpr (o.kind == VK_STS || o.kind == VK_KEY) {
        const u32x2 g = {o.kind == VK_STS ? lj.stat : lj.key, tag};
        asm volatile("buffer_store_dwordx2 %0, %1, %2, %3 offen sc1" :: "v"(g), "v"(vg),
                     "s"(c.srdS), "s"(o.kind == VK_STS ? WS_ST : WS_KEY) : "memory");
      } else if constexpr (o.kind == VK_GAT) {
        asm volatile("buffer_load_dwordx2 %0, %1, %2, %3 offen offset:%c4 sc1" : "=&v"(o.q ? g1 : g0) : "v"(8u * (row0 + l32)),
                     "s"(c.srdS), "s"(WS_ST), "i"(256 * o.q) : "memory");
      } else if constexpr (o.kind == VK_XNS) {
        if constexpr (o.q < 2) asm volatile("buffer_store_dwordx4 %0, %1, %2, %3 offen offset:%c4 sc1\n\ts_nop 1" :: "v"(lj.a[o.q & 1]), "v"(vl), "s"(c.srdS), "s"(srow + WS_XN), "i"(512 * o.q) : "memory");
        else                   asm volatile("buffer_store_dword %0, %1, %2, %3 offen offset:1024 sc1" :: "v"(lj.a2), "v"(vl3), "s"(c.srdS), "s"(srow + WS_XN) : "memory");
      }
    };
    auto hook = [&](auto P_) __attribute__((always_inline)) {
      constexpr int pos = decltype(P_)::value;
      if constexpr (pos == PS_B) ps_touch(psc);
      if constexpr (pos == PS_A) ps_touch(psc & 3u);
      if constexpr (pos == 1) {
        asm volatile("s_waitcnt vmcnt(%5)" : "+v"(gen), "+v"(lj.a[0]), "+v"(lj.a[1]), "+v"(lj.a2), "+v"(cw) : "i"(vq_wait(VK_CW, 0, pos + 1)) : "memory");
        tag = rfl(gen) * 1664525u + 1013904223u;
        asm volatile("" : "+s"(tag));
      } else if constexpr (pos == 32 + 13) asm volatile("" : "+v"(lj.w[0]), "+v"(lj.w[1]), "+v"(lj.w2));
      else if constexpr (pos == -13) {
        c.vkb[0] = c.ldsb + KRING + 256u * (lane & 15u) + 16u * ((lane >> 4) ^ (lane & 15u));
        asm volatile("" : "+v"(c.vkb[0]));
      } else if constexpr (pos == -14) {
#pragma unroll
        for (int j = 1; j < 4; ++j) c.vkb[j] = c.vkb[0] ^ (64u * j);
        asm volatile("" : "+v"(c.vkb[1]), "+v"(c.vkb[2]), "+v"(c.vkb[3]));
      } else if constexpr (pos == -15) {
        c.vvb[0] = c.ldsb + VRING + (((lane & 3u) << 3) | (((lane >> 2) & 3u) << 6) | (((lane >> 4) & 1u) * 288u) | (hi << 11));
        c.vvb[1] = c.vvb[0] ^ 32u;
        asm volatile("" : "+v"(c.vvb[0]), "+v"(c.vvb[1]));
      } else if constexpr (pos == -16) {
#pragma unroll
        for (int par = 0; par < 2; ++par) c.vksrc[par] = KRS * (lane >> 4) + 16u * ((lane & 15u) ^ (4u * par + (lane >> 4)));
        c.ksx[0] = c.vksrc[0] ^ 128u; c.ksx[1] = c.vksrc[1] ^ 128u;
        asm volatile("" : "+v"(c.vksrc[0]), "+v"(c.vksrc[1]), "+v"(c.ksx[0]), "+v"(c.ksx[1]));
      } else if constexpr (pos == -17) {
        c.vvsrc = KRS * ((lane >> 2) & 7u) + 64u * hi + 16u * ((lane & 3u) ^ ((lane >> 3) & 2u)); c.vvsrc0 = c.vvsrc + 896u;
        asm volatile("" : "+v"(c.vvsrc), "+v"(c.vvsrc0));
      } else if constexpr (pos == -12) {
        const unsigned rs = moe ? FF * 2u : ret ? DM * 2u : 64u;
        const unsigned r16 = ret ? ((lane >> 2) & 3u) | (lane & 16u) | ((lane >> 3) & 4u) : lane >> 2;
        c.pfv = rs * r16 + 16u * ((lane & 3u) ^ ((lane >> 4) & 2u));
        asm volatile("" : "+v"(c.pfv));
      } else if constexpr (pos == -10) {
        const unsigned w = rfl(wid), fb = (blk & 7u) * 18u + (blk >> 3);
        pb0 = 64u * fb + 16u * FF * 2u * (w & 1u); pb1 = ((blk - 144u) * 4u + w) * 18432u - 9216u * (w & 1u); pb2 = ((blk - 208u) * 4u + w) * 27648u; pb3 = 18432u * w;
        const u16 *a0 = wg, *a1 = wu;
        asm volatile("s_cmp_lt_u32 %1, 2\n\ts_cselect_b64 %0, %2, %3" : "=s"(pbase) : "s"(w), "s"(a0), "s"(a1) : "scc");
        asm volatile("" : "+s"(pb0), "+s"(pb1), "+s"(pb2), "+s"(pb3));
      } else if constexpr (pos == -11) {
        const u16 *a0 = wo, *a1 = wd; const unsigned b = blk;
        unsigned long long pb; unsigned nb, st, np;
        asm volatile("s_cmp_lt_u32 %8, 208\n\ts_cselect_b64 %0, %9, %10\n\ts_cselect_b32 %5, %5, %6\n\ts_mov_b32 %1, %12\n\ts_cselect_b32 %2, 64, %13\n\ts_cselect_b32 %7, %7, 0\n\ts_cselect_b32 %3, %11, %14\n\t"
                     "s_cmp_lt_u32 %8, 144\n\ts_cselect_b64 %0, %15, %0\n\ts_cselect_b32 %5, %4, %5\n\ts_cselect_b32 %1, %16, %1\n\ts_cselect_b32 %2, %17, %2"
                     : "=&s"(pb), "=&s"(nb), "=&s"(st), "=&s"(np), "+s"(pb0), "+s"(pb1), "+s"(pb2), "+s"(pb3)
                     : "s"(b), "s"(a0), "s"(a1), "s"(18432u), "s"(27648u), "s"(1024u), "s"(20480u), "s"(pbase), "s"(559u * (FF * 2u) + 64u), "s"(FF * 2u * 32u) : "scc");
        pbase = pb; pb0 = nb; pb2 = np; c.pfst = st;
      } else if constexpr (pos == -18) {
        c.srdP = mk_srd4((const void*)(pbase + pb1), pb0);
        c.pfo = 0u - c.pfst;
        c.pfm0 = c.ldsb + SD_BASE + pb3; c.pfend = c.pfm0 + pb2;
        asm volatile("" : "+s"(c.srdP), "+s"(c.pfst), "+s"(c.pfo), "+s"(c.pfm0), "+s"(c.pfend));
      } else if constexpr (pos == -19) {
        unsigned kvo = kvh; asm volatile("" : "+s"(kvo));
        c.srdK = __builtin_amdgcn_make_buffer_rsrc((void*)((const char*)Kc + (size_t)kvo * SEQ * 256), (short)0, SEQ * 256, SRD_WORD3);
        c.srdV = __builtin_amdgcn_make_buffer_rsrc((void*)((const char*)Vc + (size_t)kvo * SEQ * 256 - 896), (short)0, SEQ * 256 + 896, SRD_WORD3);
        asm volatile("" : "+s"(c.srdK), "+s"(c.srdV));
      } else if constexpr (pos == -20) {
        unsigned kvo = kvh; asm volatile("" : "+s"(kvo));
        const unsigned id = ((blk >> 3) & 15u) * 4u + rfl(wid);
        const u16 *a0 = Kc, *a1 = Vc; unsigned long long pb;
        asm volatile("s_cmp_lt_u32 %1, 32\n\ts_cselect_b64 %0, %2, %3" : "=s"(pb) : "s"(id), "s"(a0), "s"(a1) : "scc");
        c.srdF = __builtin_amdgcn_make_buffer_rsrc((void*)(pb + (size_t)kvo * SEQ * 256), (short)0, SEQ * 256, SRD_WORD3);
        c.sfo = (c.sofk & ~(KTILEB - 1u)) + KTILEB * (LAH + ((id >> 4) & 1u) - (id >> 5)) + 1024u * (id & 15u);
        asm volatile("" : "+s"(c.srdF), "+s"(c.sfo));
      } else if constexpr (pos == 192 + 0) {
        asm volatile("s_waitcnt vmcnt(%2)" : "+v"(g0), "+v"(g1) : "i"(vq_wait(VK_GAT, 0, pos + 1)) : "memory");
        if (__builtin_expect(!__all(g0[1] == tag && g1[1] == tag), 0)) {
          const unsigned stg = WS_ST + 8u * (row0 + l32); const u32x4 sr = c.srdS;
#pragma nounroll
          for (unsigned spin = 0; spin < (1u << 20) && !__all(g0[1] == tag && g1[1] == tag); ++spin) {
            __builtin_amdgcn_s_sleep(6);
            asm volatile("buffer_load_dwordx2 %0, %2, %3, 0 offen sc1\n\tbuffer_load_dwordx2 %1, %2, %3, 0 offen offset:256 sc1\n\ts_waitcnt vmcnt(0)" : "=&v"(g0), "=&v"(g1) : "v"(stg), "s"(sr) : "memory");
          }
        }
      } else if constexpr (pos == 192 + 2 || pos == 192 + 3) {
        constexpr int q = pos == 192 + 3;
        qf.a[q] = C * rcp_nr(bflo((q ? g1 : g0)[0]));
        asm volatile("" : "+v"(qf.a[q]));
      } else if constexpr (pos == 192 + 5 || pos == 192 + 25) {
        constexpr int q = pos == 192 + 25;
        const unsigned n = hi == 0u ? ((q ? g1 : g0)[0] >> 16) ^ 0x8000u : 0u;
        qf.nb[q] = u32x4{n | (n << 16), n, 0u, 0u};
        asm volatile("" : "+v"(qf.nb[q]));
      } else if constexpr (pos == 192 + 30) {
        const f32x4 cs = p.cs;
        asm volatile("ds_write_b128 %0, %1" :: "v"(csw + 0u), "v"(cs) : "memory");
      } else if constexpr (pos == -21) {
        vg = l32 == 0u ? 8u * lrow : 0x40000000u; asm volatile("" : "+v"(vg));
        unsigned a0 = 128u * wid + 64u * (lane & 1u) + 16u * (lane >> 4), a1 = 1024u + 16u * tid;
        asm volatile("" : "+v"(a0), "+v"(a1));
        csw = c.ldsb + QP_CS + ((lane & 14u) == 0u ? a0 : a1);
        asm volatile("" : "+v"(csw));
      }
    };
    asm volatile("" :: "s"(ws), "s"(Kc), "s"(Vc), "s"(wo), "s"(wg), "s"(wu), "s"(wd), "s"(out), "s"(w_r));
    vq_issue<-3>(vm);
    asm volatile("s_branch 1f\n.Lps_ret: s_setpc_b64 vcc\n.Lps_tab:\n.irp n,0,1,2,3,4,5,6,7,8,9,10,11,12,13,14,15,16,17,18,19,20,21,22,23\ns_branch .Lpe_\\n\n.endr\n1:" ::: "memory");
    psc = rfl(wid) + 16u - 4u * (((blk - 144u) >> 31) + ((blk - 208u) >> 31) + ((blk - RED1) >> 31));
    c.srdS = mk_srd4(ws, WS_END);
    asm volatile("" : "+s"(psc), "+s"(c.srdS));
    hook(IC(-5));
    hook(IC(-10)); hook(IC(-11)); hook(IC(-12)); hook(IC(-13)); hook(IC(-14)); hook(IC(-15)); hook(IC(-16)); hook(IC(-17)); hook(IC(-18)); hook(IC(-19)); hook(IC(-20)); hook(IC(-21));
    vq_issue<-2>(vm);
    GAP();
    vq_issue<-1>(vm);
    p.vwb = c.ldsb + ((((lane & 1u) << 5) | ((lane & 2u) << 2)) | (((lane >> 2) & 3u) << 6) | (lane & 16u) | (hi << 8));
#pragma unroll
    for (int ks = 0; ks < 4; ++ks) p.vxb[ks] = c.ldsb + 0x4000u + 8192u * wid + 128u * l32 + 16u * ((2u * ks + hi) ^ ((l32 >> 1) & 7u));
    p.vwc = p.vwb + 512u * wid;
    { const unsigned on = (lane & 15u) == ((lane >> 4) & 1u) ? 0x3f803f80u : 0u; p.sel = u32x4{on, on, on, on}; }
    GAP();
    asm volatile("s_waitcnt vmcnt(%0)\n\ts_barrier" :: "i"(vq_wait(VK_S, 0, 0)) : "memory");
    qp_rds<0, 8>(p, make_iseq<20>{});
    qp_iters(p, vm, hook, lj, lrow, make_iseq<7>{});
    asm volatile("s_waitcnt lgkmcnt(5)" ::: "memory");
#pragma unroll
    for (int d = 0; d < 4; ++d) qf.ca[d] = u32x4{0u, 0u, 0u, 0u};
    GAP();
    qj_gaps(p, c, qf, vm, c.ldsb + QP_CS + 4u * l32, make_iseq<NJ>{});
  }
  {
    const unsigned on = (lane & 2u) == 0u ? 0x3f803f80u : 0u; c.ONE = u32x4{on, on, on, on}; c.IDX = (lane & 1u) ? 0xeeeeeeeeu : 0x44444444u;
    c.L[0] = c.L[1] = c.L[2] = c.L[3] = f32x4{0.f, 0.f, 0.f, 0.f};
    asm volatile("" : "+v"(c.ONE), "+v"(c.IDX), "+v"(c.L[0]), "+v"(c.L[1]), "+v"(c.L[2]), "+v"(c.L[3]));
  }
  unsigned rstate = 3u;
  auto route = [&]() __attribute__((always_inline)) {
    unsigned lq;
    asm volatile("v_mbcnt_lo_u32_b32 %0, -1, 0\n\tv_mbcnt_hi_u32_b32 %0, -1, %0" : "=v"(lq));
    const unsigned w = rfl(wid), kb_ = c.ldsb + MO_KEY + 4096u * w;
    bool req = rstate == 0u;
    if (rstate == 1u) {
      u32x4 g[4];
      asm volatile("ds_read_b128 %0, %4\n\tds_read_b128 %1, %4 offset:1024\n\tds_read_b128 %2, %4 offset:2048\n\tds_read_b128 %3, %4 offset:3072\n\ts_waitcnt lgkmcnt(0)"
                   : "=&v"(g[0]), "=&v"(g[1]), "=&v"(g[2]), "=&v"(g[3]) : "v"(kb_ + 16u * lq) : "memory");
      bool ok = true;
#pragma unroll
      for (int j = 0; j < 4; ++j) ok = ok && g[j][1] == tag && g[j][3] == tag;
      if (__all(ok)) {
        unsigned k[8] = {g[0][0], g[0][2], g[1][0], g[1][2], g[2][0], g[2][2], g[3][0], g[3][2]};
        ce(k[0], k[1]); ce(k[2], k[3]); ce(k[4], k[5]); ce(k[6], k[7]);
        ce(k[0], k[2]); ce(k[1], k[3]); ce(k[4], k[6]); ce(k[5], k[7]);
        ce(k[1], k[2]); ce(k[5], k[6]); ce(k[0], k[4]); ce(k[3], k[7]);
        ce(k[1], k[5]); ce(k[2], k[6]);
        ce(k[1], k[4]); ce(k[3], k[6]);
        ce(k[2], k[4]); ce(k[3], k[5]);
        ce(k[3], k[4]);
        unsigned cnd = 0u;
#pragma nounroll
        for (unsigned r = 0; r < (unsigned)NSEL; ++r) {
          unsigned m = k[0], t;
          t = (unsigned)__builtin_amdgcn_update_dpp(0, (int)m, 0xB1, 0xf, 0xf, false); m = t > m ? t : m;
          t = (unsigned)__builtin_amdgcn_update_dpp(0, (int)m, 0x4E, 0xf, 0xf, false); m = t > m ? t : m;
          t = (unsigned)__builtin_amdgcn_update_dpp(0, (int)m, 0x141, 0xf, 0xf, false); m = t > m ? t : m;
          t = (unsigned)__builtin_amdgcn_update_dpp(0, (int)m, 0x140, 0xf, 0xf, false); m = t > m ? t : m;
          cnd = (lq & 15u) == (unsigned)r ? m : cnd;
          const bool pp = k[0] == m;
#pragma unroll
          for (int i = 0; i < 7; ++i) k[i] = pp ? k[i + 1] : k[i];
        }
        lds_st32(c.ldsb + MO_RS + ((lq & 8u) == 0u ? 64u + 128u * w + 2u * (lq & 48u) + 4u * (lq & 7u) : 640u + 4u * lq), cnd);
        asm volatile("s_waitcnt lgkmcnt(0)" ::: "memory");
        lds_st32(c.ldsb + MO_RS + 4u * w, 1u);
        rstate = 2u;
      } else req = true;
    } else if (rstate == 2u) {
      u32x4 fl;
      asm volatile("ds_read_b128 %0, %1\n\ts_waitcnt lgkmcnt(0)" : "=v"(fl) : "v"(c.ldsb + MO_RS) : "memory");
      if (rfl(fl[0] & fl[1] & fl[2] & fl[3]) != 0u) {
        unsigned cv, rank = 0u;
        asm volatile("ds_read_b32 %0, %1" : "=v"(cv) : "v"(c.ldsb + MO_RS + 64u + 128u * w + 4u * (lq & 31u)) : "memory");
#pragma nounroll
        for (unsigned h = 0; h < 4u; ++h) {
          u32x4 c4v[4];
          asm volatile("ds_read_b128 %0, %5\n\tds_read_b128 %1, %5 offset:16\n\tds_read_b128 %2, %5 offset:32\n\tds_read_b128 %3, %5 offset:48\n\ts_waitcnt lgkmcnt(0)"
                       : "=&v"(c4v[0]), "=&v"(c4v[1]), "=&v"(c4v[2]), "=&v"(c4v[3]), "+v"(cv) : "v"(c.ldsb + MO_RS + 64u + 256u * (lq >> 5) + 64u * h) : "memory");
#pragma unroll
          for (int j = 0; j < 4; ++j)
#pragma unroll
            for (int e = 0; e < 4; ++e) rank += c4v[j][e] > cv ? 1u : 0u;
        }
        auto pr = __builtin_amdgcn_permlane32_swap(rank, rank, false, false);
        rank = pr[0] + pr[1];
        {
          const float rs = rbf(__builtin_amdgcn_rcpf(1.f + __builtin_amdgcn_exp2f(key_val(cv) * -1.4426950408889634f)));
          const u32x2 gr = {(__float_as_uint(rs) & 0xffff0000u) | (unsigned)key_row(cv), tag};
          const unsigned vo = lq < 32u && rank < 8u ? 8u * rank : 0x40000000u, vg = (lq | w) == 0u ? 4u * H_G : 0x40000000u;
          asm volatile("s_nop 4\n\tbuffer_store_dwordx2 %0, %1, %3, 0 offen sc1\n\tbuffer_store_dword %4, %2, %3, 0 offen sc1\n\ts_nop 1" :: "v"(gr), "v"(vo), "v"(vg), "s"(c.srdS), "v"(gr[1]) : "memory");
        }
        rstate = 3u;
      }
    }
    if (req) {
#pragma unroll
      for (int j = 0; j < 4; ++j)
        asm volatile("s_mov_b32 m0, %0\n\ts_nop 4\n\tbuffer_load_dwordx4 %1, %2, %3 offen sc1 lds" :: "s"(kb_ + 1024u * j), "v"(16u * lq), "s"(c.srdS), "s"(WS_KEY + 4096u * w + 1024u * j) : "m0", "memory");
      rstate = 1u;
    }
  };
  const unsigned wids = rfl(wid);
  auto opq = [&](unsigned v) __attribute__((always_inline)) { asm volatile("" : "+s"(v)); return v; };
  auto lane_id = [&]() __attribute__((always_inline)) { unsigned z = 0u; asm volatile("" : "+s"(z)); return __builtin_amdgcn_mbcnt_hi(~0u, __builtin_amdgcn_mbcnt_lo(~0u, z)); };
  auto hand_req = [&](const unsigned m0, const unsigned word0, const unsigned nb) __attribute__((always_inline)) {
    const unsigned vo = c.vksrc[0]; u32x4 sr = c.srdS; sr[2] = 4u * word0 + nb;
    asm volatile("s_mov_b32 m0, %0\n\ts_nop 0\n\tbuffer_load_dwordx4 %1, %2, %3 offen sc1 lds" :: "s"(m0), "v"(vo), "s"(sr), "s"(4u * word0) : "m0", "memory");
  };
  auto pf_at = [&](auto M_, auto G_, auto A_) __attribute__((always_inline)) {
    constexpr int d = decltype(M_)::value - decltype(G_)::value;
    if constexpr (d == -decltype(A_)::value) asm volatile("s_add_u32 %0, %0, %1" : "+s"(c.pfo) : "s"(c.pfst) : "scc");
    if constexpr (d == -1) asm volatile("s_mov_b32 m0, %0" :: "s"(c.pfm0) : "m0", "memory");
    if constexpr (d == 0) { GAP(); c.pfm0 += 0x400u; asm volatile("" :: "s"(c.pfm0)); GAP(); asm volatile("buffer_load_dwordx4 %0, %1, %2 offen sc1 lds" :: "v"(c.pfv), "s"(c.srdP), "s"(c.pfo) : "memory"); }
  };
  auto m0_set = [&](const unsigned m0) __attribute__((always_inline)) { asm volatile("s_mov_b32 m0, %0" :: "s"(m0) : "m0", "memory"); };
  auto m0_imm = [&](auto C_) __attribute__((always_inline)) { asm volatile("s_mov_b32 m0, %0" :: "i"(decltype(C_)::value) : "m0", "memory"); };
  auto lim = [&](const unsigned n) __attribute__((always_inline)) { c.srdL[2] = n; asm volatile("" : "+s"(c.srdL)); };
  auto dma16 = [&](auto W_) __attribute__((always_inline)) { const unsigned vo = c.vksrc[0]; const u32x4 sr = c.srdL; if constexpr (decltype(W_)::value == 0) asm volatile("buffer_load_dwordx4 %0, %1, 0 offen sc1 lds" :: "v"(vo), "s"(sr) : "memory"); else asm volatile("buffer_load_dwordx4 %0, %1, %2 offen sc1 lds" :: "v"(vo), "s"(sr), "s"(4u * decltype(W_)::value) : "memory"); };
  auto flag_st = [&](const unsigned so, const unsigned tg) __attribute__((always_inline)) {
    const unsigned vo = c.vvsrc; const u32x4 sr = c.srdL; asm volatile("buffer_store_dword %0, %1, %2, %3 offen sc1" :: "v"(tg), "v"(vo), "s"(sr), "s"(so) : "memory");
  };
  auto warm_at = [&](auto M_, auto G_) __attribute__((always_inline)) {
    constexpr int d = decltype(M_)::value - decltype(G_)::value;
    if constexpr (d == -1) m0_set(c.pdum);
    if constexpr (d == 0) { const unsigned vo = c.vksrc[0], so = cwo; const u32x4 sr = srdC; asm volatile("buffer_load_dwordx4 %0, %1, %2 offen lds" :: "v"(vo), "s"(sr), "s"(so) : "memory"); }
  };
  auto is_cls = [&](const unsigned k) __attribute__((always_inline)) { asm volatile("" : "+s"(psc)); return psc == 4u * k; };
  int t1, t2;
  if constexpr (PS_B == 999) ps_touch(psc);
  GAP();
  phaseA_end<2>(c);
  {
    auto hook = [&](auto M_) __attribute__((always_inline)) {
      constexpr int M = decltype(M_)::value;
      warm_at(M_, IC(40));
      SC(FB(0), c.srdL, c.srdS) SC(2, psc, psc & ~3u) SC(4, t1, 0xcc860 >> psc) SC(5, t1, t1 & 15) SC(7, t2, 0x08000 >> psc) SC(9, t2, t2 & 15) SC(10, t2, t2 + 14)
    };
    phaseB_<1, true, true, 2>(c, hook, make_iseq<NGB>{});
  }
  GAP();
  if (__builtin_expect(opq(blk) == RTB, 0)) {
    {
      const unsigned w = opq(wids), kb_ = c.ldsb + MO_KEY + 4096u * w;
      RTS r; unsigned sk;
      auto hookA2 = [&](auto M_) __attribute__((always_inline)) {
        constexpr int M = decltype(M_)::value, n = fa_of(M);
        if constexpr (n == 0) { r.lq = lane_id(); asm volatile("" : "+v"(r.lq)); }
        if constexpr (n == 1) { sk = WS_KEY + 4096u * w; asm volatile("" : "+s"(sk)); }
        if constexpr (n == 2) { r.va = 16u * r.lq; m0_set(kb_); asm volatile("" : "+v"(r.va)); }
        if constexpr (n == 4 || n == 5 || n == 7 || n == 9) {
          constexpr int j = n == 4 ? 0 : n == 5 ? 1 : n == 7 ? 2 : 3;
          const unsigned v = r.va, so = sk; const u32x4 sr = c.srdS;
          asm volatile("buffer_load_dwordx4 %0, %1, %3 offen offset:%c2 sc1 lds" :: "v"(v), "s"(sr), "i"(1024 * j), "s"(so) : "memory");
        }
      };
      phaseAh<0, 7>(c, 2, hookA2);
      phaseB<0, 2>(c);
      auto hookA3 = [&](auto M_) __attribute__((always_inline)) { if constexpr (decltype(M_)::value == FA(0)) { r.va += kb_; asm volatile("" : "+v"(r.va)); } };
      phaseAh<1, 2>(c, 3, hookA3);
      auto steps = [&](auto I0_, auto G0_, auto M_) __attribute__((always_inline)) {
        constexpr int M = decltype(M_)::value, I = decltype(I0_)::value + M - 100 - decltype(G0_)::value;
        if constexpr (M >= 100 + decltype(G0_)::value && I < RT_END) rt_step<I>(r, tag);
      };
      auto hookB3 = [&](auto M_) __attribute__((always_inline)) {
        constexpr int M = decltype(M_)::value;
        if constexpr (M == 2 || M == 4 || M == 5 || M == 7) {
          constexpr int j = M == 2 ? 0 : M == 7 ? 3 : M - 3;
          u32x4& d = r.g[j]; const unsigned v = r.va;
          asm volatile("ds_read_b128 %0, %1 offset:%2" : "=v"(d) : "v"(v), "i"(1024 * j) : "memory");
        }
        if constexpr (M == FB(0)) { u32x4 (&g)[4] = r.g; asm volatile("s_waitcnt lgkmcnt(%4)" : "+v"(g[0]), "+v"(g[1]), "+v"(g[2]), "+v"(g[3]) : "i"(kyo(7, FB(0))) : "memory"); }
        steps(IC(0), IC(FB(0)), M_);
      };
      phaseBh<1, 2>(c, hookB3);
      auto hookA4 = [&](auto M_) __attribute__((always_inline)) {
        constexpr int M = decltype(M_)::value;
        steps(IC(NGB - FB(0)), IC(0), M_);
        if constexpr (M == FA(6)) { r.va = (r.lq & 8u) == 0u ? 64u + 128u * w + 2u * (r.lq & 48u) + 4u * (r.lq & 7u) : 640u + 4u * r.lq; asm volatile("" : "+v"(r.va)); }
        if constexpr (M == FA(8)) lds_st32(c.ldsb + MO_RS + r.va, r.cnd);
        if constexpr (M == FA(9)) lds_st32(c.ldsb + MO_RS + 4u * w, __all(r.bad == 0u) ? 1u : 0u);
        if constexpr (M == FA(10)) { r.va = c.ldsb + MO_RS + 64u + 256u * (r.lq >> 5); r.m = c.ldsb + MO_RS + 64u + 128u * w + 4u * (r.lq & 31u); r.rank = 0u; asm volatile("" : "+v"(r.va), "+v"(r.m), "+v"(r.rank)); }
      };
      static_assert(NGB - FB(0) + FA(6) >= RT_END, "");
      phaseAh<0, 3>(c, 4, hookA4);
      auto hookB4 = [&](auto M_) __attribute__((always_inline)) {
        constexpr int M = decltype(M_)::value;
        if constexpr (M == 0) { unsigned& d = r.cv; u32x4& f = r.g[3]; const unsigned v = r.m, v2 = c.ldsb + MO_RS; asm volatile("ds_read_b32 %0, %2\n\tds_read_b128 %1, %3" : "=&v"(d), "=&v"(f) : "v"(v), "v"(v2) : "memory"); }
        if constexpr (M < 60 && M % 4 == 0) { u32x4& d = r.g[(M >> 2) % 3]; const unsigned v = r.va; asm volatile("ds_read_b128 %0, %1 offset:%2" : "=v"(d) : "v"(v), "i"(4 * M) : "memory"); }
        if constexpr (M >= 5 && M < 65 && M % 4 == 1) { u32x4 &d = r.g[((M - 5) >> 2) % 3], &f = r.g[3]; unsigned& cv = r.cv; asm volatile("s_waitcnt lgkmcnt(%3)" : "+v"(d), "+v"(cv), "+v"(f) : "i"(kyo(M - 5, M) + (M < 61)) : "memory"); }
        if constexpr (M >= 105 && M < 165) { unsigned long long& pm = r.pm[M & 1]; const unsigned a = r.g[((M - 105) >> 2) % 3][(M - 105) & 3], cv = r.cv; asm volatile("v_cmp_gt_u32_e64 %0, %1, %2" : "=s"(pm) : "v"(a), "v"(cv)); }
        if constexpr (M >= 106 && M < 166) cnt_s(r.rank, r.pm[(M - 1) & 1]);
        if constexpr (M == 165) { r.bad = r.g[3][0] & r.g[3][1] & r.g[3][2] & r.g[3][3]; asm volatile("" : "+v"(r.bad)); }
        if constexpr (M == 66) { u32x4& d = r.g[0]; const unsigned v = r.va; asm volatile("ds_read_b128 %0, %1 offset:240" : "=v"(d) : "v"(v) : "memory"); }
      };
      phaseBh<0, 2>(c, hookB4);
      auto hookA5 = [&](auto M_) __attribute__((always_inline)) {
        constexpr int M = decltype(M_)::value;
        if constexpr (M == 4) { u32x4& d = r.g[0]; asm volatile("s_waitcnt lgkmcnt(3)" : "+v"(d) :: "memory"); }
        if constexpr (M >= 104 && M < 108) { r.rank += r.g[0][M - 104] > r.cv ? 1u : 0u; asm volatile("" : "+v"(r.rank)); }
        if constexpr (M == FA(1)) { auto pr = __builtin_amdgcn_permlane32_swap(r.rank, r.rank, false, false); r.rank = pr[0] + pr[1]; asm volatile("" : "+v"(r.rank)); }
        if constexpr (M == FA(2)) { r.rs = key_val(r.cv) * -1.4426950408889634f; asm volatile("" : "+v"(r.rs)); }
        if constexpr (M == FA(3)) { r.rs = 1.f + __builtin_amdgcn_exp2f(r.rs); asm volatile("" : "+v"(r.rs)); }
        if constexpr (M == FA(4)) { r.rs = rbf(__builtin_amdgcn_rcpf(r.rs)); asm volatile("" : "+v"(r.rs)); }
        if constexpr (M == FA(5)) { r.m = (__float_as_uint(r.rs) & 0xffff0000u) | (unsigned)key_row(r.cv); r.bad = (r.bad - 1u) & 0x40000000u; asm volatile("" : "+v"(r.m), "+v"(r.bad)); }
        if constexpr (M == FA(7)) { r.va = (r.lq < 32u && r.rank < 8u ? 8u * r.rank : 0x40000000u) | r.bad; asm volatile("" : "+v"(r.va)); }
        if constexpr (M == 100 + FA(8)) { r.cnd = ((r.lq | w) == 0u ? 4u * H_G : 0x40000000u) | r.bad; asm volatile("" : "+v"(r.cnd)); }
        if constexpr (M == FA(9)) {
          const u32x2 gr = {r.m, tag}; const unsigned vo = r.va, vg = r.cnd; const u32x4 sr = c.srdS;
          asm volatile("buffer_store_dwordx2 %0, %1, %3, 0 offen sc1\n\tbuffer_store_dword %4, %2, %3, 0 offen sc1" :: "v"(gr), "v"(vo), "v"(vg), "s"(sr), "v"(gr[1]) : "memory");
        }
      };
      phaseAh<1, 4>(c, 5, hookA5);
      rstate = rfl(r.bad) ? 0u : 3u;
      if (rstate == 0u) lds_st32(c.ldsb + MO_RS + 4u * w, 0u);
      phaseB<1, 2>(c);
    }
#pragma nounroll
    for (int t = 6; t < 14; t += 2) {
      phaseA<0, 3>(c, t);     phaseB<0, 2>(c);
      phaseA<1, 2>(c, t + 1); phaseB<1, 2>(c);
      if (rstate != 3u) {
        route();
        if (t == 12)
#pragma nounroll
          for (unsigned spin = 0; spin < (1u << 20) && rstate != 3u; ++spin) {
            asm volatile("s_waitcnt vmcnt(0)" ::: "memory");
            route();
            __builtin_amdgcn_s_sleep(8);
          }
      }
      GAP();
    }
    t1 = 0;
  }
  GAP();
  {
#pragma nounroll
    for (int t = 0; __builtin_expect(t != t1, 1);) {
      t += 2;
      phaseA<0, 3>(c, t);     phaseB<0, 2>(c);
      phaseA<1, 2>(c, t + 1); phaseB<1, 2>(c);
    }
  }
  GAP();
  constexpr auto SG = [](int i) constexpr { constexpr int g[10] = {14, 20, 26, 31, 36, 41, 46, 52, 57, 61}; return g[i + 1]; };
  constexpr auto sg_of = [](int m) constexpr { constexpr int g[10] = {14, 20, 26, 31, 36, 41, 46, 52, 57, 61}; for (int i = 0; i < 10; ++i) if (g[i] == m) return i - 1; return -2; };
  if (__builtin_expect(is_cls(1u), 1)) {
    PS_AT(40);
    unsigned w, b, fbb, s0, s1, sx, mx, va, vo, vw, vx;
    {
      auto hook = [&](auto M_) __attribute__((always_inline)) {
        constexpr int M = decltype(M_)::value;
        SC(0, w, wids) SC(1, b, blk) SC(2, fbb, b & 7u) SC(3, fbb, fbb * 18u) SC(4, s0, b >> 3) SC(5, fbb, fbb + s0)
        SC(7, sx, w << 7) SC(8, sx, sx + WS_XN) SC(9, mx, w << 10) SC(10, mx, mx + MO_XR)
        SC(11, s0, w >> 1) SC(12, s0, s0 * 36864u) SC(13, s0, s0 + SD_BASE) SC(15, s1, w & 1u) SC(16, s1, s1 << 5)
        VC(FA(0), va, (c.vksrc[0] >> 1) & 0x38u) VC(FA(2), vo, (c.vksrc[0] >> 1) & 64u) VC(FA(4), vx, (c.vksrc[0] >> 4) & 0x30u) VC(FA(5), vo, vo | vx) VC(FA(5), va, va + MO_C)
        VC(FA(9), vw, c.vvb[0] & 0xffu) VC(FA(9), vx, c.vvb[0] & 32u) VC(FA(11), vw, vw + 16u * vx) VC(FA(11), vw, vw ^ s1)
      };
      phaseAh<0, 3>(c, 8, hook);
    }
    {
      auto hook = [&](auto M_) __attribute__((always_inline)) {
        constexpr int M = decltype(M_)::value;
        if constexpr (M == 38) lim(64u);
        if constexpr (M == 39) m0_imm(IC(MO_C));
        if constexpr (M == 40) dma16(IC(0));
        VC(FB(0), vx, 9u * (c.vvb[0] & 2048u)) VC(FB(1), vw, vw + vx + s0) VC(FB(1), vx, c.vksrc[0] + MO_XR)
      };
      phaseBh<0, 2>(c, hook);
    }
    {
      u32x2 sv; unsigned long long ok;
      constexpr auto yo = [](int g0, int g1) constexpr { return 2 * (pcnt(PA0, 'T', g1) - pcnt(PA0, 'T', g0)) + pcnt(PA0, 'X', g1) - pcnt(PA0, 'X', g0); };
      auto hook = [&](auto M_) __attribute__((always_inline)) {
        constexpr int M = decltype(M_)::value;
        if constexpr (M == FA(5)) asm volatile("s_waitcnt vmcnt(2)\n\tds_read_b64 %0, %1" : "=v"(sv) : "v"(va) : "memory");
        if constexpr (M == 100 + FA(6)) { asm volatile("s_waitcnt lgkmcnt(%1)" : "+v"(sv) : "i"(yo(FA(5), FA(6))) : "memory"); ok = __ballot(sv[1] != tag); asm volatile("" : "+s"(ok)); }
        if constexpr (M == 39) {
          if (__builtin_expect(ok != 0ull, 0)) {
#pragma nounroll
            for (unsigned spin = 0; spin < (1u << 20) && !__all(sv[1] == tag); ++spin) {
              __builtin_amdgcn_s_sleep(8);
              hand_req(c.ldsb + MO_C, 0u, 64u);
              asm volatile("s_waitcnt vmcnt(0)\n\tds_read_b64 %0, %1\n\ts_waitcnt lgkmcnt(0)" : "=v"(sv) : "v"(va) : "memory");
            }
          }
        }
        VC(141, va, sv[0] & 0xffffu) VC(143, vo, __umul24(va, XRS) + vo)
        if constexpr (M == 46 || M == 52 || M == 62) m0_set(mx);
        SC(49, mx, mx + 4096u) SC(50, sx, sx + 512u) SC(55, mx, mx + 4096u) SC(56, sx, sx + 512u)
        if constexpr (M == 100 + FA(8) || M == FA(9) || M == FA(11)) { const unsigned v = vo, so = sx; const u32x4 r = c.srdS; asm volatile("buffer_load_dwordx4 %0, %1, %2 offen sc1 lds" :: "v"(v), "s"(r), "s"(so) : "memory"); }
      };
      phaseAh<1, 5>(c, 9, hook);
    }
    unsigned vd, vyo, sy;
    {
      unsigned lq;
      auto hook = [&](auto M_) __attribute__((always_inline)) {
        constexpr int M = decltype(M_)::value;
        VC(FB(0), lq, lane_id()) VC(FB(1), c.pfv, 16u * (lq & 3u)) VC(FB(3), c.pfv, __umul24((lq >> 2) & 3u, XRS) + c.pfv) VC(DB(8), c.pfv, 2u * (lq & 32u) + c.pfv)
        VC(FB(4), c.pfv, __umul24(lq & 16u, XRS) + c.pfv) VC(DB(9), vyo, (lq & 48u) << 3) VC(FB(6), vyo, ((lq & 7u) << 4) | vyo) VC(FB(8), vyo, ((lq & 8u) << 27) | vyo)
        SA(2, c.pfo, sha<3>(fbb, w)) SC(4, c.pfo, c.pfo * (4u * XRS)) SC(5, c.pfo, c.pfo - 128u) SC(7, c.pfst, 128u) SC(9, c.pfm0, 18432u * w) SC(10, c.pfm0, c.pfm0 + SD_BASE)
        SC(12, sy, fbb * 36u) SC(15, sy, sy + w) SC(18, sy, sy << 9) SC(20, sy, sy + WS_YP) SC(33, c.srdP, mk_srd4(wd, DM * FF * 2))
      };
      phaseBh<1, 0>(c, hook);
    }
    f32x4 gu;
    auto gate_up = [&](auto H_) __attribute__((always_inline)) {
      constexpr int HH = decltype(H_)::value;
      bf16x4 wl[2], wh[2]; bf16x8 xf[2];
      auto hook = [&](auto M_) __attribute__((always_inline)) {
        constexpr int M = decltype(M_)::value, j = sg_of(M % 100);
        if constexpr (M < 100 && j >= 0) asm volatile("s_waitcnt lgkmcnt(%3)" : "+v"(wl[j & 1]), "+v"(wh[j & 1]), "+v"(xf[j & 1]) : "i"(kyo(SG(j - 1) + 1, M)) : "memory");
        if constexpr (M >= 100 && j >= 0) {
          const bf16x8 wfr = __builtin_shufflevector(wl[j & 1], wh[j & 1], 0, 1, 2, 3, 4, 5, 6, 7);
          if constexpr (HH == 0 && j == 0) asm volatile("mf16 %0, %1, %2, 0" : "=&v"(gu) : "v"(wfr), "v"(xf[j & 1]));
          else                             asm volatile("mf16 %0, %1, %2, %0" : "+v"(gu) : "v"(wfr), "v"(xf[j & 1]));
        }
        if constexpr (M >= 100 && j >= -1 && j < 8) {
          constexpr int n = (j + 1) & 1, i = 9 * HH + j + 1;
          asm volatile("ds_read_b64_tr_b16 %0, %1 offset:%2" : "=v"(wl[n]) : "v"(vw), "i"(1024 * i) : "memory");
          asm volatile("ds_read_b64_tr_b16 %0, %1 offset:%2" : "=v"(wh[n]) : "v"(vw), "i"(1024 * i + 256) : "memory");
          asm volatile("ds_read_b128 %0, %1 offset:%2" : "=v"(xf[n]) : "v"(vx), "i"(1024 * (i >> 1) + 128 * (i & 1)) : "memory");
        }
        if constexpr (HH == 1) {
          pf_at(M_, IC(DB(8)), IC(1)); pf_at(M_, IC(DB(9)), IC(1)); pf_at(M_, IC(FB(6)), IC(2)); pf_at(M_, IC(FB(8)), IC(2));
        }
      };
      phaseBh<HH, 0>(c, hook);
    };
    unsigned vh;
    {
      auto hook = [&](auto M_) __attribute__((always_inline)) {
        constexpr int M = decltype(M_)::value;
        SC(0, s0, w >> 1) SC(1, s0, s0 << 9) SC(2, mx, s0 + s1) SC(3, mx, mx + SD_BASE) SC(4, s1, s1 >> 2) SA(5, s0, sha<1>(s0, s1)) SC(7, s0, s0 + MO_H) SC(8, s1, w << 10) SA(9, sx, (shi<2, MO_HH>(w)))
        VC(FA(0), vd, 72u * (c.vksrc[0] & 0x300u)) VC(FA(2), vd, vd + (c.vvb[0] & 0xdfu) + mx)
      };
      phaseAh<0, 1>(c, 10, hook);
    }
    gate_up(std::integral_constant<int, 0>{});
    phaseA<1, 0>(c, 11);
    gate_up(std::integral_constant<int, 1>{});
    {
      auto hook = [&](auto M_) __attribute__((always_inline)) {
        constexpr int M = decltype(M_)::value;
        pf_at(M_, IC(FA(0)), IC(2)); pf_at(M_, IC(FA(2)), IC(2)); pf_at(M_, IC(FA(4)), IC(2)); pf_at(M_, IC(FA(5)), IC(2)); pf_at(M_, IC(FA(11)), IC(2));
        VC(100 + FA(6), vx, vx + s1) VC(100 + FA(8), vh, c.vksrc[0] + s0)
        if constexpr (M == FA(9)) { const f32x4 g4 = gu; const unsigned v = vx; asm volatile("ds_write_b128 %0, %1 offset:%2" :: "v"(v), "v"(g4), "i"(MO_H - MO_XR) : "memory"); }
      };
      phaseAh<0, 10>(c, 12, hook);
    }
    {
      f32x2 g, u, s;
      auto hook = [&](auto M_) __attribute__((always_inline)) {
        constexpr int M = decltype(M_)::value;
        if constexpr (M == FB(0)) asm volatile("ds_read_b64 %0, %2\n\tds_read_b64 %1, %2 offset:2048" : "=&v"(g), "=&v"(u) : "v"(vh) : "memory");
        if constexpr (M == 116) { vh = c.vksrc[0] + sx; asm volatile("" : "+v"(vh)); }
        if constexpr (M == FB(1)) {
          asm volatile("s_waitcnt lgkmcnt(%2)" : "+v"(g), "+v"(u) : "i"(kyo(FB(0), FB(1))) : "memory");
          const unsigned pg = cvtpk(g[0], g[1]);
          g[0] = bflo(pg); g[1] = bfhi(pg);
          asm volatile("" : "+v"(g));
        }
        if constexpr (M == FB(2)) { const unsigned pu = cvtpk(u[0], u[1]); u[0] = bflo(pu); u[1] = bfhi(pu); asm volatile("" : "+v"(u)); }
        if constexpr (M == FB(3)) { s[0] = g[0] * -1.4426950408889634f; s[1] = g[1] * -1.4426950408889634f; asm volatile("" : "+v"(s)); }
        if constexpr (M == DB(8) || M == FB(4)) { constexpr int e = M == FB(4); s[e] = __builtin_amdgcn_exp2f(s[e]); asm volatile("" : "+v"(s)); }
        if constexpr (M == DB(9)) { float s0 = s[0], s1 = s[1]; add_v(s0, 1.f); add_v(s1, 1.f); s = f32x2{s0, s1}; }
        if constexpr (M == FB(5) || M == FB(6)) { constexpr int e = M == FB(6); s[e] = __builtin_amdgcn_rcpf(s[e]); asm volatile("" : "+v"(s)); }
        if constexpr (M == FB(7)) { float g0 = g[0], g1 = g[1]; mul_v(g0, s[0]); mul_v(g1, s[1]); g = f32x2{g0, g1}; }
        if constexpr (M == FB(8)) { const unsigned ps = cvtpk(g[0], g[1]); g[0] = bflo(ps); g[1] = bfhi(ps); asm volatile("" : "+v"(g)); }
        if constexpr (M == 164 || M == 165) { float x = u[M & 1]; mul_v(x, g[M & 1]); u[M & 1] = x; }
        if constexpr (M == FB(9)) { const unsigned hv = cvtpk(u[0], u[1]); asm volatile("ds_write_b32 %0, %1" :: "v"(vh), "v"(hv) : "memory"); }
      };
      phaseBh<0, 0>(c, hook);
    }
    {
      bf16x4 dl[2], dh[2]; f32x4 y[3]; u32x4 hwv;
      { auto hookA = [&](auto M_) __attribute__((always_inline)) { constexpr int M = decltype(M_)::value; VC(FA(0), vh, c.vksrc[0] + MO_HH) }; phaseAh<1, 0>(c, 13, hookA); }
      constexpr auto st_of = [](int m) constexpr { constexpr int g[9] = {25, 33, 42, 43, 47, 50, 56, 62, 67}; for (int i = 0; i < 9; ++i) if (g[i] == m) return i; return -1; };
      auto hook = [&](auto M_) __attribute__((always_inline)) {
        constexpr int M = decltype(M_)::value, i = sg_of(M % 100), is = st_of(M), ik = st_of(M - 101);
        if constexpr (M == FB(0)) asm volatile("ds_read_b128 %0, %1" : "=v"(hwv) : "v"(vh) : "memory");
        if constexpr (M < 100 && i >= 0) asm volatile("s_waitcnt lgkmcnt(%3)" : "+v"(dl[i & 1]), "+v"(dh[i & 1]), "+v"(hwv) : "i"(kyo(SG(i - 1) + 1, M)) : "memory");
        if constexpr (M >= 100 && i >= 0) {
          const bf16x8 d8 = __builtin_shufflevector(dl[i & 1], dh[i & 1], 0, 1, 2, 3, 4, 5, 6, 7);
          asm volatile("mf16 %0, %1, %2, 0" : "=&v"(y[i % 3]) : "v"(d8), "v"(hwv));
        }
        if constexpr (M >= 100 && i >= -1 && i < 8) {
          asm volatile("ds_read_b64_tr_b16 %0, %1 offset:%2" : "=v"(dl[(i + 1) & 1]) : "v"(vd), "i"(1024 * (i + 1)) : "memory");
          asm volatile("ds_read_b64_tr_b16 %0, %1 offset:%2" : "=v"(dh[(i + 1) & 1]) : "v"(vd), "i"(1024 * (i + 1) + 256) : "memory");
        }
        if constexpr (is >= 0) {
          const unsigned vo = vyo; const u32x4 sr = c.srdS; const f32x4 yv = y[is % 3]; const unsigned so = sy + 4096u * (is >> 1);
          if constexpr (is < 8) asm volatile("buffer_store_dwordx4 %0, %1, %2, %3 offen offset:%c4 sc1" :: "v"(yv), "v"(vo), "s"(sr), "s"(so), "i"(2048 * (is & 1)) : "memory");
          else                  asm volatile("buffer_store_dwordx4 %0, %1, %2, %3 offen sc1\n\ts_nop 1" :: "v"(yv), "v"(vo), "s"(sr), "s"(so) : "memory");
        }
        if constexpr (ik >= 0) { const f32x4 yv = y[ik % 3]; asm volatile("" :: "v"(yv)); }
      };
      phaseBh<1, 0>(c, hook);
    }
  }
  GAP();
  PS_AT(41);
  if (__builtin_expect(is_cls(2u), 0)) {
    PS_AT(42);
    constexpr unsigned NTS = 128u * 4608u;
    unsigned w, b, vw[2], v0, v1, vs, vn, st0, v0s, so;
    bf16x4 wl[2], wh[2];
    auto rd = [&](auto I_) __attribute__((always_inline)) {
      constexpr int i = decltype(I_)::value;
      bf16x4 &l = wl[i & 1], &h = wh[i & 1]; const unsigned v = vw[i & 1];
      asm volatile("ds_read_b64_tr_b16 %0, %1 offset:%2" : "=v"(l) : "v"(v), "i"(1024 * (i >> 1)) : "memory");
      asm volatile("ds_read_b64_tr_b16 %0, %1 offset:%2" : "=v"(h) : "v"(v), "i"(1024 * (i >> 1) + 256) : "memory");
    };
    auto st = [&](auto I_, auto P_, auto Y_) __attribute__((always_inline)) {
      constexpr int i = decltype(I_)::value, P = decltype(P_)::value, u = 2 * i, r = u % 9;
      constexpr auto at = [](int x) constexpr { return (unsigned)(x / 9) * NTS + 2048u * ((x % 9) >> 2) + 64u * ((x % 9) & 3); };
      const u32x4 sr = c.srdS;
      if constexpr (P == 0) { so = st0 + at(r == 8 ? u + 1 : u); asm volatile("" : "+s"(so)); }
      if constexpr (P == 1) {
        asm volatile("s_waitcnt lgkmcnt(%2)" : "+v"(wl[i & 1]), "+v"(wh[i & 1]) : "i"(decltype(Y_)::value) : "memory");
        const u32x4 x = __builtin_bit_cast(u32x4, __builtin_shufflevector(wl[i & 1], wh[i & 1], 0, 1, 2, 3, 4, 5, 6, 7));
        const unsigned vo = r == 8 ? v0 | (v1 ^ 0x40000000u) : r == 7 ? v0 | v1 : (r & 3) == 3 ? vn + (v1 >> 30) * 1792u : vn, s_ = so;
        asm volatile("buffer_store_dwordx4 %0, %1, %2, %3 offen sc1" :: "v"(x), "v"(vo), "s"(sr), "s"(s_) : "memory");
      }
      if constexpr (P == 2) {
        const u32x4 x = __builtin_bit_cast(u32x4, __builtin_shufflevector(wl[i & 1], wh[i & 1], 0, 1, 2, 3, 4, 5, 6, 7));
        if constexpr (r >= 7) {
          const u32x2 e = {x[0], x[2]}, o2 = {x[1], x[3]}; const unsigned v2 = vs | (r == 8 ? v1 : v1 ^ 0x40000000u), s0 = st0;
          asm volatile("buffer_store_dwordx2 %0, %2, %3, %4 offen sc1\n\tbuffer_store_dwordx2 %1, %2, %3, %4 offen offset:128 sc1" :: "v"(e), "v"(o2), "v"(v2), "s"(sr), "s"(s0 + (unsigned)(u / 9) * NTS) : "memory");
        } else asm volatile("" :: "v"(x));
      }
    };
    auto hookB = [&](auto I0_, auto M_) __attribute__((always_inline)) {
      constexpr int M = decltype(M_)::value, i0 = decltype(I0_)::value;
      constexpr int rg[6] = {7, 16, 33, 42, 50, 53}, sg[6] = {14, 25, 43, 48, 56, 62};
      auto one = [&](auto N_) __attribute__((always_inline)) {
        constexpr int n = decltype(N_)::value;
        if constexpr (M == rg[n]) rd(IC(i0 + n));
        if constexpr (M == sg[n] - 1) st(IC(i0 + n), IC(0), IC(0));
        if constexpr (M == sg[n]) st(IC(i0 + n), IC(1), IC(kyo(rg[n], sg[n]) + (n < 5 && rg[n < 5 ? n + 1 : n] < sg[n] ? 2 : 0)));
        if constexpr (M == 101 + sg[n]) st(IC(i0 + n), IC(2), IC(0));
      };
      for_seq(one, make_iseq<6>{});
    };
    {
      auto hook = [&](auto M_) __attribute__((always_inline)) {
        constexpr int M = decltype(M_)::value;
        SC(0, b, blk) SC(1, b, b - 144u) SC(6, w, wids) SC(2, st0, b & 51u) SC(3, so, b & 4u) SA(4, st0, sha<1>(so, st0)) SC(5, so, (b >> 3) & 1u) SA(7, st0, sha<2>(so, st0)) SC(8, so, w & 1u) SC(9, v0s, so << 8)
        SC(10, st0, st0 + v0s) SC(11, st0, st0 * 9216u) SC(12, st0, st0 + WS_WOT) SC(13, v0s, w & 2u) SA(15, so, sha<1>(v0s, so)) SC(16, so, so * 9216u) SC(17, so, so + SD_BASE) SC(18, v0s, v0s << 8)
        VC(FA(0), vw[0], c.vvb[0] & 0xffu) VC(FA(0), v0, c.vvb[0] & 32u) VC(FA(2), vw[0], vw[0] + 16u * v0) VC(FA(2), v0, c.vvb[0] & 2048u) VC(120, vw[0], __umul24(v0, 9u) + vw[0])
        VC(FA(4), vw[0], vw[0] + so) VC(FA(4), vw[1], vw[0] ^ 32u) VC(FA(5), v1, lane_id())
        VC(132, vs, v1 & 7u) VC(135, v0, v1 & 32u) VC(100 + FA(6), vs, 2u * vs + v0) VC(138, v0, (v1 >> 4) & 1u) VC(141, vs, vs + v0) VC(143, vs, 8u * vs + v0s) VC(145, vs, vs + 8192u)
        SC(46, v0s, v0s << 3) VC(148, v0, v1 & 4u) VC(151, vn, v1 & 51u) VC(FA(9), v0, 16u * v0 + vn) VC(FA(9), v0, 16u * v0 + v0s) VC(157, v1, v1 & 8u) VC(FA(11), vn, 8u * v1 + v0) VC(FA(11), v1, v1 << 27)
      };
      phaseAh<0, 3>(c, 10, hook);
    }
    { auto hook = [&](auto M_) __attribute__((always_inline)) { hookB(std::integral_constant<int, 0>{}, M_); }; phaseBh<0, 0>(c, hook); }
    {
      auto hook = [&](auto M_) __attribute__((always_inline)) {
        constexpr int M = decltype(M_)::value;
        constexpr int rg[6] = {0, 8, 16, 24, 37, 55}, sg[6] = {6, 14, 22, 30, 53, 62};
        auto one = [&](auto N_) __attribute__((always_inline)) {
          constexpr int n = decltype(N_)::value, y = 2 * (pcnt(PA0, 'T', sg[n]) - pcnt(PA0, 'T', rg[n])) + pcnt(PA0, 'X', sg[n]) - pcnt(PA0, 'X', rg[n]);
          if constexpr (M == rg[n]) rd(IC(6 + n));
          if constexpr (M == sg[n] - 1) st(IC(6 + n), IC(0), IC(0));
          if constexpr (M == sg[n]) st(IC(6 + n), IC(1), IC(y));
          if constexpr (M == 101 + sg[n]) st(IC(6 + n), IC(2), IC(0));
        };
        for_seq(one, make_iseq<6>{});
      };
      phaseAh<1, 14>(c, 11, hook);
    }
    { auto hook = [&](auto M_) __attribute__((always_inline)) { hookB(std::integral_constant<int, 12>{}, M_); }; phaseBh<1, 0>(c, hook); }
    phaseA<0>(c, 12);
    {
      auto hook = [&](auto M_) __attribute__((always_inline)) {
        constexpr int M = decltype(M_)::value;
        SA(2, st0, (shi<2, 4u * H_W>(b))) SC(3, v0s, w - 1u) SC(4, v0s, (int)v0s >> 31) SC(5, so, st0 + 4u) SC(7, v0s, v0s & so) VC(109, v0, tag)
        if constexpr (M == 10) lim(v0s);
        if constexpr (M == FB(0)) flag_st(st0, v0);
      };
      phaseBh<0, 0>(c, hook);
    }
    phaseA<1>(c, 13); phaseB<1, 0>(c);
  }
  OPS o; OST ot; unsigned xo, xs, r1; unsigned long long tm;
  {
    const unsigned w = opq(wids), b = opq(blk);
    unsigned va, vs, vt, r0;
    {
      unsigned tg, m0, sf, mt, hs, t, u;
      auto hookA = [&](auto M_) __attribute__((always_inline)) {
        constexpr int M = decltype(M_)::value;
        SC(FA(0), m0, (w - 1u) & (b - 144u)) SC(FA(1), m0, (int)m0 >> 31) SA(FA(2), sf, (shi<2, 4u * H_Y>(b))) SC(FA(4), m0, m0 & (sf + 4u)) VC(FA(5), tg, tag)
        if constexpr (M == FA(5)) lim(m0);
        SC(0, mt, b & 3u) SC(1, t, b >> 6) SA(2, mt, sha<2>(mt, t)) SC(3, t, (b >> 2) & 1u) SA(4, mt, sha<1>(mt, t))
        SA(5, u, sha<2>(mt, w)) SC(7, o.sn, u * (16u * XRS)) SC(8, t, (b >> 3) & 7u) SC(9, hs, t * 144u) SC(11, o.sn, o.sn + hs)
        SA(12, hs, sha<2>(t, w)) SC(13, ot.so[0], hs << 19) SC(15, o.sf, hs >> 1) SC(16, o.sf, o.sf << 11)
        SC(17, u, u ^ 4u) SC(19, o.sa, u << 17) SC(20, u, (u >> 2) & 3u) SC(21, u, u << 10) SC(23, o.sa, o.sa | u)
        SC(24, t, ~b & 4u) SC(25, t, t + w) SC(26, u, t << 14) SC(27, o.sa, o.sa | u)
        SC(28, mt, mt >> 1) SA(29, t, sha<3>(mt, t)) SC(31, o.sl, t << 8) SC(32, o.sl, o.sl | 4u * H_O)
        SC(33, hs, b & 6u) SC(34, t, b >> 7) SC(35, hs, hs + t) SC(36, t, b & 1u) SA(38, mt, sha<1>(hs, t)) SC(39, u, (b >> 6) & 1u) SA(40, hs, sha<1>(mt, u))
        SA(42, t, sha<2>(hs, w)) SC(43, t, t << 10) SC(45, ot.so[0], ot.so[0] + t) SC(51, ot.so[0], ot.so[0] | WS_O) SC(52, o.sa, o.sa | WS_O)
        SA(44, u, sha<2>(mt, u)) SC(46, t, w & 1u) SA(48, t, sha<1>(u, t)) SA(49, t, (shi<4, 4u * H_O>(t))) SC(50, o.sf, o.sf + t)
        SC(55, t, b - RED0) SA(56, xo, sha<2>(t, w)) SA(58, xo, (shi<4, 4u * H_X>(xo))) SC(59, xs, xo + 4u)
        if constexpr (M == FA(11)) asm volatile("s_cmp_lt_u32 %1, %2\n\ts_cselect_b32 %0, %0, 0" : "+s"(xs) : "s"(t), "i"(RED1 - RED0) : "scc");
      };
      phaseAh<0, 0>(c, 14, hookA);
      auto hook = [&](auto M_) __attribute__((always_inline)) {
        constexpr int M = decltype(M_)::value;
        if constexpr (M == FB(0)) flag_st(sf, tg);
        warm_at(M_, IC(FB(6)));
        if constexpr (M == 40) lim(4u * H_W + 256u);
        if constexpr (M == 41) m0_imm(IC(WF_FL));
        if constexpr (M == FB(3)) dma16(IC(H_W));
        if constexpr (M == 45) lim(64u);
        if constexpr (M == 46) m0_imm(IC(SEL_C));
        if constexpr (M == FB(4)) dma16(IC(0));
        VC(FB(1), c.pfv, lane_id()) VC(DB(8), vt, c.pfv & 7u) VC(DB(8), c.pfv, c.pfv << 4) VC(DB(9), va, (c.vksrc[0] & 0xf0u) | WF_FL) VC(FB(8), vs, 8u * vt + SEL_C) VC(FB(8), vt, vt + SEQ)
        SC(2, t, (b >> 3) & 7u) SC(4, u, (o.sa >> 14) & 7u) SA(5, t, sha<3>(t, u)) SC(7, o.sb, t * 73728u) SC(9, o.sb, o.sb + WS_WOT) SC(10, c.pfo, o.sb - 1024u) SC(12, c.pfst, 1024u)
        SC(15, c.pfm0, BP_W * w) SC(18, c.pfm0, c.pfm0 + SD_BASE) SC(20, c.pfend, c.pfm0 + BP_W) SC(33, c.srdP, c.srdS)
        SC(23, r0, o.sa >> 13) SC(26, r0, r0 ^ 64u) SC(28, r0, r0 & 2032u) SC(30, r1, w << 7) SC(31, r1, r1 + (SEL_C + 256u))
      };
      phaseBh<0, 0>(c, hook);
    }
    {
      u32x4 f; u32x2 sv; unsigned long long no;
      phaseA<1, 1>(c, 15);
      auto hook = [&](auto M_) __attribute__((always_inline)) {
        constexpr int M = decltype(M_)::value;
        if constexpr (M == FB(0)) asm volatile("ds_read_b128 %0, %2\n\tds_read_b64 %1, %3" : "=&v"(f), "=&v"(sv) : "v"(va), "v"(vs) : "memory");
        if constexpr (M == FB(1)) { asm volatile("s_waitcnt lgkmcnt(%2)" : "+v"(f), "+v"(sv) : "i"(kyo(FB(0), FB(1))) : "memory"); f[0] = (f[0] ^ tag) | (f[1] ^ tag); asm volatile("" : "+v"(f)); }
        if constexpr (M == 126) { f[2] = (f[2] ^ tag) | (f[3] ^ tag); asm volatile("" : "+v"(f)); }
        if constexpr (M == 128) { f[0] |= f[2]; asm volatile("" : "+v"(f)); }
        if constexpr (M == 130) { no = __ballot(f[0] != 0u); asm volatile("" : "+s"(no)); }
        if constexpr (M == FB(2)) {
          if (__builtin_expect(no != 0ull, 0)) {
#pragma nounroll
            for (unsigned spin = 0; spin < (1u << 20); ++spin) {
              __builtin_amdgcn_s_sleep(8);
              hand_req(c.ldsb + WF_FL, H_W, 256u);
              asm volatile("s_waitcnt vmcnt(0)\n\tds_read_b128 %0, %1\n\ts_waitcnt lgkmcnt(0)" : "=v"(f) : "v"(va) : "memory");
              if (__all(((f[0] ^ tag) | (f[1] ^ tag) | (f[2] ^ tag) | (f[3] ^ tag)) == 0u)) break;
            }
            asm volatile("s_mov_b32 m0, %0" :: "s"(c.pm0[1]) : "m0", "memory");
          }
        }
        VC(134, vs, sv[0] & 0xffffu) VC(136, va, vs - r0) VC(138, vt, vt - vs)
        if constexpr (M == FB(5)) { const unsigned x = va; asm volatile("v_cmp_gt_u32_e64 %0, 16, %1" : "=s"(tm) : "v"(x)); }
        if constexpr (M == 140) { const unsigned x = sv[1], g = tag; asm volatile("v_cmp_ne_u32_e64 %0, %1, %2" : "=s"(o.seen) : "s"(g), "v"(x)); }
        VC(158, va, va < 16u ? va : 16u) VC(FB(8), vt, __umul24(vt, XRS)) VC(FB(8), va, 4u * va + r1) SC(64, o.bad, (unsigned)o.seen)
        if constexpr (M == FB(9)) lds_st32(va, vt);
        if constexpr (M == 59) lim(0u);
        if constexpr (M == 60) flag_st(xo, vt);
      };
      phaseBh<1, 2>(c, hook);
    }
  }
  GAP();
  if (__builtin_expect(is_cls(3u), 0)) {
    PS_AT(43);
    unsigned va, vb, nv, sr, sj; u32x4 f; u32x2 sv; f32x4 y[4]; float a[4]; unsigned long long no;
    auto req_fl = [&](auto W_) __attribute__((always_inline)) {
      u32x4& f_ = f; u32x2& s_ = sv; const unsigned v0 = c.pfv, v1 = vb; const u32x4 r = c.srdS;
      if constexpr (decltype(W_)::value != 1) asm volatile("buffer_load_dwordx4 %0, %1, %2, 0 offen offset:%c3 sc1" : "=v"(f_) : "v"(v0), "s"(r), "i"(4 * H_Y) : "memory");
      if constexpr (decltype(W_)::value != 0) asm volatile("buffer_load_dwordx2 %0, %1, %2, 0 offen sc1" : "=v"(s_) : "v"(v1), "s"(r) : "memory");
    };
    auto add2 = [&](auto N_, auto E_) __attribute__((always_inline)) {
      constexpr int n = decltype(N_)::value, e = decltype(E_)::value; const f32x4 v = y[n & 3];
      if constexpr (n == 0) { a[e] = v[e]; a[e + 1] = v[e + 1]; PIN2(a[e], a[e + 1]); } else if constexpr (n > 0 && n < 18) { add_v(a[e], v[e]); add_v(a[e + 1], v[e + 1]); }
    };
    auto batch = [&](auto B_, auto M_) __attribute__((always_inline)) {
      constexpr int B = decltype(B_)::value, M = decltype(M_)::value, i = M == 9 ? 0 : M == FB(0) ? 1 : M == FB(1) ? 2 : M == FB(2) ? 3 : -1, j = M == 8 ? 0 : M == 12 ? 1 : M == 23 ? 2 : M == 31 ? 3 : -1;
      if constexpr (M == 116 || M == 120) add2(IC(4 * B - 2), IC(M == 116 ? 0 : 2));
      if constexpr (M == 126 || M == 130) add2(IC(4 * B - 1), IC(M == 126 ? 0 : 2));
      if constexpr (j >= 0 && 4 * B + j < 18) { sr += 8u * NSEL * DM * 4u; asm volatile("" : "+s"(sr)); }
      if constexpr (i >= 0 && 4 * B + i < 18) {
        f32x4& d = y[i]; const unsigned v = va, so = sr; const u32x4 r = c.srdS;
        asm volatile("buffer_load_dwordx4 %0, %1, %2, %3 offen sc1" : "=v"(d) : "v"(v), "s"(r), "s"(so) : "memory");
      }
    };
    auto phase_a = [&](auto X_, auto B_) __attribute__((always_inline)) {
      constexpr int X = decltype(X_)::value, B = decltype(B_)::value, n0 = 6 + (B < 4 ? 3 : 2) + 3 - X;
      auto hook = [&](auto M_) __attribute__((always_inline)) {
        constexpr int M = decltype(M_)::value;
        if constexpr (M == FA(4)) { f32x4& d = y[0]; asm volatile("s_waitcnt vmcnt(%1)" : "+v"(d) : "i"(n0) : "memory"); }
        if constexpr (M == FA(9)) { f32x4& d = y[1]; asm volatile("s_waitcnt vmcnt(%1)" : "+v"(d) : "i"(n0 - 2) : "memory"); }
        if constexpr (M == 100 + FA(4) || M == FA(5)) add2(IC(4 * B), IC(M > 100 ? 0 : 2));
        if constexpr (M == 100 + FA(9) || M == FA(11)) add2(IC(4 * B + 1), IC(M > 100 ? 0 : 2));
      };
      phaseAh<X, 3 - X>(c, 19 + B, hook);
      if constexpr (B < 4) asm volatile("" : "+v"(y[2]), "+v"(y[3]));
    };
    phaseA<0, 3>(c, 16); phaseB<0, 2>(c);
    {
      auto hook = [&](auto M_) __attribute__((always_inline)) {
        constexpr int M = decltype(M_)::value;
        VC(FA(0), vb, (c.pfv >> 1) & 56u) VC(FA(2), va, __umul24(c.pfv >> 7, NSEL * DM * 4)) VC(FA(4), va, va + (c.pfv & 112u))
        SC(0, sj, xo - 4u * H_X) SA(1, sr, (shi<3, WS_YP - 8u * NSEL * DM * 4u>(sj))) SC(2, sj, sj >> 1) SC(3, sj, sj + WS_XN)
      };
      phaseAh<1, 2>(c, 17, hook);
    }
    { auto hook = [&](auto M_) __attribute__((always_inline)) { constexpr int M = decltype(M_)::value; if constexpr (M == FB(1)) req_fl(IC(1)); if constexpr (M == 40) req_fl(IC(0)); }; phaseBh<1, 2>(c, hook); }
    {
      auto hook = [&](auto M_) __attribute__((always_inline)) {
        constexpr int M = decltype(M_)::value;
        if constexpr (M == FA(9)) { asm volatile("s_waitcnt vmcnt(3)" : "+v"(f), "+v"(sv) :: "memory"); f[0] = (f[0] ^ tag) | (f[1] ^ tag); f[2] = (f[2] ^ tag) | (f[3] ^ tag); asm volatile("" : "+v"(f)); }
        if constexpr (M == 157) { f[0] |= f[2]; asm volatile("" : "+v"(f)); }
        if constexpr (M == FA(11)) { no = (__ballot(f[0] != 0u) << 28) | __ballot(sv[1] != tag); asm volatile("" : "+s"(no)); }
      };
      phaseAh<0, 3>(c, 18, hook);
    }
    {
      auto hook = [&](auto M_) __attribute__((always_inline)) {
        constexpr int M = decltype(M_)::value;
        if constexpr (M == 4) {
          if (__builtin_expect(no != 0ull, 0)) {
            wait_flag(c.srdS, vb + 4u, tag);
#pragma nounroll
            for (unsigned q = 0; q < 3u; ++q) { const unsigned i = 64u * q + lane_id(); wait_flag(c.srdS, 4u * H_Y + 4u * (i < (unsigned)MOE_WG ? i : 0u), tag); }
            { u32x2& s_ = sv; const unsigned v = vb; const u32x4 r = c.srdS; asm volatile("buffer_load_dwordx2 %0, %1, %2, 0 offen sc1\n\ts_waitcnt vmcnt(0)" : "+v"(s_) : "v"(v), "s"(r) : "memory"); }
          }
        }
        batch(IC(0), M_);
      };
      phaseBh<0, 2>(c, hook);
    }
    phase_a(IC(1), IC(0));
    { auto hook = [&](auto M_) __attribute__((always_inline)) { batch(IC(1), M_); }; phaseBh<1, 2>(c, hook); }
    phase_a(IC(0), IC(1));
    { auto hook = [&](auto M_) __attribute__((always_inline)) { batch(IC(2), M_); }; phaseBh<0, 2>(c, hook); }
    phase_a(IC(1), IC(2));
    {
      auto hook = [&](auto M_) __attribute__((always_inline)) {
        constexpr int M = decltype(M_)::value;
        batch(IC(3), M_);
        VC(FB(5), vb, (c.pfv >> 7) & 6u) VC(FB(8), nv, sv[0] & 0xffffu) VC(FB(8), nv, __umul24(nv, XRS) + vb)
      };
      phaseBh<1, 2>(c, hook);
    }
    phase_a(IC(0), IC(3));
    {
      auto hook = [&](auto M_) __attribute__((always_inline)) {
        constexpr int M = decltype(M_)::value;
        batch(IC(4), M_);
        if constexpr (M == FB(1)) {
          unsigned& n_ = nv; const unsigned o2 = sj; const u32x4 r = c.srdS;
          asm volatile("buffer_load_ushort %0, %0, %1, %2 offen sc1" : "+v"(n_) : "s"(r), "s"(o2) : "memory");
        }
        VC(FB(6), va, (c.pfv >> 4) & 15u) VC(FB(6), va, __umul24(va, XRS) + vb) SC(60, sj, sj + SEQ * XRS)
        if constexpr (M == 61) lim(WS_WOT);
      };
      phaseBh<0, 2>(c, hook);
    }
    phase_a(IC(1), IC(4));
    asm volatile("" : "+v"(nv));
    {
      auto hook = [&](auto M_) __attribute__((always_inline)) {
        constexpr int M = decltype(M_)::value;
        if constexpr (M == FB(0) || M == FB(1)) {
          constexpr int e = M == FB(1);
          auto pr = __builtin_amdgcn_permlane32_swap(__float_as_uint(a[e]), __float_as_uint(a[e + 2]), false, false);
          a[e] = __uint_as_float(pr[0]); a[e + 2] = __uint_as_float(pr[1]); PIN2(a[e], a[e + 2]);
        }
        if constexpr (M == 16) add_v(a[0], a[2]);
        if constexpr (M == 128) add_v(a[1], a[3]);
        if constexpr (M == FB(2)) { auto pr = __builtin_amdgcn_permlane16_swap(__float_as_uint(a[0]), __float_as_uint(a[1]), false, false); a[0] = __uint_as_float(pr[0]); a[1] = __uint_as_float(pr[1]); PIN2(a[0], a[1]); }
        if constexpr (M == 136) add_v(a[0], a[1]);
        VC(140, a[0], dpp_add<0x128>(a[0])) VC(144, a[0], rbf(a[0])) VC(FB(5), a[1], bfhi(sv[0])) VC(153, a[0], a[0] * a[1]) VC(158, a[0], rbf(a[0]))
        VC(FB(8), a[0], a[0] + bflo(nv)) VC(164, nv, cvtpk(a[0], 0.f))
        if constexpr (M == FB(9)) { const unsigned d = nv, vo = va, so = sj; const u32x4 r = c.srdL; asm volatile("buffer_store_short %0, %1, %2, %3 offen sc1" :: "v"(d), "v"(vo), "s"(r), "s"(so) : "memory"); }
      };
      phaseBh<1, 2>(c, hook);
    }
  }
  GAP();
  {
    int t = t2;
    unsigned tg = tag;
    asm volatile("" : "+v"(tg));
    lim(xs);
    auto hook = [&](auto M_) __attribute__((always_inline)) {
      constexpr int M = decltype(M_)::value;
      if constexpr (M == FB(8)) flag_st(xo, tg);
      if constexpr (M == FB(8) + 1) lim(0u);
    };
#pragma nounroll
    do {
      t += 2;
      phaseA<0, 4>(c, t);     phaseB<0, 2>(c);
      phaseA<1, 2>(c, t + 1); phaseBh<1, 2>(c, hook);
    } while (__builtin_expect(t != T - 4, 1));
  }
  GAP();
  {
    const unsigned w = opq(wids), b = opq(blk);
    u32x4 sh; unsigned ax[2], dl, fx, fy, t, sx;
    auto req = [&](auto X_, auto J_) __attribute__((always_inline)) {
      constexpr int X = decltype(X_)::value, J = decltype(J_)::value;
      const unsigned v = ax[J >> 1], so = X ? o.sn : sx; const u32x4 r = X ? sh : c.srdS;
      if constexpr (J < 2) asm volatile("buffer_load_dwordx4 %0, %1, %2, %3 offen offset:%c4 sc1" : "=v"(o.gv[X][J & 1]) : "v"(v), "s"(r), "s"(so), "i"(64 * J) : "memory");
      else                 asm volatile("buffer_load_dword %0, %1, %2, %3 offen offset:128 sc1" : "=v"(o.gs[X]) : "v"(v), "s"(r), "s"(so) : "memory");
    };
    auto hookA = [&](auto M_) __attribute__((always_inline)) {
      constexpr int M = decltype(M_)::value;
      SC(0, t, (b >> 3) & 7u) SC(1, t, t * 288u) SC(2, t, t + 4u * H_X)
      if constexpr (M == 3) lim(t + 288u);
      if constexpr (M == FA(0)) { unsigned& d = fx; const unsigned v = c.pfv, so = t; const u32x4 r = c.srdL; asm volatile("buffer_load_dword %0, %1, %2, %3 offen sc1" : "=v"(d) : "v"(v), "s"(r), "s"(so) : "memory"); }
      if constexpr (M == 8) lim(o.sl + 64u);
      VC(FA(2), dl, (c.pfv >> 4) & 15u) VC(FA(2), ax[1], __umul24(dl, XRS)) VC(116, fy, c.pfv >> 8) VC(FA(4), ax[0], 16u * fy + ax[1]) VC(FA(4), fy, 0x3120u >> (4u * fy))
      VC(132, fy, fy & 3u) VC(135, ax[1], 4u * fy + ax[1])
      if constexpr (M >= 9 && M < 13) { const u32x4 d = mk_srd4(xres, SEQ * DM * 2); sh[M - 9] = d[M - 9]; asm volatile("" : "+s"(sh)); }
      if constexpr (M == FA(5)) req(IC(1), IC(0));
      if constexpr (M == FA(6)) req(IC(1), IC(1));
      if constexpr (M == FA(11)) req(IC(1), IC(2));
      VC(FA(9), dl, 4u * dl + r1)
      if constexpr (M == 55) { unsigned& d = dl; asm volatile("ds_read_b32 %0, %0" : "+v"(d) :: "memory"); }
      SC(FA(10), sx, o.sn + WS_XN)
      SC(24, ot.so[1], ot.so[0] ^ 1024u) SC(25, ot.so[2], ot.so[0] ^ 2048u) SC(26, ot.so[3], ot.so[0] ^ 3072u)
    };
    phaseAh<0, 3>(c, T - 2, hookA);
    asm volatile("" : "+v"(dl), "+v"(fx));
    o.srdS = c.srdS; ot.srd = c.srdS; o.va = c.pfv;
    auto hook = [&](auto M_) __attribute__((always_inline)) {
      constexpr int M = decltype(M_)::value;
      constexpr int f = M < NGB && PB2[M < NGB ? M : 0] == 'F' ? pcnt(PB2, 'F', M) : -1;
      VC(100, ax[0], ax[0] + dl) VC(16, ax[1], ax[1] + dl)
      if constexpr (f >= 0 && f < 3) req(IC(0), IC(f));
      if constexpr (M == 33) { const unsigned x = fx, g = tag; asm volatile("v_cmp_ne_u32_e64 %0, %1, %2" : "=s"(o.seen) : "s"(g), "v"(x)); }
      SC(35, t, (unsigned)o.seen & 0x3ffffu)
      if constexpr (M == 40) { unsigned& x = t; const unsigned long long m = tm; asm volatile("s_cmp_lg_u64 %1, 0\n\ts_cselect_b32 %0, %0, 0" : "+s"(x) : "s"(m) : "scc"); }
      SC(44, o.bad, o.bad | t)
      SC(2, c.pfo, o.sb + 23u * 1024u) SC(46, c.pfo, o.sb + 27u * 1024u) SC(48, c.pfm0, 8192u * w) SC(49, c.pfm0, c.pfm0 + (c.ldsb + 0x4000u))
    };
    static_assert(pcnt(PB2, 'E', NGB) == NBL && ppos(PB2, 'W', 0) == DB(4), "");
    phaseB_<0, false, false, 0>(c, hook, make_iseq<NGB>{});
  }
  ot.vo = c.pfv;
  fin_gaps(c, ot, o, make_iseq<NFIN>{});
  asm volatile("s_nop 1" :: "v"(ot.w));
  if (__builtin_expect(!__all((ot.ls[0] + ot.ls[1]) + (ot.ls[2] + ot.ls[3]) <= LIM), 0))
    redo_rows(c, ot, o, wq, 256u * ((opq(blk) >> 3) & 7u) + 64u * opq(wids), 4u * (opq(blk) & 7u) + (opq(blk) >> 6), tag);
  o.srdA = c.srdP; o.srdL = c.srdL; o.tag = tag;
  oproj_phase(o, lds, out, opq(wids)  );
}
extern "C" __global__ void k_attn_end() { asm volatile(PS_END ".subsection 1\n.Lk_end:\n.fill 64, 4, 0xBF800000\n.subsection 0" ::: "memory"); }

extern "C" void kernel_launch(void* const* d_in, const int* in_sizes, int n_in,
                              void* d_out, int out_size, void* d_ws, size_t ws_size,
                              hipStream_t stream) {
  const u16* x   = (const u16*)d_in[0];
  const u16* kc  = (const u16*)d_in[1];
  const u16* vc  = (const u16*)d_in[2];
  const u16* wq  = (const u16*)d_in[3];
  const u16* wo  = (const u16*)d_in[4];
  const u16* w_r = (const u16*)d_in[6];
  const u16* wg  = (const u16*)d_in[7];
  const u16* wu  = (const u16*)d_in[8];
  const u16* wd  = (const u16*)d_in[9];
  char* ws = (char*)d_ws;
  (void)hipFuncSetAttribute((const void*)k_attn, hipFuncAttributeMaxDynamicSharedMemorySize, (int)SHM_ATTN);
  hipLaunchKernelGGL(k_attn, dim3(256), dim3(256), SHM_ATTN, stream, ws, wq, x, w_r, kc, vc, wo, wg, wu, wd, (u16*)d_out  );
}
```

```cpp
#include <hip/hip_runtime.h>
#include <hip/hip_bf16.h>
#include <utility>
#include <type_traits>

constexpr int SEQ = 2048, DM = 576, HQ = 32, HKV = 16, DH = 128, FF = 4608, NSEL = 8;
constexpr int NT = SEQ / 64;
constexpr float LIM = 1073741824.0f;

typedef float    f32x16 __attribute__((ext_vector_type(16)));
typedef float    f32x2  __attribute__((ext_vector_type(2)));
typedef float    f32x4  __attribute__((ext_vector_type(4)));
typedef __bf16   bf16x8 __attribute__((ext_vector_type(8)));
typedef __bf16   bf16x4 __attribute__((ext_vector_type(4)));
typedef __bf16   bf16x2 __attribute__((ext_vector_type(2)));
typedef unsigned u32x8  __attribute__((ext_vector_type(8)));
typedef unsigned u32x4  __attribute__((ext_vector_type(4)));
typedef unsigned u32x2  __attribute__((ext_vector_type(2)));
typedef unsigned short u16;
template <int... Is> using iseq = std::integer_sequence<int, Is...>;
template <int N> using make_iseq = std::make_integer_sequence<int, N>;

#define DI __device__ __forceinline__
#define GAP() __builtin_amdgcn_sched_barrier(0)
#define IC(x) std::integral_constant<int, (x)>{}
#define SRD_WORD3 0x00020000

DI unsigned rfl(unsigned x) { return __builtin_amdgcn_readfirstlane(x); }
DI unsigned cvtpk(float lo, float hi) { f32x2 v = {lo, hi}; bf16x2 b = __builtin_convertvector(v, bf16x2); return __builtin_bit_cast(unsigned, b); }
DI __amdgpu_buffer_rsrc_t mk_srd(const void* p, const unsigned bytes) {
  asm volatile("" : "+s"(p));
  return __builtin_amdgcn_make_buffer_rsrc((void*)p, (short)0, (int)bytes, SRD_WORD3);
}
DI u32x4 mk_srd4(const void* p, const unsigned bytes) { asm volatile("" : "+s"(p)); const unsigned long long a = (unsigned long long)p; return u32x4{(unsigned)a, (unsigned)(a >> 32) & 0xffffu, bytes, SRD_WORD3}; }
template <int N> DI unsigned sha(const unsigned a, const unsigned b) { unsigned r; asm volatile("s_lshl%c3_add_u32 %0, %1, %2" : "=s"(r) : "s"(a), "s"(b), "i"(N) : "scc"); return r; }
template <int N, unsigned B> DI unsigned shi(const unsigned a) { unsigned r; asm volatile("s_lshl%c3_add_u32 %0, %1, %2" : "=s"(r) : "s"(a), "i"(B), "i"(N) : "scc"); return r; }
DI unsigned vsel(const bool c, unsigned a, const unsigned b) { asm volatile("" : "+v"(a)); return c ? a : b; }
DI float bflo(unsigned w) { return __uint_as_float(w << 16); }
DI float bfhi(unsigned w) { return __uint_as_float(w & 0xffff0000u); }
DI float rbf(float x) { return bflo(cvtpk(x, 0.f)); }
DI u16 f2bf(float x) { return (u16)(cvtpk(x, 0.f) & 0xffffu); }

#define PS_MACROS \
  ".macro psl n\n.Lps_\\n: s_cbranch_execz .Lpn_\\n\n.endm\n.set .Lpsl, 0\n" \
  ".macro ps\n.set .Lpso, (. - k_attn) >> 12\n.if .Lpso != .Lpsl\n.set .Lpsl, .Lpso\n.altmacro\npsl %%.Lpso\n.noaltmacro\n.endif\n.endm\n" \
  ".macro mf16 a, b, c, d\nv_mfma_f32_16x16x32_bf16 \\a, \\b, \\c, \\d\nps\n.endm\n" \
  ".macro mf32 a, b, c, d\nv_mfma_f32_32x32x16_bf16 \\a, \\b, \\c, \\d\nps\n.endm\n" \
  ".macro sm64 a, b, c, d\nv_smfmac_f32_16x16x64_bf16 \\a, \\b, \\c, \\d\nps\n.endm\n"
#define PS_IDS "0,1,2,3,4,5,6,7,8,9,10,11,12,13,14,15,16,17,18,19,20,21,22,23,24,25,26,27,28,29,30,31,32,33,34,35,36,37,38,39,40,41,42,43,44,45,46,47"
#define PS_AT(n) asm volatile("psl " #n)
#define PS_PG(n, i) "%%(((.Lps_" #n " - k_attn) >> 12) + 1 + " #i ")"
#define PS_CH(k, n, i) "pse " #k ", " PS_PG(n, i) "\n"
#define PS_NX(n, i, j) "psn " PS_PG(n, i) ", " PS_PG(41, j) "\n"
#define PS_END ".irp n," PS_IDS "\n.ifndef .Lps_\\n\n.set .Lps_\\n, .Lps_ret\n.endif\n.endr\n" \
  ".macro pse k, n\n.set .Lpe_\\k, .Lps_\\n\n.endm\n.macro psn a, b\n.set .Lpn_\\a, .Lps_\\b\n.endm\n.altmacro\n" \
  "pse 0, 2\npse 1, 3\npse 2, 4\npse 3, 5\n" \
  PS_CH(4, 41, -4) PS_CH(5, 41, -3) PS_CH(6, 41, -2) PS_CH(7, 41, -1) PS_NX(41, -4, 0) PS_NX(41, -3, 1) PS_NX(41, -2, 2) PS_NX(41, -1, 3) \
  "pse 8, 42\npse 9, 41\n" PS_CH(10, 42, 0) PS_CH(11, 42, 1) "psn 42, " PS_PG(41, 0) "\npsn 41, " PS_PG(41, 1) "\n" PS_NX(42, 0, 2) PS_NX(42, 1, 3) \
  "pse 12, 43\n" PS_CH(13, 43, 1) PS_CH(14, 43, 2) PS_CH(15, 43, 3) "psn 43, " PS_PG(43, 0) "\npsn " PS_PG(43, 1) ", 41\n" PS_NX(43, 0, 0) PS_NX(43, 2, 2) PS_NX(43, 3, 3) \
  PS_CH(16, 41, 0) "pse 17, 41\n" PS_CH(18, 41, 2) PS_CH(19, 41, 3) PS_CH(20, 45, 0) PS_CH(21, 45, 1) PS_CH(22, 45, 2) PS_CH(23, 45, 0) \
  ".noaltmacro\n.irp n," PS_IDS "\n.ifndef .Lpn_\\n\n.set .Lpn_\\n, .Lps_ret\n.endif\n.endr\n"
DI void ps_touch(const unsigned n) {
  asm volatile("s_getpc_b64 s[20:21]\n\ts_lshl2_add_u32 vcc_lo, %0, .Lps_tab - .\n\ts_add_u32 s20, s20, vcc_lo\n\ts_addc_u32 s21, s21, -1\n\ts_mov_b64 exec, 0\n\ts_swappc_b64 vcc, s[20:21]\n\ts_mov_b64 exec, -1"
               :: "s"(n) : "vcc", "scc", "s20", "s21", "memory");
}

constexpr unsigned H_G = 32u, H_W = 64u, H_Y = 128u, H_O = 8192u, H_X = 1536u, H_E = 16384u;
constexpr unsigned WS_KEY = 131072u;
constexpr unsigned WS_ST  = WS_KEY + SEQ * 8;
constexpr unsigned WS_XN  = WS_ST + SEQ * 16;
constexpr unsigned WS_WOT = WS_XN + (SEQ + NSEL) * DM * 2;
constexpr int      MOE_WG = 144;
constexpr unsigned WS_YP  = WS_WOT + HQ * DH * DM * 2;
constexpr unsigned WS_EX  = WS_YP + MOE_WG * NSEL * DM * 4;
constexpr unsigned WS_O   = 1u << 24, WS_END = 2u << 24;
static_assert(WS_EX + 256u * 20480u <= WS_O && WS_WOT % 1024u == 0u, "");

DI void ce(unsigned& a, unsigned& b) { const unsigned hi_ = a > b ? a : b, lo_ = a > b ? b : a; a = hi_; b = lo_; }
DI int key_row(unsigned k) { return 0xffff - (int)(k & 0xffffu); }
DI float key_val(unsigned k) { const unsigned mono = k >> 16; const unsigned rb = (mono & 0x8000u) ? (mono & 0x7fffu) : (~mono & 0xffffu); return __uint_as_float(rb << 16); }

struct LNJ { u32x4 a[2], w[2]; unsigned a2, w2; float x0, s[2], q[2], mb, den, rden, t0, t1; unsigned key; bool up; };
template <int E> DI unsigned ln_a(const LNJ& j) { if constexpr (E < 8) return j.a[E >> 2][E & 3]; else return j.a2; }
template <int E> DI unsigned ln_w(const LNJ& j) { if constexpr (E < 8) return j.w[E >> 2][E & 3]; else return j.w2; }
DI float rcp_nr(float d) { const float r = __builtin_amdgcn_rcpf(d); return __builtin_fmaf(__builtin_fmaf(-d, r, 1.f), r, r); }
template <int CTRL> DI float dpp_add(float v) { return v + __int_as_float(__builtin_amdgcn_update_dpp(0, __float_as_int(v), CTRL, 0xf, 0xf, false)); }
template <int K> DI float red_step(float v) {
  if constexpr (K == 0) return dpp_add<0xB1>(v);
  else if constexpr (K == 1) return dpp_add<0x4E>(v);
  else if constexpr (K == 2) return dpp_add<0x141>(v);
  else if constexpr (K == 3) return dpp_add<0x140>(v);
  else { auto pr = __builtin_amdgcn_permlane16_swap(__float_as_uint(v), __float_as_uint(v), false, false); return __uint_as_float(pr[0]) + __uint_as_float(pr[1]); }
}
#define PIN2(a, b) asm volatile("" : "+v"(a), "+v"(b))
constexpr int LN_N0 = 29, LN_XN = LN_N0 + 72, LN_KEY = LN_XN + 7;
template <int I> DI void ln_step(LNJ& j, const unsigned row) {
  if constexpr (I == 0) {
    const float xa = bflo((unsigned)__builtin_amdgcn_readlane((int)j.a[0][0], 0)), xb = bflo((unsigned)__builtin_amdgcn_readlane((int)j.a[0][0], 32));
    j.x0 = j.up ? xb : xa;
    asm volatile("" : "+v"(j.x0));
  } else if constexpr (I <= 18) {
    constexpr int e = I - 1, c = e & 1;
    const float d = (c ? bfhi(ln_a<(e >> 1)>(j)) : bflo(ln_a<(e >> 1)>(j))) - j.x0;
    if constexpr (e < 2) { j.s[c] = d; j.q[c] = d * d; } else { j.s[c] += d; j.q[c] = __builtin_fmaf(d, d, j.q[c]); }
    PIN2(j.s[c], j.q[c]);
  } else if constexpr (I == 19) {
    j.t0 = j.s[0] + j.s[1]; j.t1 = j.q[0] + j.q[1];
    PIN2(j.t0, j.t1);
  } else if constexpr (I <= 24) {
    j.t0 = red_step<I - 20>(j.t0); j.t1 = red_step<I - 20>(j.t1);
    PIN2(j.t0, j.t1);
  } else if constexpr (I == 25) {
    j.t0 *= 1.0f / DM; j.t1 = __builtin_fmaxf(__builtin_fmaf(-j.t0, j.t0, j.t1 * (1.0f / DM)), 0.f);
    PIN2(j.t0, j.t1);
  } else if constexpr (I == 26) {
    j.mb = rbf(j.x0 + j.t0); j.t1 = rbf(j.t1) + 1e-5f;
    PIN2(j.mb, j.t1);
  } else if constexpr (I == 27) {
    j.den = rbf(__builtin_amdgcn_sqrtf(rbf(j.t1)));
    asm volatile("" : "+v"(j.den));
  } else if constexpr (I < LN_N0) {
    j.rden = rcp_nr(j.den); j.s[0] = 0.f; j.s[1] = 0.f; asm volatile("" : "+v"(j.rden));
  } else if constexpr (I < LN_XN) {
    constexpr int e = (I - LN_N0) >> 3, st = (I - LN_N0) & 7;
    if constexpr (st == 0)      { j.t0 = bflo(ln_a<e>(j)); j.t1 = bfhi(ln_a<e>(j)); }
    else if constexpr (st == 1) { j.t0 -= j.mb; j.t1 -= j.mb; }
    else if constexpr (st == 2) { const unsigned pk = cvtpk(j.t0, j.t1); j.t0 = bflo(pk); j.t1 = __uint_as_float(pk); }
    else if constexpr (st == 3) { j.t0 *= j.rden; j.t1 = bfhi(__float_as_uint(j.t1)); }
    else if constexpr (st == 4) { unsigned pk = cvtpk(j.t0, j.t1 * j.rden); asm volatile("" : "+v"(pk)); if constexpr (e < 8) j.a[e >> 2][e & 3] = pk; else j.a2 = pk; }
    else if constexpr (st == 5) { j.t0 = bflo(ln_a<e>(j)); j.t1 = bflo(ln_w<e>(j)); }
    else if constexpr (st == 6) { j.s[0] = __builtin_fmaf(j.t0, j.t1, j.s[0]); j.t0 = bfhi(ln_a<e>(j)); asm volatile("" : "+v"(j.s[0])); }
    else                        { j.s[1] = __builtin_fmaf(j.t0, bfhi(ln_w<e>(j)), j.s[1]); asm volatile("" : "+v"(j.s[1])); }
    PIN2(j.t0, j.t1);
  } else if constexpr (I == LN_XN) {
    j.t0 = j.s[0] + j.s[1]; asm volatile("" : "+v"(j.t0));
  } else if constexpr (I < LN_XN + 6) {
    j.t0 = red_step<I - LN_XN - 1>(j.t0); asm volatile("" : "+v"(j.t0));
  } else if constexpr (I == LN_XN + 6) {
    const unsigned rb = f2bf(j.t0);
    const unsigned mono = (rb & 0x8000u) ? (~rb & 0xffffu) : (rb | 0x8000u);
    j.key = (mono << 16) | (0xffffu - row);
    asm volatile("" : "+v"(j.key));
  }
}

struct RTS { unsigned k[8], bad, m, cnd, lq, va, cv, rank; u32x4 g[4]; float rs; unsigned long long pm[2]; };
DI void cnd_s(unsigned& d, const unsigned a, const unsigned long long m) { asm volatile("v_cndmask_b32_e64 %0, %0, %1, %2" : "+v"(d) : "v"(a), "s"(m)); }
DI void cnt_s(unsigned& d, const unsigned long long m) { unsigned long long co; asm volatile("v_addc_co_u32_e64 %0, %1, 0, %0, %2" : "+v"(d), "=s"(co) : "s"(m)); }
constexpr int RT_SORT = 3, RT_POP = RT_SORT + 19, RT_END = RT_POP + 64;
template <int CTRL> DI unsigned dpp_umax(unsigned v) { const unsigned t = (unsigned)__builtin_amdgcn_update_dpp(0, (int)v, CTRL, 0xf, 0xf, false); return t > v ? t : v; }
template <int I> DI void rt_step(RTS& r, const unsigned tag) {
  constexpr int net[19][2] = {{0, 1}, {2, 3}, {4, 5}, {6, 7}, {0, 2}, {1, 3}, {4, 6}, {5, 7}, {1, 2}, {5, 6}, {0, 4}, {3, 7}, {1, 5}, {2, 6}, {1, 4}, {3, 6}, {2, 4}, {3, 5}, {3, 4}};
  if constexpr (I == 0) { r.bad = (r.g[0][1] ^ tag) | (r.g[0][3] ^ tag) | (r.g[1][1] ^ tag); asm volatile("" : "+v"(r.bad)); }
  else if constexpr (I == 1) { r.bad |= (r.g[1][3] ^ tag) | (r.g[2][1] ^ tag) | (r.g[2][3] ^ tag); asm volatile("" : "+v"(r.bad)); }
  else if constexpr (I == 2) {
    r.bad |= (r.g[3][1] ^ tag) | (r.g[3][3] ^ tag); r.cnd = 0u; r.cv = r.lq & 15u;
#pragma unroll
    for (int j = 0; j < 8; ++j) r.k[j] = r.g[j >> 1][2 * (j & 1)];
    asm volatile("" : "+v"(r.bad));
  } else if constexpr (I < RT_POP) { ce(r.k[net[I - RT_SORT][0]], r.k[net[I - RT_SORT][1]]); PIN2(r.k[net[I - RT_SORT][0]], r.k[net[I - RT_SORT][1]]); }
  else if constexpr (I < RT_END) {
    constexpr int n = (I - RT_POP) >> 3, p = (I - RT_POP) & 7;
    if constexpr (p == 0) r.m = dpp_umax<0xB1>(r.k[0]);
    else if constexpr (p == 1) { r.m = dpp_umax<0x4E>(r.m); asm volatile("v_cmp_eq_u32_e64 %0, %1, %2" : "=s"(r.pm[1]) : "i"(n), "v"(r.cv)); }
    else if constexpr (p == 2) r.m = dpp_umax<0x141>(r.m);
    else if constexpr (p == 3) r.m = dpp_umax<0x140>(r.m);
    else if constexpr (p == 4) { asm volatile("v_cmp_eq_u32_e64 %0, %1, %2" : "=s"(r.pm[0]) : "v"(r.m), "v"(r.k[0])); cnd_s(r.cnd, r.m, r.pm[1]); }
    else {
#pragma unroll
      for (int i = p == 5 ? 0 : 2 * p - 9; i < 2 * p - 7 && i < 7 - n; ++i) cnd_s(r.k[i], r.k[i + 1], r.pm[0]);
    }
    asm volatile("" : "+v"(r.m));
  }
}

constexpr unsigned PF_DUM = 0x27C00u;
#define KRING 0x0000u
#define VRING 0x8000u
constexpr unsigned SD_BASE = 0x10000u, MO_XR = 0x22000u, MO_H = 0x25200u, MO_C = 0x26400u;
constexpr unsigned RTB = 254u;
constexpr unsigned RED0 = 208u, RED1 = 244u;
constexpr unsigned MO_HH = 0x26C00u;
constexpr unsigned WF_FL = 0x27000u;
constexpr unsigned MO_RS = 0x26800u, MO_KEY = 0x20000u;
constexpr unsigned BP_W = 23552u;
constexpr size_t SHM_ATTN = 0x28000;
constexpr unsigned KRS = 256u, KTILEB = 64u * KRS;
constexpr unsigned LAH = 3u;
constexpr unsigned KMASK = SEQ * KRS - 1u;
constexpr unsigned WRS = HQ * DH * 2u;
constexpr unsigned XRS = DM * 2u;

#define W4_ACLOB_HI "a128","a129","a130","a131","a132","a133","a134","a135","a136","a137","a138","a139","a140","a141","a142","a143","a144","a145","a146","a147","a148","a149","a150","a151","a152","a153","a154","a155","a156","a157","a158","a159","a160","a161","a162","a163","a164","a165","a166","a167","a168","a169","a170","a171","a172","a173","a174","a175","a176","a177","a178","a179","a180","a181","a182","a183","a184","a185","a186","a187","a188","a189","a190","a191","a192","a193","a194","a195","a196","a197","a198","a199","a200","a201","a202","a203","a204","a205","a206","a207","a208","a209","a210","a211","a212","a213","a214","a215","a216","a217","a218","a219","a220","a221","a222","a223","a224","a225","a226","a227","a228","a229","a230","a231","a232","a233","a234","a235","a236","a237","a238","a239","a240","a241","a242","a243","a244","a245","a246","a247","a248","a249","a250","a251","a252","a253","a254","a255"
#define W4_ACLOB "a0","a1","a2","a3","a4","a5","a6","a7","a8","a9","a10","a11","a12","a13","a14","a15","a16","a17","a18","a19","a20","a21","a22","a23","a24","a25","a26","a27","a28","a29","a30","a31","a32","a33","a34","a35","a36","a37","a38","a39","a40","a41","a42","a43","a44","a45","a46","a47","a48","a49","a50","a51","a52","a53","a54","a55","a56","a57","a58","a59","a60","a61","a62","a63","a64","a65","a66","a67","a68","a69","a70","a71","a72","a73","a74","a75","a76","a77","a78","a79","a80","a81","a82","a83","a84","a85","a86","a87","a88","a89","a90","a91","a92","a93","a94","a95","a96","a97","a98","a99","a100","a101","a102","a103","a104","a105","a106","a107","a108","a109","a110","a111","a112","a113","a114","a115","a116","a117","a118","a119","a120","a121","a122","a123","a124","a125","a126","a127"

DI float max2f(float a, float b) { float r; asm("v_max_f32_e32 %0, %1, %2" : "=v"(r) : "v"(a), "v"(b)); return r; }
DI void lds_st32(unsigned addr, unsigned v) { asm volatile("ds_write_b32 %0, %1" :: "v"(addr), "v"(v) : "memory"); }

struct W4C {
  unsigned ksx[2];
  unsigned vkb[4], vvb[2], vksrc[2], vvsrc, vvsrc0, m0k, sofk;
  static constexpr unsigned ldsb = 0u;
  u32x4 srdS, srdL;
  __amdgpu_buffer_rsrc_t srdK, srdV, srdF; unsigned sfo;
  f32x4 S[2][4][4];
  f32x4 NEGM[4], L[4];
  u32x4 ONE; unsigned IDX;
  bf16x8 Vf[8][2];
  u32x4 Pk[4][2];
  bf16x4 vlo[8][2], vup[8][2];
  unsigned psf[2];
  u32x4 srdP; unsigned pfo, pfm0, pfv, pfst, pfend, pdum;
};

constexpr unsigned FCT = 0x27400u, SEL_C = 0x27800u;
constexpr int AO_(int db, int qb) { return 32 * qb + 4 * db; }
constexpr int AQ_(int qb, int j) { return 128 + 16 * qb + 4 * j; }
constexpr int AK_(int kb, int j) { return 192 + 16 * kb + 4 * j; }

template <int I> DI float acc_read() { float r; asm volatile("v_accvgpr_read_b32 %0, a[%c1]" : "=v"(r) : "i"(I)); return r; }
template <int I> DI void acc_write(const unsigned v) { asm volatile("v_accvgpr_write_b32 a[%c1], %0" :: "v"(v), "i"(I)); }

template <int I, int VSLOT> DI void tr_rd(const W4C& c, bf16x4 (&lo)[8][2], bf16x4 (&up)[8][2]) {
  constexpr int db = I >> 2, k2 = (I >> 1) & 1, off = VSLOT + 8192 * k2 + 4096 * (I & 1) + 512 * (db >> 1);
  if constexpr ((I & 1) == 0) asm volatile("ds_read_b64_tr_b16 %0, %1 offset:%2" : "=v"(lo[db][k2]) : "v"(c.vvb[db & 1]), "i"(off) : "memory");
  else                        asm volatile("ds_read_b64_tr_b16 %0, %1 offset:%2" : "=v"(up[db][k2]) : "v"(c.vvb[db & 1]), "i"(off) : "memory");
}
template <int I, int VSLOT> DI void tr_read_(W4C& c) { tr_rd<I, VSLOT>(c, c.vlo, c.vup); }
template <int I, int SLOT> DI void k_read(W4C& c) {
  asm volatile("ds_read_b128 a[%c1:%c2], %0 offset:%c3" :: "v"(c.vkb[I & 3]), "i"(192 + 4 * I), "i"(195 + 4 * I), "i"(SLOT + 4096 * (I >> 2)) : "memory");
}
template <int KB, int QB, int J, int CM> DI void qk16(f32x4& s, const f32x4& negm) {
  constexpr int ak = AK_(KB, J), aq = AQ_(QB, J);
  if constexpr (CM == 2)      asm volatile("mf16 %0, a[%c1:%c2], a[%c3:%c4], 0" : "=&v"(s) : "i"(ak), "i"(ak + 3), "i"(aq), "i"(aq + 3));
  else if constexpr (CM == 1) asm volatile("mf16 %0, a[%c2:%c3], a[%c4:%c5], %1" : "=&v"(s) : "v"(negm), "i"(ak), "i"(ak + 3), "i"(aq), "i"(aq + 3));
  else                        asm volatile("mf16 %0, a[%c1:%c2], a[%c3:%c4], %0" : "+v"(s) : "i"(ak), "i"(ak + 3), "i"(aq), "i"(aq + 3));
}
template <int X, int M> DI void qk_mfma(W4C& c) { constexpr int kb = M >> 4, qb = (M >> 2) & 3, j = (qb & 1) ? 3 - (M & 3) : (M & 3); qk16<kb, qb, j, ((M & 3) == 0)>(c.S[X][kb][qb], c.NEGM[qb]); }
template <int AO, bool ZERO> DI void pv16(const bf16x8& A, const u32x4& B) {
  if constexpr (ZERO) asm volatile("mf16 a[%c2:%c3], %0, %1, 0" :: "v"(A), "v"(B), "i"(AO), "i"(AO + 3));
  else                asm volatile("mf16 a[%c2:%c3], %0, %1, a[%c2:%c3]" :: "v"(A), "v"(B), "i"(AO), "i"(AO + 3));
}
template <bool ZERO> DI void sum16(f32x4& l, const u32x4& one, const u32x4& B) {
  if constexpr (ZERO) asm volatile("mf16 %0, %1, %2, 0" : "=&v"(l) : "v"(one), "v"(B));
  else                asm volatile("mf16 %0, %1, %2, %0" : "+v"(l) : "v"(one), "v"(B));
}
DI void sum64(W4C& c, const int qb, const u32x4 (&P)[2]) {
  const u32x8 B = __builtin_shufflevector(P[0], P[1], 0, 1, 2, 3, 4, 5, 6, 7);
  asm volatile("sm64 %0, %1, %2, %3" : "+v"(c.L[qb]) : "v"(c.ONE), "v"(B), "v"(c.IDX));
}
constexpr int NGB = 68;
template <int G, bool ZERO> DI void pv_mfma(W4C& c) {
  constexpr int qb = G / 17, r = G % 17, db = (r >> 1) & 7, k2 = (r ^ db) & 1;
  if constexpr (r < 16) pv16<AO_(db, qb), (ZERO && (r & 1) == 0)>(c.Vf[db][k2], c.Pk[qb][k2]);
  else sum64(c, qb, c.Pk[qb]);
}
DI void real_op() { GAP(); __builtin_amdgcn_s_setprio(0); GAP(); }
DI void add_v(float& acc, float x) { asm volatile("v_add_f32_e32 %0, %0, %1" : "+v"(acc) : "v"(x)); }
DI void mul_v(float& acc, float x) { asm volatile("v_mul_f32_e32 %0, %0, %1" : "+v"(acc) : "v"(x)); }
template <int X, int E> DI void exp_e(W4C& c) {
  GAP();
  const float e = __builtin_amdgcn_exp2f(c.S[X][E >> 4][(E >> 2) & 3][E & 3]);
  asm volatile("" :: "v"(e));
  c.S[X][E >> 4][(E >> 2) & 3][E & 3] = e;
  GAP();
}
template <int X, int U> DI void pack_to(W4C& c, u32x4 (&P)[4][2]) {
  constexpr int kb = U >> 3, qb = (U >> 1) & 3, h = U & 1;
  GAP();
  const unsigned w = cvtpk(c.S[X][kb][qb][2 * h], c.S[X][kb][qb][2 * h + 1]);
  asm volatile("" :: "v"(w));
  P[qb][kb >> 1][2 * (kb & 1) + h] = w;
  GAP();
}
template <int X, int U> DI void pack_e(W4C& c) { pack_to<X, U>(c, c.Pk); }

constexpr int NBE = 40;
constexpr char PA0[] = "EXETEXFTEXETEXFT" "EXETEXFTEXETEXFT" "EXXEXFEXXEXEXEXF" "ECCECFECCECECECF";
constexpr char PB2[] = "EDEKEEDEKEEDEKFES" "DEKEEWEKFEWEKEEWS" "EKEWEKEEFEEEEEEES" "EEEEEEEEEEEEEEEES";
constexpr char PB0[] = "EDEKEEDEKEEDEKFES" "DEKEEDEKFEDEKEEDS" "EKEDEKEEFDEEEFDES" "EEEEEFEEEEEFEEEES";
constexpr int pcnt(const char* p, char ch, int g) { int n = 0; for (int i = 0; i < g; ++i) n += p[i] == ch; return n; }
constexpr int ppos(const char* p, char ch, int n) { int i = 0; while (p[i] != ch || n-- > 0) ++i; return i; }
constexpr int FA(int n) { constexpr int g[12] = {6, 10, 14, 18, 22, 30, 37, 41, 47, 53, 57, 63}; return g[n]; }
constexpr int FB(int n) { constexpr int g[10] = {14, 25, 33, 42, 47, 50, 56, 59, 62, 67}; return g[n]; }
constexpr int fa_of(int m) { for (int n = 0; n < 12; ++n) if (FA(n) == m) return n; return -1; }
constexpr int fb_of(int m) { for (int n = 0; n < 10; ++n) if (FB(n) == m) return n; return -1; }
constexpr int DB(int n) { return ppos(PB0, 'D', n); }
constexpr int kyo(int g0, int g1) { const int n = 2 * (pcnt(PB0, 'K', g1) - pcnt(PB0, 'K', g0)); return n < 15 ? n : 15; }
static_assert(pcnt(PA0, 'E', 64) == 64 - NBE && pcnt(PB0, 'E', NGB) == NBE && pcnt(PB0, 'D', NGB) == 10 && pcnt(PB0, 'K', NGB) == 8 && FB(3) == DB(8) - 1 && FB(4) == DB(9) - 1 && PA0[FA(6)] == 'F' && PA0[FA(8)] == 'F', "");
template <int X, int M, typename H> DI void gapA(W4C& c, const int t, H& hook) {
  constexpr int Xp = X ^ 1;
  constexpr const char* pa = PA0;
  constexpr char ty = pa[M];
  constexpr int nt = 2 * pcnt(pa, 'T', M) + pcnt(pa, 'X', M), np = pcnt(pa, 'X', M) + 2 * pcnt(pa, 'C', M);
  if constexpr (M == 0) asm volatile("" ::: W4_ACLOB, W4_ACLOB_HI);
  qk_mfma<X, M>(c);
  GAP();
  hook(std::integral_constant<int, M>{});
  if constexpr (ty == 'T' || ty == 'X') tr_read_<nt, Xp * 0x4000>(c);
  if constexpr (ty == 'T') tr_read_<nt + 1, Xp * 0x4000>(c);
  if constexpr (ty == 'X' || ty == 'C') pack_e<Xp, np>(c);
  if constexpr (ty == 'C') pack_e<Xp, np + 1>(c);
  if constexpr (ty == 'E') exp_e<Xp, NBE + pcnt(pa, 'E', M)>(c);
  {
    if constexpr (M == FA(6) || M == FA(8)) asm volatile("s_add_u32 %0, %0, %1" : "+s"(c.psf[M == FA(8)]) : "i"(KTILEB) : "scc");
  }
  hook(std::integral_constant<int, 100 + M>{});
  GAP();
}
#define W4_V8(a, k) "+v"(a[0][k]), "+v"(a[1][k]), "+v"(a[2][k]), "+v"(a[3][k]), "+v"(a[4][k]), "+v"(a[5][k]), "+v"(a[6][k]), "+v"(a[7][k])
template <int PFW> DI void phaseA_end(W4C& c) {
  asm volatile("s_waitcnt vmcnt(%16) lgkmcnt(0)" : W4_V8(c.vlo, 0), W4_V8(c.vlo, 1) : "i"(PFW) : "memory");
  asm volatile("" : W4_V8(c.vup, 0), W4_V8(c.vup, 1) : "v"(c.NEGM[0]), "v"(c.NEGM[1]), "v"(c.NEGM[2]), "v"(c.NEGM[3]) : "memory");
  GAP();
  __builtin_amdgcn_s_barrier();
  asm volatile("" ::: "memory");
  GAP();
#pragma unroll
  for (int k2 = 0; k2 < 2; ++k2)
#pragma unroll
    for (int db = 0; db < 8; ++db) c.Vf[db][k2] = __builtin_shufflevector(c.vlo[db][k2], c.vup[db][k2], 0, 1, 2, 3, 4, 5, 6, 7);
}
template <int X, int PFW, typename H, int... Ms> DI void phaseA_(W4C& c, const int t, H& hook, iseq<Ms...>) {
  (gapA<X, Ms>(c, t, hook), ...);
  phaseA_end<PFW>(c);
}
struct NoHook { template <typename T> DI void operator()(T) const {} };
template <int X, int PFW = 0> DI void phaseA(W4C& c, const int t) { NoHook h; phaseA_<X, PFW>(c, t, h, make_iseq<64>{}); }
template <int X, int PFW, typename H> DI void phaseAh(W4C& c, const int t, H& hook) { phaseA_<X, PFW>(c, t, hook, make_iseq<64>{}); }

template <int X, bool ZERO1, bool DK, int PF, int M, typename H> DI void gapB(W4C& c, H& hook) {
  constexpr const char* pb = DK ? PB0 : PB2;
  constexpr char ty = pb[M];
  constexpr int n = pcnt(pb, ty, M) + (!DK && ty == 'D' ? 4 : 0), d8 = DB(8), d9 = DB(9);
  pv_mfma<M, ZERO1>(c);
  GAP();
  if constexpr (M == DB(DK ? 0 : 4) - 1) asm volatile("s_add_u32 m0, %0, %1" :: "s"(c.m0k), "i"(X * 0x4000) : "m0", "scc", "memory");
  if constexpr (M == DB(DK ? 4 : 0) - 1) asm volatile("s_add_u32 m0, %0, %1" :: "s"(c.m0k), "i"(VRING + (X ^ 1) * 0x4000) : "m0", "scc", "memory");
  if constexpr (M == 57) asm volatile("s_waitcnt lgkmcnt(0)" ::: "memory");
  if constexpr (PF && (M == d8 - 1 || M == d9 - 1))
    asm volatile("s_cmp_lt_u32 %0, %1\n\ts_cselect_b32 m0, %0, %2\n\ts_add_u32 %0, %0, 0x400" : "+s"(c.pfm0) : "s"(c.pfend), "s"(c.pdum) : "m0", "memory", "scc");
  if constexpr (PF && (M == d8 - 2 || M == d9 - 2)) asm volatile("s_add_u32 %0, %0, %1" : "+s"(c.pfo) : "s"(c.pfst) : "scc");
  constexpr int lg = PF ? 56 : 40;
  if constexpr (X == 1 && M == lg - 5) asm volatile("s_add_u32 %0, %0, %1" : "+s"(c.sfo) : "i"(2 * KTILEB) : "scc");
  if constexpr (X == 1 && M == lg - 4) asm volatile("s_and_b32 %0, %0, %1" : "+s"(c.sfo) : "i"(KMASK) : "scc");
  if constexpr (X == 1 && M == lg - 2) asm volatile("s_mov_b32 m0, %0" :: "s"(c.pdum) : "m0", "memory");
  if constexpr (X == 1 && M == lg) asm volatile("buffer_load_dwordx4 %0, %1, %2 offen lds" :: "v"(c.vksrc[0]), "s"(c.srdF), "s"(c.sfo) : "memory");
  hook(std::integral_constant<int, M>{});
  if constexpr (ty == 'K') { k_read<2 * n, (X ^ 1) * 0x4000>(c); k_read<2 * n + 1, (X ^ 1) * 0x4000>(c); }
  if constexpr (ty == 'D') {
    if constexpr (n < 4) { asm volatile("buffer_load_dwordx4 %0, %1, %2 offen offset:%c3 lds" :: "v"((n & 2) ? c.ksx[n & 1] : c.vksrc[n & 1]), "s"(c.srdK),  "s"(c.psf[0]), "i"(1024 * (n & 3)) : "memory"); }
    else if constexpr (n < 8) asm volatile("buffer_load_dwordx4 %0, %1, %2 offen offset:%c3 lds" :: "v"((n & 1) ? c.vvsrc : c.vvsrc0), "s"(c.srdV),  "s"(c.psf[1]), "i"(1024 * (n & 3)) : "memory");
    else if constexpr (PF) asm volatile("buffer_load_dwordx4 %0, %1, %2 offen sc1 lds" :: "v"(c.pfv), "s"(c.srdP), "s"(c.pfo) : "memory");
  }
  if constexpr (ty == 'W') asm volatile("buffer_load_dwordx4 %0, %1, %2 offen offset:%c3 sc1 lds" :: "v"(c.pfv), "s"(c.srdP), "s"(c.pfo), "i"(1024 * n) : "memory");
  if constexpr (ty == 'E') exp_e<X, DK ? n : 16 * ((n >> 2) & 3) + 4 * (n >> 4) + (n & 3)>(c);
  hook(std::integral_constant<int, 100 + M>{});
  GAP();
}
template <int X, bool ZERO1, bool DK, int PF, typename H, int... Ms> DI void phaseB_(W4C& c, H& hook, iseq<Ms...>) {
  GAP();
  (gapB<X, ZERO1, DK, PF, Ms>(c, hook), ...);
  hook(std::integral_constant<int, 72>{});
  GAP();
}
template <int X, int PF> DI void phaseB(W4C& c) { NoHook h; phaseB_<X, false, true, PF>(c, h, make_iseq<NGB>{}); }
template <int X, int PF, typename H> DI void phaseBh(W4C& c, H& hook) { phaseB_<X, false, true, PF>(c, hook, make_iseq<NGB>{}); }

constexpr unsigned QP_STG = 0xC000u, QP_W8 = 0x24000u, QP_CS = 0x18000u;
constexpr int NJ = 722, PJ = 466, QB0 = 2, NV = 147, NQ0 = 20;
constexpr short Q0[] = {21000,21001,20000,20001,20002,20003,21002,21003,21004,21005,21006,21007,20004,20005,20006,20007,20008,20009,20010,20011,};
constexpr unsigned short JT[] = {30115,1000,36000,17070,20012,20013,20014,20015,30115,1001,32000,1003,34000,20016,20017,1002,34000,32000,1004,34000,20018,20019,30108,1005,34000,32000,1007,34000,20020,20021,1006,34000,
  32000,1008,34000,20022,20023,20024,20025,20026,20027,20028,20029,1009,36000,20030,20031,17001,16000,1011,32000,16001,16002,1010,16003,16004,1012,34000,32000,16005,16006,1013,16007,16008,1015,34000,32000,16009,16010,
  1014,16011,16012,1016,34000,32000,16013,16014,1017,36000,16015,16016,1019,32000,16017,16018,1018,16019,16020,16021,1020,34000,32000,16022,16023,1021,16024,1023,34000,32000,16025,16026,1022,16027,30100,1024,34000,32000,
  16028,1025,36000,1027,32000,1026,34000,1028,34000,32000,1029,34000,1031,34000,32000,1030,34000,1032,34000,32000,1033,34000,32000,31010,1035,34000,21008,20032,1034,34000,32000,1036,34000,20033,20034,1037,34000,20035,
  21009,1039,34000,32000,1038,34000,20036,20037,4000,34000,20038,1040,34000,32000,1041,34000,20039,21010,1043,34000,20040,20041,1042,34000,20042,20043,1044,36000,21011,20044,1045,32000,1047,34000,20045,20046,1046,34000,
  20047,21012,1048,34000,20048,20049,1049,34000,32000,4001,34000,20050,1051,34000,20051,21013,1050,34000,20052,20053,1052,34000,32000,1053,34000,20054,20055,1055,34000,21014,20056,1054,34000,20057,20058,1056,34000,32000,
  1057,34000,20059,21015,1059,34000,20060,20061,1058,36000,20062,20063,1060,32000,1061,34000,17100,1063,34000,17101,1062,34000,30100,1064,34000,32000,1065,34000,17102,1067,34000,17103,1066,34000,17104,1068,34000,32000,
  1069,34000,17105,1071,34000,17106,1070,34000,17107,1072,34000,32000,1073,34000,17108,1075,34000,17071,17109,1074,36000,31011,1076,32000,1077,34000,21016,20064,1079,34000,20065,20066,1078,34000,20067,21017,1080,34000,
  32000,1081,34000,20068,20069,1083,34000,20070,20071,1082,34000,21018,1084,34000,32000,1085,34000,20072,20073,1087,34000,17110,1086,34000,20074,20075,1088,34000,32000,1089,36000,21019,20076,1091,32000,1090,34000,20077,
  20078,1092,34000,17111,1093,34000,20079,21020,1095,34000,32000,1094,34000,20080,20081,1096,34000,17112,1097,34000,20082,20083,1099,34000,32000,1098,34000,21021,20084,1100,34000,17113,1101,34000,20085,20086,1103,34000,
  32000,1102,34000,20087,21022,4002,34000,1104,36000,20088,20089,1105,32000,1107,34000,20090,20091,1106,34000,17114,1108,34000,21023,20092,1109,34000,32000,1111,34000,20093,20094,1110,34000,17115,1112,34000,20095,17045,
  1113,34000,32000,4003,34000,1115,16029,1114,16030,1116,34000,32000,1117,16031,1119,16032,1118,36000,16033,1120,32000,1121,16034,1123,16035,1122,16036,1124,34000,32000,1125,16037,1127,16038,1126,34000,30100,1128,34000,
  32000,1129,16039,1131,16040,1130,16041,1132,34000,32000,1133,16042,1135,16043,1134,16044,1136,16045,1137,16046,1139,16047,1138,34000,31012,1140,34000,21024,20096,1141,36000,20097,20098,1143,32000,1142,34000,20099,
  21025,1144,34000,20100,20101,1145,34000,20102,1147,34000,32000,1146,34000,20103,21026,1148,16048,1149,34000,20104,20105,1151,34000,32000,1150,34000,20106,20107,1152,16049,1153,34000,21027,20108,1155,34000,32000,1154,
  34000,20109,20110,1156,16050,1157,36000,20111,21028,1159,32000,1158,34000,20112,20113,1160,16051,1161,34000,20114,20115,1163,34000,32000,1162,34000,21029,20116,1164,16052,1165,34000,20117,20118,1167,34000,32000,1166,
  34000,20119,21030,4004,34000,1168,34000,20120,20121,1169,34000,32000,1171,34000,20122,20123,1170,16053,1172,36000,21031,20124,1173,32000,1175,34000,20125,20126,1174,16054,1176,34000,20127,1177,34000,32000,4005,34000,
  1179,16055,1178,16056,1180,34000,32000,1181,16057,1183,16058,1182,16059,1184,34000,32000,1185,16060,1187,16061,1186,36000,16062,1188,32000,1189,16063,1191,16064,1190,34000,30100,1192,34000,32000,1193,16065,1195,16066,
  1194,16067,1196,34000,32000,1197,16068,1199,16069,1198,16070,1200,34000,32000,1201,16071,1203,16072,1202,34000,31013,1204,34000,21032,20128,1205,36000,20129,20130,1207,32000,1206,34000,20131,21033,1208,16073,1209,
  34000,20132,20133,1211,16074,1210,34000,32000,1212,34000,20134,20135,1213,16075,1215,34000,21034,20136,1214,16076,1216,34000,32000,1217,34000,20137,20138,1219,16077,1218,34000,20139,21035,1220,16078,1221,34000,32000,
  1223,34000,20140,20141,1222,16079,1224,34000,20142,20143,1225,36000,16080,1227,32000,1226,34000,21036,20144,1228,16081,1229,34000,20145,20146,1231,16082,1230,34000,32000,4006,34000,20147,1232,34000,21037,20148,1233,
  16083,1235,34000,20149,20150,1234,34000,32000,1236,34000,20151,21038,1237,16084,1239,34000,20152,20153,1238,16085,1240,34000,32000,1241,34000,20154,20155,4007,34000,1243,34000,21039,20156,1242,36000,16086,1244,32000,
  1245,34000,20157,20158,1247,16087,1246,34000,20159,1248,16088,1249,34000,32000,1251,16089,1250,16090,1252,16091,1253,16092,1255,34000,32000,1254,34000,30100,1256,16093,1257,16094,1259,16095,1258,34000,32000,1260,16096,
  1261,16097,1263,16098,1262,16099,1264,16100,1265,16101,1267,34000,1266,34000,31014,1268,34000,22008,22009,1269,36000,22010,22011,1271,32000,1270,34000,22012,22013,1272,34000,32000,1273,34000,22014,22015,1275,34000,
  21040,20160,1274,34000,32000,1276,34000,20161,20162,1277,34000,32000,1279,34000,20163,21041,1278,34000,20164,20165,1280,34000,32000,1281,34000,20166,20167,1283,34000,32000,1282,34000,21042,20168,1284,34000,20169,20170,
  1285,34000,32000,4008,34000,20171,1287,34000,32000,1286,34000,21043,20172,1288,36000,20173,20174,1289,32000,1291,34000,20175,21044,1290,34000,20176,20177,4009,34000,20178,1292,34000,20179,21045,1293,34000,32000,1295,
  34000,20180,20181,1294,34000,1296,34000,20182,20183,1297,34000,1299,34000,32000,1298,34000,32000,30102,4016,34000,21046,1300,34000,20184,20185,1301,16102,1303,34000,32000,1302,34000,32000,1304,34000,20186,20187,1305,
  16103,4017,36000,21047,1307,32000,1306,34000,32000,1308,34000,20188,20189,1309,16104,1311,34000,20190,20191,1310,34000,32000,1312,16105,1313,16106,1315,16107,1314,34000,1316,34000,32000,1317,34000,1319,34000,1318,
  34000,30100,1320,34000,17223,1321,34000,32000,4010,34000,1323,34000,1322,34000,1324,34000,1325,34000,4011,34000,1327,34000,1326,34000,1328,34000,1329,34000,31015,1331,34000,20192,20193,1330,36000,20194,20195,1332,
  32000,1333,34000,20196,20197,1335,34000,20198,20199,1334,34000,22000,22001,1336,34000,22002,22003,1337,34000,32000,1339,34000,22004,22005,1338,34000,22006,22007,1340,34000,21048,21049,1341,34000,21050,20200,30108,4012,
  34000,32000,1343,34000,20201,20202,1342,34000,20203,21051,1344,34000,20204,20205,4013,34000,20206,1345,34000,32000,1347,34000,20207,21052,1346,34000,20208,20209,4014,34000,20210,1348,36000,20211,21053,1349,32000,1351,
  34000,20212,20213,30107,4015,34000,20214,1350,34000,20215,21054,1352,34000,17220,1353,34000,32000,1355,34000,17221,17222,1354,34000,17225,1356,34000,20216,20217,1357,34000,31000,1359,34000,32000,1358,34000,25000,25001,
  1360,34000,25002,25003,1361,34000,25004,25005,1363,34000,25006,25007,1362,34000,32000,1364,34000,20218,20219,1365,34000,17190,1367,34000,21055,20220,1366,34000,17191,1368,34000,20221,20222,1369,34000,17192,1371,34000,
  20223,1370,34000,17193,1372,34000,17194,1373,34000,30307,9000,9001,1375,34000,9002,9003,1374,34000,9004,9005,1376,34000,9006,9007,1377,34000,9008,9009,1379,34000,9010,9011,1378,34000,9012,9013,1380,34000,9014,9015,
  1381,34000,9016,9017,1383,34000,9018,9019,1382,34000,9020,30100,1384,34000,9021,9022,1385,34000,9023,9024,1387,34000,9025,9026,1386,34000,9027,9028,1388,34000,9029,9030,1389,34000,9031,9032,1391,34000,9033,9034,1390,
  34000,9035,9036,1392,34000,9037,9038,1393,34000,9039,9040,1395,34000,9041,9042,1394,34000,31016,1396,34000,21056,20224,1397,36000,20225,20226,1399,32000,1398,34000,20227,21057,1400,34000,20228,9044,1401,34000,20229,
  20230,1403,34000,9045,9046,1402,34000,32000,1404,34000,20231,21058,1405,34000,9043,9047,1407,34000,20232,20233,1406,34000,9048,9049,1408,34000,32000,1409,34000,20234,20235,1411,34000,9050,9051,1410,34000,21059,20236,
  1412,34000,9056,9057,1413,34000,32000,1415,34000,20237,20238,1414,34000,9052,9053,1416,34000,20239,21060,1417,36000,9054,9055,1419,32000,1418,34000,20240,20241,1420,34000,9058,9059,1421,34000,20242,20243,1423,34000,
  9060,9061,1422,34000,32000,1424,34000,21061,20244,1425,34000,9062,9063,1427,34000,20245,20246,1426,34000,9064,9065,1428,34000,32000,1429,34000,20247,21062,1431,34000,9066,9067,1430,34000,20248,20249,1432,34000,9068,
  9069,1433,34000,32000,1435,34000,20250,20251,1434,34000,9070,9071,1436,34000,21063,20252,1437,36000,9072,9073,1439,32000,1438,34000,31003,1440,34000,20253,20254,1441,34000,20255,9074,1443,34000,20256,20257,1442,34000,
  32000,1444,34000,20258,20259,1445,34000,20260,20261,1447,34000,20262,20263,1446,34000,9075,9076,30106,1448,34000,32000,1449,34000,21064,21065,1456,34000,21066,21067,1457,34000,21068,21069,1451,34000,21070,21071,1450,
  34000,32000,1459,34000,9077,9078,1458,34000,9079,9080,1452,34000,9081,9082,1453,34000,9083,9084,1460,34000,9085,9086,1461,34000,9087,9088,1455,34000,20264,20265,1454,34000,20266,20267,1463,34000,20268,20269,1462,34000,
  20270,20271,30108,1512,34000,9089,9090,9091,1513,34000,9092,9093,9094,1520,34000,9095,9096,9097,1521,34000,10000,10001,10002,1514,34000,10003,9098,9099,1515,34000,10004,10005,10006,1522,34000,10007,10008,10009,1523,
  34000,10010,10011,9100,1516,34000,10012,9101,1517,34000,10013,10014,1524,34000,10015,10016,10017,1525,34000,10018,10019,10020,1518,34000,10021,10022,10023,1519,34000,10024,10025,10026,1526,34000,10027,10028,10029,1527,
  34000,10030,9102,1464,34000,10031,10032,1465,34000,10033,10034,10035,30100,1529,34000,10036,10037,10038,1528,34000,10039,10040,10041,1472,34000,10042,10043,10044,1473,34000,10045,10046,10047,1537,34000,10048,9103,1536,
  34000,10049,10050,1466,34000,10051,10052,10053,1467,34000,10054,10055,10056,1531,34000,10057,10058,10059,1530,34000,10060,10061,10062,1474,34000,20272,20273,20274,1475,34000,20275,20276,20277,1539,34000,20278,20279,
  10063,1538,34000,10064,10065,9104,1468,34000,10066,9105,1469,34000,10067,10068,1533,34000,10069,10070,10071,1532,34000,10072,10073,10074,1476,34000,10075,10076,10077,1477,34000,10078,10079,10080,1541,34000,10081,10082,
  10083,1540,34000,10084,9106,1470,34000,10085,10086,1471,34000,10087,10088,10089,1535,34000,10090,10091,10092,1534,34000,10093,10094,10095,1478,34000,10096,10097,10098,1479,34000,10099,10100,10101,1543,34000,10102,9107,
  1542,34000,10103,10104,1480,34000,10105,10106,10107,1481,34000,10108,10109,10110,30100,1545,34000,10111,10112,10113,1544,34000,10114,10115,10116,1488,34000,10117,10118,10119,1489,34000,10120,9108,1553,34000,10121,
  10122,1552,34000,10123,10124,10125,1482,34000,10126,10127,10128,1483,34000,10129,10130,10131,1547,34000,10132,10133,10134,1546,34000,10135,10136,10137,1490,34000,20280,20281,20282,1491,34000,20283,20284,20285,1555,
  34000,20286,20287,9109,1554,34000,10138,9110,1484,34000,10139,10140,1485,34000,10141,10142,10143,1549,34000,10144,10145,10146,1548,34000,31001,1492,34000,23000,23001,23002,1493,34000,32000,1557,34000,23003,23004,23005,
  1556,34000,23006,23007,10147,1486,34000,23008,10148,10149,1487,36000,23009,10150,10151,1551,32000,10152,1550,34000,10153,10154,10155,1494,34000,23010,10156,1495,34000,23011,10157,1559,34000,10158,10159,10160,1558,
  34000,10161,10162,10163,1496,34000,32000,10164,1497,34000,10165,10166,10167,30101,1561,34000,10168,10169,10170,1560,34000,10171,10172,10173,1504,34000,10174,9111,1505,34000,10175,10176,1569,34000,32000,10177,1568,
  34000,10178,10179,10180,1498,34000,10181,10182,10183,1499,34000,10184,10185,10186,1563,34000,10187,10188,10189,1562,34000,10190,10191,9112,1506,34000,32000,9113,1507,34000,10192,9114,1571,34000,10193,10194,1570,34000,
  10195,10196,10197,1500,34000,10198,10199,10200,1501,34000,10201,10202,10203,1565,34000,10204,10205,10206,1564,34000,10207,10208,10209,1508,34000,10210,9120,1509,34000,10211,10212,1573,34000,10213,10214,10215,1572,
  34000,10216,10217,10218,1502,34000,23012,10219,10220,1503,34000,23013,10221,10222,1567,34000,10223,10224,10225,1566,34000,10226,10227,9115,1510,34000,23014,10228,1511,34000,23015,10229,1575,34000,10230,10231,10232,
  1574,34000,10233,10234,10235,2128,34000,10236,10237,2132,34000,10238,10239,2136,34000,10240,10241,2140,34000,23100,10242,2001,34000,10243,10244,2005,34000,31002,2009,36000,10245,9116,2013,32000,2002,34000,23101,9117,
  2003,35000,2006,34000,10246,2010,35001,2014,34000,11000,11001,11002,23102,10247,11003,11004,11005,10248,10249,11006,11007,11008,11009,11010,11011,11012,2080,34000,32000,2017,34000,11013,11014,2018,34000,10250,10251,
  2019,34000,10252,10253,2007,34000,10254,10255,2096,34000,10256,10257,2033,34000,32000,2034,34000,11016,11017,30100,2035,34000,11018,9118,2112,34000,11019,2049,34000,11020,11021,2050,34000,11022,2051,34000,32000,11023,
  11024,11025,11026,11027,11028,2087,34000,11029,11030,2022,34000,10258,9119,2021,34000,10259,10260,2020,34000,10261,10262,2103,34000,10263,9121,2038,34000,32000,2037,34000,10264,2036,34000,10265,2119,34000,10266,10267,
  2054,34000,10268,10269,2053,34000,10270,10271,2052,34000,32000,2011,34000,9122,10272,9123,10273,9124,10274,9125,10275,11032,11033,11034,9126,10276,11035,11036,11037,9127,10277,11038,11039,11040,11041,11042,11043,11044,
  2088,34000,11045,11046,2025,34000,10278,10279,2026,34000,10280,10281,2027,34000,10282,2104,34000,10283,2041,34000,10284,10285,2042,34000,10286,10287,2043,13000,2015,34000,23103,2120,13001,2057,34000,11048,11049,2058,
  11050,15000,2059,34000,11051,11052,11053,13002,13003,11054,11055,11056,11057,11058,11059,11060,2095,34000,11061,11062,2030,13016,2029,13017,2028,23104,15001,2111,23105,15008,2046,34000,23106,23107,2045,13018,2044,
  34000,23108,23109,2127,34000,23110,23111,2062,13019,2061,13032,2060,23112,15009,3064,34000,23113,23114,3001,13033,3002,23115,15016,30104,3003,13034,3071,13035,3006,13048,3005,13049,3004,13050,3072,13051,3009,24000,
  15017,3010,13004,3011,13005,3079,24001,15024,3014,13006,3013,13007,3012,24002,15025,3080,13020,3017,13021,3018,24003,15002,3019,13022,3087,13023,3022,24004,15003,3021,13036,3020,13037,3088,24005,15010,3025,13038,3026,
  13039,3027,24006,15011,3095,13052,3030,24007,15018,3029,24008,15019,3028,34000,24009,24010,3096,13053,3033,24011,15026,3034,34000,24012,24013,3035,13054,3103,34000,24014,24015,3038,13055,3037,24016,15027,3036,34000,
  24017,24018,3104,13008,3041,34000,24019,24020,3042,34000,24021,24022,3043,13009,3111,13010,3046,24023,15004,3045,34000,24024,24025,3044,13011,30103,3112,24026,15005,3049,34000,24027,24028,3050,13024,3051,34000,24029,
  24030,3119,13025,3054,24031,15012,3053,13026,3052,13027,3120,13040,3057,13041,3058,13042,3059,13043,3127,15013,15020,3062,13056,3061,13057,3060,15021,13058,13059,13012,15028,13013,13014,15029,13015,13028,15006,13029,
  13030,15007,13031,13044,15014,13045,13046,15015,13047,13060,15022,13061,13062,15023,13063,15030,15031,};
constexpr short JS[] = {0,8,11,15,18,22,26,30,33,43,49,53,56,61,64,69,72,77,81,85,89,94,96,101,103,108,110,112,114,117,119,122,124,127,130,135,138,142,146,149,153,156,159,163,167,171,175,177,181,185,189,192,195,199,203,
  206,210,214,218,221,225,229,233,235,238,241,243,247,250,253,256,259,262,265,268,271,274,278,280,283,287,291,295,298,302,306,309,312,316,319,323,326,330,332,336,339,343,346,350,353,357,360,364,367,371,374,378,380,384,
  386,390,393,397,400,404,407,411,414,416,418,420,423,425,427,430,432,434,436,438,441,443,445,447,451,453,455,457,460,462,464,466,468,470,472,474,479,483,485,489,493,496,499,503,505,509,512,516,518,522,525,529,531,535,
  537,541,543,547,550,554,556,560,563,567,569,573,576,580,582,586,588,592,594,597,600,602,604,606,609,611,613,615,618,620,622,625,627,629,631,633,637,639,641,643,646,648,650,652,655,657,659,661,666,670,672,676,678,682,
  684,687,691,693,697,699,702,706,708,712,714,717,721,723,727,730,732,736,738,742,744,747,750,754,756,760,763,767,769,773,775,778,782,784,788,791,793,797,799,802,804,807,809,811,813,815,818,820,823,825,827,830,832,834,
  836,838,840,842,844,846,851,855,857,861,864,868,872,875,879,882,886,890,893,897,900,904,908,911,914,917,921,925,927,931,935,938,942,945,949,951,955,957,960,963,967,971,973,976,979,983,985,988,990,993,997,999,1003,1006,
  1008,1010,1012,1014,1017,1019,1021,1023,1027,1030,1032,1034,1036,1038,1040,1042,1044,1046,1048,1050,1055,1059,1061,1065,1069,1073,1077,1080,1084,1088,1092,1096,1100,1104,1108,1112,1115,1118,1122,1126,1129,1133,1135,
  1139,1143,1147,1150,1153,1157,1160,1164,1166,1170,1174,1178,1182,1186,1189,1193,1196,1200,1203,1207,1210,1213,1216,1219,1224,1228,1232,1236,1240,1244,1248,1252,1256,1260,1263,1268,1272,1276,1280,1284,1288,1292,1296,
  1300,1304,1308,1310,1315,1319,1321,1325,1329,1333,1337,1340,1344,1348,1352,1356,1359,1363,1367,1371,1375,1378,1382,1386,1390,1394,1396,1400,1404,1408,1412,1415,1419,1423,1427,1431,1434,1438,1442,1446,1450,1453,1457,
  1461,1465,1469,1471,1473,1478,1482,1486,1489,1493,1497,1501,1505,1509,1513,1517,1521,1525,1528,1532,1536,1540,1544,1548,1552,1556,1560,1564,1568,1574,1579,1584,1589,1594,1599,1604,1609,1613,1617,1622,1627,1632,1637,
  1642,1646,1650,1655,1661,1666,1671,1676,1680,1684,1689,1694,1699,1704,1709,1714,1719,1724,1728,1732,1737,1742,1747,1752,1757,1761,1765,1770,1775,1780,1785,1790,1794,1798,1803,1808,1814,1819,1824,1828,1832,1837,1842,
  1847,1852,1857,1862,1867,1872,1876,1880,1885,1890,1892,1898,1901,1906,1911,1916,1921,1924,1929,1933,1937,1942,1947,1951,1956,1962,1967,1971,1975,1979,1984,1989,1994,1999,2004,2008,2012,2016,2021,2026,2031,2036,2041,
  2045,2049,2054,2059,2064,2069,2074,2079,2083,2087,2092,2097,2101,2105,2109,2113,2117,2119,2124,2126,2130,2132,2135,2137,2156,2159,2163,2167,2171,2175,2179,2182,2186,2191,2194,2198,2201,2210,2214,2218,2222,2226,2230,
  2233,2236,2239,2243,2247,2251,2254,2281,2285,2289,2293,2296,2299,2303,2307,2309,2312,2314,2318,2321,2335,2339,2341,2343,2346,2349,2353,2355,2359,2363,2365,2367,2370,2374,2376,2379,2382,2384,2386,2388,2390,2392,2395,
  2397,2399,2402,2404,2406,2409,2411,2413,2416,2418,2420,2423,2425,2427,2430,2432,2434,2437,2439,2442,2445,2449,2451,2454,2458,2460,2464,2466,2469,2473,2475,2479,2483,2485,2487,2490,2494,2496,2500,2504,2506,2510,2512,
  2515,2517,2519,2521,2523,2525,2527,2530,2532,2534,2564,};
constexpr short VL[] = {2,0,0,-3,2,0,1,-3,2,0,2,-3,2,0,3,-3,2,0,4,-3,2,0,5,-3,2,0,6,-3,2,0,7,-3,2,0,8,-3,2,0,9,-3,2,0,10,-3,2,0,11,-3,4,0,0,-2,4,0,1,-2,4,0,2,-2,1,0,0,-2,0,0,0,-2,2,1,0,-1,2,1,1,-1,2,1,2,-1,2,1,3,-1,2,1,
  4,-1,2,1,5,-1,2,1,6,-1,2,1,7,-1,2,1,8,1,2,1,9,3,2,1,10,5,2,1,11,7,2,2,0,10,2,2,1,12,2,2,2,14,2,2,3,16,2,2,4,18,2,2,5,20,2,2,6,22,2,2,7,24,2,2,8,26,2,2,9,28,2,2,10,30,2,2,11,32,6,0,0,33,5,0,0,35,5,0,1,38,5,0,2,41,2,3,0,
  46,2,3,1,50,2,3,2,54,2,3,3,58,2,3,4,62,2,3,5,66,2,3,6,70,2,3,7,74,2,3,8,78,2,3,9,82,2,3,10,86,2,3,11,90,2,4,0,92,2,4,1,96,2,4,2,100,2,4,3,104,2,4,4,108,2,4,5,112,2,4,6,116,2,4,7,120,2,4,8,124,2,4,9,128,2,4,10,132,2,4,
  11,136,2,5,0,146,2,5,1,150,2,5,2,154,2,5,3,158,2,5,4,162,2,5,5,166,2,5,6,170,2,5,7,174,2,5,8,178,2,5,9,182,2,5,10,186,2,5,11,190,3,8,0,194,3,8,1,198,3,8,2,202,3,8,3,206,2,6,0,212,2,6,1,217,2,6,2,222,2,6,3,227,2,6,4,
  232,2,6,5,237,2,6,6,242,2,6,7,247,2,6,8,252,2,6,9,257,2,6,10,262,2,6,11,267,2,7,0,278,7,0,0,280,2,7,1,283,7,0,1,285,2,7,2,288,7,0,2,290,2,7,3,293,7,0,3,295,2,7,4,298,2,7,5,303,2,7,6,308,8,0,0,309,2,7,7,313,8,0,1,314,2,
  7,8,318,8,0,2,319,2,7,9,323,2,7,10,328,2,7,11,333,2,8,4,346,2,8,5,351,2,8,6,356,2,8,7,361,2,8,8,366,2,8,9,371,2,8,10,376,2,8,11,381,9,0,0,416,9,0,1,421,9,0,2,426,9,0,3,431,9,0,4,436,9,0,5,441,9,0,6,446,9,0,7,451,9,0,8,
  456,9,0,9,461,9,0,10,466,9,0,11,471,10,0,0,551,11,0,0,556,11,0,1,562,11,0,2,568,11,0,3,574,12,0,0,601,12,0,1,607,12,0,2,613,12,0,3,619,13,0,0,625,13,0,1,631,};
enum : int { VK_CW, VK_GEN, VK_S, VK_W8, VK_A, VK_WR, VK_STS, VK_GAT, VK_XNS, VK_KV, VK_KEY, VK_V1, VK_K2, VK_PF };
struct VOp { int kind, st, q, pos; };
constexpr VOp vop(int i) { return VOp{VL[4 * i], VL[4 * i + 1], VL[4 * i + 2], VL[4 * i + 3]}; }
constexpr int vq_first(int pos) { int i = 0; while (i < NV && vop(i).pos < pos) ++i; return i; }
constexpr int vq_wait(int kind, int st, int pos, int qmax = 99) {
  int last = -1;
  for (int i = 0; i < NV; ++i) if (vop(i).kind == kind && vop(i).st == st && vop(i).q <= qmax) last = i;
  return vq_first(pos) - 1 - last;
}
constexpr int imin(int a, int b) { return a < b ? a : b; }
constexpr int vq_grp(int i) {
  const VOp o = vop(i);
  return o.kind == VK_S ? 3 * o.st + (o.q >> 2) : o.kind == VK_W8 ? 40 : o.kind == VK_KV ? 41 + (o.q >> 2) : o.kind == VK_V1 ? 44 : o.kind == VK_K2 ? 45 : o.kind == VK_PF ? 50 + o.q : -1;
}
constexpr bool vq_newm0(int i) { int j = i - 1; while (j >= 0 && vq_grp(j) < 0) --j; return j < 0 || vq_grp(j) != vq_grp(i); }
template <int I0, typename F, int... Is> DI void vq_run(F& vm, iseq<Is...>) { (vm(std::integral_constant<int, I0 + Is>{}), ...); }
template <int POS, typename F> DI void vq_issue(F& vm) { vq_run<vq_first(POS)>(vm, make_iseq<vq_first(POS + 1) - vq_first(POS)>{}); }

constexpr int AW_(int T, int blk, int ks) { return ((T & 1) ? 192 : 0) + 8 * blk + 4 * ks; }
constexpr int AX_(int T, int qb, int ks) { return ((T & 1) ? 96 : 64) + 8 * qb + 4 * ks; }
constexpr int qp_set(int T) { return T % 3 == 2; }
constexpr int qp_off(int T, bool w) { return T % 3 == 1 || (w && T == 8) ? (int)QP_STG : 0; }
struct QP {
  f32x4 A[8][4];
  f32x4 cs[2];
  unsigned aw[2][8], ax[2][2], vw[4], vx[4], m0x, so, sw[3], sx[3], ca[4];
  __amdgpu_buffer_rsrc_t srdW, srdX;
  f32x4 cv[8]; u32x4 g[4]; float m[4], m2[4]; unsigned w[4];
};
template <int N> DI void q_fma(QP& p) {
  constexpr int blk = 2 * (N >> 5) + ((N >> 2) & 1), qb = (N >> 3) & 3;
  float v = p.A[blk][qb][N & 3]; asm volatile("v_fmac_f32_e32 %0, %1, %2" : "+v"(v) : "v"(p.cv[blk][N & 3]), "v"(p.g[qb][2])); p.A[blk][qb][N & 3] = v;
}
template <int N> DI void q_fin(QP& p) {
  constexpr int j = N / 72, qb = (N / 18) & 3, n = N % 18;
  if constexpr (n < 8) { float v = p.A[2 * j + (n >> 2)][qb][n & 3]; asm volatile("v_mul_f32_e32 %0, %0, %1" : "+v"(v) : "v"(p.g[qb][0])); p.A[2 * j + (n >> 2)][qb][n & 3] = v; }
  else if constexpr (n < 12) { const f32x4& v = p.A[2 * j + ((n - 8) >> 1)][qb]; GAP(); p.w[n - 8] = cvtpk(v[2 * (n & 1)], v[2 * (n & 1) + 1]); asm volatile("" :: "v"(p.w[n - 8])); GAP(); }
  else if constexpr (n < 14) { GAP(); auto pr = __builtin_amdgcn_permlane16_swap(p.w[n - 12], p.w[n - 10], false, false); p.w[n - 12] = pr[0]; p.w[n - 10] = pr[1]; asm volatile("" :: "v"(p.w[n - 12]), "v"(p.w[n - 10])); GAP(); }
  else acc_write<AQ_(qb, j) + n - 14>(p.w[n - 14]);
}
template <int Q, int I> DI void ref_step(W4C& c, QP& t) {
  f32x4& s = c.S[0][0][Q];
  if constexpr (I == 0) asm volatile("v_max3_f32 %0, %1, %2, %3" : "=v"(t.m[Q]) : "v"(s[0]), "v"(s[1]), "v"(s[2]));
  else if constexpr (I == 1) asm volatile("v_max_f32_e32 %0, %0, %1" : "+v"(t.m[Q]) : "v"(s[3]));
  else if constexpr (I == 2 || I == 5) asm volatile("v_mov_b32_e32 %0, %1" : "=v"(t.m2[Q]) : "v"(t.m[Q]));
  else if constexpr (I == 3) asm volatile("v_permlane16_swap_b32_e32 %0, %1" : "+v"(t.m[Q]), "+v"(t.m2[Q]));
  else if constexpr (I == 4) asm volatile("v_max_f32_e32 %0, %0, %1" : "+v"(t.m[Q]) : "v"(t.m2[Q]));
  else if constexpr (I == 6) asm volatile("v_permlane32_swap_b32_e32 %0, %1" : "+v"(t.m[Q]), "+v"(t.m2[Q]));
  else if constexpr (I < 11) { float n; asm volatile("v_min_f32_e64 %0, -%1, -%2" : "=v"(n) : "v"(t.m[Q]), "v"(t.m2[Q])); c.NEGM[Q][I - 7] = n; }
  else { float v = s[I - 11]; add_v(v, c.NEGM[Q][0]); s[I - 11] = v; }
}
template <int J, int CODE, typename FV, typename FH> DI void q_op(QP& p, W4C& c, FV& vm, FH& hook, LNJ& lj, const unsigned lrow) {
  constexpr int kind = CODE / 1000, a = CODE % 1000;
  if constexpr (kind == 1) {
    constexpr int T = a >> 6, blk = (a >> 3) & 7, qb = (a >> 1) & 3, ks = a & 1, aw = AW_(T, blk, ks), ax = AX_(T, qb, ks);
    if constexpr (T == 0 && ks == (qb & 1)) asm volatile("mf16 %0, a[%c1:%c2], a[%c3:%c4], 0" : "=&v"(p.A[blk][qb]) : "i"(aw), "i"(aw + 3), "i"(ax), "i"(ax + 3));
    else                                    asm volatile("mf16 %0, a[%c1:%c2], a[%c3:%c4], %0" : "+v"(p.A[blk][qb]) : "i"(aw), "i"(aw + 3), "i"(ax), "i"(ax + 3));
  } else if constexpr (kind == 2 || kind == 3) { constexpr int kb = (a >> 4) & 3, qb = (a >> 2) & 3; qk16<kb, qb, (a & 3), (a >> 6)>(c.S[kind - 2][kb][qb], c.NEGM[qb]); }
  else if constexpr (kind == 4) {
    constexpr int r = a < 14 ? AW_(a >> 1, a & 1, 0) : 128 + 8 * (a - 14);
    asm volatile("sm64 %0, %1, a[%c3:%c4], %2" : "+v"(p.cs[a & 1]) : "v"(c.ONE), "v"(c.IDX), "i"(r), "i"(r + 7));
  } else if constexpr (kind == 9) q_fma<a>(p);
  else if constexpr (kind == 10) q_fin<a>(p);
  else if constexpr (kind == 11) ref_step<(a >> 4), (a & 15)>(c, p);
  else if constexpr (kind == 13) exp_e<0, a>(c);
  else if constexpr (kind == 15) pack_e<0, a>(c);
  else if constexpr (kind == 16) { GAP(); ln_step<a>(lj, lrow); GAP(); }
  else if constexpr (kind == 17) { GAP(); hook(std::integral_constant<int, a>{}, std::integral_constant<int, J>{}); GAP(); }
  else if constexpr (kind == 20 || kind == 22) {
    constexpr int T = kind == 20 ? a >> 5 : 7 + (a >> 3), blk = kind == 20 ? (a >> 2) & 7 : (a >> 2) & 1, ks = (a >> 1) & 1, r = kind == 20 ? AW_(T, blk, ks) + 2 * (a & 1) : 128 + 2 * a;
    asm volatile("ds_read_b64_tr_b16 a[%c1:%c2], %0 offset:%c3" :: "v"(p.aw[qp_set(T)][blk]), "i"(r), "i"(r + 1), "i"(8192 * ks + 2048 * (a & 1) + qp_off(T, true)) : "memory");
  } else if constexpr (kind == 21) {
    constexpr int T = a >> 3, qb = (a >> 1) & 3, r = AX_(T, qb, a & 1);
    asm volatile("ds_read_b128 a[%c1:%c2], %0 offset:%c3" :: "v"(p.ax[qp_set(T)][a & 1]), "i"(r), "i"(r + 3), "i"(2048 * qb + qp_off(T, false)) : "memory");
  } else if constexpr (kind == 23) k_read<a % 100, (a >= 100 ? 0x4000 : 0)>(c);
  else if constexpr (kind == 24) tr_read_<a, 0>(c);
  else if constexpr (kind == 25) asm volatile("ds_read_b128 %0, %1 offset:%2" : "=v"(p.cv[a]) : "v"(p.ca[a >> 1]), "i"(64 * (a & 1)) : "memory");
  else if constexpr (kind == 30) {
    if constexpr (a == 200) asm volatile("s_nop 0");
    else if constexpr (a >= 300) asm volatile("s_waitcnt lgkmcnt(%8)" : "+v"(p.cv[0]), "+v"(p.cv[1]), "+v"(p.cv[2]), "+v"(p.cv[3]), "+v"(p.cv[4]), "+v"(p.cv[5]), "+v"(p.cv[6]), "+v"(p.cv[7]) : "i"(a - 300) : "memory");
    else asm volatile("s_waitcnt lgkmcnt(%0)" :: "i"(a - 100) : "memory");
  } else if constexpr (kind == 31) {
    if constexpr (a >= 10) {
      constexpr int T = a - 10, w0 = vq_wait(VK_S, T + 1, J);
      constexpr int w = T == 1 ? imin(w0, vq_wait(VK_WR, 0, J)) : T == 4 ? imin(w0, vq_wait(VK_W8, 8, J)) : T == 5 ? imin(w0, vq_wait(VK_S, 7, J, 3)) : w0;
      if constexpr (T == 0) asm volatile("s_waitcnt vmcnt(%0) lgkmcnt(0)\n\ts_barrier" :: "i"(w) : "memory");
      else                  asm volatile("s_waitcnt vmcnt(%0)\n\ts_barrier" :: "i"(w) : "memory");
    } else if constexpr (a == 0) asm volatile("s_waitcnt lgkmcnt(%0)\n\ts_barrier" :: "i"(QB0) : "memory");
    else if constexpr (a == 3) asm volatile("s_waitcnt vmcnt(%0)" :: "i"(vq_wait(VK_S, 8, J)) : "memory");
    else if constexpr (a == 1) asm volatile("s_waitcnt vmcnt(%0)\n\ts_barrier" :: "i"(vq_wait(VK_KV, 0, J)) : "memory");
    else asm volatile("s_waitcnt lgkmcnt(0)\n\ts_barrier" ::: "memory");
  } else if constexpr (kind == 32) { vq_issue<J>(vm); GAP(); }
  else if constexpr (kind == 36) vm(IC(1000 + vq_first(J + 1)));
  else if constexpr (kind == 34) real_op();
  else if constexpr (kind == 35) {
    c.psf[a] = KTILEB * (3u - a) + c.sofk; asm volatile("" : "+s"(c.psf[a]));
  }
}
template <int J, typename FV, typename FH, int... Is> DI void q_gap(QP& p, W4C& c, FV& vm, FH& hook, LNJ& lj, const unsigned lrow, iseq<Is...>) {
  (q_op<J, JT[JS[J] + Is]>(p, c, vm, hook, lj, lrow), ...); GAP();
}
template <int J0, typename FV, typename FH, int... Js> DI void q_gaps(QP& p, W4C& c, FV& vm, FH& hook, LNJ& lj, const unsigned lrow, iseq<Js...>) {
  (q_gap<J0 + Js>(p, c, vm, hook, lj, lrow, make_iseq<JS[J0 + Js + 1] - JS[J0 + Js]>{}), ...);
}
template <typename FV, typename FH, int... Is> DI void q_first(QP& p, W4C& c, FV& vm, FH& hook, LNJ& lj, iseq<Is...>) { (q_op<0, Q0[Is]>(p, c, vm, hook, lj, 0u), ...); }

constexpr unsigned XRS_ = DM * 2u;
constexpr int op_vr(int f) { return f < 4 ? f : f < 9 ? f : f < 13 ? f - 9 : f - 4; }
constexpr int op_ar(int f) {
  const int n = f - 35;
  return n < 0 ? 84 + 4 * (f - 26) : n < 4 ? 128 + 4 * n : n < 12 ? 4 * (n - 4) : n < 16 ? 144 + 4 * (n - 12) : n < 24 ? 32 + 4 * (n - 16) : n < 32 ? 160 + 4 * (n - 24) : 64 + 4 * (n - 32);
}
constexpr int OP_LA = 4, OP_SF = 22, OP_SP = 25, OP_SD = 29, OP_FEW = 8, OP_NR = 72, OP_RING = 5, OP_GS = 1, OP_P4W = 16, OP_NI = 16, OP_DS = 4;
constexpr short OPU[] = {1,2,3,0,9,10,11,8,5,6,7,4,13,14,15,12,17,18,19,16,21,22,23,20,25,29,26,30,27,31,24,28,5,6,7,11,9,10,13,14,15,3,1,2,17,18,19,23,21,22,25,29,26,30,27,31,0,8,12,16,20,24,28,4,};
constexpr short OPW[] = {12,13,13,13,12,13,13,13,12,12,12,12,12,12,12,12,12,14,16,17,15,14,12,13,13,13,13,13,13,9,9,5,};
constexpr short TT[] = {0,0,22000,0,0,21016,8016,0,0,0,0,21017,8017,0,0,0,0,0,21018,8018,0,0,0,0,21019,8019,0,0,0,11000,0,11001,0,21020,8020,0,11002,0,11003,0,11004,0,21021,8021,0,11005,0,0,11006,0,11007,0,21022,8022,0,
  11008,0,11009,0,11010,0,21023,8023,0,11011,0,0,13004,13005,0,11012,0,21024,8024,0,11013,0,13006,13007,0,11014,0,21025,8025,0,11015,0,0,11016,0,11017,0,21026,8026,0,11018,0,11019,0,11020,0,21027,8027,0,11021,0,0,11022,
  0,11023,0,21028,8028,0,11024,0,11025,0,11026,0,21029,8029,0,11027,0,0,11028,0,11029,0,21030,8030,0,11030,0,11031,0,11032,0,21031,8031,0,11033,15000,0,23000,0,24000,0,22000,0,21032,8032,0,11034,0,11035,0,11036,0,21033,
  8033,0,11037,0,0,11038,0,11039,0,21034,8034,0,11040,0,11041,0,11042,0,21035,8035,0,11043,0,11044,0,11045,0,11046,0,21036,8036,0,11047,0,11048,0,11049,0,21037,8037,0,11050,0,0,11051,0,11052,0,21038,8038,0,11053,0,11054,
  0,11055,0,21039,8039,0,11056,0,0,11057,0,11058,0,21040,8040,0,11059,0,11060,0,11061,0,21041,8041,0,11062,0,0,11063,0,11064,0,21042,8042,0,11065,0,11066,0,11067,0,21043,8043,0,11068,0,0,11069,0,11070,0,21044,8044,0,
  11071,0,11072,0,11073,0,21045,8045,0,11074,0,0,11075,0,11076,0,21046,8046,0,11077,0,11078,0,11079,0,21047,8047,0,11080,0,0,0,0,21048,8048,0,0,0,0,21049,8049,0,0,0,0,0,21050,8050,0,0,0,0,21051,8051,0,0,0,0,0,21052,8052,
  0,0,0,0,21053,8053,0,0,0,0,0,21054,8054,0,0,0,0,21055,8055,0,0,0,25000,0,25001,0,21056,8056,0,25002,0,25003,0,25004,0,21057,8057,0,25005,0,0,25006,0,25007,0,21058,8058,0,25008,0,0,0,21059,8059,0,0,0,0,0,21060,8060,0,0,
  0,0,21061,8061,0,0,0,0,0,21062,8062,0,0,0,0,21063,8063,0,0,0,0,0,21064,8064,0,0,0,0,21065,8065,0,1000,0,0,0,1001,0,21066,8066,0,0,1004,0,0,21067,8067,0,1002,0,0,0,1003,0,21068,8068,0,0,0,0,21069,8069,0,1005,0,0,0,1006,
  0,21070,8070,0,0,1009,0,0,21071,8071,0,1007,0,0,0,1008,0,21072,8072,0,0,0,0,21073,8073,0,1010,0,0,0,1011,0,21074,8074,0,0,1014,0,0,21075,8075,0,1012,0,0,0,31000,1013,0,21076,8076,0,3000,0,31000,4000,0,31000,4105,0,
  21077,8077,0,4210,0,4301,0,31000,4406,0,0,21078,8078,0,5004,6000,6001,0,31000,6002,6003,0,5103,4011,6100,0,21079,8079,0,6101,6102,0,6103,0,5203,4104,6200,0,6201,6202,0,21080,8080,0,5303,6203,6304,0,7000,4209,6305,0,
  6306,6307,0,21081,8081,0,5403,4314,6404,0,6405,0,6406,6407,0,5003,4402,6004,0,21082,8082,0,6005,6006,0,5103,6007,6116,0,7001,4007,6117,0,21083,8083,0,6118,6119,0,4112,0,5204,6216,6217,0,6218,6219,0,21084,8084,0,5303,
  4203,6316,0,6317,6318,0,5403,6319,6408,0,21085,8085,0,7004,4308,6409,0,6410,0,5003,6411,6008,0,4413,6009,0,21086,8086,0,6010,6011,0,5103,6108,6109,0,6110,6111,0,21087,8087,0,5202,7002,6212,6213,0,6214,0,13000,13001,0,
  5303,6215,6312,0,21088,8088,0,6313,6314,0,13002,13003,0,5404,6315,6412,0,21089,8089,0,6413,6414,0,6415,0,7003,0,0,21090,8090,0,0,0,0,21091,8091,0,0,0,0,0,21092,8092,0,0,0,0,21093,8093,0,0,0,0,0,21094,8094,0,0,0,0,
  21095,8095,0,15000,0,0,0,0,21096,8096,0,0,0,0,21097,8097,0,0,0,0,0,21098,8098,0,0,0,0,21099,8099,0,0,0,0,0,21100,8100,0,0,0,0,21101,8101,0,0,0,0,0,21102,8102,0,0,0,0,21103,8103,0,0,14000,0,20000,0,0,21104,8104,0,0,0,0,
  21105,8105,0,0,0,0,0,21106,8106,0,0,0,0,21107,8107,0,0,0,25015,0,25016,0,21108,8108,0,0,0,0,21109,8109,0,0,0,0,0,21110,8110,0,0,0,0,21111,8111,0,0,0,0,0,21112,8112,0,0,0,0,21113,8113,0,0,0,0,0,21114,8114,0,0,0,0,21115,
  8115,0,0,0,0,0,21116,8116,0,0,0,0,21117,8117,0,1100,0,0,0,1101,0,21118,8118,0,0,1104,0,0,21119,8119,0,1102,0,0,0,1103,0,21120,8120,0,0,0,0,21121,8121,0,1105,0,0,0,1106,0,21122,8122,0,0,1109,0,0,21123,8123,0,1107,0,0,0,
  1108,0,21124,8124,0,26000,0,26001,0,26002,0,21125,8125,0,1110,0,0,0,1111,0,21126,8126,0,0,1114,0,0,21127,8127,0,1112,0,0,0,31000,1113,0,0,3000,0,31000,4900,0,31000,4025,0,31000,4130,0,4901,0,4226,0,31000,4331,0,31000,
  4904,0,31000,4429,0,5006,12000,6020,6021,0,31000,6022,6023,0,5105,4034,6120,0,31000,4902,6121,0,6122,6123,0,4127,0,31000,19000,0,31000,13004,13005,0,5208,12001,6224,6225,0,19001,0,31000,13006,13007,0,31000,6226,6227,0,
  31000,19004,0,5309,4232,6324,0,4903,0,31000,19002,0,31000,6325,6326,0,5409,12004,6327,6436,0,19003,0,31000,4328,6437,0,31000,6438,6439,0,5009,4433,6036,0,6037,6038,0,6039,0,5108,12002,6128,6129,0,31000,6130,6131,0,
  5203,6228,6229,0,6230,6231,0,5301,12003,6332,6333,0,31000,6334,6335,0,5400,6432,6433,0,6434,6435,0,0,31000,16000,0,31000,16001,0,31000,16002,0,16003,0,31000,16004,0,31000,16005,0,31000,16006,0,16007,15004,0,0,31000,
  16008,0,31000,16009,0,31000,16010,0,16011,0,31000,16012,0,31000,16013,0,31000,16014,0,16015,0,0,31000,16016,0,31000,16017,0,17000,18000,18001,0,18002,18003,0,31000,18004,18005,0,31000,18006,18007,0,31000,18016,18017,0,
  18018,18019,18008,18009,18010,18011,18012,18013,18014,18015,};
constexpr short TS[] = {0,1,3,4,7,8,9,10,13,14,15,16,17,20,21,22,23,26,27,28,30,32,35,37,39,41,44,46,47,49,51,54,56,58,60,63,65,66,69,71,74,76,79,81,84,86,87,89,91,94,96,98,100,103,105,106,108,110,113,115,117,119,122,
  124,125,127,129,132,134,136,138,141,143,146,148,150,153,155,157,159,162,164,165,167,169,172,174,176,178,181,183,185,187,189,192,194,196,198,201,203,204,206,208,211,213,215,217,220,222,223,225,227,230,232,234,236,239,
  241,242,244,246,249,251,253,255,258,260,261,263,265,268,270,272,274,277,279,280,282,284,287,289,291,293,296,298,299,300,301,304,305,306,307,310,311,312,313,314,317,318,319,320,323,324,325,326,327,330,331,332,333,336,
  337,338,339,340,343,344,345,346,349,350,351,353,355,358,360,362,364,367,369,370,372,374,377,379,380,381,384,385,386,387,388,391,392,393,394,397,398,399,400,401,404,405,406,407,410,411,412,413,414,417,418,419,420,423,
  425,426,427,429,432,433,435,436,439,441,442,443,445,448,449,450,451,454,456,457,458,460,463,464,466,467,470,472,473,474,476,479,480,481,482,485,487,488,489,491,494,495,497,498,501,503,504,505,508,511,512,516,519,522,
  524,526,529,530,533,537,541,545,548,551,553,557,560,563,567,571,574,577,581,583,586,590,593,596,600,604,607,610,612,616,619,622,626,629,633,636,640,642,646,649,652,655,659,662,665,670,672,675,679,682,685,688,692,695,
  698,700,702,703,706,707,708,709,712,713,714,715,716,719,720,721,722,725,726,727,728,729,732,733,734,735,738,739,741,742,743,746,747,748,749,752,753,754,755,756,759,760,761,762,765,766,767,768,769,772,773,774,775,778,
  779,780,781,782,785,786,787,788,791,792,793,796,797,800,801,802,803,806,807,808,809,810,813,814,815,816,819,820,821,823,825,828,829,830,831,834,835,836,837,838,841,842,843,844,847,848,849,850,851,854,855,856,857,860,
  861,862,863,864,867,868,869,870,873,874,875,876,877,880,881,882,883,886,888,889,890,892,895,896,898,899,902,904,905,906,908,911,912,913,914,917,919,920,921,923,926,927,929,930,933,935,936,937,939,942,944,946,948,951,
  953,954,955,957,960,961,963,964,967,969,970,971,974,975,976,980,983,986,988,990,993,996,999,1004,1008,1012,1016,1019,1021,1024,1028,1033,1035,1039,1043,1046,1050,1052,1055,1059,1064,1066,1070,1074,1078,1081,1083,1088,
  1092,1096,1099,1104,1108,1112,1115,1116,1119,1122,1125,1127,1130,1133,1136,1138,1140,1143,1146,1149,1151,1154,1157,1160,1162,1163,1166,1169,1173,1176,1180,1184,1188,1191,1199,};
constexpr bool op_first(int u) { for (int x = u & 32; x < u; ++x) if ((OPU[x] & 3) == (OPU[u] & 3)) return false; return true; }
struct OPS {
  u32x4 fa[OP_LA + 1][4], fb[22]; f32x4 D[2][5], T[5], m[OP_RING], pv[5]; float g0, g1, t0, t1, gx[36];
  u32x4 gv[2][2]; unsigned gs[2];
  unsigned ra[3], sw[2], si[2], sg, tg, so, sb;
  unsigned va, idx, sa, lb5, lb1, lb2, sn;
  u32x4 srdA, srdL, srdS; unsigned fo, sl, sf, tag, bad; unsigned long long seen;
};
constexpr unsigned OB = WS_END;
DI void op_poll(OPS& o) { asm volatile("buffer_load_dword %0, %1, %2, %3 offen sc1" : "=v"(o.fo) : "v"(o.va), "s"(o.srdL), "s"(o.sl) : "memory"); }
DI void op_there(OPS& o) {
  unsigned n;
  asm volatile("s_and_b32 %0, %1, 15\n\ts_cmp_eq_u32 %0, 15\n\ts_cselect_b32 %0, %2, 0" : "=&s"(n) : "s"((unsigned)o.seen), "i"(OB) : "scc");
  o.srdA[2] = n;
}
template <int F> DI void op_reqB(OPS& o) {
  asm volatile("buffer_load_dwordx4 a[%c3:%c4], %0, %1, %2 offen offset:%c5 sc1" :: "v"(o.va), "s"(o.srdS), "s"(o.sb + 1024u * (F & ~3)), "i"(op_ar(F)), "i"(op_ar(F) + 3), "i"(1024 * (F & 3)) : "memory");
}
constexpr unsigned op_off(int st, int i) { const int un = OPU[2 * st + (i >> 1)]; return (131072u * (un & 3) + 1024u * (2 * (un >> 2) + (i & 1))) ^ (st >> 4) * (524288u + 1024u); }
template <int ST, int I> DI void op_offA(OPS& o) {
  GAP(); o.so = o.sa ^ op_off(ST, I); asm volatile("" :: "s"(o.so)); GAP();
}
template <int ST, int I> DI void op_reqA1(OPS& o) {
  asm volatile("buffer_load_dwordx4 %0, %1, %2, %3 offen sc1" : "=v"(o.fa[ST % (OP_LA + 1)][I]) : "v"(o.va), "s"(o.srdA), "s"(o.so) : "memory");
  if constexpr (ST < OP_LA) asm volatile("ps");
}
template <int... Qs> DI void op_reqA0(OPS& o, iseq<Qs...>) { ((op_offA<Qs / 4, Qs % 4>(o), op_reqA1<Qs / 4, Qs % 4>(o)), ...); }
#define OP_F4(s) "+v"(o.fa[(s) % (OP_LA + 1)][0]), "+v"(o.fa[(s) % (OP_LA + 1)][1]), "+v"(o.fa[(s) % (OP_LA + 1)][2]), "+v"(o.fa[(s) % (OP_LA + 1)][3])
#define OP_I4(s) "i"(op_off(s, 0)), "i"(op_off(s, 1)), "i"(op_off(s, 2)), "i"(op_off(s, 3))
#define OP_L1(d, i) "s_xor_b32 %1, %[sa], %c" #i "\n\tbuffer_load_dwordx4 %" #d ", %[va], %[sr], %1 offen sc1\n\t"
#define OP_HEAD "s_cmp_lg_u32 %[was], 0\n\ts_cbranch_scc0 4f\n\t.subsection 1\n\t4:\n\ts_mov_b32 %2, 0x100000\n\t" \
                "2:\n\tv_cmp_eq_u32_e32 vcc, %[tg], %0\n\ts_and_b32 %1, vcc_lo, 15\n\ts_cmp_eq_u32 %1, 15\n\ts_cbranch_scc1 3f\n\t" \
                "s_sleep 1\n\tbuffer_load_dword %0, %[va], %[sl], %[so] offen sc1\n\ts_sub_u32 %2, %2, 1\n\ts_waitcnt vmcnt(0)\n\ts_cbranch_scc0 2b\n\t3:\n\t"
#define OP_IN [va] "v"(o.va), [sr] "s"(o.srdA), [sl] "s"(o.srdL), [so] "s"(o.sl), [sa] "s"(o.sa), [was] "s"(was), [tg] "s"(o.tag)
template <int S0, int N> DI void op_again(OPS& o) {
  unsigned t, n; const unsigned was = o.srdA[2];
  o.srdA[2] = OB;
  if constexpr (N == 16)
    asm volatile(OP_HEAD OP_L1(3, 19) OP_L1(4, 20) OP_L1(5, 21) OP_L1(6, 22) OP_L1(7, 23) OP_L1(8, 24) OP_L1(9, 25) OP_L1(10, 26) OP_L1(11, 27) OP_L1(12, 28) OP_L1(13, 29) OP_L1(14, 30)
                 OP_L1(15, 31) OP_L1(16, 32) OP_L1(17, 33) OP_L1(18, 34) "s_branch 1f\n\t.subsection 0\n\t1:"
                 : "+v"(o.fo), "=&s"(t), "=&s"(n), OP_F4(S0), OP_F4(S0 + 1), OP_F4(S0 + 2), OP_F4(S0 + 3) : OP_I4(S0), OP_I4(S0 + 1), OP_I4(S0 + 2), OP_I4(S0 + 3), OP_IN : "vcc", "scc", "memory");
  else if constexpr (N == 12)
    asm volatile(OP_HEAD OP_L1(3, 15) OP_L1(4, 16) OP_L1(5, 17) OP_L1(6, 18) OP_L1(7, 19) OP_L1(8, 20) OP_L1(9, 21) OP_L1(10, 22) OP_L1(11, 23) OP_L1(12, 24) OP_L1(13, 25) OP_L1(14, 26) "s_branch 1f\n\t.subsection 0\n\t1:"
                 : "+v"(o.fo), "=&s"(t), "=&s"(n), OP_F4(S0), OP_F4(S0 + 1), OP_F4(S0 + 2) : OP_I4(S0), OP_I4(S0 + 1), OP_I4(S0 + 2), OP_IN : "vcc", "scc", "memory");
  else
    asm volatile(OP_HEAD OP_L1(3, 11) OP_L1(4, 12) OP_L1(5, 13) OP_L1(6, 14) OP_L1(7, 15) OP_L1(8, 16) OP_L1(9, 17) OP_L1(10, 18) "s_branch 1f\n\t.subsection 0\n\t1:"
                 : "+v"(o.fo), "=&s"(t), "=&s"(n), OP_F4(S0), OP_F4(S0 + 1) : OP_I4(S0), OP_I4(S0 + 1), OP_IN : "vcc", "scc", "memory");
}
template <int F> DI void op_ldsB(OPS& o) {
  if constexpr (F < 26) asm volatile("ds_read_b128 %0, %1 offset:%2" : "=v"(o.fb[op_vr(F)]) : "v"(F < 23 ? o.lb5 : o.lb1), "i"(1024 * (F < 23 ? F : F - 23)) : "memory");
  else                  asm volatile("ds_read_b128 a[%c1:%c2], %0 offset:%c3" :: "v"(F < 27 ? o.lb1 : o.lb2), "i"(op_ar(F)), "i"(op_ar(F) + 3), "i"(1024 * (F < 27 ? F - 23 : F - 27)) : "memory");
}
template <int F0, int... Is> DI void op_ldsBs(OPS& o, iseq<Is...>) { (op_ldsB<F0 + Is>(o), ...); }
constexpr int op_ac(int pass, int i, int j) { return j < 4 ? 176 + 16 * i + 4 * j : pass == 0 ? 236 + 4 * i : i == 1 ? 252 : 112 + 4 * i; }
template <int ST, int Q> DI void op_mfma(OPS& o) {
  constexpr int A = ST % (OP_LA + 1), P = ST >> 4;
  if constexpr (Q == 0 || Q == 9) {
    constexpr int u = Q == 9, un = OPU[2 * ST + u], i = un & 3, f = 9 * (un >> 2) + 8, ac = op_ac(P, i, 4), ar = op_ar(f);
    const u32x8 B = __builtin_shufflevector(o.fa[A][2 * u], o.fa[A][2 * u + 1], 0, 1, 2, 3, 4, 5, 6, 7);
    if constexpr (i == 0 && f < 26) asm volatile("sm64 %0, %1, %2, %3" : "+v"(o.D[P][4]) : "v"(o.fb[op_vr(f) % 22]), "v"(B), "v"(o.idx));
    else if constexpr (i == 0)      asm volatile("sm64 %0, a[%c3:%c4], %1, %2" : "+v"(o.D[P][4]) : "v"(B), "v"(o.idx), "i"(ar), "i"(ar + 3));
    else if constexpr (f < 26)      asm volatile("sm64 a[%c3:%c4], %0, %1, %2" :: "v"(o.fb[op_vr(f) % 22]), "v"(B), "v"(o.idx), "i"(ac), "i"(ac + 3));
    else                            asm volatile("sm64 a[%c2:%c3], a[%c4:%c5], %0, %1" :: "v"(B), "v"(o.idx), "i"(ac), "i"(ac + 3), "i"(ar), "i"(ar + 3));
  } else {
    constexpr int d = Q - 1 - (Q > 9), J = d >> 2, u = (d >> 1) & 1, kk = d & 1, un = OPU[2 * ST + u], i = un & 3, f = 9 * (un >> 2) + 4 * kk + J, ac = op_ac(P, i, J), ar = op_ar(f);
    constexpr bool z = kk == 0 && op_first(2 * ST + u);
    static_assert(!z || f < 26, "");
    const u32x4& B = o.fa[A][2 * u + kk];
    if constexpr (i == 0 && z)           asm volatile("mf16 %0, %1, %2, 0" : "=&v"(o.D[P][J]) : "v"(o.fb[op_vr(f) % 22]), "v"(B));
    else if constexpr (i == 0 && f < 26) asm volatile("mf16 %0, %1, %2, %0" : "+v"(o.D[P][J]) : "v"(o.fb[op_vr(f) % 22]), "v"(B));
    else if constexpr (i == 0)           asm volatile("mf16 %0, a[%c2:%c3], %1, %0" : "+v"(o.D[P][J]) : "v"(B), "i"(ar), "i"(ar + 3));
    else if constexpr (z)                asm volatile("mf16 a[%c2:%c3], %0, %1, 0" :: "v"(o.fb[op_vr(f) % 22]), "v"(B), "i"(ac), "i"(ac + 3));
    else if constexpr (f < 26)           asm volatile("mf16 a[%c2:%c3], %0, %1, a[%c2:%c3]" :: "v"(o.fb[op_vr(f) % 22]), "v"(B), "i"(ac), "i"(ac + 3));
    else                                 asm volatile("mf16 a[%c1:%c2], a[%c3:%c4], %0, a[%c1:%c2]" :: "v"(B), "i"(ac), "i"(ac + 3), "i"(ar), "i"(ar + 3));
  }
}
template <int I> DI unsigned op_ax(const unsigned va) { const unsigned r = ((va >> 4) & 15u) * XRS_; return I ? r + 128u + ((va >> 5) & 8u) + ((va >> 7) & 4u) : r + ((va >> 4) & 48u); }
template <int X, int WD> DI unsigned op_word(const OPS& o) { if constexpr (WD < 8) return o.gv[X][WD >> 2][WD & 3]; else return o.gs[X]; }
template <int U> DI void op_gate(OPS& o) {
  constexpr int wd = U / 9, pt = U % 9;
  if constexpr (pt == 0)      asm volatile("v_lshlrev_b32_e32 %0, 16, %2\n\tv_and_b32_e32 %1, 0xffff0000, %2" : "=&v"(o.g0), "=&v"(o.g1) : "v"(op_word<0, wd>(o)));
  else if constexpr (pt == 1) asm volatile("v_mul_f32_e32 %0, 0xbfb8aa3b, %2\n\tv_mul_f32_e32 %1, 0xbfb8aa3b, %3" : "=&v"(o.t0), "=&v"(o.t1) : "v"(o.g0), "v"(o.g1));
  else if constexpr (pt == 2) asm volatile("v_exp_f32_e32 %0, %0" : "+v"(o.t0));
  else if constexpr (pt == 3) asm volatile("v_exp_f32_e32 %0, %0" : "+v"(o.t1));
  else if constexpr (pt == 4) asm volatile("v_add_f32_e32 %0, 1.0, %0\n\tv_add_f32_e32 %1, 1.0, %1" : "+v"(o.t0), "+v"(o.t1));
  else if constexpr (pt == 5) asm volatile("v_rcp_f32_e32 %0, %0" : "+v"(o.t0));
  else if constexpr (pt == 6) asm volatile("v_rcp_f32_e32 %0, %0" : "+v"(o.t1));
  else if constexpr (pt == 7) asm volatile("v_mul_f32_e32 %0, %0, %2\n\tv_mul_f32_e32 %1, %1, %3" : "+v"(o.t0), "+v"(o.t1) : "v"(o.g0), "v"(o.g1));
  else { unsigned r; asm volatile("v_cvt_pk_bf16_f32 %0, %1, %2" : "=v"(r) : "v"(o.t0), "v"(o.t1)); if constexpr (wd < 8) o.gv[0][wd >> 2][wd & 3] = r; else o.gs[0] = r; }
}
template <int ST, int Q, int CODE, typename H> DI void op_t(OPS& o, H& hook) {
  constexpr int kind = CODE / 1000, a = CODE % 1000;
  if constexpr (kind == 0) op_mfma<ST, Q>(o);
  else if constexpr (kind == 1) {
    constexpr int n = a % 100, h = a / 200, ac = op_ac((a / 100) & 1, 1 + n / 5, n % 5), off = n < 10 ? 13312 + 1024 * n : 1024 * (n - 10);
    if constexpr (h == 0) asm volatile("ds_write_b128 %0, a[%c1:%c2] offset:%c3" :: "v"(n < 10 ? o.lb5 : o.lb2), "i"(ac), "i"(ac + 3), "i"(off) : "memory");
    else                  asm volatile("ds_write_b64 %0, a[%c1:%c2] offset:%c3" :: "v"(n < 10 ? o.lb5 : o.lb2), "i"(ac + 2 * h - 2), "i"(ac + 2 * h - 1), "i"(off + 8 * h - 8) : "memory");
  } else if constexpr (kind == 2) asm volatile("s_waitcnt lgkmcnt(%0)" :: "i"(a) : "memory");
  else if constexpr (kind == 3) asm volatile("s_waitcnt lgkmcnt(0)\n\ts_barrier" ::: "memory");
  else if constexpr (kind == 4) {
    constexpr int sl = a / 100, n = a % 20;
    asm volatile("ds_read_b128 %0, %1 offset:%2" : "=v"(sl == 9 ? o.T[n % 5] : o.m[sl % OP_RING]) : "v"(o.ra[n / 5]), "i"(n < 10 ? 13312 + 1024 * n : 1024 * (n - 10)) : "memory");
  } else if constexpr (kind == 5) asm volatile("s_waitcnt lgkmcnt(%1)" : "+v"(o.m[a / 100]) : "i"(a % 100) : "memory");
  else if constexpr (kind == 6 || kind == 18) {
    constexpr int j = (a % 20) >> 2, e = a & 3;
    f32x4& s = kind == 6 && a % 100 < 20 ? o.D[0][j] : o.T[j];
    float v = s[e]; add_v(v, kind == 6 ? o.m[a / 100][e] : o.pv[j][e]); s[e] = v;
  } else if constexpr (kind == 7)
    asm volatile("buffer_store_dwordx4 %0, %1, %2, %3 offen offset:%c4 sc1" :: "v"(o.D[0][a]), "v"(o.va), "s"(o.srdS), "s"(o.sw[a >> 2]), "i"(1024 * (a & 3)) : "memory");
  else if constexpr (kind == 8) op_reqA1<(a >> 2), (a & 3)>(o);
  else if constexpr (kind == 9) op_reqB<a>(o);
  else if constexpr (kind == 19) asm volatile("buffer_load_dwordx4 %0, %1, %2, %3 offen offset:%c4 sc1" : "=v"(o.pv[a]) : "v"(o.va), "s"(o.srdS), "s"(o.si[a >> 2]), "i"(1024 * (a & 3)) : "memory");
  else if constexpr (kind == 20) asm volatile("buffer_store_dword %0, %1, %2, %3 offen sc1" :: "v"(o.tg), "v"(o.va), "s"(o.srdL), "s"(o.sg) : "memory");
  else if constexpr (kind == 25) { GAP(); hook(IC(a)); GAP(); }
  else if constexpr (kind == 26) {
    if constexpr (a < 2) asm volatile("ds_read_b128 %0, %1 offset:%2" : "=v"(o.gv[1][a]) : "v"(o.lb5), "i"(4096 + 1024 * a) : "memory");
    else                 asm volatile("ds_read_b32 %0, %1 offset:6144" : "=v"(o.gs[1]) : "v"(o.lb5) : "memory");
  } else if constexpr (kind == 22) op_poll(o);
  else if constexpr (kind == 23) asm volatile("v_cmp_eq_u32_e64 %0, %1, %2" : "=s"(o.seen) : "s"(o.tag), "v"(o.fo));
  else if constexpr (kind == 24) op_there(o);
  else if constexpr (kind == 21) op_offA<(a >> 2), (a & 3)>(o);
  else if constexpr (kind == 31) real_op();
  else if constexpr (kind == 11) op_gate<a>(o);
  else if constexpr (kind == 12) asm volatile("" : "+v"(o.T[a]));
  else if constexpr (kind == 13) op_ldsB<9 * (a >> 2) + (a & 3)>(o);
  else if constexpr (kind == 14) asm volatile("s_barrier" ::: "memory");
  else if constexpr (kind == 15) asm volatile("s_waitcnt lgkmcnt(%4)" : "+v"(o.fb[0]), "+v"(o.fb[1]), "+v"(o.fb[2]), "+v"(o.fb[3]) : "i"(a) : "memory");
  else if constexpr (kind == 16) asm volatile("v_lshlrev_b32_e32 %0, 16, %2\n\tv_and_b32_e32 %1, 0xffff0000, %2" : "=&v"(o.gx[2 * a]), "=v"(o.gx[2 * a + 1]) : "v"(op_word<a / 9, a % 9>(o)));
  else if constexpr (kind == 17) asm volatile("s_waitcnt vmcnt(0)" : "+v"(o.pv[0]), "+v"(o.pv[1]), "+v"(o.pv[2]), "+v"(o.pv[3]), "+v"(o.pv[4]) :: "memory");
}
template <int ST, int Q, typename H, int... Is> DI void op_ts(OPS& o, H& hook, iseq<Is...>) { (op_t<ST, Q, TT[TS[18 * ST + Q] + Is]>(o, hook), ...); }
template <int ST, typename H, int... Qs> DI void op_qs(OPS& o, H& hook, iseq<Qs...>) { (op_ts<ST, Qs>(o, hook, make_iseq<TS[18 * ST + Qs + 1] - TS[18 * ST + Qs]>{}), ...); }

template <typename F, int... Is> DI void for_seq(F& f, iseq<Is...>) { (f(std::integral_constant<int, Is>{}), ...); }
DI void wait_flag(const u32x4 srd, const unsigned p, const unsigned tag) {
#pragma nounroll
  for (unsigned spin = 0; spin < (1u << 20); ++spin) {
    unsigned v;
    asm volatile("buffer_load_dword %0, %1, %2, 0 offen sc1\n\ts_waitcnt vmcnt(0)" : "=v"(v) : "v"(p), "s"(srd) : "memory");
    if (__all(v == tag)) break;
    __builtin_amdgcn_s_sleep(1);
  }
}
DI unsigned op_k0(const unsigned blk, const unsigned w) { return ((blk >> 3) & 7u) * 128u + (~blk & 4u) * 16u + 16u * w; }

constexpr int NFIN = 200, NBL = 45, FVW = 49, FVS = 0, FNR = 37;
constexpr short FT[] = {5064,31000,23000,23001,23002,5001,31000,23003,23004,23005,5002,31000,23006,23007,23008,5003,31000,23009,23010,23011,5083,31000,23012,23013,23014,5018,31000,23015,16000,16001,5017,16008,16009,
  16016,5016,16017,16024,16025,5096,31000,23016,23017,23018,5033,31000,23019,23020,23021,5034,31000,23022,23023,23024,5035,31000,23025,23026,23027,5115,31000,25003,5050,31000,23028,23029,23030,5049,31000,25002,5048,
  31000,23031,13000,20015,1000,31000,25001,22000,1001,13001,13002,20031,1003,31000,25000,1002,15000,16002,20047,1004,13003,13016,1005,15001,16003,20063,1007,13017,13018,1006,15008,16010,29010,20076,1008,27000,13019,1009,
  31000,26000,15009,20088,1011,13032,1010,31000,26001,13033,20100,1012,15016,16011,1013,31000,26002,13034,20112,1015,13035,1014,31000,26003,3000,27001,5068,31000,26004,15017,5005,13048,13049,5006,31000,26005,5007,31000,
  24000,24001,5087,31000,26006,24002,5022,31000,24003,24004,5021,31000,26007,24005,5020,31000,24006,24007,5100,31000,24008,24009,24010,5037,24011,15024,5038,13050,13051,5039,15025,16018,5119,31000,25015,24012,5054,31000,
  24013,24014,24015,5053,31000,25014,24016,24017,24018,24019,5052,31000,25013,24020,4000,31000,24021,24022,22001,30015,2000,31000,24023,24024,24025,2001,31000,12000,30031,2003,31000,24026,24027,24028,24029,2002,31000,
  24030,24031,25012,30047,2004,31000,17000,17001,17002,17003,2005,31000,25004,17004,17005,30063,2007,31000,17006,17007,18000,18001,2006,31000,25005,18002,30076,2008,31000,18003,18004,18005,18006,2009,31000,18007,19000,
  19001,30088,2011,19002,19003,16019,16026,2010,31000,28000,17008,30100,2012,31000,17009,17010,17011,17012,2013,31000,25006,17013,30112,2015,31000,17014,17015,18008,18009,2014,31000,18010,18011,18012,1016,31000,25007,
  18013,18014,16027,13004,13005,1017,31000,18015,19004,19005,1019,19006,19007,15002,1018,31000,28001,17016,1020,31000,17017,17018,17019,1021,31000,25008,17020,1023,31000,17021,17022,17023,1022,31000,18016,18017,18018,
  1024,31000,25009,18019,1025,31000,18020,18021,18022,1027,31000,18023,19008,19009,1026,19010,19011,13006,1028,31000,28002,17024,1029,31000,17025,17026,17027,1031,31000,25010,17028,17029,1030,31000,17030,17031,18024,
  3001,31000,25011,18025,5072,31000,18026,18027,18028,18029,5009,31000,18030,18031,19012,19013,5010,19014,19015,13007,5011,31000,28003,15003,5091,13020,13021,5026,15010,13022,5025,13023,13036,5024,15011,13037,5104,15018,
  13038,5041,13039,13052,5042,15019,13053,5043,15026,13054,13055,5123,31000,25027,15027,5058,16004,16005,16012,5057,16013,16020,16021,14057,14058,5056,31000,25026,16028,4001,14059,22002,2016,16029,13008,2017,31000,12001,
  2019,31000,25025,13009,2018,31000,17032,17033,17034,2020,31000,17035,15004,13010,2021,31000,25024,17036,2023,31000,17037,17038,17039,2022,31000,18032,18033,18034,2024,31000,25016,18035,2025,31000,18036,18037,18038,
  2027,31000,18039,19016,19017,2026,31000,25017,19018,2028,19019,13011,2029,31000,28004,17040,2031,31000,17041,17042,17043,2030,31000,25018,17044,1032,31000,17045,17046,17047,1033,31000,18040,18041,18042,1035,31000,
  25019,18043,1034,31000,18044,18045,18046,1036,31000,18047,19020,19021,19022,1037,19023,15005,13024,1039,31000,28005,17048,1038,31000,17049,17050,17051,1040,31000,25020,17052,17053,1041,31000,17054,17055,18048,1043,
  31000,18049,18050,18051,18052,1042,31000,25021,18053,1044,31000,18054,18055,19024,19025,1045,19026,19027,13025,1047,31000,28006,17056,1046,31000,17057,17058,17059,3002,31000,25022,17060,5076,31000,17061,17062,17063,
  18056,5013,31000,18057,18058,18059,18060,5014,31000,25023,18061,5015,31000,18062,18063,19028,19029,5095,19030,19031,15012,5030,31000,28007,13026,5029,13027,13040,5028,15013,13041,5108,15020,13042,5045,13043,13056,5046,
  15021,13057,5047,15028,13058,13059,5127,31000,25031,15029,5062,14012,14013,5061,16006,14014,14015,14028,5060,31000,25030,16007,14029,4002,16014,14030,14031,22003,2032,31000,25029,16015,14044,2033,31000,12002,14045,
  2035,31000,25028,16022,14046,2034,31000,17064,17065,17066,17067,14047,2036,31000,25032,16023,17068,17069,2037,31000,17070,17071,18064,18065,18066,2039,31000,25033,18067,18068,18069,2038,31000,18070,18071,19032,19033,
  19034,2040,19035,14060,14061,2041,31000,28008,17072,17073,17074,2043,31000,17075,17076,17077,17078,17079,2042,31000,25034,18072,18073,18074,2044,31000,18075,18076,18077,18078,18079,2045,31000,25035,19036,19037,19038,
  2047,19039,16030,14062,14063,2046,31000,28009,17080,17081,17082,1048,31000,17083,17084,17085,17086,17087,16031,13012,13013,1049,31000,25036,18080,18081,18082,1051,31000,18083,18084,18085,18086,18087,1050,19040,19041,
  19042,19043,15006,1052,31000,28010,17088,17089,17090,1053,31000,17091,17092,17093,17094,17095,1055,31000,18088,18089,18090,18091,18092,1054,31000,18093,18094,18095,19044,19045,1056,19046,19047,13014,13015,1057,31000,
  28011,15007,13028,1059,13029,13030,15014,1058,13031,13044,15015,1060,13045,13046,1061,15022,13047,13060,1063,15023,13061,13062,1062,15030,13063,3003,15031,30200,30200,4003,31000,2048,31000,2049,31000,12003,2051,31000,
  2050,31000,17096,17097,17098,17099,2052,31000,2053,31000,17100,17101,17102,17103,18096,2055,31000,18097,18098,18099,18100,18101,2054,31000,18102,18103,19048,19049,19050,2056,19051,2057,31000,28012,17104,17105,17106,
  17107,2059,31000,17108,17109,17110,17111,18104,18105,2058,31000,18106,18107,18108,18109,18110,18111,2060,19052,19053,19054,19055,2061,31000,28013,17112,17113,17114,17115,2063,31000,17116,17117,17118,17119,18112,18113,
  2062,31000,18114,18115,18116,18117,18118,18119,19056,19057,19058,19059,28014,17120,17121,17122,17123,17124,17125,17126,17127,18120,18121,18122,18123,18124,18125,18126,18127,19060,19061,19062,19063,28015,};
constexpr short FS[] = {0,5,10,15,20,25,30,34,38,43,48,53,58,61,66,69,73,78,81,85,88,92,95,99,102,107,111,114,118,122,126,129,132,134,138,141,144,148,152,156,160,164,169,172,175,178,182,187,194,198,203,209,212,219,224,
  231,236,243,247,254,259,265,269,276,280,287,292,300,305,309,313,318,322,327,332,336,341,346,350,354,359,364,369,373,379,385,389,393,396,399,402,405,408,411,414,418,422,426,432,436,439,442,445,449,454,459,463,468,473,
  477,482,487,491,494,498,503,507,512,517,521,526,532,536,540,545,550,555,561,565,571,575,579,584,588,594,600,604,610,614,618,621,624,627,630,633,637,641,644,649,654,659,664,668,673,680,686,693,699,706,710,716,723,729,
  736,742,747,753,763,769,776,782,788,795,802,809,814,819,823,827,830,834,838,841,845,847,849,852,854,860,862,869,876,883,885,892,900,908,913,920,928,962,};
struct OST { u32x4 w, P[4][2]; float o[8], il[4], ls[4]; unsigned vo, so[4]; u32x4 srd; bf16x4 lo[8][2], up[8][2]; };
template <int U> DI void o_store(const OST& t) {
  asm volatile("buffer_store_dwordx4 %0, %1, %2, %3 offen sc1" :: "v"(t.w), "v"(t.vo), "s"(t.srd), "s"(t.so[U & 3] + 131072u * (U >> 2)) : "memory");
}
template <int CODE> DI void fin_op(W4C& c, OST& t, OPS& o) {
  constexpr int kind = CODE / 1000, a = CODE % 1000;
  if constexpr (kind == 1) pv16<AO_((a >> 1) & 7, a >> 4), false>(__builtin_shufflevector(c.vlo[(a >> 1) & 7][a & 1], c.vup[(a >> 1) & 7][a & 1], 0, 1, 2, 3, 4, 5, 6, 7), c.Pk[a >> 4][a & 1]);
  else if constexpr (kind == 2) pv16<AO_((a >> 1) & 7, a >> 4), false>(__builtin_shufflevector(t.lo[(a >> 1) & 7][a & 1], t.up[(a >> 1) & 7][a & 1], 0, 1, 2, 3, 4, 5, 6, 7), t.P[a >> 4][a & 1]);
  else if constexpr (kind == 3) sum64(c, a, c.Pk[a]);
  else if constexpr (kind == 4) sum64(c, a, t.P[a]);
  else if constexpr (kind == 5) { constexpr int kb = (a >> 4) & 3, qb = (a >> 2) & 3; qk16<kb, qb, (a & 3), (a >> 6)>(c.S[1][kb][qb], c.NEGM[qb]); }
  else if constexpr (kind == 14) exp_e<0, a>(c);
  else if constexpr (kind == 16) pack_e<0, a>(c);
  else if constexpr (kind == 22) asm volatile("" :: "v"(c.NEGM[a]));
  else if constexpr (kind == 20) { constexpr int d = a >> 4; asm volatile("s_waitcnt lgkmcnt(%4)" : "+v"(c.vlo[d][0]), "+v"(c.vlo[d][1]), "+v"(c.vup[d][0]), "+v"(c.vup[d][1]) : "i"(a & 15) : "memory"); }
  else if constexpr (kind == 23) tr_read_<a, 0>(c);
  else if constexpr (kind == 29) asm volatile("s_waitcnt vmcnt(%0) lgkmcnt(0)\n\ts_barrier" :: "i"(a) : "memory");
  else if constexpr (kind == 12) asm volatile("v_add_f32_e32 %0, %2, %3\n\tv_rcp_f32_e32 %1, %0" : "=&v"(t.ls[a]), "=v"(t.il[a]) : "v"(c.L[a][0]), "v"(c.L[a][1]), "v"(c.L[a]));
  else if constexpr (kind == 13) exp_e<1, a>(c);
  else if constexpr (kind == 15) pack_to<1, a>(c, t.P);
  else if constexpr (kind == 17) t.o[a & 7] = acc_read<a>();
  else if constexpr (kind == 18) {
    asm volatile("v_mul_f32_e32 %0, %0, %1" : "+v"(t.o[a & 7]) : "v"(t.il[a >> 5]));
    if constexpr (a >= 8 && (a & 7) == 0) asm volatile("" :: "v"(t.w));
  } else if constexpr (kind == 19) { GAP(); const unsigned w = cvtpk(t.o[2 * (a & 3)], t.o[2 * (a & 3) + 1]); asm volatile("" :: "v"(w)); t.w[a & 3] = w; GAP(); }
  else if constexpr (kind == 24) tr_rd<a, 0x4000>(c, t.lo, t.up);
  else if constexpr (kind == 25) op_reqB<35 + a>(o);
  else if constexpr (kind == 26) asm volatile("buffer_load_dwordx4 %0, %1, %2 offen offset:%c3 sc1 lds" :: "v"(c.pfv), "s"(c.srdP), "s"(c.pfo + 4096u * (a >> 2)), "i"(1024 * (a & 3)) : "memory");
  else if constexpr (kind == 27) asm volatile("s_mov_b32 m0, %0" :: "s"(c.pfm0 + 4096u * a) : "m0", "memory");
  else if constexpr (kind == 28) o_store<a>(t);
  else if constexpr (kind == 31) real_op();
  else if constexpr (a == 200) asm volatile("s_nop 0");
  else { constexpr int d = a >> 4; asm volatile("s_waitcnt lgkmcnt(%4)" : "+v"(t.lo[d][0]), "+v"(t.lo[d][1]), "+v"(t.up[d][0]), "+v"(t.up[d][1]) : "i"(a & 15) : "memory"); }
}
template <int G, int... Is> DI void fin_gap(W4C& c, OST& t, OPS& o, iseq<Is...>) { (fin_op<FT[FS[G] + Is]>(c, t, o), ...); GAP(); }
template <int... Gs> DI void fin_gaps(W4C& c, OST& t, OPS& o, iseq<Gs...>) { (fin_gap<Gs>(c, t, o, make_iseq<FS[Gs + 1] - FS[Gs]>{}), ...); }

template <int J, int H> DI void redo_q(const __amdgpu_buffer_rsrc_t srdW, const u32x4 srdN, const unsigned vw, const unsigned vx, const unsigned sw, const unsigned sx) {
  f32x4 q[4] = {};
#pragma nounroll
  for (unsigned ks = 0; ks < DM / 32; ++ks) {
    u32x4 w;
#pragma unroll
    for (int e = 0; e < 4; ++e) {
      const unsigned so = sw + (64u * J + 8u * H) + WRS * (32u * ks + 2u * e);
      w[e] = (unsigned)__builtin_amdgcn_raw_buffer_load_b16(srdW, vw, so, 0) | ((unsigned)__builtin_amdgcn_raw_buffer_load_b16(srdW, vw, so + WRS, 0) << 16);
    }
    asm volatile("s_nop 1" : "+v"(w), "+v"(q[0]), "+v"(q[1]), "+v"(q[2]), "+v"(q[3]));
#pragma unroll
    for (int qb = 0; qb < 4; ++qb) {
      u32x4 x;
      asm volatile("buffer_load_dwordx4 %0, %1, %2, %3 offen sc1\n\ts_waitcnt vmcnt(0)" : "=v"(x) : "v"(vx), "s"(srdN), "s"(sx + 64u * ks + 16u * XRS * qb) : "memory");
      sum16<false>(q[qb], w, x);
    }
  }
  asm volatile("s_nop 7" : "+v"(q[0]), "+v"(q[1]), "+v"(q[2]), "+v"(q[3]));
  const float C = 0.12751743082f;
  unsigned r[8];
#pragma unroll
  for (int qb = 0; qb < 4; ++qb) { r[2 * qb] = cvtpk(C * q[qb][0], C * q[qb][1]); r[2 * qb + 1] = cvtpk(C * q[qb][2], C * q[qb][3]); }
  asm volatile("s_nop 1" : "+v"(r[0]), "+v"(r[1]), "+v"(r[2]), "+v"(r[3]), "+v"(r[4]), "+v"(r[5]), "+v"(r[6]), "+v"(r[7]));
  acc_write<AQ_(0, J) + 2 * H>(r[0]); acc_write<AQ_(0, J) + 2 * H + 1>(r[1]); acc_write<AQ_(1, J) + 2 * H>(r[2]); acc_write<AQ_(1, J) + 2 * H + 1>(r[3]);
  acc_write<AQ_(2, J) + 2 * H>(r[4]); acc_write<AQ_(2, J) + 2 * H + 1>(r[5]); acc_write<AQ_(3, J) + 2 * H>(r[6]); acc_write<AQ_(3, J) + 2 * H + 1>(r[7]);
}
template <int I> DI void redo_kload(const unsigned vo, const __amdgpu_buffer_rsrc_t srd, const unsigned so) {
  asm volatile("buffer_load_dwordx4 a[%c3:%c4], %0, %1, %2 offen offset:%c5" :: "v"(vo), "s"(srd), "s"(so + 4096u * (I >> 2)), "i"(192 + 4 * I), "i"(195 + 4 * I), "i"(64 * (I & 3)) : "memory");
}
template <int CM, int... Ms> DI void redo_s(W4C& c, const unsigned vk, const unsigned so, f32x4 (&T)[2][4], iseq<Ms...>) {
  redo_kload<0>(vk, c.srdK, so); redo_kload<1>(vk, c.srdK, so); redo_kload<2>(vk, c.srdK, so); redo_kload<3>(vk, c.srdK, so);
  redo_kload<4>(vk, c.srdK, so); redo_kload<5>(vk, c.srdK, so); redo_kload<6>(vk, c.srdK, so); redo_kload<7>(vk, c.srdK, so);
  asm volatile("s_waitcnt vmcnt(0)" ::: "memory");
  (qk16<(Ms >> 4), ((Ms >> 2) & 3), (Ms & 3), ((Ms & 3) ? 0 : CM)>(T[Ms >> 4][(Ms >> 2) & 3], c.NEGM[(Ms >> 2) & 3]), ...);
  asm volatile("s_nop 7" : "+v"(T[0][0]), "+v"(T[0][1]), "+v"(T[0][2]), "+v"(T[0][3]), "+v"(T[1][0]), "+v"(T[1][1]), "+v"(T[1][2]), "+v"(T[1][3]));
}
template <int DB> DI void redo_pv(const W4C& c, const unsigned vv, const unsigned so, const u32x4 (&P)[4]) {
  u32x4 v;
#pragma unroll
  for (int e = 0; e < 4; ++e) {
    const unsigned s = so + (896u + 32u * DB) + KRS * (16u * (e >> 1) + 2u * (e & 1));
    v[e] = (unsigned)__builtin_amdgcn_raw_buffer_load_b16(c.srdV, vv, s, 0) | ((unsigned)__builtin_amdgcn_raw_buffer_load_b16(c.srdV, vv, s + KRS, 0) << 16);
  }
  asm volatile("s_nop 1" : "+v"(v));
  const bf16x8 vf = __builtin_bit_cast(bf16x8, v);
  pv16<AO_(DB, 0), false>(vf, P[0]); pv16<AO_(DB, 1), false>(vf, P[1]); pv16<AO_(DB, 2), false>(vf, P[2]); pv16<AO_(DB, 3), false>(vf, P[3]);
  GAP();
}
template <int U> DI void redo_unit(OST& t) {
  t.o[0] = acc_read<8 * U>(); t.o[1] = acc_read<8 * U + 1>(); t.o[2] = acc_read<8 * U + 2>(); t.o[3] = acc_read<8 * U + 3>();
  t.o[4] = acc_read<8 * U + 4>(); t.o[5] = acc_read<8 * U + 5>(); t.o[6] = acc_read<8 * U + 6>(); t.o[7] = acc_read<8 * U + 7>();
  const float il = t.il[U >> 2];
  t.w = u32x4{cvtpk(t.o[0] * il, t.o[1] * il), cvtpk(t.o[2] * il, t.o[3] * il), cvtpk(t.o[4] * il, t.o[5] * il), cvtpk(t.o[6] * il, t.o[7] * il)};
  o_store<U>(t);
  asm volatile("s_nop 1" :: "v"(t.w));
}
template <int R0, int... Is> DI void redo_zero(const unsigned z, iseq<Is...>) { (acc_write<R0 + Is>(z), ...); }
template <int... Is> DI void redo_units(OST& t, iseq<Is...>) { (redo_unit<Is>(t), ...); }
template <int F0, int... Is> DI void redo_reqs(OPS& o, iseq<Is...>) { (op_reqB<F0 + Is>(o), ...); }
DI void redo_rows(W4C& c, OST& t, OPS& o, const u16* wq, const unsigned row0, const unsigned h, const unsigned tag) {
  unsigned l;
  asm volatile("s_waitcnt vmcnt(0)\n\tv_mbcnt_lo_u32_b32 %0, -1, 0\n\tv_mbcnt_hi_u32_b32 %0, -1, %0" : "=v"(l) :: "memory");
  const unsigned c16 = l & 15u, g = l >> 4;
  wait_flag(c.srdS, WS_KEY + 8u * (row0 + l) + 4u, tag);
  {
    const __amdgpu_buffer_rsrc_t srdW = mk_srd(wq, DM * HQ * DH * 2); const u32x4 srdN = c.srdS;
    const unsigned vw = WRS * 8u * g + 16u * (c16 >> 2) + 2u * (c16 & 3u), vx = XRS * c16 + 16u * g, sw = 256u * h, sx = WS_XN + XRS * row0;
    redo_q<0, 0>(srdW, srdN, vw, vx, sw, sx); redo_q<0, 1>(srdW, srdN, vw, vx, sw, sx); redo_q<1, 0>(srdW, srdN, vw, vx, sw, sx); redo_q<1, 1>(srdW, srdN, vw, vx, sw, sx);
    redo_q<2, 0>(srdW, srdN, vw, vx, sw, sx); redo_q<2, 1>(srdW, srdN, vw, vx, sw, sx); redo_q<3, 0>(srdW, srdN, vw, vx, sw, sx); redo_q<3, 1>(srdW, srdN, vw, vx, sw, sx);
  }
  const unsigned vk = KRS * c16 + 16u * g, vv = KRS * 4u * g + 2u * c16;
  f32x4 T[2][4];
  float m[4] = {-__builtin_inff(), -__builtin_inff(), -__builtin_inff(), -__builtin_inff()};
#pragma nounroll
  for (unsigned u = 0; u < SEQ / 32; ++u) {
    redo_s<2>(c, vk, 32u * KRS * u, T, make_iseq<32>{});
#pragma unroll
    for (int q = 0; q < 4; ++q)
#pragma unroll
      for (int e = 0; e < 8; ++e) m[q] = max2f(m[q], T[e >> 2][q][e & 3]);
  }
#pragma unroll
  for (int q = 0; q < 4; ++q) {
    asm volatile("s_nop 1" : "+v"(m[q]));
    auto p1 = __builtin_amdgcn_permlane16_swap(__float_as_uint(m[q]), __float_as_uint(m[q]), false, false);
    m[q] = max2f(__uint_as_float(p1[0]), __uint_as_float(p1[1]));
    asm volatile("s_nop 1" : "+v"(m[q]));
    auto p2 = __builtin_amdgcn_permlane32_swap(__float_as_uint(m[q]), __float_as_uint(m[q]), false, false);
    m[q] = -max2f(__uint_as_float(p2[0]), __uint_as_float(p2[1]));
    c.NEGM[q] = f32x4{m[q], m[q], m[q], m[q]};
    c.L[q] = f32x4{0.f, 0.f, 0.f, 0.f};
  }
  redo_zero<0>(0u, make_iseq<128>{});
  asm volatile("s_nop 1" : "+v"(c.NEGM[0]), "+v"(c.NEGM[1]), "+v"(c.NEGM[2]), "+v"(c.NEGM[3]), "+v"(c.L[0]), "+v"(c.L[1]), "+v"(c.L[2]), "+v"(c.L[3]));
#pragma nounroll
  for (unsigned u = 0; u < SEQ / 32; ++u) {
    redo_s<1>(c, vk, 32u * KRS * u, T, make_iseq<32>{});
    u32x4 P[4];
#pragma unroll
    for (int q = 0; q < 4; ++q)
#pragma unroll
      for (int e = 0; e < 4; ++e) P[q][e] = cvtpk(__builtin_amdgcn_exp2f(T[e >> 1][q][2 * (e & 1)]), __builtin_amdgcn_exp2f(T[e >> 1][q][2 * (e & 1) + 1]));
    asm volatile("s_nop 1" : "+v"(P[0]), "+v"(P[1]), "+v"(P[2]), "+v"(P[3]));
#pragma unroll
    for (int q = 0; q < 4; ++q) sum16<false>(c.L[q], c.ONE, P[q]);
    const unsigned so = 32u * KRS * u;
    redo_pv<0>(c, vv, so, P); redo_pv<1>(c, vv, so, P); redo_pv<2>(c, vv, so, P); redo_pv<3>(c, vv, so, P);
    redo_pv<4>(c, vv, so, P); redo_pv<5>(c, vv, so, P); redo_pv<6>(c, vv, so, P); redo_pv<7>(c, vv, so, P);
  }
  asm volatile("s_nop 7" : "+v"(c.L[0]), "+v"(c.L[1]), "+v"(c.L[2]), "+v"(c.L[3]));
#pragma unroll
  for (int q = 0; q < 4; ++q) t.il[q] = __builtin_amdgcn_rcpf(c.L[q][0]);
  redo_units(t, make_iseq<16>{});
  redo_reqs<35>(o, make_iseq<FNR>{});
  asm volatile("s_waitcnt vmcnt(0)" ::: "memory");
}

DI void oproj_phase(OPS& o, char* lds, u16* __restrict__ out, const unsigned wid  ) {
  const unsigned ldsb = 0u, tag = o.tag;
  unsigned fe;
  op_poll(o);
  o.lb5 = ldsb + SD_BASE + BP_W * wid + o.va; o.lb1 = ldsb + 4096u * wid + o.va; o.lb2 = ldsb + 0x4000u + 8192u * wid + o.va;
  asm volatile("s_waitcnt vmcnt(%6)" : "+v"(o.gv[0][0]), "+v"(o.gv[0][1]), "+v"(o.gs[0]), "+v"(o.gv[1][0]), "+v"(o.gv[1][1]), "+v"(o.gs[1]) : "i"(FVW + 1) : "memory");
  static_assert(FVS == 0, "the last request of the final phase is a store");
  op_ldsBs<0>(o, make_iseq<9>{}); op_ldsBs<13>(o, make_iseq<22>{});
  asm volatile("ds_write_b128 %0, %1 offset:4096\n\tds_write_b128 %0, %2 offset:5120\n\tds_write_b32 %0, %3 offset:6144" :: "v"(o.lb5), "v"(o.gv[1][0]), "v"(o.gv[1][1]), "v"(o.gs[1]) : "memory");
  o.idx = (o.va & 128u) ? 0xeeeeeeeeu : 0x44444444u; o.D[0][4] = f32x4{0.f, 0.f, 0.f, 0.f};
  asm volatile("" : "+v"(o.idx), "+v"(o.D[0][4]));
  redo_zero<240>(0u, make_iseq<12>{});
  PS_AT(45); ps_touch(20u + wid);
  GAP();
  {
    u32x4 sr = o.srdL; sr[2] = o.sf + 4u;
    redo_reqs<35 + FNR>(o, make_iseq<OP_NR - 35 - FNR>{});
    asm volatile("s_waitcnt vmcnt(%7)\n\tbuffer_store_dword %2, %3, %4, %5 offen sc1\n\ts_waitcnt vmcnt(%7)\n\tv_cmp_eq_u32_e64 %0, %6, %1"
                 : "=s"(o.seen), "+v"(o.fo) : "v"(tag), "v"(o.va), "s"(sr), "s"(o.sf), "s"(tag), "i"(OP_NR - 34 - FNR) : "memory");
  }
  op_there(o);
  op_reqA0(o, make_iseq<OP_NI>{});
  GAP();
  unsigned blk = blockIdx.x;
  asm volatile("" : "+s"(blk));
  const unsigned sn = o.sn;
  asm volatile("s_waitcnt lgkmcnt(0)" : "+v"(o.fb[0]), "+v"(o.fb[1]), "+v"(o.fb[2]), "+v"(o.fb[3]), "+v"(o.fb[4]), "+v"(o.fb[5]), "+v"(o.fb[6]), "+v"(o.fb[7]), "+v"(o.fb[8]), "+v"(o.fb[9]), "+v"(o.fb[10]),
               "+v"(o.fb[11]), "+v"(o.fb[12]), "+v"(o.fb[13]), "+v"(o.fb[14]), "+v"(o.fb[15]), "+v"(o.fb[16]), "+v"(o.fb[17]), "+v"(o.fb[18]), "+v"(o.fb[19]), "+v"(o.fb[20]), "+v"(o.fb[21]) :: "memory");
  if (__builtin_expect(o.bad != 0u, 0)) {
    unsigned va = o.va;
    asm volatile("" : "+v"(va));
    const unsigned lane = va >> 4, row0 = (o.sa >> 13 ^ 64u) & 2032u, ax[2] = {op_ax<0>(va), op_ax<1>(va)};
    unsigned selr, an[2] = {ax[0], ax[1]};
    wait_flag(o.srdS, 8u * (lane & 7u) + 4u, tag);
    wait_flag(o.srdS, 4u * H_X + 288u * ((blk >> 3) & 7u) + 16u * (lane < 18u ? lane : 0u), tag);
    asm volatile("buffer_load_dword %0, %1, %2, 0 offen sc1\n\ts_waitcnt vmcnt(0)" : "=v"(selr) : "v"(8u * (lane & 7u)), "s"(o.srdL) : "memory");
#pragma nounroll
    for (unsigned t = 0; t < (unsigned)NSEL; ++t) {
      const unsigned ro = (((unsigned)__builtin_amdgcn_readlane(selr, t) & 0xffffu) - row0) * XRS_;
#pragma unroll
      for (int j = 0; j < 2; ++j) { const unsigned d = ax[j] - ro; an[j] = d < XRS_ ? (SEQ + t - row0) * XRS_ + d : an[j]; }
    }
    asm volatile("buffer_load_dwordx4 %0, %3, %5, %6 offen sc1\n\tbuffer_load_dwordx4 %1, %3, %5, %6 offen offset:64 sc1\n\tbuffer_load_dword %2, %4, %5, %6 offen sc1\n\ts_waitcnt vmcnt(0)"
                 : "=&v"(o.gv[0][0]), "=&v"(o.gv[0][1]), "=&v"(o.gs[0]) : "v"(an[0]), "v"(an[1]), "s"(o.srdS), "s"(sn + WS_XN) : "memory");
  }
  __amdgpu_buffer_rsrc_t srdO; unsigned ax[2], fes, t;
#define SC(g, x, e) if constexpr (M == (g)) { x = (e); asm volatile("" : "+s"(x)); }
#define SA(g, x, e) if constexpr (M == (g)) x = (e);
#define VU(g, x, e) if constexpr (M == (g)) { x = (e); asm volatile("" :: "v"(x)); GAP(); }
#define VC(g, x, e) if constexpr (M == (g)) { x = (e); asm volatile("" : "+v"(x)); }
  auto prep = [&](auto M_) __attribute__((always_inline)) {
    constexpr int M = decltype(M_)::value;
    if constexpr (M < 6) { acc_write<op_ac(1, 1 + M / 2, 4) + 2 * (M & 1)>(0u); acc_write<op_ac(1, 1 + M / 2, 4) + 2 * (M & 1) + 1>(0u); }
    VC(6, o.ra[0], o.lb5 + BP_W * ((wid ^ 1u) - wid)) VC(7, o.ra[1], o.lb5 + BP_W * ((wid ^ 2u) - wid)) VC(8, o.ra[2], o.lb2 + 8192u * ((wid ^ 3u) - wid))
    SC(9, t, 5120u * wid + WS_EX) SC(10, o.sw[0], blk * 20480u + t) SC(10, o.sw[1], o.sw[0] + 4096u) SC(11, o.si[0], (blk ^ 4u) * 20480u + t) SC(11, o.si[1], o.si[0] + 4096u)
    SC(12, o.sg, 128u * blk + 4u * H_E) SC(12, fes, o.sg ^ 512u) SC(13, t, (o.sg + 4u) & ((int)(wid - 1u) >> 31)) SC(14, srdO, mk_srd(out, SEQ * DM * 2))
    VC(15, ax[0], op_ax<0>(o.va)) VC(16, ax[1], op_ax<1>(o.va))
  };
  asm volatile("ps"); prep(IC(9)); asm volatile("ps"); prep(IC(10)); asm volatile("ps"); prep(IC(11)); asm volatile("ps"); prep(IC(12)); asm volatile("ps"); prep(IC(13)); asm volatile("ps"); prep(IC(14));
  GAP();
  op_again<0, OP_NI>(o);
  o.sl += 128u; o.srdL[2] += 128u;
  asm volatile("ps");
  auto step = [&](auto ST) __attribute__((always_inline)) {
    constexpr int st = decltype(ST)::value, A = st % (OP_LA + 1);
    if constexpr (st == 16) {
    }
    if constexpr (st == 24) {
    }
    if constexpr (st == 30) {
    }
    if constexpr (st == OP_SD) {
      asm volatile("s_waitcnt vmcnt(%1)" : "+v"(fe) : "i"(OP_FEW) : "memory");
      if (__builtin_expect(!__all(fe == tag), 0)) wait_flag(o.srdS, fes, tag);
    }
    if constexpr (st == 8) { asm volatile("s_waitcnt vmcnt(%0)" :: "i"(OP_P4W) : "memory"); op_again<8, 4 * (8 - OP_DS)>(o); }
    if constexpr (st == 28) { o.D[1][4] = f32x4{0.f, 0.f, 0.f, 0.f}; asm volatile("s_nop 0" : "+v"(o.D[1][4])); }
    asm volatile("s_waitcnt vmcnt(%4)" : "+v"(o.fa[A][0]), "+v"(o.fa[A][1]), "+v"(o.fa[A][2]), "+v"(o.fa[A][3]) : "i"(OPW[st]) : "memory");
    if constexpr (st == OP_DS) asm volatile("" : "+v"(o.fo));
    if constexpr (st == 29) asm volatile("" : "+v"(o.gv[1][0]), "+v"(o.gv[1][1]), "+v"(o.gs[1]));
    GAP();
    op_qs<st>(o, prep, make_iseq<18>{});
    GAP();
    if constexpr (st == OP_SP) { const u32x4 sh = o.srdS; const unsigned so = fes; asm volatile("buffer_load_dword %0, off, %1, %2 sc1" : "=v"(fe) : "s"(sh), "s"(so) : "memory"); }
    if constexpr (st == OP_SF - 2) { o.tg = tag; o.srdL[2] = t; asm volatile("" : "+v"(o.tg), "+s"(o.srdL)); }
    GAP();
  };
  for_seq(step, make_iseq<32>{});
  op_qs<32>(o, prep, make_iseq<1>{});
  auto fin2 = [&](const float s0, const float s1, const int wd) __attribute__((always_inline)) {
    const unsigned sp = cvtpk(s0, s1), pp = cvtpk(bflo(sp) * o.gx[2 * wd], bfhi(sp) * o.gx[2 * wd + 1]);
    return cvtpk(o.gx[18 + 2 * wd] + bflo(pp), o.gx[19 + 2 * wd] + bfhi(pp));
  };
  auto fin = [&](auto J_) __attribute__((always_inline)) {
    constexpr int j = decltype(J_)::value;
    const f32x4 s = o.D[1][2 * j] + o.T[2 * j];
    const __amdgpu_buffer_rsrc_t so = srdO; const unsigned sc = sn, va = ax[j >> 1];
    if constexpr (j < 2) {
      const f32x4 r = o.D[1][2 * j + 1] + o.T[2 * j + 1];
      const u32x4 ov = {fin2(s[0], s[1], 4 * j), fin2(s[2], s[3], 4 * j + 1), fin2(r[0], r[1], 4 * j + 2), fin2(r[2], r[3], 4 * j + 3)};
      asm volatile("buffer_store_dwordx4 %0, %1, %2, %3 offen offset:%c4\n\ts_nop 1" :: "v"(ov), "v"(va), "s"(so), "s"(sc), "i"(64 * j) : "memory");
    } else {
      auto p0 = __builtin_amdgcn_permlane32_swap(__float_as_uint(s[0]), __float_as_uint(s[2]), false, false), p1 = __builtin_amdgcn_permlane32_swap(__float_as_uint(s[1]), __float_as_uint(s[3]), false, false);
      const unsigned ov = fin2(__uint_as_float(p0[0]) + __uint_as_float(p0[1]), __uint_as_float(p1[0]) + __uint_as_float(p1[1]), 8);
      asm volatile("buffer_store_dword %0, %1, %2, %3 offen" :: "v"(ov), "v"(va), "s"(so), "s"(sc) : "memory");
    }
  };
  fin(IC(2)); fin(IC(0));
  GAP();
  fin(IC(1));
}

extern "C" __global__ __launch_bounds__(256, 1) __attribute__((aligned(4096)))
void k_attn(char* __restrict__ ws, const u16* __restrict__ wq, const u16* __restrict__ xres, const u16* __restrict__ w_r, const u16* __restrict__ Kc, const u16* __restrict__ Vc,
            const u16* __restrict__ wo, const u16* __restrict__ wg, const u16* __restrict__ wu, const u16* __restrict__ wd, u16* __restrict__ out  ) {
  asm volatile(PS_MACROS ::: "memory");
  extern __shared__ __attribute__((aligned(16))) char lds[];
  const unsigned tid = threadIdx.x, lane = tid & 63u, wid = tid >> 6, l32 = lane & 31u, hi = lane >> 5;
  constexpr int T = NT;
  const float C = 0.12751743082f;
  W4C c;
  c.pdum = c.ldsb + PF_DUM;
  asm volatile("" : "+s"(c.pdum) : "s"((unsigned)(size_t)lds));
  c.m0k = rfl(c.ldsb + KRING + 4096u * wid);
  c.sofk = rfl(KRS * 16u * wid) + (32u / 1) * KTILEB * ((blockIdx.x >> 3) & 0u);

  const unsigned blk = blockIdx.x, xg = blk & 7u, ii = blk >> 3;
  const unsigned kvh = 2u * xg + (ii >> 4), h = 2u * kvh + ((ii >> 3) & 1u), yb = ii & 7u;
  const unsigned row0 = 256u * yb + 64u * wid;
  const bool moe = blk < (unsigned)MOE_WG;
  const bool ret = !moe && blk < 208u;
  unsigned tag = 0u, gen;
  const unsigned lrow = 8u * blk + 2u * wid + hi;
  unsigned psc, cwo; u32x4 srdC;
  {
    QP p; LNJ lj; u32x4 cw; unsigned long long no; unsigned pb0, pb1, pb2, pb3, csw, vg, vs, kk, bad, l16, g16, ta, tb; unsigned long long pbase;
    lj.up = hi != 0u;
    const unsigned vl = 16u * l32 + XRS * hi, vl3 = 4u * l32 + XRS * hi, srow = XRS * 8u * blk + XRS * 2u * rfl(wid);
    {
      const unsigned r8 = (lane >> 2) & 7u, vws = WRS * ((r8 & 3u) + 8u * (r8 >> 2)) + 64u * hi + 16u * ((lane & 3u) ^ ((lane >> 4) & 1u));
      p.vw[0] = vws + 896u; p.vw[1] = vws; p.vw[2] = vws + (4u * WRS - 2048u + 896u); p.vw[3] = vws + (4u * WRS - 2048u);
#pragma unroll
      for (int q = 0; q < 4; ++q) p.vx[q] = XRS * (lane >> 3) + 16u * ((lane & 7u) ^ (4u * (q & 1) + (lane >> 4))) + (8u * XRS - 1024u) * q;
    }
    p.m0x = rfl(c.ldsb + 0x4000u + 8192u * wid);
    p.sw[1] = rfl(WRS * 16u * wid + 256u * h);
    p.sx[1] = rfl(XRS * row0);
    kk = (yb ^ (ii >> 3)) & 1u;
    asm volatile("s_nop 4" : "+s"(c.m0k), "+s"(c.sofk), "+s"(p.m0x), "+s"(p.sw[1]), "+s"(p.sx[1]), "+s"(kk));
    asm volatile("" : "+v"(p.vw[0]), "+v"(p.vw[1]), "+v"(p.vw[2]), "+v"(p.vw[3]), "+v"(p.vx[0]), "+v"(p.vx[1]), "+v"(p.vx[2]), "+v"(p.vx[3]));
    GAP();
    p.srdW = __builtin_amdgcn_make_buffer_rsrc((void*)((const char*)wq - 896), (short)0, DM * HQ * DH * 2 + 896, SRD_WORD3);
    p.srdX = __builtin_amdgcn_make_buffer_rsrc((void*)xres, (short)0, SEQ * DM * 2, SRD_WORD3);
    auto vm = [&](auto I_) __attribute__((always_inline)) {
      constexpr int I = decltype(I_)::value % 1000; constexpr VOp o = vop(I); constexpr int q = o.q & 3;
      auto dma = [&](const unsigned m0, auto MO_, const unsigned vo, const __amdgpu_buffer_rsrc_t srd, const unsigned so) __attribute__((always_inline)) {
        if constexpr (decltype(I_)::value >= 1000 || (o.pos < 0 && vq_newm0(I))) asm volatile("s_add_u32 m0, %0, %1" :: "s"(m0), "i"(decltype(MO_)::value) : "m0", "scc", "memory");
        if constexpr (decltype(I_)::value < 1000) {
          if constexpr (vq_newm0(I)) { GAP(); p.so = so; }
          if constexpr (o.pos < 0 && vq_newm0(I)) asm volatile("s_nop 0");
          const unsigned s_ = p.so;
          asm volatile("buffer_load_dwordx4 %0, %1, %2 offen offset:%c3 lds" :: "v"(vo), "s"(srd), "s"(s_), "i"(1024 * q) : "memory");
        }
      };
      constexpr int kd = o.st == 0 ? 1 : (o.st & 1) ? 2 : 0;
      if constexpr (o.kind == VK_S && o.q < 4) dma(c.m0k, IC(QP_STG * (o.st % 3)), p.vw[q], p.srdW, p.sw[kd] + (o.st << 19));
      else if constexpr (o.kind == VK_S) dma(p.m0x, IC(QP_STG * (o.st % 3) + (o.q >= 8 ? 4096u : 0u)), p.vx[q], p.srdX, p.sx[kd] + ((o.st << 7) + (o.q >= 8 ? 32u * XRS : 0u)));
      else if constexpr (o.kind == VK_W8) dma(c.m0k, IC(QP_W8), p.vw[q], p.srdW, p.sw[kd] + (o.st << 19));
      else if constexpr (o.kind == VK_KV && o.q < 8) dma(c.m0k, IC(0x4000u * (o.q >> 2)), (q & 2) ? c.ksx[q & 1] : c.vksrc[q & 1], c.srdK, KTILEB * (o.q >> 2) + c.sofk);
      else if constexpr (o.kind == VK_KV) dma(c.m0k, IC(VRING), (q & 1) ? c.vvsrc : c.vvsrc0, c.srdV, c.sofk);
      else if constexpr (o.kind == VK_V1) dma(c.m0k, IC(VRING + 0x4000), (q & 1) ? c.vvsrc : c.vvsrc0, c.srdV, KTILEB + c.sofk);
      else if constexpr (o.kind == VK_K2) dma(c.m0k, IC(0), (q & 2) ? c.ksx[q & 1] : c.vksrc[q & 1], c.srdK, KTILEB * 2u + c.sofk);
      else if constexpr (o.kind == VK_PF)
        asm volatile("s_mov_b32 m0, %0\n\ts_add_u32 %1, %1, %4\n\tbuffer_load_dwordx4 %2, %3, %1 offen sc1 lds\n\ts_add_u32 %0, %0, 0x400"
                     : "+s"(c.pfm0), "+s"(c.pfo) : "v"(c.pfv), "s"(c.srdP), "s"(c.pfst) : "m0", "memory", "scc");
      else if constexpr (o.kind == VK_CW) {
        unsigned long long pa; unsigned po;
        asm volatile("s_getpc_b64 s[20:21]\n\ts_add_u32 s20, s20, k_attn@rel32@lo+4\n\ts_addc_u32 s21, s21, k_attn@rel32@hi+12\n\ts_mov_b64 %0, s[20:21]" : "=s"(pa) :: "scc", "s20", "s21");
        asm volatile("s_mov_b32 %0, .Lk_end-k_attn" : "=s"(po));
        srdC = mk_srd4((const void*)pa, po); cwo = (blk >> 3) * 4096u + 1024u * rfl(wid);
        asm volatile("" : "+s"(srdC), "+s"(cwo));
        asm volatile("buffer_load_dwordx4 %0, %1, %2, %3 offen" : "=&v"(cw) : "v"(16u * lane), "s"(srdC), "s"(cwo) : "memory");
      } else if constexpr (o.kind == VK_GEN)
        asm volatile("buffer_load_dword %0, off, %1, 0 offset:%c2 sc1" : "=&v"(gen) : "s"(c.srdS), "i"(4 * H_G) : "memory");
      else if constexpr (o.kind == VK_A && o.q < 2)
        asm volatile("buffer_load_dwordx4 %0, %1, %2, %3 offen offset:%c4" : "=&v"(lj.a[o.q]) : "v"(vl), "s"(p.srdX), "s"(srow), "i"(512 * o.q) : "memory");
      else if constexpr (o.kind == VK_A) asm volatile("buffer_load_dword %0, %1, %2, %3 offen offset:1024" : "=&v"(lj.a2) : "v"(vl3), "s"(p.srdX), "s"(srow) : "memory");
      else if constexpr (o.kind == VK_WR && o.q < 2)
        asm volatile("buffer_load_dwordx4 %0, %1, %2, 0 offen offset:%c3" : "=&v"(lj.w[o.q]) : "v"(16u * l32), "s"(__builtin_amdgcn_make_buffer_rsrc((void*)w_r, (short)0, DM * 2, SRD_WORD3)), "i"(512 * o.q) : "memory");
      else if constexpr (o.kind == VK_WR)
        asm volatile("buffer_load_dword %0, %1, %2, 0 offen offset:1024" : "=&v"(lj.w2) : "v"(4u * l32), "s"(__builtin_amdgcn_make_buffer_rsrc((void*)w_r, (short)0, DM * 2, SRD_WORD3)) : "memory");
      else if constexpr (o.kind == VK_STS) {
        const u32x4 g = {__float_as_uint(C * lj.rden), tag, __float_as_uint(-lj.mb), tag};
        asm volatile("buffer_store_dwordx4 %0, %1, %2, %3 offen sc1\n\ts_nop 1" :: "v"(g), "v"(2u * vg), "s"(c.srdS), "s"(WS_ST) : "memory");
      } else if constexpr (o.kind == VK_KEY) {
        const u32x2 g = {lj.key, tag};
        asm volatile("buffer_store_dwordx2 %0, %1, %2, %3 offen sc1" :: "v"(g), "v"(vg), "s"(c.srdS), "s"(WS_KEY) : "memory");
      } else if constexpr (o.kind == VK_GAT) {
        asm volatile("buffer_load_dwordx4 %0, %1, %2, 0 offen offset:%c3 sc1" : "=&v"(p.g[o.q]) : "v"(vs), "s"(c.srdS), "i"(256 * o.q) : "memory");
      } else if constexpr (o.kind == VK_XNS) {
        if constexpr (o.q < 2) asm volatile("buffer_store_dwordx4 %0, %1, %2, %3 offen offset:%c4 sc1\n\ts_nop 1" :: "v"(lj.a[o.q & 1]), "v"(vl), "s"(c.srdS), "s"(srow + WS_XN), "i"(512 * o.q) : "memory");
        else                   asm volatile("buffer_store_dword %0, %1, %2, %3 offen offset:1024 sc1" :: "v"(lj.a2), "v"(vl3), "s"(c.srdS), "s"(srow + WS_XN) : "memory");
      }
    };
    auto hook = [&](auto P_, auto J_) __attribute__((always_inline)) {
      constexpr int pos = decltype(P_)::value, J = decltype(J_)::value;
      if constexpr (pos == 70) ps_touch(psc);
      if constexpr (pos == 71) ps_touch(psc & 3u);
      if constexpr (pos == 1) {
        asm volatile("s_waitcnt vmcnt(%5)" : "+v"(gen), "+v"(lj.a[0]), "+v"(lj.a[1]), "+v"(lj.a2), "+v"(cw) : "i"(vq_wait(VK_CW, 0, J + 1)) : "memory");
        tag = rfl(gen) * 1664525u + 1013904223u;
        asm volatile("" : "+s"(tag));
      } else if constexpr (pos == 45) asm volatile("" : "+v"(lj.w[0]), "+v"(lj.w[1]), "+v"(lj.w2));
      else if constexpr (pos >= 100 && pos < 130) {
        constexpr int M = pos - 100;
        VC(0, l16, tid & 15u) VC(0, g16, (tid >> 4) & 3u) VC(1, ta, l16 ^ g16) VC(1, tb, (tid >> 4) & 12u) VU(2, tb, ta ^ tb) VC(2, tb, 16u * l16 + tb) VU(3, c.vkb[0], tb << 4) VC(3, c.vkb[1], c.vkb[0] ^ 64u)
        VC(4, c.vkb[2], c.vkb[0] ^ 128u) VC(4, c.vkb[3], c.vkb[0] ^ 192u) VU(5, ta, 16u * g16 + ta) VC(5, c.vksrc[0], ta << 4) VC(6, c.vksrc[1], c.vksrc[0] ^ 64u) VC(6, c.ksx[0], c.vksrc[0] ^ 128u)
        VC(7, c.ksx[1], c.vksrc[0] ^ 192u) VC(7, ta, l16 & 3u) VU(8, tb, l16 & 12u) VC(8, tb, 2u * tb + ta) VU(9, l16, g16 & 1u) VC(9, tb, __umul24(l16, 36u) + tb) VU(10, g16, g16 & 2u) VC(10, tb, (g16 << 7) + tb)
        VC(11, c.vvb[0], 8u * tb + VRING) VC(12, c.vvb[1], c.vvb[0] ^ 32u) VC(12, l16, l16 << 1) VU(13, ta, ta ^ l16) VC(13, ta, 2u * g16 + ta) VU(14, tb, tid & 28u) VC(14, ta, 4u * tb + ta)
        VU(15, c.vvsrc, ta << 4) VC(15, c.vvsrc0, c.vvsrc + 896u)
      } else if constexpr (pos == -12) {
        const unsigned rs = moe ? FF * 2u : ret ? DM * 2u : 64u;
        const unsigned r16 = ret ? ((lane >> 2) & 3u) | (lane & 16u) | ((lane >> 3) & 4u) : lane >> 2;
        c.pfv = rs * r16 + 16u * ((lane & 3u) ^ ((lane >> 4) & 2u));
        asm volatile("" : "+v"(c.pfv));
      } else if constexpr (pos == -10) {
        const unsigned w = rfl(wid), fb = (blk & 7u) * 18u + (blk >> 3);
        pb0 = 64u * fb + 16u * FF * 2u * (w & 1u); pb1 = ((blk - 144u) * 4u + w) * 18432u - 9216u * (w & 1u); pb2 = ((blk - 208u) * 4u + w) * 27648u; pb3 = 18432u * w;
        const u16 *a0 = wg, *a1 = wu;
        asm volatile("s_cmp_lt_u32 %1, 2\n\ts_cselect_b64 %0, %2, %3" : "=s"(pbase) : "s"(w), "s"(a0), "s"(a1) : "scc");
        asm volatile("" : "+s"(pb0), "+s"(pb1), "+s"(pb2), "+s"(pb3));
      } else if constexpr (pos == -11) {
        const u16 *a0 = wo, *a1 = wd; const unsigned b = blk;
        unsigned long long pb; unsigned nb, st, np;
        asm volatile("s_cmp_lt_u32 %8, 208\n\ts_cselect_b64 %0, %9, %10\n\ts_cselect_b32 %5, %5, %6\n\ts_mov_b32 %1, %12\n\ts_cselect_b32 %2, 64, %13\n\ts_cselect_b32 %7, %7, 0\n\ts_cselect_b32 %3, %11, %14\n\t"
                     "s_cmp_lt_u32 %8, 144\n\ts_cselect_b64 %0, %15, %0\n\ts_cselect_b32 %5, %4, %5\n\ts_cselect_b32 %1, %16, %1\n\ts_cselect_b32 %2, %17, %2"
                     : "=&s"(pb), "=&s"(nb), "=&s"(st), "=&s"(np), "+s"(pb0), "+s"(pb1), "+s"(pb2), "+s"(pb3)
                     : "s"(b), "s"(a0), "s"(a1), "s"(18432u), "s"(27648u), "s"(1024u), "s"(20480u), "s"(pbase), "s"(559u * (FF * 2u) + 64u), "s"(FF * 2u * 32u) : "scc");
        pbase = pb; pb0 = nb; pb2 = np; c.pfst = st;
      } else if constexpr (pos == -18) {
        c.srdP = mk_srd4((const void*)(pbase + pb1), pb0);
        c.pfo = 0u - c.pfst;
        c.pfm0 = c.ldsb + SD_BASE + pb3; c.pfend = c.pfm0 + pb2;
        asm volatile("" : "+s"(c.srdP), "+s"(c.pfst), "+s"(c.pfo), "+s"(c.pfm0), "+s"(c.pfend));
      } else if constexpr (pos == -19) {
        unsigned kvo = kvh; asm volatile("" : "+s"(kvo));
        c.srdK = __builtin_amdgcn_make_buffer_rsrc((void*)((const char*)Kc + (size_t)kvo * SEQ * 256), (short)0, SEQ * 256, SRD_WORD3);
        c.srdV = __builtin_amdgcn_make_buffer_rsrc((void*)((const char*)Vc + (size_t)kvo * SEQ * 256 - 896), (short)0, SEQ * 256 + 896, SRD_WORD3);
        asm volatile("" : "+s"(c.srdK), "+s"(c.srdV));
      } else if constexpr (pos == -20) {
        unsigned kvo = kvh; asm volatile("" : "+s"(kvo));
        const unsigned id = ((blk >> 3) & 15u) * 4u + rfl(wid);
        const u16 *a0 = Kc, *a1 = Vc; unsigned long long pb;
        asm volatile("s_cmp_lt_u32 %1, 32\n\ts_cselect_b64 %0, %2, %3" : "=s"(pb) : "s"(id), "s"(a0), "s"(a1) : "scc");
        c.srdF = __builtin_amdgcn_make_buffer_rsrc((void*)(pb + (size_t)kvo * SEQ * 256), (short)0, SEQ * 256, SRD_WORD3);
        c.sfo = (c.sofk & ~(KTILEB - 1u)) + KTILEB * (LAH + ((id >> 4) & 1u) - (id >> 5)) + 1024u * (id & 15u);
        asm volatile("" : "+s"(c.srdF), "+s"(c.sfo));
      } else if constexpr (pos == 190) {
        asm volatile("s_waitcnt vmcnt(%4)" : "+v"(p.g[0]), "+v"(p.g[1]), "+v"(p.g[2]), "+v"(p.g[3]) : "i"(vq_wait(VK_GAT, 0, J + 1)) : "memory");
        bad = (p.g[0][1] ^ tag) | (p.g[0][3] ^ tag); csw = (p.g[1][1] ^ tag) | (p.g[1][3] ^ tag); PIN2(bad, csw);
      } else if constexpr (pos == 191 || pos == 192) { bad |= csw; csw = (p.g[pos - 189][1] ^ tag) | (p.g[pos - 189][3] ^ tag); PIN2(bad, csw); }
      else if constexpr (pos == 193) { no = __ballot((bad | csw) != 0u); asm volatile("" : "+s"(no)); }
      else if constexpr (pos == 194) {
        if (__builtin_expect(no != 0ull, 0)) {
          const unsigned v = vs; const u32x4 sr = c.srdS;
#pragma nounroll
          for (unsigned spin = 0; spin < (1u << 20); ++spin) {
            asm volatile("buffer_load_dwordx4 %0, %4, %5, 0 offen sc1\n\tbuffer_load_dwordx4 %1, %4, %5, 0 offen offset:256 sc1\n\tbuffer_load_dwordx4 %2, %4, %5, 0 offen offset:512 sc1\n\t"
                         "buffer_load_dwordx4 %3, %4, %5, 0 offen offset:768 sc1\n\ts_waitcnt vmcnt(0)" : "+v"(p.g[0]), "+v"(p.g[1]), "+v"(p.g[2]), "+v"(p.g[3]) : "v"(v), "s"(sr) : "memory");
            unsigned x = 0u;
#pragma unroll
            for (int q = 0; q < 4; ++q) x |= (p.g[q][1] ^ tag) | (p.g[q][3] ^ tag);
            if (__all(x == 0u)) break;
            __builtin_amdgcn_s_sleep(4);
          }
        }
      } else if constexpr (pos == 220) { lj.t0 = p.cs[0][0] + p.cs[0][1]; asm volatile("" :: "v"(lj.t0), "v"(p.cs[0])); GAP(); lj.t1 = p.cs[1][0] + p.cs[1][1]; asm volatile("" :: "v"(lj.t1), "v"(p.cs[1])); }
      else if constexpr (pos == 221) { lj.t0 = (lane & 16u) ? lj.t1 : lj.t0; asm volatile("" :: "v"(lj.t0)); }
      else if constexpr (pos == 222) lds_st32(p.ca[1], __float_as_uint(lj.t0));
      else if constexpr (pos == 223) { p.ca[2] = p.ca[0] ^ 256u; p.ca[3] = p.ca[0] ^ 384u; PIN2(p.ca[2], p.ca[3]); }
      else if constexpr (pos == 225) { p.ca[1] = p.ca[0] ^ 128u; asm volatile("" : "+v"(p.ca[1])); }
      else if constexpr (pos == -21) {
        vg = l32 == 0u ? 8u * lrow : 0x20000000u; vs = WS_ST + 16u * (row0 + (lane & 15u));
        p.ca[0] = QP_CS + 128u * wid + (lane & 48u); p.ca[1] = QP_CS + 128u * wid + 4u * l32;
        p.sw[0] = p.sw[1] - (kk << 19); p.sw[2] = p.sw[1] + (kk << 19); p.sx[0] = p.sx[1] - (kk << 7); p.sx[2] = p.sx[1] + (kk << 7);
        asm volatile("" : "+v"(vg), "+v"(vs), "+v"(p.ca[0]), "+v"(p.ca[1]), "+s"(p.sw[0]), "+s"(p.sw[2]), "+s"(p.sx[0]), "+s"(p.sx[2]));
        const unsigned a0 = ((lane & 1u) << 3) | ((lane & 2u) << 4) | (((lane >> 2) & 3u) << 6) | (((lane >> 4) & 1u) * 272u) | (hi << 12) | (wid << 9);
#pragma unroll
        for (int b = 0; b < 8; ++b) { p.aw[0][b] = a0 ^ (16u * (b & 1) + 512u * (b >> 1)); p.aw[1][b] = p.aw[0][b] + 2u * QP_STG; PIN2(p.aw[0][b], p.aw[1][b]); }
#pragma unroll
        for (int ks = 0; ks < 2; ++ks) { p.ax[0][ks] = 0x4000u + 8192u * wid + 128u * (lane & 15u) + 16u * ((4u * ks + (lane >> 4)) ^ ((lane >> 1) & 7u)); p.ax[1][ks] = p.ax[0][ks] + 2u * QP_STG; PIN2(p.ax[0][ks], p.ax[1][ks]); }
        const unsigned on = (lane & 2u) == 0u ? 0x3f803f80u : 0u; c.ONE = u32x4{on, on, on, on}; c.IDX = (lane & 1u) ? 0xeeeeeeeeu : 0x44444444u;
        p.cs[0] = p.cs[1] = f32x4{0.f, 0.f, 0.f, 0.f};
        asm volatile("" : "+v"(c.ONE), "+v"(c.IDX), "+v"(p.cs[0]), "+v"(p.cs[1]) :: W4_ACLOB, W4_ACLOB_HI);
      }
    };
    asm volatile("" :: "s"(ws), "s"(Kc), "s"(Vc), "s"(wo), "s"(wg), "s"(wu), "s"(wd), "s"(out), "s"(w_r));
    vq_issue<-3>(vm);
    asm volatile("s_branch 1f\n.Lps_ret: s_setpc_b64 vcc\n.Lps_tab:\n.irp n,0,1,2,3,4,5,6,7,8,9,10,11,12,13,14,15,16,17,18,19,20,21,22,23\ns_branch .Lpe_\\n\n.endr\n1:" ::: "memory");
    psc = rfl(wid) + 16u - 4u * (((blk - 144u) >> 31) + ((blk - 208u) >> 31) + ((blk - RED1) >> 31));
    c.srdS = mk_srd4(ws, WS_END);
    asm volatile("" : "+s"(psc), "+s"(c.srdS));
    hook(IC(-10), IC(0)); hook(IC(-11), IC(0)); hook(IC(-12), IC(0)); hook(IC(-18), IC(0)); hook(IC(-19), IC(0)); hook(IC(-20), IC(0)); hook(IC(-21), IC(0));
    vq_issue<-2>(vm);
    GAP();
    vq_issue<-1>(vm);
    GAP();
    asm volatile("s_waitcnt vmcnt(%0)\n\ts_barrier" :: "i"(vq_wait(VK_S, 0, 0)) : "memory");
    q_first(p, c, vm, hook, lj, make_iseq<NQ0>{});
    q_gaps<0>(p, c, vm, hook, lj, lrow, make_iseq<240>{});
    q_gaps<240>(p, c, vm, hook, lj, lrow, make_iseq<PJ - 240>{});
    q_gaps<PJ>(p, c, vm, hook, lj, lrow, make_iseq<150>{});
    q_gaps<PJ + 150>(p, c, vm, hook, lj, lrow, make_iseq<NJ - PJ - 150>{});
  }
  {
    c.L[0] = c.L[1] = c.L[2] = c.L[3] = f32x4{0.f, 0.f, 0.f, 0.f};
    asm volatile("" : "+v"(c.L[0]), "+v"(c.L[1]), "+v"(c.L[2]), "+v"(c.L[3]));
  }
  unsigned rstate = 3u;
  auto route = [&]() __attribute__((always_inline)) {
    unsigned lq;
    asm volatile("v_mbcnt_lo_u32_b32 %0, -1, 0\n\tv_mbcnt_hi_u32_b32 %0, -1, %0" : "=v"(lq));
    const unsigned w = rfl(wid), kb_ = c.ldsb + MO_KEY + 4096u * w;
    bool req = rstate == 0u;
    if (rstate == 1u) {
      u32x4 g[4];
      asm volatile("ds_read_b128 %0, %4\n\tds_read_b128 %1, %4 offset:1024\n\tds_read_b128 %2, %4 offset:2048\n\tds_read_b128 %3, %4 offset:3072\n\ts_waitcnt lgkmcnt(0)"
                   : "=&v"(g[0]), "=&v"(g[1]), "=&v"(g[2]), "=&v"(g[3]) : "v"(kb_ + 16u * lq) : "memory");
      bool ok = true;
#pragma unroll
      for (int j = 0; j < 4; ++j) ok = ok && g[j][1] == tag && g[j][3] == tag;
      if (__all(ok)) {
        unsigned k[8] = {g[0][0], g[0][2], g[1][0], g[1][2], g[2][0], g[2][2], g[3][0], g[3][2]};
        ce(k[0], k[1]); ce(k[2], k[3]); ce(k[4], k[5]); ce(k[6], k[7]);
        ce(k[0], k[2]); ce(k[1], k[3]); ce(k[4], k[6]); ce(k[5], k[7]);
        ce(k[1], k[2]); ce(k[5], k[6]); ce(k[0], k[4]); ce(k[3], k[7]);
        ce(k[1], k[5]); ce(k[2], k[6]);
        ce(k[1], k[4]); ce(k[3], k[6]);
        ce(k[2], k[4]); ce(k[3], k[5]);
        ce(k[3], k[4]);
        unsigned cnd = 0u;
#pragma nounroll
        for (unsigned r = 0; r < (unsigned)NSEL; ++r) {
          unsigned m = k[0], t;
          t = (unsigned)__builtin_amdgcn_update_dpp(0, (int)m, 0xB1, 0xf, 0xf, false); m = t > m ? t : m;
          t = (unsigned)__builtin_amdgcn_update_dpp(0, (int)m, 0x4E, 0xf, 0xf, false); m = t > m ? t : m;
          t = (unsigned)__builtin_amdgcn_update_dpp(0, (int)m, 0x141, 0xf, 0xf, false); m = t > m ? t : m;
          t = (unsigned)__builtin_amdgcn_update_dpp(0, (int)m, 0x140, 0xf, 0xf, false); m = t > m ? t : m;
          cnd = (lq & 15u) == (unsigned)r ? m : cnd;
          const bool pp = k[0] == m;
#pragma unroll
          for (int i = 0; i < 7; ++i) k[i] = pp ? k[i + 1] : k[i];
        }
        lds_st32(c.ldsb + MO_RS + ((lq & 8u) == 0u ? 64u + 128u * w + 2u * (lq & 48u) + 4u * (lq & 7u) : 640u + 4u * lq), cnd);
        asm volatile("s_waitcnt lgkmcnt(0)" ::: "memory");
        lds_st32(c.ldsb + MO_RS + 4u * w, 1u);
        rstate = 2u;
      } else req = true;
    } else if (rstate == 2u) {
      u32x4 fl;
      asm volatile("ds_read_b128 %0, %1\n\ts_waitcnt lgkmcnt(0)" : "=v"(fl) : "v"(c.ldsb + MO_RS) : "memory");
      if (rfl(fl[0] & fl[1] & fl[2] & fl[3]) != 0u) {
        unsigned cv, rank = 0u;
        asm volatile("ds_read_b32 %0, %1" : "=v"(cv) : "v"(c.ldsb + MO_RS + 64u + 128u * w + 4u * (lq & 31u)) : "memory");
#pragma nounroll
        for (unsigned h = 0; h < 4u; ++h) {
          u32x4 c4v[4];
          asm volatile("ds_read_b128 %0, %5\n\tds_read_b128 %1, %5 offset:16\n\tds_read_b128 %2, %5 offset:32\n\tds_read_b128 %3, %5 offset:48\n\ts_waitcnt lgkmcnt(0)"
                       : "=&v"(c4v[0]), "=&v"(c4v[1]), "=&v"(c4v[2]), "=&v"(c4v[3]), "+v"(cv) : "v"(c.ldsb + MO_RS + 64u + 256u * (lq >> 5) + 64u * h) : "memory");
#pragma unroll
          for (int j = 0; j < 4; ++j)
#pragma unroll
            for (int e = 0; e < 4; ++e) rank += c4v[j][e] > cv ? 1u : 0u;
        }
        auto pr = __builtin_amdgcn_permlane32_swap(rank, rank, false, false);
        rank = pr[0] + pr[1];
        {
          const float rs = rbf(__builtin_amdgcn_rcpf(1.f + __builtin_amdgcn_exp2f(key_val(cv) * -1.4426950408889634f)));
          const u32x2 gr = {(__float_as_uint(rs) & 0xffff0000u) | (unsigned)key_row(cv), tag};
          const unsigned vo = lq < 32u && rank < 8u ? 8u * rank : 0x40000000u, vg = (lq | w) == 0u ? 4u * H_G : 0x40000000u;
          asm volatile("s_nop 4\n\tbuffer_store_dwordx2 %0, %1, %3, 0 offen sc1\n\tbuffer_store_dword %4, %2, %3, 0 offen sc1\n\ts_nop 1" :: "v"(gr), "v"(vo), "v"(vg), "s"(c.srdS), "v"(gr[1]) : "memory");
        }
        rstate = 3u;
      }
    }
    if (req) {
#pragma unroll
      for (int j = 0; j < 4; ++j)
        asm volatile("s_mov_b32 m0, %0\n\ts_nop 4\n\tbuffer_load_dwordx4 %1, %2, %3 offen sc1 lds" :: "s"(kb_ + 1024u * j), "v"(16u * lq), "s"(c.srdS), "s"(WS_KEY + 4096u * w + 1024u * j) : "m0", "memory");
      rstate = 1u;
    }
  };
  const unsigned wids = rfl(wid);
  auto opq = [&](unsigned v) __attribute__((always_inline)) { asm volatile("" : "+s"(v)); return v; };
  auto lane_id = [&]() __attribute__((always_inline)) { unsigned z = 0u; asm volatile("" : "+s"(z)); return __builtin_amdgcn_mbcnt_hi(~0u, __builtin_amdgcn_mbcnt_lo(~0u, z)); };
  auto hand_req = [&](const unsigned m0, const unsigned word0, const unsigned nb) __attribute__((always_inline)) {
    const unsigned vo = c.vksrc[0]; u32x4 sr = c.srdS; sr[2] = 4u * word0 + nb;
    asm volatile("s_mov_b32 m0, %0\n\ts_nop 0\n\tbuffer_load_dwordx4 %1, %2, %3 offen sc1 lds" :: "s"(m0), "v"(vo), "s"(sr), "s"(4u * word0) : "m0", "memory");
  };
  auto pf_at = [&](auto M_, auto G_, auto A_) __attribute__((always_inline)) {
    constexpr int d = decltype(M_)::value - decltype(G_)::value;
    if constexpr (d == -decltype(A_)::value) asm volatile("s_add_u32 %0, %0, %1" : "+s"(c.pfo) : "s"(c.pfst) : "scc");
    if constexpr (d == -1) asm volatile("s_mov_b32 m0, %0" :: "s"(c.pfm0) : "m0", "memory");
    if constexpr (d == 0) { GAP(); c.pfm0 += 0x400u; asm volatile("" :: "s"(c.pfm0)); GAP(); asm volatile("buffer_load_dwordx4 %0, %1, %2 offen sc1 lds" :: "v"(c.pfv), "s"(c.srdP), "s"(c.pfo) : "memory"); }
  };
  auto m0_set = [&](const unsigned m0) __attribute__((always_inline)) { asm volatile("s_mov_b32 m0, %0" :: "s"(m0) : "m0", "memory"); };
  auto m0_imm = [&](auto C_) __attribute__((always_inline)) { asm volatile("s_mov_b32 m0, %0" :: "i"(decltype(C_)::value) : "m0", "memory"); };
  auto lim = [&](const unsigned n) __attribute__((always_inline)) { c.srdL[2] = n; asm volatile("" : "+s"(c.srdL)); };
  auto dma16 = [&](auto W_) __attribute__((always_inline)) { const unsigned vo = c.vksrc[0]; const u32x4 sr = c.srdL; if constexpr (decltype(W_)::value == 0) asm volatile("buffer_load_dwordx4 %0, %1, 0 offen sc1 lds" :: "v"(vo), "s"(sr) : "memory"); else asm volatile("buffer_load_dwordx4 %0, %1, %2 offen sc1 lds" :: "v"(vo), "s"(sr), "s"(4u * decltype(W_)::value) : "memory"); };
  auto flag_st = [&](const unsigned so, const unsigned tg) __attribute__((always_inline)) {
    const unsigned vo = c.vvsrc; const u32x4 sr = c.srdL; asm volatile("buffer_store_dword %0, %1, %2, %3 offen sc1" :: "v"(tg), "v"(vo), "s"(sr), "s"(so) : "memory");
  };
  auto warm_at = [&](auto M_, auto G_) __attribute__((always_inline)) {
    constexpr int d = decltype(M_)::value - decltype(G_)::value;
    if constexpr (d == -1) m0_set(c.pdum);
    if constexpr (d == 0) { const unsigned vo = c.vksrc[0], so = cwo; const u32x4 sr = srdC; asm volatile("buffer_load_dwordx4 %0, %1, %2 offen lds" :: "v"(vo), "s"(sr), "s"(so) : "memory"); }
  };
  auto is_cls = [&](const unsigned k) __attribute__((always_inline)) { asm volatile("" : "+s"(psc)); return psc == 4u * k; };
  int t1, t2;
  GAP();
  phaseA_end<2>(c);
  {
    auto hook = [&](auto M_) __attribute__((always_inline)) {
      constexpr int M = decltype(M_)::value;
      warm_at(M_, IC(40));
      SC(FB(0), c.srdL, c.srdS) SC(2, psc, psc & ~3u) SC(4, t1, 0xcc860 >> psc) SC(5, t1, t1 & 15) SC(7, t2, 0x08000 >> psc) SC(9, t2, t2 & 15) SC(10, t2, t2 + 14)
    };
    phaseB_<1, true, true, 2>(c, hook, make_iseq<NGB>{});
  }
  GAP();
  if (__builtin_expect(opq(blk) == RTB, 0)) {
    {
      const unsigned w = opq(wids), kb_ = c.ldsb + MO_KEY + 4096u * w;
      RTS r; unsigned sk;
      auto hookA2 = [&](auto M_) __attribute__((always_inline)) {
        constexpr int M = decltype(M_)::value, n = fa_of(M);
        if constexpr (n == 0) { r.lq = lane_id(); asm volatile("" : "+v"(r.lq)); }
        if constexpr (n == 1) { sk = WS_KEY + 4096u * w; asm volatile("" : "+s"(sk)); }
        if constexpr (n == 2) { r.va = 16u * r.lq; m0_set(kb_); asm volatile("" : "+v"(r.va)); }
        if constexpr (n == 4 || n == 5 || n == 7 || n == 9) {
          constexpr int j = n == 4 ? 0 : n == 5 ? 1 : n == 7 ? 2 : 3;
          const unsigned v = r.va, so = sk; const u32x4 sr = c.srdS;
          asm volatile("buffer_load_dwordx4 %0, %1, %3 offen offset:%c2 sc1 lds" :: "v"(v), "s"(sr), "i"(1024 * j), "s"(so) : "memory");
        }
      };
      phaseAh<0, 7>(c, 2, hookA2);
      phaseB<0, 2>(c);
      auto hookA3 = [&](auto M_) __attribute__((always_inline)) { if constexpr (decltype(M_)::value == FA(0)) { r.va += kb_; asm volatile("" : "+v"(r.va)); } };
      phaseAh<1, 2>(c, 3, hookA3);
      auto steps = [&](auto I0_, auto G0_, auto M_) __attribute__((always_inline)) {
        constexpr int M = decltype(M_)::value, I = decltype(I0_)::value + M - 100 - decltype(G0_)::value;
        if constexpr (M >= 100 + decltype(G0_)::value && I < RT_END) rt_step<I>(r, tag);
      };
      auto hookB3 = [&](auto M_) __attribute__((always_inline)) {
        constexpr int M = decltype(M_)::value;
        if constexpr (M == 2 || M == 4 || M == 5 || M == 7) {
          constexpr int j = M == 2 ? 0 : M == 7 ? 3 : M - 3;
          u32x4& d = r.g[j]; const unsigned v = r.va;
          asm volatile("ds_read_b128 %0, %1 offset:%2" : "=v"(d) : "v"(v), "i"(1024 * j) : "memory");
        }
        if constexpr (M == FB(0)) { u32x4 (&g)[4] = r.g; asm volatile("s_waitcnt lgkmcnt(%4)" : "+v"(g[0]), "+v"(g[1]), "+v"(g[2]), "+v"(g[3]) : "i"(kyo(7, FB(0))) : "memory"); }
        steps(IC(0), IC(FB(0)), M_);
      };
      phaseBh<1, 2>(c, hookB3);
      auto hookA4 = [&](auto M_) __attribute__((always_inline)) {
        constexpr int M = decltype(M_)::value;
        steps(IC(NGB - FB(0)), IC(0), M_);
        if constexpr (M == FA(6)) { r.va = (r.lq & 8u) == 0u ? 64u + 128u * w + 2u * (r.lq & 48u) + 4u * (r.lq & 7u) : 640u + 4u * r.lq; asm volatile("" : "+v"(r.va)); }
        if constexpr (M == FA(8)) lds_st32(c.ldsb + MO_RS + r.va, r.cnd);
        if constexpr (M == FA(9)) lds_st32(c.ldsb + MO_RS + 4u * w, __all(r.bad == 0u) ? 1u : 0u);
        if constexpr (M == FA(10)) { r.va = c.ldsb + MO_RS + 64u + 256u * (r.lq >> 5); r.m = c.ldsb + MO_RS + 64u + 128u * w + 4u * (r.lq & 31u); r.rank = 0u; asm volatile("" : "+v"(r.va), "+v"(r.m), "+v"(r.rank)); }
      };
      static_assert(NGB - FB(0) + FA(6) >= RT_END, "");
      phaseAh<0, 3>(c, 4, hookA4);
      auto hookB4 = [&](auto M_) __attribute__((always_inline)) {
        constexpr int M = decltype(M_)::value;
        if constexpr (M == 0) { unsigned& d = r.cv; u32x4& f = r.g[3]; const unsigned v = r.m, v2 = c.ldsb + MO_RS; asm volatile("ds_read_b32 %0, %2\n\tds_read_b128 %1, %3" : "=&v"(d), "=&v"(f) : "v"(v), "v"(v2) : "memory"); }
        if constexpr (M < 60 && M % 4 == 0) { u32x4& d = r.g[(M >> 2) % 3]; const unsigned v = r.va; asm volatile("ds_read_b128 %0, %1 offset:%2" : "=v"(d) : "v"(v), "i"(4 * M) : "memory"); }
        if constexpr (M >= 5 && M < 65 && M % 4 == 1) { u32x4 &d = r.g[((M - 5) >> 2) % 3], &f = r.g[3]; unsigned& cv = r.cv; asm volatile("s_waitcnt lgkmcnt(%3)" : "+v"(d), "+v"(cv), "+v"(f) : "i"(kyo(M - 5, M) + (M < 61)) : "memory"); }
        if constexpr (M >= 105 && M < 165) { unsigned long long& pm = r.pm[M & 1]; const unsigned a = r.g[((M - 105) >> 2) % 3][(M - 105) & 3], cv = r.cv; asm volatile("v_cmp_gt_u32_e64 %0, %1, %2" : "=s"(pm) : "v"(a), "v"(cv)); }
        if constexpr (M >= 106 && M < 166) cnt_s(r.rank, r.pm[(M - 1) & 1]);
        if constexpr (M == 165) { r.bad = r.g[3][0] & r.g[3][1] & r.g[3][2] & r.g[3][3]; asm volatile("" : "+v"(r.bad)); }
        if constexpr (M == 66) { u32x4& d = r.g[0]; const unsigned v = r.va; asm volatile("ds_read_b128 %0, %1 offset:240" : "=v"(d) : "v"(v) : "memory"); }
      };
      phaseBh<0, 2>(c, hookB4);
      auto hookA5 = [&](auto M_) __attribute__((always_inline)) {
        constexpr int M = decltype(M_)::value;
        if constexpr (M == 4) { u32x4& d = r.g[0]; asm volatile("s_waitcnt lgkmcnt(3)" : "+v"(d) :: "memory"); }
        if constexpr (M >= 104 && M < 108) { r.rank += r.g[0][M - 104] > r.cv ? 1u : 0u; asm volatile("" : "+v"(r.rank)); }
        if constexpr (M == FA(1)) { auto pr = __builtin_amdgcn_permlane32_swap(r.rank, r.rank, false, false); r.rank = pr[0] + pr[1]; asm volatile("" : "+v"(r.rank)); }
        if constexpr (M == FA(2)) { r.rs = key_val(r.cv) * -1.4426950408889634f; asm volatile("" : "+v"(r.rs)); }
        if constexpr (M == FA(3)) { r.rs = 1.f + __builtin_amdgcn_exp2f(r.rs); asm volatile("" : "+v"(r.rs)); }
        if constexpr (M == FA(4)) { r.rs = rbf(__builtin_amdgcn_rcpf(r.rs)); asm volatile("" : "+v"(r.rs)); }
        if constexpr (M == FA(5)) { r.m = (__float_as_uint(r.rs) & 0xffff0000u) | (unsigned)key_row(r.cv); r.bad = (r.bad - 1u) & 0x40000000u; asm volatile("" : "+v"(r.m), "+v"(r.bad)); }
        if constexpr (M == FA(7)) { r.va = (r.lq < 32u && r.rank < 8u ? 8u * r.rank : 0x40000000u) | r.bad; asm volatile("" : "+v"(r.va)); }
        if constexpr (M == 100 + FA(8)) { r.cnd = ((r.lq | w) == 0u ? 4u * H_G : 0x40000000u) | r.bad; asm volatile("" : "+v"(r.cnd)); }
        if constexpr (M == FA(9)) {
          const u32x2 gr = {r.m, tag}; const unsigned vo = r.va, vg = r.cnd; const u32x4 sr = c.srdS;
          asm volatile("buffer_store_dwordx2 %0, %1, %3, 0 offen sc1\n\tbuffer_store_dword %4, %2, %3, 0 offen sc1" :: "v"(gr), "v"(vo), "v"(vg), "s"(sr), "v"(gr[1]) : "memory");
        }
      };
      phaseAh<1, 4>(c, 5, hookA5);
      rstate = rfl(r.bad) ? 0u : 3u;
      if (rstate == 0u) lds_st32(c.ldsb + MO_RS + 4u * w, 0u);
      phaseB<1, 2>(c);
    }
#pragma nounroll
    for (int t = 6; t < 14; t += 2) {
      phaseA<0, 3>(c, t);     phaseB<0, 2>(c);
      phaseA<1, 2>(c, t + 1); phaseB<1, 2>(c);
      if (rstate != 3u) {
        route();
        if (t == 12)
#pragma nounroll
          for (unsigned spin = 0; spin < (1u << 20) && rstate != 3u; ++spin) {
            asm volatile("s_waitcnt vmcnt(0)" ::: "memory");
            route();
            __builtin_amdgcn_s_sleep(8);
          }
      }
      GAP();
    }
    t1 = 0;
  }
  GAP();
  {
#pragma nounroll
    for (int t = 0; __builtin_expect(t != t1, 1);) {
      t += 2;
      phaseA<0, 3>(c, t);     phaseB<0, 2>(c);
      phaseA<1, 2>(c, t + 1); phaseB<1, 2>(c);
    }
  }
  GAP();
  constexpr auto SG = [](int i) constexpr { constexpr int g[10] = {14, 20, 26, 31, 36, 41, 46, 52, 57, 61}; return g[i + 1]; };
  constexpr auto sg_of = [](int m) constexpr { constexpr int g[10] = {14, 20, 26, 31, 36, 41, 46, 52, 57, 61}; for (int i = 0; i < 10; ++i) if (g[i] == m) return i - 1; return -2; };
  if (__builtin_expect(is_cls(1u), 1)) {
    unsigned w, b, fbb, s0, s1, sx, mx, va, vo, vw, vx;
    {
      auto hook = [&](auto M_) __attribute__((always_inline)) {
        constexpr int M = decltype(M_)::value;
        SC(0, w, wids) SC(1, b, blk) SC(2, fbb, b & 7u) SC(3, fbb, fbb * 18u) SC(4, s0, b >> 3) SC(5, fbb, fbb + s0)
        SC(7, sx, w << 7) SC(8, sx, sx + WS_XN) SC(9, mx, w << 10) SC(10, mx, mx + MO_XR)
        SC(11, s0, w >> 1) SC(12, s0, s0 * 36864u) SC(13, s0, s0 + SD_BASE) SC(15, s1, w & 1u) SC(16, s1, s1 << 5)
        VC(FA(0), va, (c.vksrc[0] >> 1) & 0x38u) VC(FA(2), vo, (c.vksrc[0] >> 1) & 64u) VC(FA(4), vx, (c.vksrc[0] >> 4) & 0x30u) VC(FA(5), vo, vo | vx) VC(FA(5), va, va + MO_C)
        VC(FA(9), vw, c.vvb[0] & 0xffu) VC(FA(9), vx, c.vvb[0] & 32u) VC(FA(11), vw, vw + 16u * vx) VC(FA(11), vw, vw ^ s1)
      };
      phaseAh<0, 3>(c, 8, hook);
    }
    {
      auto hook = [&](auto M_) __attribute__((always_inline)) {
        constexpr int M = decltype(M_)::value;
        if constexpr (M == 38) lim(64u);
        if constexpr (M == 39) m0_imm(IC(MO_C));
        if constexpr (M == 40) dma16(IC(0));
        VC(FB(0), vx, 9u * (c.vvb[0] & 2048u)) VC(FB(1), vw, vw + vx + s0) VC(FB(1), vx, c.vksrc[0] + MO_XR)
      };
      phaseBh<0, 2>(c, hook);
    }
    {
      u32x2 sv; unsigned long long ok;
      constexpr auto yo = [](int g0, int g1) constexpr { return 2 * (pcnt(PA0, 'T', g1) - pcnt(PA0, 'T', g0)) + pcnt(PA0, 'X', g1) - pcnt(PA0, 'X', g0); };
      auto hook = [&](auto M_) __attribute__((always_inline)) {
        constexpr int M = decltype(M_)::value;
        if constexpr (M == FA(5)) asm volatile("s_waitcnt vmcnt(2)\n\tds_read_b64 %0, %1" : "=v"(sv) : "v"(va) : "memory");
        if constexpr (M == 100 + FA(6)) { asm volatile("s_waitcnt lgkmcnt(%1)" : "+v"(sv) : "i"(yo(FA(5), FA(6))) : "memory"); ok = __ballot(sv[1] != tag); asm volatile("" : "+s"(ok)); }
        if constexpr (M == 39) {
          if (__builtin_expect(ok != 0ull, 0)) {
#pragma nounroll
            for (unsigned spin = 0; spin < (1u << 20) && !__all(sv[1] == tag); ++spin) {
              __builtin_amdgcn_s_sleep(8);
              hand_req(c.ldsb + MO_C, 0u, 64u);
              asm volatile("s_waitcnt vmcnt(0)\n\tds_read_b64 %0, %1\n\ts_waitcnt lgkmcnt(0)" : "=v"(sv) : "v"(va) : "memory");
            }
          }
        }
        VC(141, va, sv[0] & 0xffffu) VC(143, vo, __umul24(va, XRS) + vo)
        if constexpr (M == 46 || M == 52 || M == 62) m0_set(mx);
        SC(49, mx, mx + 4096u) SC(50, sx, sx + 512u) SC(55, mx, mx + 4096u) SC(56, sx, sx + 512u)
        if constexpr (M == 100 + FA(8) || M == FA(9) || M == FA(11)) { const unsigned v = vo, so = sx; const u32x4 r = c.srdS; asm volatile("buffer_load_dwordx4 %0, %1, %2 offen sc1 lds" :: "v"(v), "s"(r), "s"(so) : "memory"); }
      };
      phaseAh<1, 5>(c, 9, hook);
    }
    unsigned vd, vyo, sy;
    {
      unsigned lq;
      auto hook = [&](auto M_) __attribute__((always_inline)) {
        constexpr int M = decltype(M_)::value;
        VC(FB(0), lq, lane_id()) VC(FB(1), c.pfv, 16u * (lq & 3u)) VC(FB(3), c.pfv, __umul24((lq >> 2) & 3u, XRS) + c.pfv) VC(DB(8), c.pfv, 2u * (lq & 32u) + c.pfv)
        VC(FB(4), c.pfv, __umul24(lq & 16u, XRS) + c.pfv) VC(DB(9), vyo, (lq & 48u) << 3) VC(FB(6), vyo, ((lq & 7u) << 4) | vyo) VC(FB(8), vyo, ((lq & 8u) << 27) | vyo)
        SA(2, c.pfo, sha<3>(fbb, w)) SC(4, c.pfo, c.pfo * (4u * XRS)) SC(5, c.pfo, c.pfo - 128u) SC(7, c.pfst, 128u) SC(9, c.pfm0, 18432u * w) SC(10, c.pfm0, c.pfm0 + SD_BASE)
        SC(12, sy, fbb * 36u) SC(15, sy, sy + w) SC(18, sy, sy << 9) SC(20, sy, sy + WS_YP) SC(33, c.srdP, mk_srd4(wd, DM * FF * 2))
      };
      phaseBh<1, 0>(c, hook);
    }
    f32x4 gu;
    auto gate_up = [&](auto H_) __attribute__((always_inline)) {
      constexpr int HH = decltype(H_)::value;
      bf16x4 wl[2], wh[2]; bf16x8 xf[2];
      auto hook = [&](auto M_) __attribute__((always_inline)) {
        constexpr int M = decltype(M_)::value, j = sg_of(M % 100);
        if constexpr (M < 100 && j >= 0) asm volatile("s_waitcnt lgkmcnt(%3)" : "+v"(wl[j & 1]), "+v"(wh[j & 1]), "+v"(xf[j & 1]) : "i"(kyo(SG(j - 1) + 1, M)) : "memory");
        if constexpr (M >= 100 && j >= 0) {
          const bf16x8 wfr = __builtin_shufflevector(wl[j & 1], wh[j & 1], 0, 1, 2, 3, 4, 5, 6, 7);
          if constexpr (HH == 0 && j == 0) asm volatile("mf16 %0, %1, %2, 0" : "=&v"(gu) : "v"(wfr), "v"(xf[j & 1]));
          else                             asm volatile("mf16 %0, %1, %2, %0" : "+v"(gu) : "v"(wfr), "v"(xf[j & 1]));
        }
        if constexpr (M >= 100 && j >= -1 && j < 8) {
          constexpr int n = (j + 1) & 1, i = 9 * HH + j + 1;
          asm volatile("ds_read_b64_tr_b16 %0, %1 offset:%2" : "=v"(wl[n]) : "v"(vw), "i"(1024 * i) : "memory");
          asm volatile("ds_read_b64_tr_b16 %0, %1 offset:%2" : "=v"(wh[n]) : "v"(vw), "i"(1024 * i + 256) : "memory");
          asm volatile("ds_read_b128 %0, %1 offset:%2" : "=v"(xf[n]) : "v"(vx), "i"(1024 * (i >> 1) + 128 * (i & 1)) : "memory");
        }
        if constexpr (HH == 1) {
          pf_at(M_, IC(DB(8)), IC(1)); pf_at(M_, IC(DB(9)), IC(1)); pf_at(M_, IC(FB(6)), IC(2)); pf_at(M_, IC(FB(8)), IC(2));
        }
      };
      phaseBh<HH, 0>(c, hook);
    };
    unsigned vh;
    {
      auto hook = [&](auto M_) __attribute__((always_inline)) {
        constexpr int M = decltype(M_)::value;
        SC(0, s0, w >> 1) SC(1, s0, s0 << 9) SC(2, mx, s0 + s1) SC(3, mx, mx + SD_BASE) SC(4, s1, s1 >> 2) SA(5, s0, sha<1>(s0, s1)) SC(7, s0, s0 + MO_H) SC(8, s1, w << 10) SA(9, sx, (shi<2, MO_HH>(w)))
        VC(FA(0), vd, 72u * (c.vksrc[0] & 0x300u)) VC(FA(2), vd, vd + (c.vvb[0] & 0xdfu) + mx)
      };
      phaseAh<0, 1>(c, 10, hook);
    }
    gate_up(std::integral_constant<int, 0>{});
    phaseA<1, 0>(c, 11);
    gate_up(std::integral_constant<int, 1>{});
    {
      auto hook = [&](auto M_) __attribute__((always_inline)) {
        constexpr int M = decltype(M_)::value;
        pf_at(M_, IC(FA(0)), IC(2)); pf_at(M_, IC(FA(2)), IC(2)); pf_at(M_, IC(FA(4)), IC(2)); pf_at(M_, IC(FA(5)), IC(2)); pf_at(M_, IC(FA(11)), IC(2));
        VC(100 + FA(6), vx, vx + s1) VC(100 + FA(8), vh, c.vksrc[0] + s0)
        if constexpr (M == FA(9)) { const f32x4 g4 = gu; const unsigned v = vx; asm volatile("ds_write_b128 %0, %1 offset:%2" :: "v"(v), "v"(g4), "i"(MO_H - MO_XR) : "memory"); }
      };
      phaseAh<0, 10>(c, 12, hook);
    }
    {
      f32x2 g, u, s;
      auto hook = [&](auto M_) __attribute__((always_inline)) {
        constexpr int M = decltype(M_)::value;
        if constexpr (M == FB(0)) asm volatile("ds_read_b64 %0, %2\n\tds_read_b64 %1, %2 offset:2048" : "=&v"(g), "=&v"(u) : "v"(vh) : "memory");
        if constexpr (M == 116) { vh = c.vksrc[0] + sx; asm volatile("" : "+v"(vh)); }
        if constexpr (M == FB(1)) {
          asm volatile("s_waitcnt lgkmcnt(%2)" : "+v"(g), "+v"(u) : "i"(kyo(FB(0), FB(1))) : "memory");
          const unsigned pg = cvtpk(g[0], g[1]);
          g[0] = bflo(pg); g[1] = bfhi(pg);
          asm volatile("" : "+v"(g));
        }
        if constexpr (M == FB(2)) { const unsigned pu = cvtpk(u[0], u[1]); u[0] = bflo(pu); u[1] = bfhi(pu); asm volatile("" : "+v"(u)); }
        if constexpr (M == FB(3)) { s[0] = g[0] * -1.4426950408889634f; s[1] = g[1] * -1.4426950408889634f; asm volatile("" : "+v"(s)); }
        if constexpr (M == DB(8) || M == FB(4)) { constexpr int e = M == FB(4); s[e] = __builtin_amdgcn_exp2f(s[e]); asm volatile("" : "+v"(s)); }
        if constexpr (M == DB(9)) { float s0 = s[0], s1 = s[1]; add_v(s0, 1.f); add_v(s1, 1.f); s = f32x2{s0, s1}; }
        if constexpr (M == FB(5) || M == FB(6)) { constexpr int e = M == FB(6); s[e] = __builtin_amdgcn_rcpf(s[e]); asm volatile("" : "+v"(s)); }
        if constexpr (M == FB(7)) { float g0 = g[0], g1 = g[1]; mul_v(g0, s[0]); mul_v(g1, s[1]); g = f32x2{g0, g1}; }
        if constexpr (M == FB(8)) { const unsigned ps = cvtpk(g[0], g[1]); g[0] = bflo(ps); g[1] = bfhi(ps); asm volatile("" : "+v"(g)); }
        if constexpr (M == 164 || M == 165) { float x = u[M & 1]; mul_v(x, g[M & 1]); u[M & 1] = x; }
        if constexpr (M == FB(9)) { const unsigned hv = cvtpk(u[0], u[1]); asm volatile("ds_write_b32 %0, %1" :: "v"(vh), "v"(hv) : "memory"); }
      };
      phaseBh<0, 0>(c, hook);
    }
    {
      bf16x4 dl[2], dh[2]; f32x4 y[3]; u32x4 hwv;
      { auto hookA = [&](auto M_) __attribute__((always_inline)) { constexpr int M = decltype(M_)::value; VC(FA(0), vh, c.vksrc[0] + MO_HH) }; phaseAh<1, 0>(c, 13, hookA); }
      constexpr auto st_of = [](int m) constexpr { constexpr int g[9] = {25, 33, 42, 43, 47, 50, 56, 62, 67}; for (int i = 0; i < 9; ++i) if (g[i] == m) return i; return -1; };
      auto hook = [&](auto M_) __attribute__((always_inline)) {
        constexpr int M = decltype(M_)::value, i = sg_of(M % 100), is = st_of(M), ik = st_of(M - 101);
        if constexpr (M == FB(0)) asm volatile("ds_read_b128 %0, %1" : "=v"(hwv) : "v"(vh) : "memory");
        if constexpr (M < 100 && i >= 0) asm volatile("s_waitcnt lgkmcnt(%3)" : "+v"(dl[i & 1]), "+v"(dh[i & 1]), "+v"(hwv) : "i"(kyo(SG(i - 1) + 1, M)) : "memory");
        if constexpr (M >= 100 && i >= 0) {
          const bf16x8 d8 = __builtin_shufflevector(dl[i & 1], dh[i & 1], 0, 1, 2, 3, 4, 5, 6, 7);
          asm volatile("mf16 %0, %1, %2, 0" : "=&v"(y[i % 3]) : "v"(d8), "v"(hwv));
        }
        if constexpr (M >= 100 && i >= -1 && i < 8) {
          asm volatile("ds_read_b64_tr_b16 %0, %1 offset:%2" : "=v"(dl[(i + 1) & 1]) : "v"(vd), "i"(1024 * (i + 1)) : "memory");
          asm volatile("ds_read_b64_tr_b16 %0, %1 offset:%2" : "=v"(dh[(i + 1) & 1]) : "v"(vd), "i"(1024 * (i + 1) + 256) : "memory");
        }
        if constexpr (is >= 0) {
          const unsigned vo = vyo; const u32x4 sr = c.srdS; const f32x4 yv = y[is % 3]; const unsigned so = sy + 4096u * (is >> 1);
          if constexpr (is < 8) asm volatile("buffer_store_dwordx4 %0, %1, %2, %3 offen offset:%c4 sc1" :: "v"(yv), "v"(vo), "s"(sr), "s"(so), "i"(2048 * (is & 1)) : "memory");
          else                  asm volatile("buffer_store_dwordx4 %0, %1, %2, %3 offen sc1\n\ts_nop 1" :: "v"(yv), "v"(vo), "s"(sr), "s"(so) : "memory");
        }
        if constexpr (ik >= 0) { const f32x4 yv = y[ik % 3]; asm volatile("" :: "v"(yv)); }
      };
      phaseBh<1, 0>(c, hook);
    }
  }
  GAP();
  PS_AT(41);
  if (__builtin_expect(is_cls(2u), 0)) {
    PS_AT(42);
    constexpr unsigned NTS = 128u * 4608u;
    unsigned w, b, vw[2], v0, v1, vs, vn, st0, v0s, so;
    bf16x4 wl[2], wh[2];
    auto rd = [&](auto I_) __attribute__((always_inline)) {
      constexpr int i = decltype(I_)::value;
      bf16x4 &l = wl[i & 1], &h = wh[i & 1]; const unsigned v = vw[i & 1];
      asm volatile("ds_read_b64_tr_b16 %0, %1 offset:%2" : "=v"(l) : "v"(v), "i"(1024 * (i >> 1)) : "memory");
      asm volatile("ds_read_b64_tr_b16 %0, %1 offset:%2" : "=v"(h) : "v"(v), "i"(1024 * (i >> 1) + 256) : "memory");
    };
    auto st = [&](auto I_, auto P_, auto Y_) __attribute__((always_inline)) {
      constexpr int i = decltype(I_)::value, P = decltype(P_)::value, u = 2 * i, r = u % 9;
      constexpr auto at = [](int x) constexpr { return (unsigned)(x / 9) * NTS + 2048u * ((x % 9) >> 2) + 64u * ((x % 9) & 3); };
      const u32x4 sr = c.srdS;
      if constexpr (P == 0) { so = st0 + at(r == 8 ? u + 1 : u); asm volatile("" : "+s"(so)); }
      if constexpr (P == 1) {
        asm volatile("s_waitcnt lgkmcnt(%2)" : "+v"(wl[i & 1]), "+v"(wh[i & 1]) : "i"(decltype(Y_)::value) : "memory");
        const u32x4 x = __builtin_bit_cast(u32x4, __builtin_shufflevector(wl[i & 1], wh[i & 1], 0, 1, 2, 3, 4, 5, 6, 7));
        const unsigned vo = r == 8 ? v0 | (v1 ^ 0x40000000u) : r == 7 ? v0 | v1 : (r & 3) == 3 ? vn + (v1 >> 30) * 1792u : vn, s_ = so;
        asm volatile("buffer_store_dwordx4 %0, %1, %2, %3 offen sc1" :: "v"(x), "v"(vo), "s"(sr), "s"(s_) : "memory");
      }
      if constexpr (P == 2) {
        const u32x4 x = __builtin_bit_cast(u32x4, __builtin_shufflevector(wl[i & 1], wh[i & 1], 0, 1, 2, 3, 4, 5, 6, 7));
        if constexpr (r >= 7) {
          const u32x2 e = {x[0], x[2]}, o2 = {x[1], x[3]}; const unsigned v2 = vs | (r == 8 ? v1 : v1 ^ 0x40000000u), s0 = st0;
          asm volatile("buffer_store_dwordx2 %0, %2, %3, %4 offen sc1\n\tbuffer_store_dwordx2 %1, %2, %3, %4 offen offset:128 sc1" :: "v"(e), "v"(o2), "v"(v2), "s"(sr), "s"(s0 + (unsigned)(u / 9) * NTS) : "memory");
        } else asm volatile("" :: "v"(x));
      }
    };
    auto hookB = [&](auto I0_, auto M_) __attribute__((always_inline)) {
      constexpr int M = decltype(M_)::value, i0 = decltype(I0_)::value;
      constexpr int rg[6] = {7, 16, 33, 42, 50, 53}, sg[6] = {14, 25, 43, 48, 56, 62};
      auto one = [&](auto N_) __attribute__((always_inline)) {
        constexpr int n = decltype(N_)::value;
        if constexpr (M == rg[n]) rd(IC(i0 + n));
        if constexpr (M == sg[n] - 1) st(IC(i0 + n), IC(0), IC(0));
        if constexpr (M == sg[n]) st(IC(i0 + n), IC(1), IC(kyo(rg[n], sg[n]) + (n < 5 && rg[n < 5 ? n + 1 : n] < sg[n] ? 2 : 0)));
        if constexpr (M == 101 + sg[n]) st(IC(i0 + n), IC(2), IC(0));
      };
      for_seq(one, make_iseq<6>{});
    };
    {
      auto hook = [&](auto M_) __attribute__((always_inline)) {
        constexpr int M = decltype(M_)::value;
        SC(0, b, blk) SC(1, b, b - 144u) SC(6, w, wids) SC(2, st0, b & 51u) SC(3, so, b & 4u) SA(4, st0, sha<1>(so, st0)) SC(5, so, (b >> 3) & 1u) SA(7, st0, sha<2>(so, st0)) SC(8, so, w & 1u) SC(9, v0s, so << 8)
        SC(10, st0, st0 + v0s) SC(11, st0, st0 * 9216u) SC(12, st0, st0 + WS_WOT) SC(13, v0s, w & 2u) SA(15, so, sha<1>(v0s, so)) SC(16, so, so * 9216u) SC(17, so, so + SD_BASE) SC(18, v0s, v0s << 8)
        VC(FA(0), vw[0], c.vvb[0] & 0xffu) VC(FA(0), v0, c.vvb[0] & 32u) VC(FA(2), vw[0], vw[0] + 16u * v0) VC(FA(2), v0, c.vvb[0] & 2048u) VC(120, vw[0], __umul24(v0, 9u) + vw[0])
        VC(FA(4), vw[0], vw[0] + so) VC(FA(4), vw[1], vw[0] ^ 32u) VC(FA(5), v1, lane_id())
        VC(132, vs, v1 & 7u) VC(135, v0, v1 & 32u) VC(100 + FA(6), vs, 2u * vs + v0) VC(138, v0, (v1 >> 4) & 1u) VC(141, vs, vs + v0) VC(143, vs, 8u * vs + v0s) VC(145, vs, vs + 8192u)
        SC(46, v0s, v0s << 3) VC(148, v0, v1 & 4u) VC(151, vn, v1 & 51u) VC(FA(9), v0, 16u * v0 + vn) VC(FA(9), v0, 16u * v0 + v0s) VC(157, v1, v1 & 8u) VC(FA(11), vn, 8u * v1 + v0) VC(FA(11), v1, v1 << 27)
      };
      phaseAh<0, 3>(c, 10, hook);
    }
    { auto hook = [&](auto M_) __attribute__((always_inline)) { hookB(std::integral_constant<int, 0>{}, M_); }; phaseBh<0, 0>(c, hook); }
    {
      auto hook = [&](auto M_) __attribute__((always_inline)) {
        constexpr int M = decltype(M_)::value;
        constexpr int rg[6] = {0, 8, 16, 24, 37, 55}, sg[6] = {6, 14, 22, 30, 53, 62};
        auto one = [&](auto N_) __attribute__((always_inline)) {
          constexpr int n = decltype(N_)::value, y = 2 * (pcnt(PA0, 'T', sg[n]) - pcnt(PA0, 'T', rg[n])) + pcnt(PA0, 'X', sg[n]) - pcnt(PA0, 'X', rg[n]);
          if constexpr (M == rg[n]) rd(IC(6 + n));
          if constexpr (M == sg[n] - 1) st(IC(6 + n), IC(0), IC(0));
          if constexpr (M == sg[n]) st(IC(6 + n), IC(1), IC(y));
          if constexpr (M == 101 + sg[n]) st(IC(6 + n), IC(2), IC(0));
        };
        for_seq(one, make_iseq<6>{});
      };
      phaseAh<1, 14>(c, 11, hook);
    }
    { auto hook = [&](auto M_) __attribute__((always_inline)) { hookB(std::integral_constant<int, 12>{}, M_); }; phaseBh<1, 0>(c, hook); }
    phaseA<0>(c, 12);
    {
      auto hook = [&](auto M_) __attribute__((always_inline)) {
        constexpr int M = decltype(M_)::value;
        SA(2, st0, (shi<2, 4u * H_W>(b))) SC(3, v0s, w - 1u) SC(4, v0s, (int)v0s >> 31) SC(5, so, st0 + 4u) SC(7, v0s, v0s & so) VC(109, v0, tag)
        if constexpr (M == 10) lim(v0s);
        if constexpr (M == FB(0)) flag_st(st0, v0);
      };
      phaseBh<0, 0>(c, hook);
    }
    phaseA<1>(c, 13); phaseB<1, 0>(c);
  }
  OPS o; OST ot; unsigned xo, xs, r1; unsigned long long tm;
  {
    const unsigned w = opq(wids), b = opq(blk);
    unsigned va, vs, vt, r0;
    {
      unsigned tg, m0, sf, mt, hs, t, u;
      auto hookA = [&](auto M_) __attribute__((always_inline)) {
        constexpr int M = decltype(M_)::value;
        SC(FA(0), m0, (w - 1u) & (b - 144u)) SC(FA(1), m0, (int)m0 >> 31) SA(FA(2), sf, (shi<2, 4u * H_Y>(b))) SC(FA(4), m0, m0 & (sf + 4u)) VC(FA(5), tg, tag)
        if constexpr (M == FA(5)) lim(m0);
        SC(0, mt, b & 3u) SC(1, t, b >> 6) SA(2, mt, sha<2>(mt, t)) SC(3, t, (b >> 2) & 1u) SA(4, mt, sha<1>(mt, t))
        SA(5, u, sha<2>(mt, w)) SC(7, o.sn, u * (16u * XRS)) SC(8, t, (b >> 3) & 7u) SC(9, hs, t * 144u) SC(11, o.sn, o.sn + hs)
        SA(12, hs, sha<2>(t, w)) SC(13, ot.so[0], hs << 19) SC(15, o.sf, hs >> 1) SC(16, o.sf, o.sf << 11)
        SC(17, u, u ^ 4u) SC(19, o.sa, u << 17) SC(20, u, (u >> 2) & 3u) SC(21, u, u << 10) SC(23, o.sa, o.sa | u)
        SC(24, t, ~b & 4u) SC(25, t, t + w) SC(26, u, t << 14) SC(27, o.sa, o.sa | u)
        SC(28, mt, mt >> 1) SA(29, t, sha<3>(mt, t)) SC(31, o.sl, t << 8) SC(32, o.sl, o.sl | 4u * H_O)
        SC(33, hs, b & 6u) SC(34, t, b >> 7) SC(35, hs, hs + t) SC(36, t, b & 1u) SA(38, mt, sha<1>(hs, t)) SC(39, u, (b >> 6) & 1u) SA(40, hs, sha<1>(mt, u))
        SA(42, t, sha<2>(hs, w)) SC(43, t, t << 10) SC(45, ot.so[0], ot.so[0] + t) SC(51, ot.so[0], ot.so[0] | WS_O) SC(52, o.sa, o.sa | WS_O)
        SA(44, u, sha<2>(mt, u)) SC(46, t, w & 1u) SA(48, t, sha<1>(u, t)) SA(49, t, (shi<4, 4u * H_O>(t))) SC(50, o.sf, o.sf + t)
        SC(55, t, b - RED0) SA(56, xo, sha<2>(t, w)) SA(58, xo, (shi<4, 4u * H_X>(xo))) SC(59, xs, xo + 4u)
        if constexpr (M == FA(11)) asm volatile("s_cmp_lt_u32 %1, %2\n\ts_cselect_b32 %0, %0, 0" : "+s"(xs) : "s"(t), "i"(RED1 - RED0) : "scc");
      };
      phaseAh<0, 0>(c, 14, hookA);
      auto hook = [&](auto M_) __attribute__((always_inline)) {
        constexpr int M = decltype(M_)::value;
        if constexpr (M == FB(0)) flag_st(sf, tg);
        warm_at(M_, IC(FB(6)));
        if constexpr (M == 40) lim(4u * H_W + 256u);
        if constexpr (M == 41) m0_imm(IC(WF_FL));
        if constexpr (M == FB(3)) dma16(IC(H_W));
        if constexpr (M == 45) lim(64u);
        if constexpr (M == 46) m0_imm(IC(SEL_C));
        if constexpr (M == FB(4)) dma16(IC(0));
        VC(FB(1), c.pfv, lane_id()) VC(DB(8), vt, c.pfv & 7u) VC(DB(8), c.pfv, c.pfv << 4) VC(DB(9), va, (c.vksrc[0] & 0xf0u) | WF_FL) VC(FB(8), vs, 8u * vt + SEL_C) VC(FB(8), vt, vt + SEQ)
        SC(2, t, (b >> 3) & 7u) SC(4, u, (o.sa >> 14) & 7u) SA(5, t, sha<3>(t, u)) SC(7, o.sb, t * 73728u) SC(9, o.sb, o.sb + WS_WOT) SC(10, c.pfo, o.sb - 1024u) SC(12, c.pfst, 1024u)
        SC(15, c.pfm0, BP_W * w) SC(18, c.pfm0, c.pfm0 + SD_BASE) SC(20, c.pfend, c.pfm0 + BP_W) SC(33, c.srdP, c.srdS)
        SC(23, r0, o.sa >> 13) SC(26, r0, r0 ^ 64u) SC(28, r0, r0 & 2032u) SC(30, r1, w << 7) SC(31, r1, r1 + (SEL_C + 256u))
      };
      phaseBh<0, 0>(c, hook);
    }
    {
      u32x4 f; u32x2 sv; unsigned long long no;
      phaseA<1, 1>(c, 15);
      auto hook = [&](auto M_) __attribute__((always_inline)) {
        constexpr int M = decltype(M_)::value;
        if constexpr (M == FB(0)) asm volatile("ds_read_b128 %0, %2\n\tds_read_b64 %1, %3" : "=&v"(f), "=&v"(sv) : "v"(va), "v"(vs) : "memory");
        if constexpr (M == FB(1)) { asm volatile("s_waitcnt lgkmcnt(%2)" : "+v"(f), "+v"(sv) : "i"(kyo(FB(0), FB(1))) : "memory"); f[0] = (f[0] ^ tag) | (f[1] ^ tag); asm volatile("" : "+v"(f)); }
        if constexpr (M == 126) { f[2] = (f[2] ^ tag) | (f[3] ^ tag); asm volatile("" : "+v"(f)); }
        if constexpr (M == 128) { f[0] |= f[2]; asm volatile("" : "+v"(f)); }
        if constexpr (M == 130) { no = __ballot(f[0] != 0u); asm volatile("" : "+s"(no)); }
        if constexpr (M == FB(2)) {
          if (__builtin_expect(no != 0ull, 0)) {
#pragma nounroll
            for (unsigned spin = 0; spin < (1u << 20); ++spin) {
              __builtin_amdgcn_s_sleep(8);
              hand_req(c.ldsb + WF_FL, H_W, 256u);
              asm volatile("s_waitcnt vmcnt(0)\n\tds_read_b128 %0, %1\n\ts_waitcnt lgkmcnt(0)" : "=v"(f) : "v"(va) : "memory");
              if (__all(((f[0] ^ tag) | (f[1] ^ tag) | (f[2] ^ tag) | (f[3] ^ tag)) == 0u)) break;
            }
            asm volatile("s_add_u32 m0, %0, %1" :: "s"(c.m0k), "i"(VRING) : "m0", "scc", "memory");
          }
        }
        VC(134, vs, sv[0] & 0xffffu) VC(136, va, vs - r0) VC(138, vt, vt - vs)
        if constexpr (M == FB(5)) { const unsigned x = va; asm volatile("v_cmp_gt_u32_e64 %0, 16, %1" : "=s"(tm) : "v"(x)); }
        if constexpr (M == 140) { const unsigned x = sv[1], g = tag; asm volatile("v_cmp_ne_u32_e64 %0, %1, %2" : "=s"(o.seen) : "s"(g), "v"(x)); }
        VC(158, va, va < 16u ? va : 16u) VC(FB(8), vt, __umul24(vt, XRS)) VC(FB(8), va, 4u * va + r1) SC(64, o.bad, (unsigned)o.seen)
        if constexpr (M == FB(9)) lds_st32(va, vt);
        if constexpr (M == 59) lim(0u);
        if constexpr (M == 60) flag_st(xo, vt);
      };
      phaseBh<1, 2>(c, hook);
    }
  }
  GAP();
  if (__builtin_expect(is_cls(3u), 0)) {
    PS_AT(43);
    unsigned va, vb, nv, s0, sr, sj; u32x4 f; u32x2 sv; f32x4 y[4]; float a[4]; unsigned long long no;
    auto req_fl = [&](auto W_) __attribute__((always_inline)) {
      u32x4& f_ = f; u32x2& s_ = sv; const unsigned v0 = c.pfv, v1 = vb; const u32x4 r = c.srdS;
      if constexpr (decltype(W_)::value != 1) asm volatile("buffer_load_dwordx4 %0, %1, %2, 0 offen offset:%c3 sc1" : "=v"(f_) : "v"(v0), "s"(r), "i"(4 * H_Y) : "memory");
      if constexpr (decltype(W_)::value != 0) asm volatile("buffer_load_dwordx2 %0, %1, %2, 0 offen sc1" : "=v"(s_) : "v"(v1), "s"(r) : "memory");
    };
    auto add2 = [&](auto N_, auto E_) __attribute__((always_inline)) {
      constexpr int n = decltype(N_)::value, e = decltype(E_)::value; const f32x4 v = y[n & 3];
      if constexpr (n == 0) { a[e] = v[e]; a[e + 1] = v[e + 1]; PIN2(a[e], a[e + 1]); } else if constexpr (n > 0 && n < 18) { add_v(a[e], v[e]); add_v(a[e + 1], v[e + 1]); }
    };
    auto batch = [&](auto B_, auto M_) __attribute__((always_inline)) {
      constexpr int B = decltype(B_)::value, M = decltype(M_)::value, i = M == 9 ? 0 : M == FB(0) ? 1 : M == FB(1) ? 2 : M == FB(2) ? 3 : -1, j = M == 8 ? 0 : M == 12 ? 1 : M == 23 ? 2 : M == 31 ? 3 : -1;
      if constexpr (M == 116 || M == 120) add2(IC(4 * B - 2), IC(M == 116 ? 0 : 2));
      if constexpr (M == 126 || M == 130) add2(IC(4 * B - 1), IC(M == 126 ? 0 : 2));
      if constexpr (j >= 0 && 4 * B + j < 18) { sr += 8u * NSEL * DM * 4u; asm volatile("" : "+s"(sr)); }
      if constexpr (i >= 0 && 4 * B + i < 18) {
        f32x4& d = y[i]; const unsigned v = va, so = sr; const u32x4 r = c.srdS;
        asm volatile("buffer_load_dwordx4 %0, %1, %2, %3 offen sc1" : "=v"(d) : "v"(v), "s"(r), "s"(so) : "memory");
      }
    };
    auto phase_a = [&](auto X_, auto B_) __attribute__((always_inline)) {
      constexpr int X = decltype(X_)::value, B = decltype(B_)::value, n0 = 6 + (B < 4 ? 3 : 2) + 3 - X;
      auto hook = [&](auto M_) __attribute__((always_inline)) {
        constexpr int M = decltype(M_)::value;
        if constexpr (M == FA(4)) { f32x4& d = y[0]; asm volatile("s_waitcnt vmcnt(%1)" : "+v"(d) : "i"(n0) : "memory"); }
        if constexpr (M == FA(9)) { f32x4& d = y[1]; asm volatile("s_waitcnt vmcnt(%1)" : "+v"(d) : "i"(n0 - 2) : "memory"); }
        if constexpr (M == 100 + FA(4) || M == FA(5)) add2(IC(4 * B), IC(M > 100 ? 0 : 2));
        if constexpr (M == 100 + FA(9) || M == FA(11)) add2(IC(4 * B + 1), IC(M > 100 ? 0 : 2));
      };
      phaseAh<X, 3 - X>(c, 19 + B, hook);
      if constexpr (B < 4) asm volatile("" : "+v"(y[2]), "+v"(y[3]));
    };
    phaseA<0, 3>(c, 16); phaseB<0, 2>(c);
    {
      auto hook = [&](auto M_) __attribute__((always_inline)) {
        constexpr int M = decltype(M_)::value;
        VC(FA(0), vb, (c.pfv >> 1) & 56u) VC(FA(2), va, __umul24(c.pfv >> 7, NSEL * DM * 4)) VC(FA(4), va, va + (c.pfv & 112u))
        SC(0, sj, xo - 4u * H_X) SA(1, sr, (shi<3, WS_YP - 8u * NSEL * DM * 4u>(sj))) SC(2, sj, sj >> 1) SC(3, sj, sj + WS_XN)
      };
      phaseAh<1, 2>(c, 17, hook);
    }
    { auto hook = [&](auto M_) __attribute__((always_inline)) { constexpr int M = decltype(M_)::value; if constexpr (M == FB(1)) req_fl(IC(1)); if constexpr (M == 40) req_fl(IC(0)); }; phaseBh<1, 2>(c, hook); }
    {
      auto hook = [&](auto M_) __attribute__((always_inline)) {
        constexpr int M = decltype(M_)::value;
        if constexpr (M == FA(9)) { asm volatile("s_waitcnt vmcnt(3)" : "+v"(f), "+v"(sv) :: "memory"); f[0] = (f[0] ^ tag) | (f[1] ^ tag); f[2] = (f[2] ^ tag) | (f[3] ^ tag); asm volatile("" : "+v"(f)); }
        if constexpr (M == 157) { f[0] |= f[2]; asm volatile("" : "+v"(f)); }
        if constexpr (M == FA(11)) { no = (__ballot(f[0] != 0u) << 28) | __ballot(sv[1] != tag); asm volatile("" : "+s"(no)); }
      };
      phaseAh<0, 3>(c, 18, hook);
    }
    {
      auto hook = [&](auto M_) __attribute__((always_inline)) {
        constexpr int M = decltype(M_)::value;
        if constexpr (M == 4) {
          if (__builtin_expect(no != 0ull, 0)) {
            wait_flag(c.srdS, vb + 4u, tag);
#pragma nounroll
            for (unsigned q = 0; q < 3u; ++q) { const unsigned i = 64u * q + lane_id(); wait_flag(c.srdS, 4u * H_Y + 4u * (i < (unsigned)MOE_WG ? i : 0u), tag); }
            { u32x2& s_ = sv; const unsigned v = vb; const u32x4 r = c.srdS; asm volatile("buffer_load_dwordx2 %0, %1, %2, 0 offen sc1\n\ts_waitcnt vmcnt(0)" : "+v"(s_) : "v"(v), "s"(r) : "memory"); }
          }
        }
        batch(IC(0), M_);
        if constexpr (M == 16) { unsigned& d = s0; const unsigned x = sv[0]; asm volatile("v_mov_b32_e32 %0, %1" : "=v"(d) : "v"(x)); }
      };
      phaseBh<0, 2>(c, hook);
    }
    phase_a(IC(1), IC(0));
    { auto hook = [&](auto M_) __attribute__((always_inline)) { batch(IC(1), M_); }; phaseBh<1, 2>(c, hook); }
    phase_a(IC(0), IC(1));
    { auto hook = [&](auto M_) __attribute__((always_inline)) { batch(IC(2), M_); }; phaseBh<0, 2>(c, hook); }
    phase_a(IC(1), IC(2));
    { auto hook = [&](auto M_) __attribute__((always_inline)) { batch(IC(3), M_); }; phaseBh<1, 2>(c, hook); }
    phase_a(IC(0), IC(3));
    {
      auto hook = [&](auto M_) __attribute__((always_inline)) {
        constexpr int M = decltype(M_)::value;
        batch(IC(4), M_);
        VC(123, vb, (c.pfv >> 7) & 6u) VC(FB(1), nv, s0 & 0xffffu) VC(FB(1), nv, __umul24(nv, XRS) + vb)
        if constexpr (M == FB(2)) {
          unsigned& n_ = nv; const unsigned o2 = sj; const u32x4 r = c.srdS;
          asm volatile("buffer_load_ushort %0, %0, %1, %2 offen sc1" : "+v"(n_) : "s"(r), "s"(o2) : "memory");
        }
        VC(FB(6), va, (c.pfv >> 4) & 15u) VC(FB(6), va, __umul24(va, XRS) + vb) SC(60, sj, sj + SEQ * XRS)
        if constexpr (M == 61) lim(WS_WOT);
      };
      phaseBh<0, 2>(c, hook);
    }
    phase_a(IC(1), IC(4));
    asm volatile("" : "+v"(nv));
    {
      auto hook = [&](auto M_) __attribute__((always_inline)) {
        constexpr int M = decltype(M_)::value;
        if constexpr (M == FB(0) || M == FB(1)) {
          constexpr int e = M == FB(1);
          auto pr = __builtin_amdgcn_permlane32_swap(__float_as_uint(a[e]), __float_as_uint(a[e + 2]), false, false);
          a[e] = __uint_as_float(pr[0]); a[e + 2] = __uint_as_float(pr[1]); PIN2(a[e], a[e + 2]);
        }
        if constexpr (M == 16) add_v(a[0], a[2]);
        if constexpr (M == 128) add_v(a[1], a[3]);
        if constexpr (M == FB(2)) { auto pr = __builtin_amdgcn_permlane16_swap(__float_as_uint(a[0]), __float_as_uint(a[1]), false, false); a[0] = __uint_as_float(pr[0]); a[1] = __uint_as_float(pr[1]); PIN2(a[0], a[1]); }
        if constexpr (M == 136) add_v(a[0], a[1]);
        VC(140, a[0], dpp_add<0x128>(a[0])) VC(144, a[0], rbf(a[0])) VC(FB(5), a[1], bfhi(s0)) VC(153, a[0], a[0] * a[1]) VC(158, a[0], rbf(a[0]))
        VC(FB(8), a[0], a[0] + bflo(nv)) VC(164, nv, cvtpk(a[0], 0.f))
        if constexpr (M == FB(9)) { const unsigned d = nv, vo = va, so = sj; const u32x4 r = c.srdL; asm volatile("buffer_store_short %0, %1, %2, %3 offen sc1" :: "v"(d), "v"(vo), "s"(r), "s"(so) : "memory"); }
      };
      phaseBh<1, 2>(c, hook);
    }
  }
  GAP();
  {
    int t = t2;
    unsigned tg = tag;
    asm volatile("" : "+v"(tg));
    lim(xs);
    auto hook = [&](auto M_) __attribute__((always_inline)) {
      constexpr int M = decltype(M_)::value;
      if constexpr (M == FB(8)) flag_st(xo, tg);
      if constexpr (M == FB(8) + 1) lim(0u);
    };
#pragma nounroll
    do {
      t += 2;
      phaseA<0, 4>(c, t);     phaseB<0, 2>(c);
      phaseA<1, 2>(c, t + 1); phaseBh<1, 2>(c, hook);
    } while (__builtin_expect(t != T - 4, 1));
  }
  GAP();
  {
    const unsigned w = opq(wids), b = opq(blk);
    u32x4 sh; unsigned ax[2], dl, fx, fy, t, sx;
    auto req = [&](auto X_, auto J_) __attribute__((always_inline)) {
      constexpr int X = decltype(X_)::value, J = decltype(J_)::value;
      const unsigned v = ax[J >> 1], so = X ? o.sn : sx; const u32x4 r = X ? sh : c.srdS;
      if constexpr (J < 2) asm volatile("buffer_load_dwordx4 %0, %1, %2, %3 offen offset:%c4 sc1" : "=v"(o.gv[X][J & 1]) : "v"(v), "s"(r), "s"(so), "i"(64 * J) : "memory");
      else                 asm volatile("buffer_load_dword %0, %1, %2, %3 offen offset:128 sc1" : "=v"(o.gs[X]) : "v"(v), "s"(r), "s"(so) : "memory");
    };
    auto hookA = [&](auto M_) __attribute__((always_inline)) {
      constexpr int M = decltype(M_)::value;
      SC(0, t, (b >> 3) & 7u) SC(1, t, t * 288u) SC(2, t, t + 4u * H_X)
      if constexpr (M == 3) lim(t + 288u);
      if constexpr (M == FA(0)) { unsigned& d = fx; const unsigned v = c.pfv, so = t; const u32x4 r = c.srdL; asm volatile("buffer_load_dword %0, %1, %2, %3 offen sc1" : "=v"(d) : "v"(v), "s"(r), "s"(so) : "memory"); }
      if constexpr (M == 8) lim(o.sl + 64u);
      VC(FA(2), dl, (c.pfv >> 4) & 15u) VC(FA(2), ax[1], __umul24(dl, XRS)) VC(116, fy, c.pfv >> 8) VC(FA(4), ax[0], 16u * fy + ax[1]) VC(FA(4), fy, 0x3120u >> (4u * fy))
      VC(132, fy, fy & 3u) VC(135, ax[1], 4u * fy + ax[1])
      if constexpr (M >= 9 && M < 13) { const u32x4 d = mk_srd4(xres, SEQ * DM * 2); sh[M - 9] = d[M - 9]; asm volatile("" : "+s"(sh)); }
      if constexpr (M == FA(5)) req(IC(1), IC(0));
      if constexpr (M == FA(6)) req(IC(1), IC(1));
      if constexpr (M == FA(11)) req(IC(1), IC(2));
      VC(FA(9), dl, 4u * dl + r1)
      if constexpr (M == 55) { unsigned& d = dl; asm volatile("ds_read_b32 %0, %0" : "+v"(d) :: "memory"); }
      SC(FA(10), sx, o.sn + WS_XN)
      SC(24, ot.so[1], ot.so[0] ^ 1024u) SC(25, ot.so[2], ot.so[0] ^ 2048u) SC(26, ot.so[3], ot.so[0] ^ 3072u)
    };
    phaseAh<0, 3>(c, T - 2, hookA);
    asm volatile("" : "+v"(dl), "+v"(fx));
    o.srdS = c.srdS; ot.srd = c.srdS; o.va = c.pfv;
    auto hook = [&](auto M_) __attribute__((always_inline)) {
      constexpr int M = decltype(M_)::value;
      constexpr int f = M < NGB && PB2[M < NGB ? M : 0] == 'F' ? pcnt(PB2, 'F', M) : -1;
      VC(100, ax[0], ax[0] + dl) VC(16, ax[1], ax[1] + dl)
      if constexpr (f >= 0 && f < 3) req(IC(0), IC(f));
      if constexpr (M == 33) { const unsigned x = fx, g = tag; asm volatile("v_cmp_ne_u32_e64 %0, %1, %2" : "=s"(o.seen) : "s"(g), "v"(x)); }
      SC(35, t, (unsigned)o.seen & 0x3ffffu)
      if constexpr (M == 40) { unsigned& x = t; const unsigned long long m = tm; asm volatile("s_cmp_lg_u64 %1, 0\n\ts_cselect_b32 %0, %0, 0" : "+s"(x) : "s"(m) : "scc"); }
      SC(44, o.bad, o.bad | t)
      SC(2, c.pfo, o.sb + 23u * 1024u) SC(46, c.pfo, o.sb + 27u * 1024u) SC(48, c.pfm0, 8192u * w) SC(49, c.pfm0, c.pfm0 + (c.ldsb + 0x4000u))
    };
    static_assert(pcnt(PB2, 'E', NGB) == NBL && ppos(PB2, 'W', 0) == DB(4), "");
    phaseB_<0, false, false, 0>(c, hook, make_iseq<NGB>{});
  }
  ot.vo = c.pfv;
  fin_gaps(c, ot, o, make_iseq<NFIN>{});
  asm volatile("s_nop 1" :: "v"(ot.w));
  if (__builtin_expect(!__all((ot.ls[0] + ot.ls[1]) + (ot.ls[2] + ot.ls[3]) <= LIM), 0))
    redo_rows(c, ot, o, wq, 256u * ((opq(blk) >> 3) & 7u) + 64u * opq(wids), 4u * (opq(blk) & 7u) + (opq(blk) >> 6), tag);
  o.srdA = c.srdP; o.srdL = c.srdL; o.tag = tag;
  oproj_phase(o, lds, out, opq(wids)  );
}
extern "C" __global__ void k_attn_end() { asm volatile(PS_END ".subsection 1\n.Lk_end:\n.fill 64, 4, 0xBF800000\n.subsection 0" ::: "memory"); }

extern "C" void kernel_launch(void* const* d_in, const int* in_sizes, int n_in,
                              void* d_out, int out_size, void* d_ws, size_t ws_size,
                              hipStream_t stream) {
  const u16* x   = (const u16*)d_in[0];
  const u16* kc  = (const u16*)d_in[1];
  const u16* vc  = (const u16*)d_in[2];
  const u16* wq  = (const u16*)d_in[3];
  const u16* wo  = (const u16*)d_in[4];
  const u16* w_r = (const u16*)d_in[6];
  const u16* wg  = (const u16*)d_in[7];
  const u16* wu  = (const u16*)d_in[8];
  const u16* wd  = (const u16*)d_in[9];
  char* ws = (char*)d_ws;
  (void)hipFuncSetAttribute((const void*)k_attn, hipFuncAttributeMaxDynamicSharedMemorySize, (int)SHM_ATTN);
  hipLaunchKernelGGL(k_attn, dim3(256), dim3(256), SHM_ATTN, stream, ws, wq, x, w_r, kc, vc, wo, wg, wu, wd, (u16*)d_out  );
}
```

```cpp
#include <hip/hip_runtime.h>
#include <hip/hip_bf16.h>
#include <utility>
#include <type_traits>

constexpr int SEQ = 2048, DM = 576, HQ = 32, HKV = 16, DH = 128, FF = 4608, NSEL = 8;
constexpr int NT = SEQ / 64;
constexpr float LIM = 1073741824.0f;

typedef float    f32x2  __attribute__((ext_vector_type(2)));
typedef float    f32x3  __attribute__((ext_vector_type(3)));
typedef float    f32x4  __attribute__((ext_vector_type(4)));
typedef __bf16   bf16x8 __attribute__((ext_vector_type(8)));
typedef __bf16   bf16x4 __attribute__((ext_vector_type(4)));
typedef __bf16   bf16x2 __attribute__((ext_vector_type(2)));
typedef unsigned u32x8  __attribute__((ext_vector_type(8)));
typedef unsigned u32x4  __attribute__((ext_vector_type(4)));
typedef unsigned u32x2  __attribute__((ext_vector_type(2)));
typedef unsigned short u16;
template <int... Is> using iseq = std::integer_sequence<int, Is...>;
template <int N> using make_iseq = std::make_integer_sequence<int, N>;

#define DI __device__ __forceinline__
#define GAP() __builtin_amdgcn_sched_barrier(0)
#define IC(x) std::integral_constant<int, (x)>{}
#define SRD_WORD3 0x00020000

DI unsigned rfl(unsigned x) { return __builtin_amdgcn_readfirstlane(x); }
DI unsigned cvtpk(float lo, float hi) { f32x2 v = {lo, hi}; bf16x2 b = __builtin_convertvector(v, bf16x2); return __builtin_bit_cast(unsigned, b); }
DI __amdgpu_buffer_rsrc_t mk_srd(const void* p, const unsigned bytes) {
  asm volatile("" : "+s"(p));
  return __builtin_amdgcn_make_buffer_rsrc((void*)p, (short)0, (int)bytes, SRD_WORD3);
}
DI u32x4 mk_srd4(const void* p, const unsigned bytes) { asm volatile("" : "+s"(p)); const unsigned long long a = (unsigned long long)p; return u32x4{(unsigned)a, (unsigned)(a >> 32) & 0xffffu, bytes, SRD_WORD3}; }
template <int N> DI unsigned sha(const unsigned a, const unsigned b) { unsigned r; asm volatile("s_lshl%c3_add_u32 %0, %1, %2" : "=s"(r) : "s"(a), "s"(b), "i"(N) : "scc"); return r; }
template <int N, unsigned B> DI unsigned shi(const unsigned a) { unsigned r; asm volatile("s_lshl%c3_add_u32 %0, %1, %2" : "=s"(r) : "s"(a), "i"(B), "i"(N) : "scc"); return r; }
DI float bflo(unsigned w) { return __uint_as_float(w << 16); }
DI float bfhi(unsigned w) { return __uint_as_float(w & 0xffff0000u); }
DI float rbf(float x) { return bflo(cvtpk(x, 0.f)); }
DI u16 f2bf(float x) { return (u16)(cvtpk(x, 0.f) & 0xffffu); }

#define PS_MACROS \
  ".macro psl n\n.Lps_\\n: s_cbranch_execz .Lpn_\\n\n.endm\n.set .Lpsl, 0\n" \
  ".macro ps\n.set .Lpso, (. - k_attn) >> 12\n.if .Lpso != .Lpsl\n.set .Lpsl, .Lpso\n.altmacro\npsl %%.Lpso\n.noaltmacro\n.endif\n.endm\n" \
  ".macro mf16 a, b, c, d\nv_mfma_f32_16x16x32_bf16 \\a, \\b, \\c, \\d\nps\n.endm\n" \
  ".macro sm64 a, b, c, d\nv_smfmac_f32_16x16x64_bf16 \\a, \\b, \\c, \\d\nps\n.endm\n"
#define PS_IDS "0,1,2,3,4,5,6,7,8,9,10,11,12,13,14,15,16,17,18,19,20,21,22,23,24,25,26,27,28,29,30,31,32,33,34,35,36,37,38,39,40,41,42,43,44,45,46,47"
#define PS_AT(n) asm volatile("psl " #n)
#define PS_PG(n, i) "%%(((.Lps_" #n " - k_attn) >> 12) + 1 + " #i ")"
#define PS_CH(k, n, i) "pse " #k ", " PS_PG(n, i) "\n"
#define PS_NX(n, i, j) "psn " PS_PG(n, i) ", " PS_PG(41, j) "\n"
#define PS_END ".irp n," PS_IDS "\n.ifndef .Lps_\\n\n.set .Lps_\\n, .Lps_ret\n.endif\n.endr\n" \
  ".macro pse k, n\n.set .Lpe_\\k, .Lps_\\n\n.endm\n.macro psn a, b\n.set .Lpn_\\a, .Lps_\\b\n.endm\n.altmacro\n" \
  "pse 0, 2\npse 1, 3\npse 2, 4\npse 3, 5\n" \
  PS_CH(4, 41, -4) PS_CH(5, 41, -3) PS_CH(6, 41, -2) PS_CH(7, 41, -1) PS_NX(41, -4, 0) PS_NX(41, -3, 1) PS_NX(41, -2, 2) PS_NX(41, -1, 3) \
  "pse 8, 42\npse 9, 41\n" PS_CH(10, 42, 0) PS_CH(11, 42, 1) "psn 42, " PS_PG(41, 0) "\npsn 41, " PS_PG(41, 1) "\n" PS_NX(42, 0, 2) PS_NX(42, 1, 3) \
  "pse 12, 43\n" PS_CH(13, 43, 1) PS_CH(14, 43, 2) PS_CH(15, 43, 3) "psn 43, " PS_PG(43, 0) "\npsn " PS_PG(43, 1) ", 41\n" PS_NX(43, 0, 0) PS_NX(43, 2, 2) PS_NX(43, 3, 3) \
  PS_CH(16, 41, 0) "pse 17, 41\n" PS_CH(18, 41, 2) PS_CH(19, 41, 3) PS_CH(20, 45, 0) PS_CH(21, 45, 1) PS_CH(22, 45, 2) PS_CH(23, 45, 0) \
  ".noaltmacro\n.irp n," PS_IDS "\n.ifndef .Lpn_\\n\n.set .Lpn_\\n, .Lps_ret\n.endif\n.endr\n"
DI void ps_touch(const unsigned n) {
  asm volatile("s_getpc_b64 s[20:21]\n\ts_lshl2_add_u32 vcc_lo, %0, .Lps_tab - .\n\ts_add_u32 s20, s20, vcc_lo\n\ts_addc_u32 s21, s21, -1\n\ts_mov_b64 exec, 0\n\ts_swappc_b64 vcc, s[20:21]\n\ts_mov_b64 exec, -1"
               :: "s"(n) : "vcc", "scc", "s20", "s21", "memory");
}

constexpr unsigned H_G = 32u, H_W = 64u, H_Y = 128u, H_O = 8192u, H_X = 1536u, H_E = 16384u;
constexpr unsigned WS_KEY = 131072u;
constexpr unsigned WS_ST  = WS_KEY + SEQ * 8;
constexpr unsigned WS_XN  = WS_ST + SEQ * 16;
constexpr unsigned WS_WOT = WS_XN + (SEQ + NSEL) * DM * 2;
constexpr int      MOE_WG = 144;
constexpr unsigned WS_YP  = WS_WOT + HQ * DH * DM * 2;
constexpr unsigned WS_EX  = WS_YP + MOE_WG * NSEL * DM * 4;
constexpr unsigned WS_O   = 1u << 24, WS_END = 2u << 24;
static_assert(WS_EX + 256u * 16384u <= WS_O && WS_WOT % 1024u == 0u, "");

DI void ce(unsigned& a, unsigned& b) { const unsigned hi_ = a > b ? a : b, lo_ = a > b ? b : a; a = hi_; b = lo_; }
DI int key_row(unsigned k) { return 0xffff - (int)(k & 0xffffu); }
DI float key_val(unsigned k) { const unsigned mono = k >> 16; const unsigned rb = (mono & 0x8000u) ? (mono & 0x7fffu) : (~mono & 0xffffu); return __uint_as_float(rb << 16); }

struct LNJ { u32x4 a[2], w[2]; unsigned a2, w2; float x0, s[2], q[2], mb, den, rden, t0, t1; unsigned key; bool up; };
template <int E> DI unsigned ln_a(const LNJ& j) { if constexpr (E < 8) return j.a[E >> 2][E & 3]; else return j.a2; }
template <int E> DI unsigned ln_w(const LNJ& j) { if constexpr (E < 8) return j.w[E >> 2][E & 3]; else return j.w2; }
DI float rcp_nr(float d) { const float r = __builtin_amdgcn_rcpf(d); return __builtin_fmaf(__builtin_fmaf(-d, r, 1.f), r, r); }
template <int CTRL> DI float dpp_add(float v) { return v + __int_as_float(__builtin_amdgcn_update_dpp(0, __float_as_int(v), CTRL, 0xf, 0xf, false)); }
template <int K> DI float red_step(float v) {
  if constexpr (K == 0) return dpp_add<0xB1>(v);
  else if constexpr (K == 1) return dpp_add<0x4E>(v);
  else if constexpr (K == 2) return dpp_add<0x141>(v);
  else if constexpr (K == 3) return dpp_add<0x140>(v);
  else { auto pr = __builtin_amdgcn_permlane16_swap(__float_as_uint(v), __float_as_uint(v), false, false); return __uint_as_float(pr[0]) + __uint_as_float(pr[1]); }
}
#define PIN2(a, b) asm volatile("" : "+v"(a), "+v"(b))
constexpr int LN_N0 = 29, LN_XN = LN_N0 + 72;
template <int I> DI void ln_step(LNJ& j, const unsigned row) {
  if constexpr (I == 0) {
    const float xa = bflo((unsigned)__builtin_amdgcn_readlane((int)j.a[0][0], 0)), xb = bflo((unsigned)__builtin_amdgcn_readlane((int)j.a[0][0], 32));
    j.x0 = j.up ? xb : xa;
    asm volatile("" : "+v"(j.x0));
  } else if constexpr (I <= 18) {
    constexpr int e = I - 1, c = e & 1;
    const float d = (c ? bfhi(ln_a<(e >> 1)>(j)) : bflo(ln_a<(e >> 1)>(j))) - j.x0;
    if constexpr (e < 2) { j.s[c] = d; j.q[c] = d * d; } else { j.s[c] += d; j.q[c] = __builtin_fmaf(d, d, j.q[c]); }
    PIN2(j.s[c], j.q[c]);
  } else if constexpr (I == 19) {
    j.t0 = j.s[0] + j.s[1]; j.t1 = j.q[0] + j.q[1];
    PIN2(j.t0, j.t1);
  } else if constexpr (I <= 24) {
    j.t0 = red_step<I - 20>(j.t0); j.t1 = red_step<I - 20>(j.t1);
    PIN2(j.t0, j.t1);
  } else if constexpr (I == 25) {
    j.t0 *= 1.0f / DM; j.t1 = __builtin_fmaxf(__builtin_fmaf(-j.t0, j.t0, j.t1 * (1.0f / DM)), 0.f);
    PIN2(j.t0, j.t1);
  } else if constexpr (I == 26) {
    j.mb = rbf(j.x0 + j.t0); j.t1 = rbf(j.t1) + 1e-5f;
    PIN2(j.mb, j.t1);
  } else if constexpr (I == 27) {
    j.den = rbf(__builtin_amdgcn_sqrtf(rbf(j.t1)));
    asm volatile("" : "+v"(j.den));
  } else if constexpr (I < LN_N0) {
    j.rden = rcp_nr(j.den); j.s[0] = 0.f; j.s[1] = 0.f; asm volatile("" : "+v"(j.rden));
  } else if constexpr (I < LN_XN) {
    constexpr int e = (I - LN_N0) >> 3, st = (I - LN_N0) & 7;
    if constexpr (st == 0)      { j.t0 = bflo(ln_a<e>(j)); j.t1 = bfhi(ln_a<e>(j)); }
    else if constexpr (st == 1) { j.t0 -= j.mb; j.t1 -= j.mb; }
    else if constexpr (st == 2) { const unsigned pk = cvtpk(j.t0, j.t1); j.t0 = bflo(pk); j.t1 = __uint_as_float(pk); }
    else if constexpr (st == 3) { j.t0 *= j.rden; j.t1 = bfhi(__float_as_uint(j.t1)); }
    else if constexpr (st == 4) { unsigned pk = cvtpk(j.t0, j.t1 * j.rden); asm volatile("" : "+v"(pk)); if constexpr (e < 8) j.a[e >> 2][e & 3] = pk; else j.a2 = pk; }
    else if constexpr (st == 5) { j.t0 = bflo(ln_a<e>(j)); j.t1 = bflo(ln_w<e>(j)); }
    else if constexpr (st == 6) { j.s[0] = __builtin_fmaf(j.t0, j.t1, j.s[0]); j.t0 = bfhi(ln_a<e>(j)); asm volatile("" : "+v"(j.s[0])); }
    else                        { j.s[1] = __builtin_fmaf(j.t0, bfhi(ln_w<e>(j)), j.s[1]); asm volatile("" : "+v"(j.s[1])); }
    PIN2(j.t0, j.t1);
  } else if constexpr (I == LN_XN) {
    j.t0 = j.s[0] + j.s[1]; asm volatile("" : "+v"(j.t0));
  } else if constexpr (I < LN_XN + 6) {
    j.t0 = red_step<I - LN_XN - 1>(j.t0); asm volatile("" : "+v"(j.t0));
  } else if constexpr (I == LN_XN + 6) {
    const unsigned rb = f2bf(j.t0);
    const unsigned mono = (rb & 0x8000u) ? (~rb & 0xffffu) : (rb | 0x8000u);
    j.key = (mono << 16) | (0xffffu - row);
    asm volatile("" : "+v"(j.key));
  }
}

struct RTS { unsigned k[8], bad, m, cnd, lq, va, cv, rank; u32x4 g[4]; float rs; unsigned long long pm[2]; };
DI void cnd_s(unsigned& d, const unsigned a, const unsigned long long m) { asm volatile("v_cndmask_b32_e64 %0, %0, %1, %2" : "+v"(d) : "v"(a), "s"(m)); }
DI void cnt_s(unsigned& d, const unsigned long long m) { unsigned long long co; asm volatile("v_addc_co_u32_e64 %0, %1, 0, %0, %2" : "+v"(d), "=s"(co) : "s"(m)); }
constexpr int RT_SORT = 3, RT_POP = RT_SORT + 19, RT_END = RT_POP + 64;
template <int CTRL> DI unsigned dpp_umax(unsigned v) { const unsigned t = (unsigned)__builtin_amdgcn_update_dpp(0, (int)v, CTRL, 0xf, 0xf, false); return t > v ? t : v; }
template <int I> DI void rt_step(RTS& r, const unsigned tag) {
  constexpr int net[19][2] = {{0, 1}, {2, 3}, {4, 5}, {6, 7}, {0, 2}, {1, 3}, {4, 6}, {5, 7}, {1, 2}, {5, 6}, {0, 4}, {3, 7}, {1, 5}, {2, 6}, {1, 4}, {3, 6}, {2, 4}, {3, 5}, {3, 4}};
  if constexpr (I == 0) { r.bad = (r.g[0][1] ^ tag) | (r.g[0][3] ^ tag) | (r.g[1][1] ^ tag); asm volatile("" : "+v"(r.bad)); }
  else if constexpr (I == 1) { r.bad |= (r.g[1][3] ^ tag) | (r.g[2][1] ^ tag) | (r.g[2][3] ^ tag); asm volatile("" : "+v"(r.bad)); }
  else if constexpr (I == 2) {
    r.bad |= (r.g[3][1] ^ tag) | (r.g[3][3] ^ tag); r.cnd = 0u; r.cv = r.lq & 15u;
#pragma unroll
    for (int j = 0; j < 8; ++j) r.k[j] = r.g[j >> 1][2 * (j & 1)];
    asm volatile("" : "+v"(r.bad));
  } else if constexpr (I < RT_POP) { ce(r.k[net[I - RT_SORT][0]], r.k[net[I - RT_SORT][1]]); PIN2(r.k[net[I - RT_SORT][0]], r.k[net[I - RT_SORT][1]]); }
  else if constexpr (I < RT_END) {
    constexpr int n = (I - RT_POP) >> 3, p = (I - RT_POP) & 7;
    if constexpr (p == 0) r.m = dpp_umax<0xB1>(r.k[0]);
    else if constexpr (p == 1) { r.m = dpp_umax<0x4E>(r.m); asm volatile("v_cmp_eq_u32_e64 %0, %1, %2" : "=s"(r.pm[1]) : "i"(n), "v"(r.cv)); }
    else if constexpr (p == 2) r.m = dpp_umax<0x141>(r.m);
    else if constexpr (p == 3) r.m = dpp_umax<0x140>(r.m);
    else if constexpr (p == 4) { asm volatile("v_cmp_eq_u32_e64 %0, %1, %2" : "=s"(r.pm[0]) : "v"(r.m), "v"(r.k[0])); cnd_s(r.cnd, r.m, r.pm[1]); }
    else {
#pragma unroll
      for (int i = p == 5 ? 0 : 2 * p - 9; i < 2 * p - 7 && i < 7 - n; ++i) cnd_s(r.k[i], r.k[i + 1], r.pm[0]);
    }
    asm volatile("" : "+v"(r.m));
  }
}

constexpr unsigned PF_DUM = 0x27C00u;
#define KRING 0x0000u
#define VRING 0x8000u
constexpr unsigned SD_BASE = 0x10000u, MO_XR = 0x22000u, MO_H = 0x25200u, MO_C = 0x26400u;
constexpr unsigned RTB = 254u;
constexpr unsigned RED0 = 208u, RED1 = 244u;
constexpr unsigned MO_HH = 0x26C00u;
constexpr unsigned WF_FL = 0x27000u;
constexpr unsigned MO_RS = 0x26800u, MO_KEY = 0x20000u;
constexpr unsigned BP_W = 23552u;
constexpr size_t SHM_ATTN = 0x28000;
constexpr unsigned KRS = 256u, KTILEB = 64u * KRS;
constexpr unsigned LAH = 3u;
constexpr unsigned WRS = HQ * DH * 2u;
constexpr unsigned XRS = DM * 2u;

#define W4_ACLOB_HI "a128","a129","a130","a131","a132","a133","a134","a135","a136","a137","a138","a139","a140","a141","a142","a143","a144","a145","a146","a147","a148","a149","a150","a151","a152","a153","a154","a155","a156","a157","a158","a159","a160","a161","a162","a163","a164","a165","a166","a167","a168","a169","a170","a171","a172","a173","a174","a175","a176","a177","a178","a179","a180","a181","a182","a183","a184","a185","a186","a187","a188","a189","a190","a191","a192","a193","a194","a195","a196","a197","a198","a199","a200","a201","a202","a203","a204","a205","a206","a207","a208","a209","a210","a211","a212","a213","a214","a215","a216","a217","a218","a219","a220","a221","a222","a223","a224","a225","a226","a227","a228","a229","a230","a231","a232","a233","a234","a235","a236","a237","a238","a239","a240","a241","a242","a243","a244","a245","a246","a247","a248","a249","a250","a251","a252","a253","a254","a255"
#define W4_ACLOB "a0","a1","a2","a3","a4","a5","a6","a7","a8","a9","a10","a11","a12","a13","a14","a15","a16","a17","a18","a19","a20","a21","a22","a23","a24","a25","a26","a27","a28","a29","a30","a31","a32","a33","a34","a35","a36","a37","a38","a39","a40","a41","a42","a43","a44","a45","a46","a47","a48","a49","a50","a51","a52","a53","a54","a55","a56","a57","a58","a59","a60","a61","a62","a63","a64","a65","a66","a67","a68","a69","a70","a71","a72","a73","a74","a75","a76","a77","a78","a79","a80","a81","a82","a83","a84","a85","a86","a87","a88","a89","a90","a91","a92","a93","a94","a95","a96","a97","a98","a99","a100","a101","a102","a103","a104","a105","a106","a107","a108","a109","a110","a111","a112","a113","a114","a115","a116","a117","a118","a119","a120","a121","a122","a123","a124","a125","a126","a127"

DI float max2f(float a, float b) { float r; asm("v_max_f32_e32 %0, %1, %2" : "=v"(r) : "v"(a), "v"(b)); return r; }
DI void lds_st32(unsigned addr, unsigned v) { asm volatile("ds_write_b32 %0, %1" :: "v"(addr), "v"(v) : "memory"); }

struct W4C {
  unsigned ksx[2];
  unsigned vkb[4], vvb[2], vksrc[2], vvsrc, vvsrc0, m0k, sofk;
  static constexpr unsigned ldsb = 0u;
  u32x4 srdS, srdL;
  __amdgpu_buffer_rsrc_t srdK, srdV, srdF; unsigned sfo;
  f32x4 S[2][4][4];
  f32x4 NEGM[4], L[4];
  u32x4 ONE; unsigned IDX;
  bf16x8 Vf[8][2];
  u32x4 Pk[4][2];
  bf16x4 vlo[8][2], vup[8][2];
  unsigned psf[2];
  u32x4 srdP; unsigned pfo, pfm0, pfv, pfst, pfend, pdum;
};

constexpr unsigned FCT = 0x27400u, SEL_C = 0x27800u;
constexpr int AO_(int db, int qb) { return 32 * qb + 4 * db; }
constexpr int AQ_(int qb, int j) { return 128 + 16 * qb + 4 * j; }
constexpr int AK_(int kb, int j) { return 192 + 16 * kb + 4 * j; }

template <int I> DI float acc_read() { float r; asm volatile("v_accvgpr_read_b32 %0, a[%c1]" : "=v"(r) : "i"(I)); return r; }
template <int I> DI void acc_write(const unsigned v) { asm volatile("v_accvgpr_write_b32 a[%c1], %0" :: "v"(v), "i"(I)); }

template <int I, int VSLOT> DI void tr_rd(const W4C& c, bf16x4 (&lo)[8][2], bf16x4 (&up)[8][2]) {
  constexpr int db = I >> 2, k2 = (I >> 1) & 1, off = VSLOT + 8192 * k2 + 4096 * (I & 1) + 512 * (db >> 1);
  if constexpr ((I & 1) == 0) asm volatile("ds_read_b64_tr_b16 %0, %1 offset:%2" : "=v"(lo[db][k2]) : "v"(c.vvb[db & 1]), "i"(off) : "memory");
  else                        asm volatile("ds_read_b64_tr_b16 %0, %1 offset:%2" : "=v"(up[db][k2]) : "v"(c.vvb[db & 1]), "i"(off) : "memory");
}
template <int I, int VSLOT> DI void tr_read_(W4C& c) { tr_rd<I, VSLOT>(c, c.vlo, c.vup); }
template <int I, int SLOT> DI void k_read(W4C& c) {
  asm volatile("ds_read_b128 a[%c1:%c2], %0 offset:%c3" :: "v"(c.vkb[I & 3]), "i"(192 + 4 * I), "i"(195 + 4 * I), "i"(SLOT + 4096 * (I >> 2)) : "memory");
}
template <int KB, int QB, int J, int CM> DI void qk16(f32x4& s, const f32x4& negm) {
  constexpr int ak = AK_(KB, J), aq = AQ_(QB, J);
  if constexpr (CM == 2)      asm volatile("mf16 %0, a[%c1:%c2], a[%c3:%c4], 0" : "=&v"(s) : "i"(ak), "i"(ak + 3), "i"(aq), "i"(aq + 3));
  else if constexpr (CM == 1) asm volatile("mf16 %0, a[%c2:%c3], a[%c4:%c5], %1" : "=&v"(s) : "v"(negm), "i"(ak), "i"(ak + 3), "i"(aq), "i"(aq + 3));
  else                        asm volatile("mf16 %0, a[%c1:%c2], a[%c3:%c4], %0" : "+v"(s) : "i"(ak), "i"(ak + 3), "i"(aq), "i"(aq + 3));
}
template <int X, int M> DI void qk_mfma(W4C& c) { constexpr int kb = M >> 4, qb = (M >> 2) & 3, j = (qb & 1) ? 3 - (M & 3) : (M & 3); qk16<kb, qb, j, ((M & 3) == 0)>(c.S[X][kb][qb], c.NEGM[qb]); }
template <int AO, bool ZERO> DI void pv16(const bf16x8& A, const u32x4& B) {
  if constexpr (ZERO) asm volatile("mf16 a[%c2:%c3], %0, %1, 0" :: "v"(A), "v"(B), "i"(AO), "i"(AO + 3));
  else                asm volatile("mf16 a[%c2:%c3], %0, %1, a[%c2:%c3]" :: "v"(A), "v"(B), "i"(AO), "i"(AO + 3));
}
template <bool ZERO> DI void sum16(f32x4& l, const u32x4& one, const u32x4& B) {
  if constexpr (ZERO) asm volatile("mf16 %0, %1, %2, 0" : "=&v"(l) : "v"(one), "v"(B));
  else                asm volatile("mf16 %0, %1, %2, %0" : "+v"(l) : "v"(one), "v"(B));
}
DI void sum64(W4C& c, const int qb, const u32x4 (&P)[2]) {
  const u32x8 B = __builtin_shufflevector(P[0], P[1], 0, 1, 2, 3, 4, 5, 6, 7);
  asm volatile("sm64 %0, %1, %2, %3" : "+v"(c.L[qb]) : "v"(c.ONE), "v"(B), "v"(c.IDX));
}
constexpr int NGB = 68;
template <int G, bool ZERO> DI void pv_mfma(W4C& c) {
  constexpr int qb = G / 17, r = G % 17, db = (r >> 1) & 7, k2 = (r ^ db) & 1;
  if constexpr (r < 16) pv16<AO_(db, qb), (ZERO && (r & 1) == 0)>(c.Vf[db][k2], c.Pk[qb][k2]);
  else sum64(c, qb, c.Pk[qb]);
}
DI void real_op() { GAP(); __builtin_amdgcn_s_setprio(0); GAP(); }
DI void add_v(float& acc, float x) { asm volatile("v_add_f32_e32 %0, %0, %1" : "+v"(acc) : "v"(x)); }
DI void mul_v(float& acc, float x) { asm volatile("v_mul_f32_e32 %0, %0, %1" : "+v"(acc) : "v"(x)); }
template <int X, int E> DI void exp_e(W4C& c) {
  GAP();
  const float e = __builtin_amdgcn_exp2f(c.S[X][E >> 4][(E >> 2) & 3][E & 3]);
  asm volatile("" :: "v"(e));
  c.S[X][E >> 4][(E >> 2) & 3][E & 3] = e;
  GAP();
}
template <int X, int U> DI void pack_to(W4C& c, u32x4 (&P)[4][2]) {
  constexpr int kb = U >> 3, qb = (U >> 1) & 3, h = U & 1;
  GAP();
  const unsigned w = cvtpk(c.S[X][kb][qb][2 * h], c.S[X][kb][qb][2 * h + 1]);
  asm volatile("" :: "v"(w));
  P[qb][kb >> 1][2 * (kb & 1) + h] = w;
  GAP();
}
template <int X, int U> DI void pack_e(W4C& c) { pack_to<X, U>(c, c.Pk); }

constexpr int NBE = 40;
constexpr char PA0[] = "EXETEXFTEXETEXFT" "EXETEXFTEXETEXFT" "EXXEXFEXXEXEXEXF" "ECCECFECCECECECF";
constexpr char PB2[] = "EDEKEEDEKEEDEKFES" "DEKEEWEKFEWEKEEWS" "EKEWEKEEFEEEEEEES" "EEEEEEEEEEEEEEEES";
constexpr char PB0[] = "EDEKEEDEKEEDEKFES" "DEKEEDEKFEDEKEEDS" "EKEDEKEEFDEEEFDES" "EEEEEFEEEEEFEEEES";
constexpr int pcnt(const char* p, char ch, int g) { int n = 0; for (int i = 0; i < g; ++i) n += p[i] == ch; return n; }
constexpr int ppos(const char* p, char ch, int n) { int i = 0; while (p[i] != ch || n-- > 0) ++i; return i; }
constexpr int FA(int n) { constexpr int g[12] = {6, 10, 14, 18, 22, 30, 37, 41, 47, 53, 57, 63}; return g[n]; }
constexpr int FB(int n) { constexpr int g[10] = {14, 25, 33, 42, 47, 50, 56, 59, 62, 67}; return g[n]; }
constexpr int fa_of(int m) { for (int n = 0; n < 12; ++n) if (FA(n) == m) return n; return -1; }
constexpr int fb_of(int m) { for (int n = 0; n < 10; ++n) if (FB(n) == m) return n; return -1; }
constexpr int DB(int n) { return ppos(PB0, 'D', n); }
constexpr int kyo(int g0, int g1) { const int n = 2 * (pcnt(PB0, 'K', g1) - pcnt(PB0, 'K', g0)); return n < 15 ? n : 15; }
static_assert(pcnt(PA0, 'E', 64) == 64 - NBE && pcnt(PB0, 'E', NGB) == NBE && pcnt(PB0, 'D', NGB) == 10 && pcnt(PB0, 'K', NGB) == 8 && FB(3) == DB(8) - 1 && FB(4) == DB(9) - 1 && PA0[FA(6)] == 'F' && PA0[FA(8)] == 'F', "");
template <int X, int M, typename H> DI void gapA(W4C& c, const int t, H& hook) {
  constexpr int Xp = X ^ 1;
  constexpr const char* pa = PA0;
  constexpr char ty = pa[M];
  constexpr int nt = 2 * pcnt(pa, 'T', M) + pcnt(pa, 'X', M), np = pcnt(pa, 'X', M) + 2 * pcnt(pa, 'C', M);
  if constexpr (M == 0) asm volatile("" ::: W4_ACLOB, W4_ACLOB_HI);
  qk_mfma<X, M>(c);
  GAP();
  hook(std::integral_constant<int, M>{});
  if constexpr (ty == 'T' || ty == 'X') tr_read_<nt, Xp * 0x4000>(c);
  if constexpr (ty == 'T') tr_read_<nt + 1, Xp * 0x4000>(c);
  if constexpr (ty == 'X' || ty == 'C') pack_e<Xp, np>(c);
  if constexpr (ty == 'C') pack_e<Xp, np + 1>(c);
  if constexpr (ty == 'E') exp_e<Xp, NBE + pcnt(pa, 'E', M)>(c);
  {
    if constexpr (M == FA(6) || M == FA(8)) asm volatile("s_add_u32 %0, %0, %1" : "+s"(c.psf[M == FA(8)]) : "i"(KTILEB) : "scc");
  }
  hook(std::integral_constant<int, 100 + M>{});
  GAP();
}
#define W4_V8(a, k) "+v"(a[0][k]), "+v"(a[1][k]), "+v"(a[2][k]), "+v"(a[3][k]), "+v"(a[4][k]), "+v"(a[5][k]), "+v"(a[6][k]), "+v"(a[7][k])
template <int PFW> DI void phaseA_end(W4C& c) {
  asm volatile("s_waitcnt vmcnt(%16) lgkmcnt(0)" : W4_V8(c.vlo, 0), W4_V8(c.vlo, 1) : "i"(PFW) : "memory");
  asm volatile("" : W4_V8(c.vup, 0), W4_V8(c.vup, 1) : "v"(c.NEGM[0]), "v"(c.NEGM[1]), "v"(c.NEGM[2]), "v"(c.NEGM[3]) : "memory");
  GAP();
  __builtin_amdgcn_s_barrier();
  asm volatile("" ::: "memory");
  GAP();
#pragma unroll
  for (int k2 = 0; k2 < 2; ++k2)
#pragma unroll
    for (int db = 0; db < 8; ++db) c.Vf[db][k2] = __builtin_shufflevector(c.vlo[db][k2], c.vup[db][k2], 0, 1, 2, 3, 4, 5, 6, 7);
}
template <int X, int PFW, typename H, int... Ms> DI void phaseA_(W4C& c, const int t, H& hook, iseq<Ms...>) {
  (gapA<X, Ms>(c, t, hook), ...);
  phaseA_end<PFW>(c);
}
struct NoHook { template <typename T> DI void operator()(T) const {} };
template <int X, int PFW = 0> DI void phaseA(W4C& c, const int t) { NoHook h; phaseA_<X, PFW>(c, t, h, make_iseq<64>{}); }
template <int X, int PFW, typename H> DI void phaseAh(W4C& c, const int t, H& hook) { phaseA_<X, PFW>(c, t, hook, make_iseq<64>{}); }

template <int X, bool ZERO1, bool DK, int PF, int M, typename H> DI void gapB(W4C& c, H& hook) {
  constexpr const char* pb = DK ? PB0 : PB2;
  constexpr char ty = pb[M];
  constexpr int n = pcnt(pb, ty, M) + (!DK && ty == 'D' ? 4 : 0), d8 = DB(8), d9 = DB(9);
  pv_mfma<M, ZERO1>(c);
  GAP();
  if constexpr (M == DB(DK ? 0 : 4) - 1) asm volatile("s_add_u32 m0, %0, %1" :: "s"(c.m0k), "i"(X * 0x4000) : "m0", "scc", "memory");
  if constexpr (M == DB(DK ? 4 : 0) - 1) asm volatile("s_add_u32 m0, %0, %1" :: "s"(c.m0k), "i"(VRING + (X ^ 1) * 0x4000) : "m0", "scc", "memory");
  if constexpr (M == 57) asm volatile("s_waitcnt lgkmcnt(0)" ::: "memory");
  if constexpr (PF && (M == d8 - 1 || M == d9 - 1))
    asm volatile("s_cmp_lt_u32 %0, %1\n\ts_cselect_b32 m0, %0, %2\n\ts_add_u32 %0, %0, 0x400" : "+s"(c.pfm0) : "s"(c.pfend), "s"(c.pdum) : "m0", "memory", "scc");
  if constexpr (PF && (M == d8 - 2 || M == d9 - 2)) asm volatile("s_add_u32 %0, %0, %1" : "+s"(c.pfo) : "s"(c.pfst) : "scc");
  constexpr int lg = PF ? 56 : 40;
  if constexpr (X == 1 && M == lg - 5) asm volatile("s_add_u32 %0, %0, %1" : "+s"(c.sfo) : "i"(2 * KTILEB) : "scc");
  if constexpr (X == 1 && M == lg - 2) asm volatile("s_mov_b32 m0, %0" :: "s"(c.pdum) : "m0", "memory");
  if constexpr (X == 1 && M == lg) asm volatile("buffer_load_dwordx4 %0, %1, %2 offen lds" :: "v"(c.vksrc[0]), "s"(c.srdF), "s"(c.sfo) : "memory");
  hook(std::integral_constant<int, M>{});
  if constexpr (ty == 'K') { k_read<2 * n, (X ^ 1) * 0x4000>(c); k_read<2 * n + 1, (X ^ 1) * 0x4000>(c); }
  if constexpr (ty == 'D') {
    if constexpr (n < 4) { asm volatile("buffer_load_dwordx4 %0, %1, %2 offen offset:%c3 lds" :: "v"((n & 2) ? c.ksx[n & 1] : c.vksrc[n & 1]), "s"(c.srdK),  "s"(c.psf[0]), "i"(1024 * (n & 3)) : "memory"); }
    else if constexpr (n < 8) asm volatile("buffer_load_dwordx4 %0, %1, %2 offen offset:%c3 lds" :: "v"((n & 1) ? c.vvsrc : c.vvsrc0), "s"(c.srdV),  "s"(c.psf[1]), "i"(1024 * (n & 3)) : "memory");
    else if constexpr (PF) asm volatile("buffer_load_dwordx4 %0, %1, %2 offen sc1 lds" :: "v"(c.pfv), "s"(c.srdP), "s"(c.pfo) : "memory");
  }
  if constexpr (ty == 'W') asm volatile("buffer_load_dwordx4 %0, %1, %2 offen offset:%c3 sc1 lds" :: "v"(c.pfv), "s"(c.srdP), "s"(c.pfo), "i"(1024 * n) : "memory");
  if constexpr (ty == 'E') exp_e<X, DK ? n : 16 * ((n >> 2) & 3) + 4 * (n >> 4) + (n & 3)>(c);
  hook(std::integral_constant<int, 100 + M>{});
  GAP();
}
template <int X, bool ZERO1, bool DK, int PF, typename H, int... Ms> DI void phaseB_(W4C& c, H& hook, iseq<Ms...>) {
  GAP();
  (gapB<X, ZERO1, DK, PF, Ms>(c, hook), ...);
  hook(std::integral_constant<int, 72>{});
  GAP();
}
template <int X, int PF> DI void phaseB(W4C& c) { NoHook h; phaseB_<X, false, true, PF>(c, h, make_iseq<NGB>{}); }
template <int X, int PF, typename H> DI void phaseBh(W4C& c, H& hook) { phaseB_<X, false, true, PF>(c, hook, make_iseq<NGB>{}); }

constexpr unsigned QP_STG = 0xC000u, QP_W8 = 0x24000u, QP_CS = 0x18000u;
constexpr int NJ = 722, PJ = 466, QB0 = 2, NV = 147, NQ0 = 20;
constexpr short Q0[] = {21000,21001,20000,20001,20002,20003,21002,21003,21004,21005,21006,21007,20004,20005,20006,20007,20008,20009,20010,20011,};
constexpr unsigned short JT[] = {30115,1000,36000,17070,20012,20013,20014,20015,30115,1001,32000,1003,34000,20016,20017,1002,34000,32000,1004,34000,20018,20019,30108,1005,34000,32000,1007,34000,20020,20021,1006,34000,
  32000,1008,34000,20022,20023,20024,20025,20026,20027,20028,20029,1009,36000,20030,20031,17001,16000,1011,32000,16001,16002,1010,16003,16004,1012,34000,32000,16005,16006,1013,16007,16008,1015,34000,32000,16009,16010,
  1014,16011,16012,1016,34000,32000,16013,16014,1017,36000,16015,16016,1019,32000,16017,16018,1018,16019,16020,16021,1020,34000,32000,16022,16023,1021,16024,1023,34000,32000,16025,16026,1022,16027,30100,1024,34000,32000,
  16028,1025,36000,1027,32000,1026,34000,1028,34000,32000,1029,34000,1031,34000,32000,1030,34000,1032,34000,32000,1033,34000,32000,31010,1035,34000,21008,20032,1034,34000,32000,1036,34000,20033,20034,1037,34000,20035,
  21009,1039,34000,32000,1038,34000,20036,20037,4000,34000,20038,1040,34000,32000,1041,34000,20039,21010,1043,34000,20040,20041,1042,34000,20042,20043,1044,36000,21011,20044,1045,32000,1047,34000,20045,20046,1046,34000,
  20047,21012,1048,34000,20048,20049,1049,34000,32000,4001,34000,20050,1051,34000,20051,21013,1050,34000,20052,20053,1052,34000,32000,1053,34000,20054,20055,1055,34000,21014,20056,1054,34000,20057,20058,1056,34000,32000,
  1057,34000,20059,21015,1059,34000,20060,20061,1058,36000,20062,20063,1060,32000,1061,34000,17100,1063,34000,17101,1062,34000,30100,1064,34000,32000,1065,34000,17102,1067,34000,17103,1066,34000,17104,1068,34000,32000,
  1069,34000,17105,1071,34000,17106,1070,34000,17107,1072,34000,32000,1073,34000,17108,1075,34000,17071,17109,1074,36000,31011,1076,32000,1077,34000,21016,20064,1079,34000,20065,20066,1078,34000,20067,21017,1080,34000,
  32000,1081,34000,20068,20069,1083,34000,20070,20071,1082,34000,21018,1084,34000,32000,1085,34000,20072,20073,1087,34000,17110,1086,34000,20074,20075,1088,34000,32000,1089,36000,21019,20076,1091,32000,1090,34000,20077,
  20078,1092,34000,17111,1093,34000,20079,21020,1095,34000,32000,1094,34000,20080,20081,1096,34000,17112,1097,34000,20082,20083,1099,34000,32000,1098,34000,21021,20084,1100,34000,17113,1101,34000,20085,20086,1103,34000,
  32000,1102,34000,20087,21022,4002,34000,1104,36000,20088,20089,1105,32000,1107,34000,20090,20091,1106,34000,17114,1108,34000,21023,20092,1109,34000,32000,1111,34000,20093,20094,1110,34000,17115,1112,34000,20095,17045,
  1113,34000,32000,4003,34000,1115,16029,1114,16030,1116,34000,32000,1117,16031,1119,16032,1118,36000,16033,1120,32000,1121,16034,1123,16035,1122,16036,1124,34000,32000,1125,16037,1127,16038,1126,34000,30100,1128,34000,
  32000,1129,16039,1131,16040,1130,16041,1132,34000,32000,1133,16042,1135,16043,1134,16044,1136,16045,1137,16046,1139,16047,1138,34000,31012,1140,34000,21024,20096,1141,36000,20097,20098,1143,32000,1142,34000,20099,
  21025,1144,34000,20100,20101,1145,34000,20102,1147,34000,32000,1146,34000,20103,21026,1148,16048,1149,34000,20104,20105,1151,34000,32000,1150,34000,20106,20107,1152,16049,1153,34000,21027,20108,1155,34000,32000,1154,
  34000,20109,20110,1156,16050,1157,36000,20111,21028,1159,32000,1158,34000,20112,20113,1160,16051,1161,34000,20114,20115,1163,34000,32000,1162,34000,21029,20116,1164,16052,1165,34000,20117,20118,1167,34000,32000,1166,
  34000,20119,21030,4004,34000,1168,34000,20120,20121,1169,34000,32000,1171,34000,20122,20123,1170,16053,1172,36000,21031,20124,1173,32000,1175,34000,20125,20126,1174,16054,1176,34000,20127,1177,34000,32000,4005,34000,
  1179,16055,1178,16056,1180,34000,32000,1181,16057,1183,16058,1182,16059,1184,34000,32000,1185,16060,1187,16061,1186,36000,16062,1188,32000,1189,16063,1191,16064,1190,34000,30100,1192,34000,32000,1193,16065,1195,16066,
  1194,16067,1196,34000,32000,1197,16068,1199,16069,1198,16070,1200,34000,32000,1201,16071,1203,16072,1202,34000,31013,1204,34000,21032,20128,1205,36000,20129,20130,1207,32000,1206,34000,20131,21033,1208,16073,1209,
  34000,20132,20133,1211,16074,1210,34000,32000,1212,34000,20134,20135,1213,16075,1215,34000,21034,20136,1214,16076,1216,34000,32000,1217,34000,20137,20138,1219,16077,1218,34000,20139,21035,1220,16078,1221,34000,32000,
  1223,34000,20140,20141,1222,16079,1224,34000,20142,20143,1225,36000,16080,1227,32000,1226,34000,21036,20144,1228,16081,1229,34000,20145,20146,1231,16082,1230,34000,32000,4006,34000,20147,1232,34000,21037,20148,1233,
  16083,1235,34000,20149,20150,1234,34000,32000,1236,34000,20151,21038,1237,16084,1239,34000,20152,20153,1238,16085,1240,34000,32000,1241,34000,20154,20155,4007,34000,1243,34000,21039,20156,1242,36000,16086,1244,32000,
  1245,34000,20157,20158,1247,16087,1246,34000,20159,1248,16088,1249,34000,32000,1251,16089,1250,16090,1252,16091,1253,16092,1255,34000,32000,1254,34000,30100,1256,16093,1257,16094,1259,16095,1258,34000,32000,1260,16096,
  1261,16097,1263,16098,1262,16099,1264,16100,1265,16101,1267,34000,1266,34000,31014,1268,34000,22008,22009,1269,36000,22010,22011,1271,32000,1270,34000,22012,22013,1272,34000,32000,1273,34000,22014,22015,1275,34000,
  21040,20160,1274,34000,32000,1276,34000,20161,20162,1277,34000,32000,1279,34000,20163,21041,1278,34000,20164,20165,1280,34000,32000,1281,34000,20166,20167,1283,34000,32000,1282,34000,21042,20168,1284,34000,20169,20170,
  1285,34000,32000,4008,34000,20171,1287,34000,32000,1286,34000,21043,20172,1288,36000,20173,20174,1289,32000,1291,34000,20175,21044,1290,34000,20176,20177,4009,34000,20178,1292,34000,20179,21045,1293,34000,32000,1295,
  34000,20180,20181,1294,34000,1296,34000,20182,20183,1297,34000,1299,34000,32000,1298,34000,32000,30102,4016,34000,21046,1300,34000,20184,20185,1301,16102,1303,34000,32000,1302,34000,32000,1304,34000,20186,20187,1305,
  16103,4017,36000,21047,1307,32000,1306,34000,32000,1308,34000,20188,20189,1309,16104,1311,34000,20190,20191,1310,34000,32000,1312,16105,1313,16106,1315,16107,1314,34000,1316,34000,32000,1317,34000,1319,34000,1318,
  34000,30100,1320,34000,17223,1321,34000,32000,4010,34000,1323,34000,1322,34000,1324,34000,1325,34000,4011,34000,1327,34000,1326,34000,1328,34000,1329,34000,31015,1331,34000,20192,20193,1330,36000,20194,20195,1332,
  32000,1333,34000,20196,20197,1335,34000,20198,20199,1334,34000,22000,22001,1336,34000,22002,22003,1337,34000,32000,1339,34000,22004,22005,1338,34000,22006,22007,1340,34000,21048,21049,1341,34000,21050,20200,30108,4012,
  34000,32000,1343,34000,20201,20202,1342,34000,20203,21051,1344,34000,20204,20205,4013,34000,20206,1345,34000,32000,1347,34000,20207,21052,1346,34000,20208,20209,4014,34000,20210,1348,36000,20211,21053,1349,32000,1351,
  34000,20212,20213,30107,4015,34000,20214,1350,34000,20215,21054,1352,34000,17220,1353,34000,32000,1355,34000,17221,17222,1354,34000,17225,1356,34000,20216,20217,1357,34000,31000,1359,34000,32000,1358,34000,25000,25001,
  1360,34000,25002,25003,1361,34000,25004,25005,1363,34000,25006,25007,1362,34000,32000,1364,34000,20218,20219,1365,34000,17190,1367,34000,21055,20220,1366,34000,17191,1368,34000,20221,20222,1369,34000,17192,1371,34000,
  20223,1370,34000,17193,1372,34000,17194,1373,34000,30307,9000,9001,1375,34000,9002,9003,1374,34000,9004,9005,1376,34000,9006,9007,1377,34000,9008,9009,1379,34000,9010,9011,1378,34000,9012,9013,1380,34000,9014,9015,
  1381,34000,9016,9017,1383,34000,9018,9019,1382,34000,9020,30100,1384,34000,9021,9022,1385,34000,9023,9024,1387,34000,9025,9026,1386,34000,9027,9028,1388,34000,9029,9030,1389,34000,9031,9032,1391,34000,9033,9034,1390,
  34000,9035,9036,1392,34000,9037,9038,1393,34000,9039,9040,1395,34000,9041,9042,1394,34000,31016,1396,34000,21056,20224,1397,36000,20225,20226,1399,32000,1398,34000,20227,21057,1400,34000,20228,9044,1401,34000,20229,
  20230,1403,34000,9045,9046,1402,34000,32000,1404,34000,20231,21058,1405,34000,9043,9047,1407,34000,20232,20233,1406,34000,9048,9049,1408,34000,32000,1409,34000,20234,20235,1411,34000,9050,9051,1410,34000,21059,20236,
  1412,34000,9056,9057,1413,34000,32000,1415,34000,20237,20238,1414,34000,9052,9053,1416,34000,20239,21060,1417,36000,9054,9055,1419,32000,1418,34000,20240,20241,1420,34000,9058,9059,1421,34000,20242,20243,1423,34000,
  9060,9061,1422,34000,32000,1424,34000,21061,20244,1425,34000,9062,9063,1427,34000,20245,20246,1426,34000,9064,9065,1428,34000,32000,1429,34000,20247,21062,1431,34000,9066,9067,1430,34000,20248,20249,1432,34000,9068,
  9069,1433,34000,32000,1435,34000,20250,20251,1434,34000,9070,9071,1436,34000,21063,20252,1437,36000,9072,9073,1439,32000,1438,34000,31003,1440,34000,20253,20254,1441,34000,20255,9074,1443,34000,20256,20257,1442,34000,
  32000,1444,34000,20258,20259,1445,34000,20260,20261,1447,34000,20262,20263,1446,34000,9075,9076,30106,1448,34000,32000,1449,34000,21064,21065,1456,34000,21066,21067,1457,34000,21068,21069,1451,34000,21070,21071,1450,
  34000,32000,1459,34000,9077,9078,1458,34000,9079,9080,1452,34000,9081,9082,1453,34000,9083,9084,1460,34000,9085,9086,1461,34000,9087,9088,1455,34000,20264,20265,1454,34000,20266,20267,1463,34000,20268,20269,1462,34000,
  20270,20271,30108,1512,34000,9089,9090,9091,1513,34000,9092,9093,9094,1520,34000,9095,9096,9097,1521,34000,10000,10001,10002,1514,34000,10003,9098,9099,1515,34000,10004,10005,10006,1522,34000,10007,10008,10009,1523,
  34000,10010,10011,9100,1516,34000,10012,9101,1517,34000,10013,10014,1524,34000,10015,10016,10017,1525,34000,10018,10019,10020,1518,34000,10021,10022,10023,1519,34000,10024,10025,10026,1526,34000,10027,10028,10029,1527,
  34000,10030,9102,1464,34000,10031,10032,1465,34000,10033,10034,10035,30100,1529,34000,10036,10037,10038,1528,34000,10039,10040,10041,1472,34000,10042,10043,10044,1473,34000,10045,10046,10047,1537,34000,10048,9103,1536,
  34000,10049,10050,1466,34000,10051,10052,10053,1467,34000,10054,10055,10056,1531,34000,10057,10058,10059,1530,34000,10060,10061,10062,1474,34000,20272,20273,20274,1475,34000,20275,20276,20277,1539,34000,20278,20279,
  10063,1538,34000,10064,10065,9104,1468,34000,10066,9105,1469,34000,10067,10068,1533,34000,10069,10070,10071,1532,34000,10072,10073,10074,1476,34000,10075,10076,10077,1477,34000,10078,10079,10080,1541,34000,10081,10082,
  10083,1540,34000,10084,9106,1470,34000,10085,10086,1471,34000,10087,10088,10089,1535,34000,10090,10091,10092,1534,34000,10093,10094,10095,1478,34000,10096,10097,10098,1479,34000,10099,10100,10101,1543,34000,10102,9107,
  1542,34000,10103,10104,1480,34000,10105,10106,10107,1481,34000,10108,10109,10110,30100,1545,34000,10111,10112,10113,1544,34000,10114,10115,10116,1488,34000,10117,10118,10119,1489,34000,10120,9108,1553,34000,10121,
  10122,1552,34000,10123,10124,10125,1482,34000,10126,10127,10128,1483,34000,10129,10130,10131,1547,34000,10132,10133,10134,1546,34000,10135,10136,10137,1490,34000,20280,20281,20282,1491,34000,20283,20284,20285,1555,
  34000,20286,20287,9109,1554,34000,10138,9110,1484,34000,10139,10140,1485,34000,10141,10142,10143,1549,34000,10144,10145,10146,1548,34000,31001,1492,34000,23000,23001,23002,1493,34000,32000,1557,34000,23003,23004,23005,
  1556,34000,23006,23007,10147,1486,34000,23008,10148,10149,1487,36000,23009,10150,10151,1551,32000,10152,1550,34000,10153,10154,10155,1494,34000,23010,10156,1495,34000,23011,10157,1559,34000,10158,10159,10160,1558,
  34000,10161,10162,10163,1496,34000,32000,10164,1497,34000,10165,10166,10167,30101,1561,34000,10168,10169,10170,1560,34000,10171,10172,10173,1504,34000,10174,9111,1505,34000,10175,10176,1569,34000,32000,10177,1568,
  34000,10178,10179,10180,1498,34000,10181,10182,10183,1499,34000,10184,10185,10186,1563,34000,10187,10188,10189,1562,34000,10190,10191,9112,1506,34000,32000,9113,1507,34000,10192,9114,1571,34000,10193,10194,1570,34000,
  10195,10196,10197,1500,34000,10198,10199,10200,1501,34000,10201,10202,10203,1565,34000,10204,10205,10206,1564,34000,10207,10208,10209,1508,34000,10210,9120,1509,34000,10211,10212,1573,34000,10213,10214,10215,1572,
  34000,10216,10217,10218,1502,34000,23012,10219,10220,1503,34000,23013,10221,10222,1567,34000,10223,10224,10225,1566,34000,10226,10227,9115,1510,34000,23014,10228,1511,34000,23015,10229,1575,34000,10230,10231,10232,
  1574,34000,10233,10234,10235,2128,34000,10236,10237,2132,34000,10238,10239,2136,34000,10240,10241,2140,34000,23100,10242,2001,34000,10243,10244,2005,34000,31002,2009,36000,10245,9116,2013,32000,2002,34000,23101,9117,
  2003,35000,2006,34000,10246,2010,35001,2014,34000,11000,11001,11002,23102,10247,11003,11004,11005,10248,10249,11006,11007,11008,11009,11010,11011,11012,2080,34000,32000,2017,34000,11013,11014,2018,34000,10250,10251,
  2019,34000,10252,10253,2007,34000,10254,10255,2096,34000,10256,10257,2033,34000,32000,2034,34000,11016,11017,30100,2035,34000,11018,9118,2112,34000,11019,2049,34000,11020,11021,2050,34000,11022,2051,34000,32000,11023,
  11024,11025,11026,11027,11028,2087,34000,11029,11030,2022,34000,10258,9119,2021,34000,10259,10260,2020,34000,10261,10262,2103,34000,10263,9121,2038,34000,32000,2037,34000,10264,2036,34000,10265,2119,34000,10266,10267,
  2054,34000,10268,10269,2053,34000,10270,10271,2052,34000,32000,2011,34000,9122,10272,9123,10273,9124,10274,9125,10275,11032,11033,11034,9126,10276,11035,11036,11037,9127,10277,11038,11039,11040,11041,11042,11043,11044,
  2088,34000,11045,11046,2025,34000,10278,10279,2026,34000,10280,10281,2027,34000,10282,2104,34000,10283,2041,34000,10284,10285,2042,34000,10286,10287,2043,13000,2015,34000,23103,2120,13001,2057,34000,11048,11049,2058,
  11050,15000,2059,34000,11051,11052,11053,13002,13003,11054,11055,11056,11057,11058,11059,11060,2095,34000,11061,11062,2030,13016,2029,13017,2028,23104,15001,2111,23105,15008,2046,34000,23106,23107,2045,13018,2044,
  34000,23108,23109,2127,34000,23110,23111,2062,13019,2061,13032,2060,23112,15009,3064,34000,23113,23114,3001,13033,3002,23115,15016,30104,3003,13034,3071,13035,3006,13048,3005,13049,3004,13050,3072,13051,3009,24000,
  15017,3010,13004,3011,13005,3079,24001,15024,3014,13006,3013,13007,3012,24002,15025,3080,13020,3017,13021,3018,24003,15002,3019,13022,3087,13023,3022,24004,15003,3021,13036,3020,13037,3088,24005,15010,3025,13038,3026,
  13039,3027,24006,15011,3095,13052,3030,24007,15018,3029,24008,15019,3028,34000,24009,24010,3096,13053,3033,24011,15026,3034,34000,24012,24013,3035,13054,3103,34000,24014,24015,3038,13055,3037,24016,15027,3036,34000,
  24017,24018,3104,13008,3041,34000,24019,24020,3042,34000,24021,24022,3043,13009,3111,13010,3046,24023,15004,3045,34000,24024,24025,3044,13011,30103,3112,24026,15005,3049,34000,24027,24028,3050,13024,3051,34000,24029,
  24030,3119,13025,3054,24031,15012,3053,13026,3052,13027,3120,13040,3057,13041,3058,13042,3059,13043,3127,15013,15020,3062,13056,3061,13057,3060,15021,13058,13059,13012,15028,13013,13014,15029,13015,13028,15006,13029,
  13030,15007,13031,13044,15014,13045,13046,15015,13047,13060,15022,13061,13062,15023,13063,15030,15031,};
constexpr short JS[] = {0,8,11,15,18,22,26,30,33,43,49,53,56,61,64,69,72,77,81,85,89,94,96,101,103,108,110,112,114,117,119,122,124,127,130,135,138,142,146,149,153,156,159,163,167,171,175,177,181,185,189,192,195,199,203,
  206,210,214,218,221,225,229,233,235,238,241,243,247,250,253,256,259,262,265,268,271,274,278,280,283,287,291,295,298,302,306,309,312,316,319,323,326,330,332,336,339,343,346,350,353,357,360,364,367,371,374,378,380,384,
  386,390,393,397,400,404,407,411,414,416,418,420,423,425,427,430,432,434,436,438,441,443,445,447,451,453,455,457,460,462,464,466,468,470,472,474,479,483,485,489,493,496,499,503,505,509,512,516,518,522,525,529,531,535,
  537,541,543,547,550,554,556,560,563,567,569,573,576,580,582,586,588,592,594,597,600,602,604,606,609,611,613,615,618,620,622,625,627,629,631,633,637,639,641,643,646,648,650,652,655,657,659,661,666,670,672,676,678,682,
  684,687,691,693,697,699,702,706,708,712,714,717,721,723,727,730,732,736,738,742,744,747,750,754,756,760,763,767,769,773,775,778,782,784,788,791,793,797,799,802,804,807,809,811,813,815,818,820,823,825,827,830,832,834,
  836,838,840,842,844,846,851,855,857,861,864,868,872,875,879,882,886,890,893,897,900,904,908,911,914,917,921,925,927,931,935,938,942,945,949,951,955,957,960,963,967,971,973,976,979,983,985,988,990,993,997,999,1003,1006,
  1008,1010,1012,1014,1017,1019,1021,1023,1027,1030,1032,1034,1036,1038,1040,1042,1044,1046,1048,1050,1055,1059,1061,1065,1069,1073,1077,1080,1084,1088,1092,1096,1100,1104,1108,1112,1115,1118,1122,1126,1129,1133,1135,
  1139,1143,1147,1150,1153,1157,1160,1164,1166,1170,1174,1178,1182,1186,1189,1193,1196,1200,1203,1207,1210,1213,1216,1219,1224,1228,1232,1236,1240,1244,1248,1252,1256,1260,1263,1268,1272,1276,1280,1284,1288,1292,1296,
  1300,1304,1308,1310,1315,1319,1321,1325,1329,1333,1337,1340,1344,1348,1352,1356,1359,1363,1367,1371,1375,1378,1382,1386,1390,1394,1396,1400,1404,1408,1412,1415,1419,1423,1427,1431,1434,1438,1442,1446,1450,1453,1457,
  1461,1465,1469,1471,1473,1478,1482,1486,1489,1493,1497,1501,1505,1509,1513,1517,1521,1525,1528,1532,1536,1540,1544,1548,1552,1556,1560,1564,1568,1574,1579,1584,1589,1594,1599,1604,1609,1613,1617,1622,1627,1632,1637,
  1642,1646,1650,1655,1661,1666,1671,1676,1680,1684,1689,1694,1699,1704,1709,1714,1719,1724,1728,1732,1737,1742,1747,1752,1757,1761,1765,1770,1775,1780,1785,1790,1794,1798,1803,1808,1814,1819,1824,1828,1832,1837,1842,
  1847,1852,1857,1862,1867,1872,1876,1880,1885,1890,1892,1898,1901,1906,1911,1916,1921,1924,1929,1933,1937,1942,1947,1951,1956,1962,1967,1971,1975,1979,1984,1989,1994,1999,2004,2008,2012,2016,2021,2026,2031,2036,2041,
  2045,2049,2054,2059,2064,2069,2074,2079,2083,2087,2092,2097,2101,2105,2109,2113,2117,2119,2124,2126,2130,2132,2135,2137,2156,2159,2163,2167,2171,2175,2179,2182,2186,2191,2194,2198,2201,2210,2214,2218,2222,2226,2230,
  2233,2236,2239,2243,2247,2251,2254,2281,2285,2289,2293,2296,2299,2303,2307,2309,2312,2314,2318,2321,2335,2339,2341,2343,2346,2349,2353,2355,2359,2363,2365,2367,2370,2374,2376,2379,2382,2384,2386,2388,2390,2392,2395,
  2397,2399,2402,2404,2406,2409,2411,2413,2416,2418,2420,2423,2425,2427,2430,2432,2434,2437,2439,2442,2445,2449,2451,2454,2458,2460,2464,2466,2469,2473,2475,2479,2483,2485,2487,2490,2494,2496,2500,2504,2506,2510,2512,
  2515,2517,2519,2521,2523,2525,2527,2530,2532,2534,2564,};
constexpr short VL[] = {2,0,0,-3,2,0,1,-3,2,0,2,-3,2,0,3,-3,2,0,4,-3,2,0,5,-3,2,0,6,-3,2,0,7,-3,2,0,8,-3,2,0,9,-3,2,0,10,-3,2,0,11,-3,4,0,0,-2,4,0,1,-2,4,0,2,-2,1,0,0,-2,0,0,0,-2,2,1,0,-1,2,1,1,-1,2,1,2,-1,2,1,3,-1,2,1,
  4,-1,2,1,5,-1,2,1,6,-1,2,1,7,-1,2,1,8,1,2,1,9,3,2,1,10,5,2,1,11,7,2,2,0,10,2,2,1,12,2,2,2,14,2,2,3,16,2,2,4,18,2,2,5,20,2,2,6,22,2,2,7,24,2,2,8,26,2,2,9,28,2,2,10,30,2,2,11,32,6,0,0,33,5,0,0,35,5,0,1,38,5,0,2,41,2,3,0,
  46,2,3,1,50,2,3,2,54,2,3,3,58,2,3,4,62,2,3,5,66,2,3,6,70,2,3,7,74,2,3,8,78,2,3,9,82,2,3,10,86,2,3,11,90,2,4,0,92,2,4,1,96,2,4,2,100,2,4,3,104,2,4,4,108,2,4,5,112,2,4,6,116,2,4,7,120,2,4,8,124,2,4,9,128,2,4,10,132,2,4,
  11,136,2,5,0,146,2,5,1,150,2,5,2,154,2,5,3,158,2,5,4,162,2,5,5,166,2,5,6,170,2,5,7,174,2,5,8,178,2,5,9,182,2,5,10,186,2,5,11,190,3,8,0,194,3,8,1,198,3,8,2,202,3,8,3,206,2,6,0,212,2,6,1,217,2,6,2,222,2,6,3,227,2,6,4,
  232,2,6,5,237,2,6,6,242,2,6,7,247,2,6,8,252,2,6,9,257,2,6,10,262,2,6,11,267,2,7,0,278,7,0,0,280,2,7,1,283,7,0,1,285,2,7,2,288,7,0,2,290,2,7,3,293,7,0,3,295,2,7,4,298,2,7,5,303,2,7,6,308,8,0,0,309,2,7,7,313,8,0,1,314,2,
  7,8,318,8,0,2,319,2,7,9,323,2,7,10,328,2,7,11,333,2,8,4,346,2,8,5,351,2,8,6,356,2,8,7,361,2,8,8,366,2,8,9,371,2,8,10,376,2,8,11,381,9,0,0,416,9,0,1,421,9,0,2,426,9,0,3,431,9,0,4,436,9,0,5,441,9,0,6,446,9,0,7,451,9,0,8,
  456,9,0,9,461,9,0,10,466,9,0,11,471,10,0,0,551,11,0,0,556,11,0,1,562,11,0,2,568,11,0,3,574,12,0,0,601,12,0,1,607,12,0,2,613,12,0,3,619,13,0,0,625,13,0,1,631,};
enum : int { VK_CW, VK_GEN, VK_S, VK_W8, VK_A, VK_WR, VK_STS, VK_GAT, VK_XNS, VK_KV, VK_KEY, VK_V1, VK_K2, VK_PF };
struct VOp { int kind, st, q, pos; };
constexpr VOp vop(int i) { return VOp{VL[4 * i], VL[4 * i + 1], VL[4 * i + 2], VL[4 * i + 3]}; }
constexpr int vq_first(int pos) { int i = 0; while (i < NV && vop(i).pos < pos) ++i; return i; }
constexpr int vq_wait(int kind, int st, int pos, int qmax = 99) {
  int last = -1;
  for (int i = 0; i < NV; ++i) if (vop(i).kind == kind && vop(i).st == st && vop(i).q <= qmax) last = i;
  return vq_first(pos) - 1 - last;
}
constexpr int imin(int a, int b) { return a < b ? a : b; }
constexpr int vq_grp(int i) {
  const VOp o = vop(i);
  return o.kind == VK_S ? 3 * o.st + (o.q >> 2) : o.kind == VK_W8 ? 40 : o.kind == VK_KV ? 41 + (o.q >> 2) : o.kind == VK_V1 ? 44 : o.kind == VK_K2 ? 45 : o.kind == VK_PF ? 50 + o.q : -1;
}
constexpr bool vq_newm0(int i) { int j = i - 1; while (j >= 0 && vq_grp(j) < 0) --j; return j < 0 || vq_grp(j) != vq_grp(i); }
template <int I0, typename F, int... Is> DI void vq_run(F& vm, iseq<Is...>) { (vm(std::integral_constant<int, I0 + Is>{}), ...); }
template <int POS, typename F> DI void vq_issue(F& vm) { vq_run<vq_first(POS)>(vm, make_iseq<vq_first(POS + 1) - vq_first(POS)>{}); }

constexpr int AW_(int T, int blk, int ks) { return ((T & 1) ? 192 : 0) + 8 * blk + 4 * ks; }
constexpr int AX_(int T, int qb, int ks) { return ((T & 1) ? 96 : 64) + 8 * qb + 4 * ks; }
constexpr int qp_set(int T) { return T % 3 == 2; }
constexpr int qp_off(int T, bool w) { return T % 3 == 1 || (w && T == 8) ? (int)QP_STG : 0; }
struct QP {
  f32x4 A[8][4];
  f32x4 cs[2];
  unsigned aw[2][8], ax[2][2], vw[4], vx[4], m0x, so, sw[3], sx[3], ca[4];
  __amdgpu_buffer_rsrc_t srdW, srdX;
  f32x4 cv[8]; u32x4 g[4]; float m[4], m2[4]; unsigned w[4];
};
template <int N> DI void q_fma(QP& p) {
  constexpr int blk = 2 * (N >> 5) + ((N >> 2) & 1), qb = (N >> 3) & 3;
  float v = p.A[blk][qb][N & 3]; asm volatile("v_fmac_f32_e32 %0, %1, %2" : "+v"(v) : "v"(p.cv[blk][N & 3]), "v"(p.g[qb][2])); p.A[blk][qb][N & 3] = v;
}
template <int N> DI void q_fin(QP& p) {
  constexpr int j = N / 72, qb = (N / 18) & 3, n = N % 18;
  if constexpr (n < 8) { float v = p.A[2 * j + (n >> 2)][qb][n & 3]; asm volatile("v_mul_f32_e32 %0, %0, %1" : "+v"(v) : "v"(p.g[qb][0])); p.A[2 * j + (n >> 2)][qb][n & 3] = v; }
  else if constexpr (n < 12) { const f32x4& v = p.A[2 * j + ((n - 8) >> 1)][qb]; GAP(); p.w[n - 8] = cvtpk(v[2 * (n & 1)], v[2 * (n & 1) + 1]); asm volatile("" :: "v"(p.w[n - 8])); GAP(); }
  else if constexpr (n < 14) { GAP(); auto pr = __builtin_amdgcn_permlane16_swap(p.w[n - 12], p.w[n - 10], false, false); p.w[n - 12] = pr[0]; p.w[n - 10] = pr[1]; asm volatile("" :: "v"(p.w[n - 12]), "v"(p.w[n - 10])); GAP(); }
  else acc_write<AQ_(qb, j) + n - 14>(p.w[n - 14]);
}
template <int Q, int I> DI void ref_step(W4C& c, QP& t) {
  f32x4& s = c.S[0][0][Q];
  if constexpr (I == 0) asm volatile("v_max3_f32 %0, %1, %2, %3" : "=v"(t.m[Q]) : "v"(s[0]), "v"(s[1]), "v"(s[2]));
  else if constexpr (I == 1) asm volatile("v_max_f32_e32 %0, %0, %1" : "+v"(t.m[Q]) : "v"(s[3]));
  else if constexpr (I == 2 || I == 5) asm volatile("v_mov_b32_e32 %0, %1" : "=v"(t.m2[Q]) : "v"(t.m[Q]));
  else if constexpr (I == 3) asm volatile("v_permlane16_swap_b32_e32 %0, %1" : "+v"(t.m[Q]), "+v"(t.m2[Q]));
  else if constexpr (I == 4) asm volatile("v_max_f32_e32 %0, %0, %1" : "+v"(t.m[Q]) : "v"(t.m2[Q]));
  else if constexpr (I == 6) asm volatile("v_permlane32_swap_b32_e32 %0, %1" : "+v"(t.m[Q]), "+v"(t.m2[Q]));
  else if constexpr (I < 11) { float n; asm volatile("v_min_f32_e64 %0, -%1, -%2" : "=v"(n) : "v"(t.m[Q]), "v"(t.m2[Q])); c.NEGM[Q][I - 7] = n; }
  else { float v = s[I - 11]; add_v(v, c.NEGM[Q][0]); s[I - 11] = v; }
}
template <int J, int CODE, typename FV, typename FH> DI void q_op(QP& p, W4C& c, FV& vm, FH& hook, LNJ& lj, const unsigned lrow) {
  constexpr int kind = CODE / 1000, a = CODE % 1000;
  if constexpr (kind == 1) {
    constexpr int T = a >> 6, blk = (a >> 3) & 7, qb = (a >> 1) & 3, ks = a & 1, aw = AW_(T, blk, ks), ax = AX_(T, qb, ks);
    if constexpr (T == 0 && ks == (qb & 1)) asm volatile("mf16 %0, a[%c1:%c2], a[%c3:%c4], 0" : "=&v"(p.A[blk][qb]) : "i"(aw), "i"(aw + 3), "i"(ax), "i"(ax + 3));
    else                                    asm volatile("mf16 %0, a[%c1:%c2], a[%c3:%c4], %0" : "+v"(p.A[blk][qb]) : "i"(aw), "i"(aw + 3), "i"(ax), "i"(ax + 3));
  } else if constexpr (kind == 2 || kind == 3) { constexpr int kb = (a >> 4) & 3, qb = (a >> 2) & 3; qk16<kb, qb, (a & 3), (a >> 6)>(c.S[kind - 2][kb][qb], c.NEGM[qb]); }
  else if constexpr (kind == 4) {
    constexpr int r = a < 14 ? AW_(a >> 1, a & 1, 0) : 128 + 8 * (a - 14);
    asm volatile("sm64 %0, %1, a[%c3:%c4], %2" : "+v"(p.cs[a & 1]) : "v"(c.ONE), "v"(c.IDX), "i"(r), "i"(r + 7));
  } else if constexpr (kind == 9) q_fma<a>(p);
  else if constexpr (kind == 10) q_fin<a>(p);
  else if constexpr (kind == 11) ref_step<(a >> 4), (a & 15)>(c, p);
  else if constexpr (kind == 13) exp_e<0, a>(c);
  else if constexpr (kind == 15) pack_e<0, a>(c);
  else if constexpr (kind == 16) { GAP(); ln_step<a>(lj, lrow); GAP(); }
  else if constexpr (kind == 17) { GAP(); hook(std::integral_constant<int, a>{}, std::integral_constant<int, J>{}); GAP(); }
  else if constexpr (kind == 20 || kind == 22) {
    constexpr int T = kind == 20 ? a >> 5 : 7 + (a >> 3), blk = kind == 20 ? (a >> 2) & 7 : (a >> 2) & 1, ks = (a >> 1) & 1, r = kind == 20 ? AW_(T, blk, ks) + 2 * (a & 1) : 128 + 2 * a;
    asm volatile("ds_read_b64_tr_b16 a[%c1:%c2], %0 offset:%c3" :: "v"(p.aw[qp_set(T)][blk]), "i"(r), "i"(r + 1), "i"(8192 * ks + 2048 * (a & 1) + qp_off(T, true)) : "memory");
  } else if constexpr (kind == 21) {
    constexpr int T = a >> 3, qb = (a >> 1) & 3, r = AX_(T, qb, a & 1);
    asm volatile("ds_read_b128 a[%c1:%c2], %0 offset:%c3" :: "v"(p.ax[qp_set(T)][a & 1]), "i"(r), "i"(r + 3), "i"(2048 * qb + qp_off(T, false)) : "memory");
  } else if constexpr (kind == 23) k_read<a % 100, (a >= 100 ? 0x4000 : 0)>(c);
  else if constexpr (kind == 24) tr_read_<a, 0>(c);
  else if constexpr (kind == 25) asm volatile("ds_read_b128 %0, %1 offset:%2" : "=v"(p.cv[a]) : "v"(p.ca[a >> 1]), "i"(64 * (a & 1)) : "memory");
  else if constexpr (kind == 30) {
    if constexpr (a == 200) asm volatile("s_nop 0");
    else if constexpr (a >= 300) asm volatile("s_waitcnt lgkmcnt(%8)" : "+v"(p.cv[0]), "+v"(p.cv[1]), "+v"(p.cv[2]), "+v"(p.cv[3]), "+v"(p.cv[4]), "+v"(p.cv[5]), "+v"(p.cv[6]), "+v"(p.cv[7]) : "i"(a - 300) : "memory");
    else asm volatile("s_waitcnt lgkmcnt(%0)" :: "i"(a - 100) : "memory");
  } else if constexpr (kind == 31) {
    if constexpr (a >= 10) {
      constexpr int T = a - 10, w0 = vq_wait(VK_S, T + 1, J);
      constexpr int w = T == 1 ? imin(w0, vq_wait(VK_WR, 0, J)) : T == 4 ? imin(w0, vq_wait(VK_W8, 8, J)) : T == 5 ? imin(w0, vq_wait(VK_S, 7, J, 3)) : w0;
      if constexpr (T == 0) asm volatile("s_waitcnt vmcnt(%0) lgkmcnt(0)\n\ts_barrier" :: "i"(w) : "memory");
      else                  asm volatile("s_waitcnt vmcnt(%0)\n\ts_barrier" :: "i"(w) : "memory");
    } else if constexpr (a == 0) asm volatile("s_waitcnt lgkmcnt(%0)\n\ts_barrier" :: "i"(QB0) : "memory");
    else if constexpr (a == 3) asm volatile("s_waitcnt vmcnt(%0)" :: "i"(vq_wait(VK_S, 8, J)) : "memory");
    else if constexpr (a == 1) asm volatile("s_waitcnt vmcnt(%0)\n\ts_barrier" :: "i"(vq_wait(VK_KV, 0, J)) : "memory");
    else asm volatile("s_waitcnt lgkmcnt(0)\n\ts_barrier" ::: "memory");
  } else if constexpr (kind == 32) { vq_issue<J>(vm); GAP(); }
  else if constexpr (kind == 36) vm(IC(1000 + vq_first(J + 1)));
  else if constexpr (kind == 34) real_op();
  else if constexpr (kind == 35) {
    c.psf[a] = KTILEB * (3u - a) + c.sofk; asm volatile("" : "+s"(c.psf[a]));
  }
}
template <int J, typename FV, typename FH, int... Is> DI void q_gap(QP& p, W4C& c, FV& vm, FH& hook, LNJ& lj, const unsigned lrow, iseq<Is...>) {
  (q_op<J, JT[JS[J] + Is]>(p, c, vm, hook, lj, lrow), ...); GAP();
}
template <int J0, typename FV, typename FH, int... Js> DI void q_gaps(QP& p, W4C& c, FV& vm, FH& hook, LNJ& lj, const unsigned lrow, iseq<Js...>) {
  (q_gap<J0 + Js>(p, c, vm, hook, lj, lrow, make_iseq<JS[J0 + Js + 1] - JS[J0 + Js]>{}), ...);
}
template <typename FV, typename FH, int... Is> DI void q_first(QP& p, W4C& c, FV& vm, FH& hook, LNJ& lj, iseq<Is...>) { (q_op<0, Q0[Is]>(p, c, vm, hook, lj, 0u), ...); }

constexpr unsigned XRS_ = DM * 2u;
constexpr int op_vr(int f) { return f < 4 ? f : f < 9 ? f : f < 13 ? f - 9 : f - 4; }
constexpr int op_ar(int f) {
  const int n = f - 35;
  return n < 0 ? 84 + 4 * (f - 26) : n < 4 ? 128 + 4 * n : n < 12 ? 4 * (n - 4) : n < 16 ? 144 + 4 * (n - 12) : n < 24 ? 32 + 4 * (n - 16) : n < 32 ? 160 + 4 * (n - 24) : 64 + 4 * (n - 32);
}
constexpr int OP_LA = 4, OP_SF = 23, OP_SP = 25, OP_SD = 29, OP_FEW = 8, OP_NR = 72, OP_RING = 5, OP_GS = 1, OP_P4W = 12, OP_NI = 12, OP_DS = 5;
constexpr short OPU[] = {1,2,3,0,9,10,11,8,5,6,7,4,13,14,15,12,17,18,19,16,21,22,23,20,25,29,26,30,27,31,24,28,5,6,7,11,9,10,13,14,15,3,1,2,17,18,19,23,21,22,25,29,26,30,27,31,0,8,12,16,20,24,28,4,};
constexpr short OPW[] = {8,8,8,9,9,8,9,9,8,8,9,9,10,10,11,11,12,13,14,15,15,14,13,12,13,13,13,13,13,9,8,4,};
constexpr short TT[] = {0,0,0,0,21012,8012,0,0,0,0,21013,8013,0,0,0,0,0,21014,8014,0,0,0,0,21015,8015,0,27001,0,0,11000,0,11001,0,21016,8016,0,11002,0,11003,0,11004,0,21017,8017,0,11005,0,0,11006,0,11007,0,21018,8018,0,
  11008,0,11009,0,11010,0,21019,8019,0,27002,11011,0,0,22000,0,13004,13005,0,21020,8020,0,11012,0,13006,13007,0,11013,0,21021,8021,0,11014,0,0,11015,0,11016,0,21022,8022,0,11017,0,11018,0,11019,0,21023,8023,0,27003,
  11020,0,0,11021,0,11022,0,21024,8024,0,11023,0,11024,0,11025,0,21025,8025,0,11026,0,0,11027,0,11028,0,21026,8026,0,11029,0,11030,0,11031,0,21027,8027,0,27004,11032,15000,0,0,11033,0,11034,0,21028,8028,0,11035,0,11036,
  0,11037,0,21029,8029,0,11038,0,0,11039,0,11040,0,21030,8030,0,11041,0,11042,0,11043,0,21031,8031,0,27005,11044,0,23000,0,24000,0,22000,0,21032,8032,0,11045,0,11046,0,11047,0,21033,8033,0,11048,0,0,11049,0,11050,0,
  21034,8034,0,11051,0,11052,0,11053,0,21035,8035,0,27006,11054,0,0,11055,0,11056,0,21036,8036,0,11057,0,11058,0,11059,0,21037,8037,0,11060,0,0,11061,0,11062,0,21038,8038,0,11063,0,11064,0,11065,0,21039,8039,0,27007,
  11066,0,0,11067,0,11068,0,21040,8040,0,11069,0,11070,0,11071,0,21041,8041,0,11072,0,0,11073,0,11074,0,21042,8042,0,11075,0,11076,0,11077,0,21043,8043,0,11078,0,0,11079,0,11080,0,21044,8044,0,0,0,0,21045,8045,0,0,0,0,0,
  21046,8046,0,0,0,0,21047,8047,0,27009,0,0,21048,8048,0,0,0,0,21049,8049,0,0,0,0,0,21050,8050,0,0,21051,8051,0,0,0,0,21052,8052,0,27010,0,0,25000,0,25001,0,21053,8053,0,25002,0,25003,0,25004,0,21054,8054,0,25005,0,0,
  25006,0,25007,0,21055,8055,0,25008,0,25019,0,0,21056,8056,0,27011,0,0,21057,8057,0,0,0,0,21058,8058,0,0,0,0,0,21059,8059,0,0,21060,8060,0,0,0,0,21061,8061,0,27012,0,0,0,0,21062,8062,0,0,0,0,21063,8063,0,1000,0,0,0,
  1001,0,21064,8064,0,0,1004,0,0,21065,8065,0,27013,1002,0,0,21066,8066,0,1003,0,0,0,21067,8067,0,0,1005,0,0,0,21068,8068,0,1006,0,21069,8069,0,0,1009,0,0,21070,8070,0,27014,1007,0,0,0,1008,0,21071,8071,0,0,0,0,21072,
  8072,0,1010,0,0,0,1011,0,21073,8073,0,0,1014,0,0,21074,8074,0,27015,1012,0,0,21075,8075,0,31000,1013,0,0,3000,0,21076,8076,0,31000,4000,0,31000,4105,0,4210,0,4301,0,21077,8077,0,31000,4406,0,21078,8078,0,5004,6000,
  6001,0,31000,6002,6003,0,5103,4011,6100,0,21079,8079,0,27016,6101,6102,0,6103,0,5203,4104,6200,0,6201,6202,0,21080,8080,0,6203,10000,0,4209,10001,0,5304,10002,6304,0,21081,8081,0,7000,6305,6306,0,6307,0,5403,4314,6404,
  0,6405,6406,0,21082,8082,0,5003,6407,6004,0,4402,6005,0,6006,6007,0,21083,8083,0,27017,4007,10004,0,10005,0,5104,10006,6116,0,7001,6117,6118,0,21084,8084,0,5203,6119,6216,0,4112,6217,0,6218,6219,0,21085,8085,0,5303,
  4203,6316,0,6317,0,6318,6319,0,4308,0,21086,8086,0,10016,0,10017,0,5404,6408,6409,0,21087,8087,0,27018,6410,6411,0,4413,0,13000,13001,0,5006,6008,6009,0,21088,8088,0,6010,6011,0,13002,13003,0,5107,6108,6109,0,21089,
  8089,0,6110,6111,0,10008,0,10009,10010,0,5206,10011,6212,0,21090,8090,0,7002,6213,6214,0,5305,6215,6312,0,6313,6314,0,21091,8091,0,27019,5404,6315,6412,0,6413,0,6414,6415,0,10012,10013,0,21092,8092,0,10014,10015,0,
  7003,0,0,21093,8093,0,0,0,0,0,21094,8094,0,0,0,0,21095,8095,0,27020,15000,0,0,0,0,21096,8096,0,0,0,0,21097,8097,0,0,0,0,0,21098,8098,0,0,0,0,21099,8099,0,27021,0,0,0,0,21100,8100,0,0,0,0,21101,8101,0,0,0,0,0,21102,
  8102,0,0,0,0,21103,8103,0,27022,0,0,0,0,21104,8104,0,0,0,0,21105,8105,0,0,0,0,0,21106,8106,0,0,0,0,21107,8107,0,27023,0,14000,0,20000,0,25015,0,21108,8108,0,25016,0,25017,0,0,21109,8109,0,0,0,0,0,21110,8110,0,0,0,0,
  21111,8111,0,27024,0,0,16000,0,16001,0,21112,8112,0,16002,0,16003,0,16004,0,21113,8113,0,16005,0,0,16006,0,16007,0,21114,8114,0,16008,0,0,0,21115,8115,0,27025,0,0,0,0,21116,8116,0,0,0,0,21117,8117,0,1100,0,0,0,1101,0,
  21118,8118,0,0,1104,0,0,21119,8119,0,27026,1102,0,0,29000,0,1103,0,21120,8120,0,0,0,0,21121,8121,0,1105,0,0,0,1106,0,21122,8122,0,0,1109,0,0,21123,8123,0,27027,1107,0,0,0,1108,0,21124,8124,0,26000,0,26001,0,26002,0,
  21125,8125,0,1110,0,0,0,1111,0,21126,8126,0,0,1114,0,0,21127,8127,0,27028,1112,0,0,0,31000,1113,0,0,3000,0,31000,4900,0,31000,4025,0,31000,4130,0,4901,0,4226,0,31000,4331,0,31000,4904,0,31000,4429,0,5006,6020,6021,0,
  28000,0,31000,6022,6023,0,28001,0,27029,5105,4034,6120,0,4902,0,31000,19000,0,31000,13004,13005,0,31000,6121,6122,0,19001,0,31000,13006,13007,0,5209,6123,6224,0,31000,19002,0,4127,6225,0,6226,0,31000,19003,0,5309,6227,
  6324,0,31000,4232,6325,0,4903,6326,0,5409,6327,6436,0,31000,4328,6437,0,31000,6438,6439,0,27030,5009,4433,6036,0,6037,0,31000,6038,6039,0,5104,6128,6129,0,31000,6130,6131,0,5203,6228,6229,0,31000,6230,6231,0,5301,6332,
  6333,0,31000,6334,6335,0,5400,6432,6433,0,6434,0,31000,6435,0,31000,16009,0,31000,16010,0,16011,0,31000,16012,0,31000,16013,0,31000,16014,0,27031,16015,15005,0,0,31000,16016,0,31000,16017,0,0,17000,30000,18000,0,31000,
  30001,18001,0,31000,18002,18003,0,31000,30002,18004,0,30003,18005,0,18006,0,31000,18007,30004,0,31000,18008,30005,0,31000,18009,18010,0,18011,30006,0,31000,18012,30007,0,31000,18013,18014,0,31000,18015,0,};
constexpr short TS[] = {0,1,2,3,6,7,8,9,12,13,14,15,16,19,20,21,22,25,27,28,30,32,35,37,39,41,44,46,47,49,51,54,56,58,60,63,66,67,69,72,75,77,80,82,85,87,88,90,92,95,97,99,101,104,107,108,110,112,115,117,119,121,124,126,
  127,129,131,134,136,138,140,143,146,148,150,152,155,157,159,161,164,166,167,169,171,174,176,178,180,183,186,188,190,192,195,197,199,201,204,206,207,209,211,214,216,218,220,223,226,227,229,231,234,236,238,240,243,245,
  246,248,250,253,255,257,259,262,265,266,268,270,273,275,277,279,282,284,285,287,289,292,294,296,298,301,303,304,306,308,311,312,313,314,317,318,319,320,321,324,325,326,327,330,332,333,336,337,338,339,342,343,344,345,
  346,349,350,353,354,355,356,359,361,362,364,366,369,371,373,375,378,380,381,383,385,388,390,392,393,396,398,399,402,403,404,405,408,409,410,411,412,415,416,419,420,421,422,425,427,428,429,430,433,434,435,436,439,441,
  442,443,445,448,449,451,452,455,458,459,462,464,465,466,469,470,472,473,474,477,479,482,483,485,486,489,492,493,494,496,499,500,501,502,505,507,508,509,511,514,515,517,518,521,524,525,528,531,532,533,537,540,543,545,
  547,550,553,556,560,564,568,571,575,577,581,584,587,590,593,597,600,604,606,610,613,616,620,623,626,629,633,635,639,643,646,650,653,656,659,663,665,668,670,673,675,677,681,684,688,690,693,697,700,703,706,710,713,716,
  718,721,725,728,732,736,739,742,747,749,752,755,758,761,763,764,767,768,769,770,771,774,775,776,777,780,782,784,785,786,789,790,791,792,795,796,797,798,799,802,803,804,805,808,810,811,812,813,816,817,818,819,822,823,
  824,825,826,829,830,831,832,835,837,838,839,840,843,844,845,846,849,850,851,852,853,856,857,858,859,862,864,865,868,870,873,875,877,878,881,882,883,884,885,888,889,890,891,894,896,897,899,901,904,906,908,910,913,915,
  916,918,920,923,925,926,927,930,932,933,934,935,938,939,940,941,944,946,947,948,950,953,954,956,957,960,963,964,966,968,971,972,973,974,977,979,980,981,983,986,987,989,990,993,996,997,998,1000,1003,1005,1007,1009,1012,
  1014,1015,1016,1018,1021,1022,1024,1025,1028,1031,1032,1033,1036,1037,1038,1042,1045,1048,1050,1052,1055,1058,1061,1065,1067,1071,1073,1078,1080,1083,1087,1091,1093,1097,1101,1104,1107,1109,1112,1116,1120,1123,1127,
  1131,1135,1140,1142,1146,1150,1154,1158,1162,1166,1170,1174,1176,1179,1182,1185,1187,1190,1193,1196,1199,1201,1204,1207,1208,1212,1216,1220,1224,1227,1229,1233,1237,1241,1244,1248,1252,1255,1256,1256,};
constexpr bool op_first(int u) { for (int x = u & 32; x < u; ++x) if ((OPU[x] & 3) == (OPU[u] & 3)) return false; return true; }
struct OPS {
  u32x4 fa[OP_LA + 1][4], fb[22]; f32x4 D[2][5], T[5], m[OP_RING], pv[4]; float g0, g1, t0, t1, gx[36];
  u32x4 gv[2][2]; unsigned gs[2];
  unsigned ra[3], sw, si, sg, tg, so, sb, v12, p3, ks[5];
  unsigned va, idx, sa, lb5, lb1, lb2, sn;
  u32x4 srdA, srdL, srdS; unsigned fo, sl, sf, tag, bad; unsigned long long seen;
};
constexpr unsigned OB = WS_END;
DI void op_poll(OPS& o) { asm volatile("buffer_load_dword %0, %1, %2, %3 offen sc1" : "=v"(o.fo) : "v"(o.va), "s"(o.srdL), "s"(o.sl) : "memory"); }
DI void op_there(OPS& o) {
  unsigned n;
  asm volatile("s_and_b32 %0, %1, 15\n\ts_cmp_eq_u32 %0, 15\n\ts_cselect_b32 %0, %2, 0" : "=&s"(n) : "s"((unsigned)o.seen), "i"(OB) : "scc");
  o.srdA[2] = n;
}
template <int F> DI void op_reqB(OPS& o) {
  asm volatile("buffer_load_dwordx4 a[%c3:%c4], %0, %1, %2 offen offset:%c5 sc1" :: "v"(o.va), "s"(o.srdS), "s"(o.sb + 1024u * (F & ~3)), "i"(op_ar(F)), "i"(op_ar(F) + 3), "i"(1024 * (F & 3)) : "memory");
}
constexpr unsigned op_off(int st, int i) { const int un = OPU[2 * st + (i >> 1)]; return (131072u * (un & 3) + 1024u * (2 * (un >> 2) + (i & 1))) ^ (st >> 4) * (524288u + 1024u); }
template <int ST, int I> DI void op_offA(OPS& o) {
  GAP(); o.so = o.sa ^ op_off(ST, I); asm volatile("" :: "s"(o.so)); GAP();
}
template <int ST, int I> DI void op_reqA1(OPS& o) {
  asm volatile("buffer_load_dwordx4 %0, %1, %2, %3 offen sc1" : "=v"(o.fa[ST % (OP_LA + 1)][I]) : "v"(o.va), "s"(o.srdA), "s"(o.so) : "memory");
  if constexpr (ST < OP_LA) asm volatile("ps");
}
template <int... Qs> DI void op_reqA0(OPS& o, iseq<Qs...>) { ((op_offA<Qs / 4, Qs % 4>(o), op_reqA1<Qs / 4, Qs % 4>(o)), ...); }
#define OP_F4(s) "+v"(o.fa[(s) % (OP_LA + 1)][0]), "+v"(o.fa[(s) % (OP_LA + 1)][1]), "+v"(o.fa[(s) % (OP_LA + 1)][2]), "+v"(o.fa[(s) % (OP_LA + 1)][3])
#define OP_I4(s) "i"(op_off(s, 0)), "i"(op_off(s, 1)), "i"(op_off(s, 2)), "i"(op_off(s, 3))
#define OP_L1(d, i) "s_xor_b32 %1, %[sa], %c" #i "\n\tbuffer_load_dwordx4 %" #d ", %[va], %[sr], %1 offen sc1\n\t"
#define OP_HEAD "s_cmp_lg_u32 %[was], 0\n\ts_cbranch_scc0 4f\n\t.subsection 1\n\t4:\n\ts_mov_b32 %2, 0x100000\n\t" \
                "2:\n\tv_cmp_eq_u32_e32 vcc, %[tg], %0\n\ts_and_b32 %1, vcc_lo, 15\n\ts_cmp_eq_u32 %1, 15\n\ts_cbranch_scc1 3f\n\t" \
                "s_sleep 1\n\tbuffer_load_dword %0, %[va], %[sl], %[so] offen sc1\n\ts_sub_u32 %2, %2, 1\n\ts_waitcnt vmcnt(0)\n\ts_cbranch_scc0 2b\n\t3:\n\t"
#define OP_IN [va] "v"(o.va), [sr] "s"(o.srdA), [sl] "s"(o.srdL), [so] "s"(o.sl), [sa] "s"(o.sa), [was] "s"(was), [tg] "s"(o.tag)
template <int S0, int N> DI void op_again(OPS& o) {
  unsigned t, n; const unsigned was = o.srdA[2];
  o.srdA[2] = OB;
  if constexpr (N == 16)
    asm volatile(OP_HEAD OP_L1(3, 19) OP_L1(4, 20) OP_L1(5, 21) OP_L1(6, 22) OP_L1(7, 23) OP_L1(8, 24) OP_L1(9, 25) OP_L1(10, 26) OP_L1(11, 27) OP_L1(12, 28) OP_L1(13, 29) OP_L1(14, 30)
                 OP_L1(15, 31) OP_L1(16, 32) OP_L1(17, 33) OP_L1(18, 34) "s_branch 1f\n\t.subsection 0\n\t1:"
                 : "+v"(o.fo), "=&s"(t), "=&s"(n), OP_F4(S0), OP_F4(S0 + 1), OP_F4(S0 + 2), OP_F4(S0 + 3) : OP_I4(S0), OP_I4(S0 + 1), OP_I4(S0 + 2), OP_I4(S0 + 3), OP_IN : "vcc", "scc", "memory");
  else if constexpr (N == 12)
    asm volatile(OP_HEAD OP_L1(3, 15) OP_L1(4, 16) OP_L1(5, 17) OP_L1(6, 18) OP_L1(7, 19) OP_L1(8, 20) OP_L1(9, 21) OP_L1(10, 22) OP_L1(11, 23) OP_L1(12, 24) OP_L1(13, 25) OP_L1(14, 26) "s_branch 1f\n\t.subsection 0\n\t1:"
                 : "+v"(o.fo), "=&s"(t), "=&s"(n), OP_F4(S0), OP_F4(S0 + 1), OP_F4(S0 + 2) : OP_I4(S0), OP_I4(S0 + 1), OP_I4(S0 + 2), OP_IN : "vcc", "scc", "memory");
  else
    asm volatile(OP_HEAD OP_L1(3, 11) OP_L1(4, 12) OP_L1(5, 13) OP_L1(6, 14) OP_L1(7, 15) OP_L1(8, 16) OP_L1(9, 17) OP_L1(10, 18) "s_branch 1f\n\t.subsection 0\n\t1:"
                 : "+v"(o.fo), "=&s"(t), "=&s"(n), OP_F4(S0), OP_F4(S0 + 1) : OP_I4(S0), OP_I4(S0 + 1), OP_IN : "vcc", "scc", "memory");
}
template <int F> DI void op_ldsB(OPS& o) {
  if constexpr (F < 26) asm volatile("ds_read_b128 %0, %1 offset:%2" : "=v"(o.fb[op_vr(F)]) : "v"(F < 23 ? o.lb5 : o.lb1), "i"(1024 * (F < 23 ? F : F - 23)) : "memory");
  else                  asm volatile("ds_read_b128 a[%c1:%c2], %0 offset:%c3" :: "v"(F < 27 ? o.lb1 : o.lb2), "i"(op_ar(F)), "i"(op_ar(F) + 3), "i"(1024 * (F < 27 ? F - 23 : F - 27)) : "memory");
}
template <int F0, int... Is> DI void op_ldsBs(OPS& o, iseq<Is...>) { (op_ldsB<F0 + Is>(o), ...); }
constexpr int op_ac(int pass, int i, int j) { return j < 4 ? 176 + 16 * i + 4 * j : pass == 0 ? 236 + 4 * i : i == 1 ? 252 : 112 + 4 * i; }
template <int ST, int Q> DI void op_mfma(OPS& o) {
  constexpr int A = ST % (OP_LA + 1), P = ST >> 4;
  if constexpr (Q == 0 || Q == 9) {
    constexpr int u = Q == 9, un = OPU[2 * ST + u], i = un & 3, f = 9 * (un >> 2) + 8, ac = op_ac(P, i, 4), ar = op_ar(f);
    const u32x8 B = __builtin_shufflevector(o.fa[A][2 * u], o.fa[A][2 * u + 1], 0, 1, 2, 3, 4, 5, 6, 7);
    if constexpr (i == 0 && f < 26) asm volatile("sm64 %0, %1, %2, %3" : "+v"(o.D[P][4]) : "v"(o.fb[op_vr(f) % 22]), "v"(B), "v"(o.idx));
    else if constexpr (i == 0)      asm volatile("sm64 %0, a[%c3:%c4], %1, %2" : "+v"(o.D[P][4]) : "v"(B), "v"(o.idx), "i"(ar), "i"(ar + 3));
    else if constexpr (f < 26)      asm volatile("sm64 a[%c3:%c4], %0, %1, %2" :: "v"(o.fb[op_vr(f) % 22]), "v"(B), "v"(o.idx), "i"(ac), "i"(ac + 3));
    else                            asm volatile("sm64 a[%c2:%c3], a[%c4:%c5], %0, %1" :: "v"(B), "v"(o.idx), "i"(ac), "i"(ac + 3), "i"(ar), "i"(ar + 3));
  } else {
    constexpr int d = Q - 1 - (Q > 9), J = d >> 2, u = (d >> 1) & 1, kk = d & 1, un = OPU[2 * ST + u], i = un & 3, f = 9 * (un >> 2) + 4 * kk + J, ac = op_ac(P, i, J), ar = op_ar(f);
    constexpr bool z = kk == 0 && op_first(2 * ST + u);
    static_assert(!z || f < 26, "");
    const u32x4& B = o.fa[A][2 * u + kk];
    if constexpr (i == 0 && z)           asm volatile("mf16 %0, %1, %2, 0" : "=&v"(o.D[P][J]) : "v"(o.fb[op_vr(f) % 22]), "v"(B));
    else if constexpr (i == 0 && f < 26) asm volatile("mf16 %0, %1, %2, %0" : "+v"(o.D[P][J]) : "v"(o.fb[op_vr(f) % 22]), "v"(B));
    else if constexpr (i == 0)           asm volatile("mf16 %0, a[%c2:%c3], %1, %0" : "+v"(o.D[P][J]) : "v"(B), "i"(ar), "i"(ar + 3));
    else if constexpr (z)                asm volatile("mf16 a[%c2:%c3], %0, %1, 0" :: "v"(o.fb[op_vr(f) % 22]), "v"(B), "i"(ac), "i"(ac + 3));
    else if constexpr (f < 26)           asm volatile("mf16 a[%c2:%c3], %0, %1, a[%c2:%c3]" :: "v"(o.fb[op_vr(f) % 22]), "v"(B), "i"(ac), "i"(ac + 3));
    else                                 asm volatile("mf16 a[%c1:%c2], a[%c3:%c4], %0, a[%c1:%c2]" :: "v"(B), "i"(ac), "i"(ac + 3), "i"(ar), "i"(ar + 3));
  }
}
template <int I> DI unsigned op_ax(const unsigned va) { const unsigned r = ((va >> 4) & 15u) * XRS_; return I ? r + 128u + ((va >> 5) & 8u) + ((va >> 7) & 4u) : r + ((va >> 4) & 48u); }
template <int X, int WD> DI unsigned op_word(const OPS& o) { if constexpr (WD < 8) return o.gv[X][WD >> 2][WD & 3]; else return o.gs[X]; }
template <int U> DI void op_gate(OPS& o) {
  constexpr int wd = U / 9, pt = U % 9;
  if constexpr (pt == 0)      asm volatile("v_lshlrev_b32_e32 %0, 16, %2\n\tv_and_b32_e32 %1, 0xffff0000, %2" : "=&v"(o.g0), "=&v"(o.g1) : "v"(op_word<0, wd>(o)));
  else if constexpr (pt == 1) asm volatile("v_mul_f32_e32 %0, 0xbfb8aa3b, %2\n\tv_mul_f32_e32 %1, 0xbfb8aa3b, %3" : "=&v"(o.t0), "=&v"(o.t1) : "v"(o.g0), "v"(o.g1));
  else if constexpr (pt == 2) asm volatile("v_exp_f32_e32 %0, %0" : "+v"(o.t0));
  else if constexpr (pt == 3) asm volatile("v_exp_f32_e32 %0, %0" : "+v"(o.t1));
  else if constexpr (pt == 4) asm volatile("v_add_f32_e32 %0, 1.0, %0\n\tv_add_f32_e32 %1, 1.0, %1" : "+v"(o.t0), "+v"(o.t1));
  else if constexpr (pt == 5) asm volatile("v_rcp_f32_e32 %0, %0" : "+v"(o.t0));
  else if constexpr (pt == 6) asm volatile("v_rcp_f32_e32 %0, %0" : "+v"(o.t1));
  else if constexpr (pt == 7) asm volatile("v_mul_f32_e32 %0, %0, %2\n\tv_mul_f32_e32 %1, %1, %3" : "+v"(o.t0), "+v"(o.t1) : "v"(o.g0), "v"(o.g1));
  else { unsigned r; asm volatile("v_cvt_pk_bf16_f32 %0, %1, %2" : "=v"(r) : "v"(o.t0), "v"(o.t1)); if constexpr (wd < 8) o.gv[0][wd >> 2][wd & 3] = r; else o.gs[0] = r; }
}
template <int ST> DI void op_wait(OPS& o) { asm volatile("s_waitcnt vmcnt(%0)" :: "i"(OPW[ST]) : "memory"); }
template <int ST, int Q, int CODE, typename H> DI void op_t(OPS& o, H& hook) {
  constexpr int kind = CODE / 1000, a = CODE % 1000;
  if constexpr (kind == 0) op_mfma<ST, Q>(o);
  else if constexpr (kind == 1) {
    constexpr int n = a % 100, ac = op_ac((a / 100) & 1, 1 + n / 5, n % 5), off = n < 10 ? 13312 + 1024 * n : 1024 * (n - 10);
    asm volatile("ds_write_b128 %0, a[%c1:%c2] offset:%c3" :: "v"(n < 10 ? o.lb5 : o.lb2), "i"(ac), "i"(ac + 3), "i"(off) : "memory");
  } else if constexpr (kind == 2) asm volatile("s_waitcnt lgkmcnt(%0)" :: "i"(a) : "memory");
  else if constexpr (kind == 3) asm volatile("s_waitcnt lgkmcnt(0)\n\ts_barrier" ::: "memory");
  else if constexpr (kind == 4) {
    constexpr int sl = a / 100, n = a % 20;
    asm volatile("ds_read_b128 %0, %1 offset:%2" : "=v"(sl == 9 ? o.T[n % 5] : o.m[sl % OP_RING]) : "v"(o.ra[n / 5]), "i"(n < 10 ? 13312 + 1024 * n : 1024 * (n - 10)) : "memory");
  } else if constexpr (kind == 5) asm volatile("s_waitcnt lgkmcnt(%0)" :: "i"(a % 100) : "memory");
  else if constexpr (kind == 6 || kind == 18) {
    constexpr int j = (a % 20) >> 2, e = a & 3;
    f32x4& s = kind == 6 && a % 100 < 20 ? o.D[0][j] : o.T[j];
    float v = s[e]; add_v(v, kind == 6 ? o.m[a / 100][e] : e == 3 ? __uint_as_float(o.p3) : o.pv[j & 3][e]); s[e] = v;
  } else if constexpr (kind == 10) {
    constexpr int j = a >> 2, e = a & 3; f32x4& h = o.D[0][4];
    if constexpr (j == 4) { float x = h[e], y = h[e + 2]; asm volatile("v_permlane32_swap_b32_e32 %0, %1" : "+v"(x), "+v"(y)); h[e] = x; h[e + 2] = y; }
    else if constexpr (e == 3) { float x; asm volatile("v_add_f32_e32 %0, %1, %2" : "=v"(x) : "v"(h[j & 1]), "v"(h[2 + (j & 1)])); o.D[0][j][3] = x; }
    else { float x = o.D[0][j][e]; asm volatile("v_perm_b32 %0, %0, %1, %2" : "+v"(x) : "v"(o.D[0][j][3]), "s"(o.ks[e])); o.D[0][j][e] = x; }
  } else if constexpr (kind == 30) {
    if constexpr (a & 1) asm volatile("v_perm_b32 %0, %1, %0, %2" : "+v"(o.p3) : "v"(o.pv[a >> 1][2]), "s"(o.ks[4]));
    else                 asm volatile("v_perm_b32 %0, %1, %2, %3" : "=v"(o.p3) : "v"(o.pv[a >> 1][1]), "v"(o.pv[a >> 1][0]), "s"(o.ks[3]));
  } else if constexpr (kind == 7) {
    if constexpr (a < 2) asm volatile("buffer_store_dwordx3 %0, %1, %2, %3 offen offset:%c4 sc1" :: "v"(__builtin_shufflevector(o.D[0][a], o.D[0][a], 0, 1, 2)), "v"(o.v12), "s"(o.srdS), "s"(o.sw), "i"(2048 + 768 * a) : "memory");
    else                 asm volatile("buffer_store_dwordx4 %0, %1, %2, %3 offen offset:%c4 sc1" :: "v"(o.D[0][a]), "v"(o.va), "s"(o.srdS), "s"(o.sw), "i"(1024 * (a & 1)) : "memory");
  }
  else if constexpr (kind == 8) op_reqA1<(a >> 2), (a & 3)>(o);
  else if constexpr (kind == 9) op_reqB<a>(o);
  else if constexpr (kind == 19) {
    if constexpr (a < 2) {
      f32x3 d; asm volatile("buffer_load_dwordx3 %0, %1, %2, %3 offen offset:%c4 sc1" : "=v"(d) : "v"(o.v12), "s"(o.srdS), "s"(o.si), "i"(2048 + 768 * a) : "memory");
      o.pv[a] = __builtin_shufflevector(d, d, 0, 1, 2, -1);
    } else asm volatile("buffer_load_dwordx4 %0, %1, %2, %3 offen offset:%c4 sc1" : "=v"(o.pv[a]) : "v"(o.va), "s"(o.srdS), "s"(o.si), "i"(1024 * (a & 1)) : "memory");
  }
  else if constexpr (kind == 20) asm volatile("buffer_store_dword %0, %1, %2, %3 offen sc1" :: "v"(o.tg), "v"(o.va), "s"(o.srdL), "s"(o.sg) : "memory");
  else if constexpr (kind == 25) { GAP(); hook(IC(a)); GAP(); }
  else if constexpr (kind == 27) op_wait<a>(o);
  else if constexpr (kind == 28 || kind == 29) { GAP(); hook(IC(CODE)); GAP(); }
  else if constexpr (kind == 26) {
    if constexpr (a < 2) asm volatile("ds_read_b128 %0, %1 offset:%2" : "=v"(o.gv[1][a]) : "v"(o.lb5), "i"(4096 + 1024 * a) : "memory");
    else                 asm volatile("ds_read_b32 %0, %1 offset:6144" : "=v"(o.gs[1]) : "v"(o.lb5) : "memory");
  } else if constexpr (kind == 22) op_poll(o);
  else if constexpr (kind == 23) asm volatile("v_cmp_eq_u32_e64 %0, %1, %2" : "=s"(o.seen) : "s"(o.tag), "v"(o.fo));
  else if constexpr (kind == 24) op_there(o);
  else if constexpr (kind == 21) op_offA<(a >> 2), (a & 3)>(o);
  else if constexpr (kind == 31) real_op();
  else if constexpr (kind == 11) op_gate<a>(o);
  else if constexpr (kind == 13) op_ldsB<9 * (a >> 2) + (a & 3)>(o);
  else if constexpr (kind == 14) asm volatile("s_barrier" ::: "memory");
  else if constexpr (kind == 15) asm volatile("s_waitcnt lgkmcnt(%0)" :: "i"(a) : "memory");
  else if constexpr (kind == 16) asm volatile("v_lshlrev_b32_e32 %0, 16, %2\n\tv_and_b32_e32 %1, 0xffff0000, %2" : "=&v"(o.gx[2 * a]), "=v"(o.gx[2 * a + 1]) : "v"(op_word<a / 9, a % 9>(o)));
  else if constexpr (kind == 17) asm volatile("s_waitcnt vmcnt(0)" : "+v"(o.pv[2]), "+v"(o.pv[3]) :: "memory");
}
template <int ST, int Q, typename H, int... Is> DI void op_ts(OPS& o, H& hook, iseq<Is...>) { (op_t<ST, Q, TT[TS[18 * ST + Q] + Is]>(o, hook), ...); }
template <int ST, typename H, int... Qs> DI void op_qs(OPS& o, H& hook, iseq<Qs...>) { (op_ts<ST, Qs>(o, hook, make_iseq<TS[18 * ST + Qs + 1] - TS[18 * ST + Qs]>{}), ...); }

template <typename F, int... Is> DI void for_seq(F& f, iseq<Is...>) { (f(std::integral_constant<int, Is>{}), ...); }
DI void wait_flag(const u32x4 srd, const unsigned p, const unsigned tag) {
#pragma nounroll
  for (unsigned spin = 0; spin < (1u << 20); ++spin) {
    unsigned v;
    asm volatile("buffer_load_dword %0, %1, %2, 0 offen sc1\n\ts_waitcnt vmcnt(0)" : "=v"(v) : "v"(p), "s"(srd) : "memory");
    if (__all(v == tag)) break;
    __builtin_amdgcn_s_sleep(1);
  }
}

constexpr int NFIN = 200, NBL = 45, FVW = 49, FVS = 0, FNR = 37;
constexpr short FT[] = {5064,31000,23000,23001,23002,5001,31000,23003,23004,23005,5002,31000,23006,23007,23008,5003,31000,23009,23010,23011,5083,31000,23012,23013,23014,5018,31000,23015,16000,16001,5017,16008,16009,
  16016,5016,16017,16024,16025,5096,31000,23016,23017,23018,5033,31000,23019,23020,23021,5034,31000,23022,23023,23024,5035,31000,23025,23026,23027,5115,31000,25003,5050,31000,23028,23029,23030,5049,31000,25002,5048,
  31000,23031,13000,20015,1000,31000,25001,22000,1001,13001,13002,20031,1003,31000,25000,1002,15000,16002,20047,1004,13003,13016,1005,15001,16003,20063,1007,13017,13018,1006,15008,16010,29010,20076,1008,27000,13019,1009,
  31000,26000,15009,20088,1011,13032,1010,31000,26001,13033,20100,1012,15016,16011,1013,31000,26002,13034,20112,1015,13035,1014,31000,26003,3000,27001,5068,31000,26004,15017,5005,13048,13049,5006,31000,26005,5007,31000,
  24000,24001,5087,31000,26006,24002,5022,31000,24003,24004,5021,31000,26007,24005,5020,31000,24006,24007,5100,31000,24008,24009,24010,5037,24011,15024,5038,13050,13051,5039,15025,16018,5119,31000,25015,24012,5054,31000,
  24013,24014,24015,5053,31000,25014,24016,24017,24018,24019,5052,31000,25013,24020,4000,31000,24021,24022,22001,30015,2000,31000,24023,24024,24025,2001,31000,12000,30031,2003,31000,24026,24027,24028,24029,2002,31000,
  24030,24031,25012,30047,2004,31000,17000,17001,17002,17003,2005,31000,25004,17004,17005,30063,2007,31000,17006,17007,18000,18001,2006,31000,25005,18002,30076,2008,31000,18003,18004,18005,18006,2009,31000,18007,19000,
  19001,30088,2011,19002,19003,16019,16026,2010,31000,28000,17008,30100,2012,31000,17009,17010,17011,17012,2013,31000,25006,17013,30112,2015,31000,17014,17015,18008,18009,2014,31000,18010,18011,18012,1016,31000,25007,
  18013,18014,16027,13004,13005,1017,31000,18015,19004,19005,1019,19006,19007,15002,1018,31000,28001,17016,1020,31000,17017,17018,17019,1021,31000,25008,17020,1023,31000,17021,17022,17023,1022,31000,18016,18017,18018,
  1024,31000,25009,18019,1025,31000,18020,18021,18022,1027,31000,18023,19008,19009,1026,19010,19011,13006,1028,31000,28002,17024,1029,31000,17025,17026,17027,1031,31000,25010,17028,17029,1030,31000,17030,17031,18024,
  3001,31000,25011,18025,5072,31000,18026,18027,18028,18029,5009,31000,18030,18031,19012,19013,5010,19014,19015,13007,5011,31000,28003,15003,5091,13020,13021,5026,15010,13022,5025,13023,13036,5024,15011,13037,5104,15018,
  13038,5041,13039,13052,5042,15019,13053,5043,15026,13054,13055,5123,31000,25027,15027,5058,16004,16005,16012,5057,16013,16020,16021,14057,14058,5056,31000,25026,16028,4001,14059,22002,2016,16029,13008,2017,31000,12001,
  2019,31000,25025,13009,2018,31000,17032,17033,17034,2020,31000,17035,15004,13010,2021,31000,25024,17036,2023,31000,17037,17038,17039,2022,31000,18032,18033,18034,2024,31000,25016,18035,2025,31000,18036,18037,18038,
  2027,31000,18039,19016,19017,2026,31000,25017,19018,2028,19019,13011,2029,31000,28004,17040,2031,31000,17041,17042,17043,2030,31000,25018,17044,1032,31000,17045,17046,17047,1033,31000,18040,18041,18042,1035,31000,
  25019,18043,1034,31000,18044,18045,18046,1036,31000,18047,19020,19021,19022,1037,19023,15005,13024,1039,31000,28005,17048,1038,31000,17049,17050,17051,1040,31000,25020,17052,17053,1041,31000,17054,17055,18048,1043,
  31000,18049,18050,18051,18052,1042,31000,25021,18053,1044,31000,18054,18055,19024,19025,1045,19026,19027,13025,1047,31000,28006,17056,1046,31000,17057,17058,17059,3002,31000,25022,17060,5076,31000,17061,17062,17063,
  18056,5013,31000,18057,18058,18059,18060,5014,31000,25023,18061,5015,31000,18062,18063,19028,19029,5095,19030,19031,15012,5030,31000,28007,13026,5029,13027,13040,5028,15013,13041,5108,15020,13042,5045,13043,13056,5046,
  15021,13057,5047,15028,13058,13059,5127,31000,25031,15029,5062,14012,14013,5061,16006,14014,14015,14028,5060,31000,25030,16007,14029,4002,16014,14030,14031,22003,2032,31000,25029,16015,14044,2033,31000,12002,14045,
  2035,31000,25028,16022,14046,2034,31000,17064,17065,17066,17067,14047,2036,31000,25032,16023,17068,17069,2037,31000,17070,17071,18064,18065,18066,2039,31000,25033,18067,18068,18069,2038,31000,18070,18071,19032,19033,
  19034,2040,19035,14060,14061,2041,31000,28008,17072,17073,17074,2043,31000,17075,17076,17077,17078,17079,2042,31000,25034,18072,18073,18074,2044,31000,18075,18076,18077,18078,18079,2045,31000,25035,19036,19037,19038,
  2047,19039,16030,14062,14063,2046,31000,28009,17080,17081,17082,1048,31000,17083,17084,17085,17086,17087,16031,13012,13013,1049,31000,25036,18080,18081,18082,1051,31000,18083,18084,18085,18086,18087,1050,19040,19041,
  19042,19043,15006,1052,31000,28010,17088,17089,17090,1053,31000,17091,17092,17093,17094,17095,1055,31000,18088,18089,18090,18091,18092,1054,31000,18093,18094,18095,19044,19045,1056,19046,19047,13014,13015,1057,31000,
  28011,15007,13028,1059,13029,13030,15014,1058,13031,13044,15015,1060,13045,13046,1061,15022,13047,13060,1063,15023,13061,13062,1062,15030,13063,3003,15031,30200,30200,4003,31000,2048,31000,2049,31000,12003,2051,31000,
  2050,31000,17096,17097,17098,17099,2052,31000,2053,31000,17100,17101,17102,17103,18096,2055,31000,18097,18098,18099,18100,18101,2054,31000,18102,18103,19048,19049,19050,2056,19051,2057,31000,28012,17104,17105,17106,
  17107,2059,31000,17108,17109,17110,17111,18104,18105,2058,31000,18106,18107,18108,18109,18110,18111,2060,19052,19053,19054,19055,2061,31000,28013,17112,17113,17114,17115,2063,31000,17116,17117,17118,17119,18112,18113,
  2062,31000,18114,18115,18116,18117,18118,18119,19056,19057,19058,19059,28014,17120,17121,17122,17123,17124,17125,17126,17127,18120,18121,18122,18123,18124,18125,18126,18127,19060,19061,19062,19063,28015,};
constexpr short FS[] = {0,5,10,15,20,25,30,34,38,43,48,53,58,61,66,69,73,78,81,85,88,92,95,99,102,107,111,114,118,122,126,129,132,134,138,141,144,148,152,156,160,164,169,172,175,178,182,187,194,198,203,209,212,219,224,
  231,236,243,247,254,259,265,269,276,280,287,292,300,305,309,313,318,322,327,332,336,341,346,350,354,359,364,369,373,379,385,389,393,396,399,402,405,408,411,414,418,422,426,432,436,439,442,445,449,454,459,463,468,473,
  477,482,487,491,494,498,503,507,512,517,521,526,532,536,540,545,550,555,561,565,571,575,579,584,588,594,600,604,610,614,618,621,624,627,630,633,637,641,644,649,654,659,664,668,673,680,686,693,699,706,710,716,723,729,
  736,742,747,753,763,769,776,782,788,795,802,809,814,819,823,827,830,834,838,841,845,847,849,852,854,860,862,869,876,883,885,892,900,908,913,920,928,962,};
struct OST { u32x4 w, P[4][2]; float o[8], il[4], ls[4]; unsigned vo, so[4]; u32x4 srd; bf16x4 lo[8][2], up[8][2]; };
template <int U> DI void o_store(const OST& t) {
  asm volatile("buffer_store_dwordx4 %0, %1, %2, %3 offen sc1" :: "v"(t.w), "v"(t.vo), "s"(t.srd), "s"(t.so[U & 3] + 131072u * (U >> 2)) : "memory");
}
template <int CODE> DI void fin_op(W4C& c, OST& t, OPS& o) {
  constexpr int kind = CODE / 1000, a = CODE % 1000;
  if constexpr (kind == 1) pv16<AO_((a >> 1) & 7, a >> 4), false>(__builtin_shufflevector(c.vlo[(a >> 1) & 7][a & 1], c.vup[(a >> 1) & 7][a & 1], 0, 1, 2, 3, 4, 5, 6, 7), c.Pk[a >> 4][a & 1]);
  else if constexpr (kind == 2) pv16<AO_((a >> 1) & 7, a >> 4), false>(__builtin_shufflevector(t.lo[(a >> 1) & 7][a & 1], t.up[(a >> 1) & 7][a & 1], 0, 1, 2, 3, 4, 5, 6, 7), t.P[a >> 4][a & 1]);
  else if constexpr (kind == 3) sum64(c, a, c.Pk[a]);
  else if constexpr (kind == 4) sum64(c, a, t.P[a]);
  else if constexpr (kind == 5) { constexpr int kb = (a >> 4) & 3, qb = (a >> 2) & 3; qk16<kb, qb, (a & 3), (a >> 6)>(c.S[1][kb][qb], c.NEGM[qb]); }
  else if constexpr (kind == 14) exp_e<0, a>(c);
  else if constexpr (kind == 16) pack_e<0, a>(c);
  else if constexpr (kind == 22) asm volatile("" :: "v"(c.NEGM[a]));
  else if constexpr (kind == 20) { constexpr int d = a >> 4; asm volatile("s_waitcnt lgkmcnt(%4)" : "+v"(c.vlo[d][0]), "+v"(c.vlo[d][1]), "+v"(c.vup[d][0]), "+v"(c.vup[d][1]) : "i"(a & 15) : "memory"); }
  else if constexpr (kind == 23) tr_read_<a, 0>(c);
  else if constexpr (kind == 29) asm volatile("s_waitcnt vmcnt(%0) lgkmcnt(0)\n\ts_barrier" :: "i"(a) : "memory");
  else if constexpr (kind == 12) asm volatile("v_add_f32_e32 %0, %2, %3\n\tv_rcp_f32_e32 %1, %0" : "=&v"(t.ls[a]), "=v"(t.il[a]) : "v"(c.L[a][0]), "v"(c.L[a][1]), "v"(c.L[a]));
  else if constexpr (kind == 13) exp_e<1, a>(c);
  else if constexpr (kind == 15) pack_to<1, a>(c, t.P);
  else if constexpr (kind == 17) t.o[a & 7] = acc_read<a>();
  else if constexpr (kind == 18) {
    asm volatile("v_mul_f32_e32 %0, %0, %1" : "+v"(t.o[a & 7]) : "v"(t.il[a >> 5]));
    if constexpr (a >= 8 && (a & 7) == 0) asm volatile("" :: "v"(t.w));
  } else if constexpr (kind == 19) { GAP(); const unsigned w = cvtpk(t.o[2 * (a & 3)], t.o[2 * (a & 3) + 1]); asm volatile("" :: "v"(w)); t.w[a & 3] = w; GAP(); }
  else if constexpr (kind == 24) tr_rd<a, 0x4000>(c, t.lo, t.up);
  else if constexpr (kind == 25) op_reqB<35 + a>(o);
  else if constexpr (kind == 26) asm volatile("buffer_load_dwordx4 %0, %1, %2 offen offset:%c3 sc1 lds" :: "v"(c.pfv), "s"(c.srdP), "s"(c.pfo + 4096u * (a >> 2)), "i"(1024 * (a & 3)) : "memory");
  else if constexpr (kind == 27) asm volatile("s_mov_b32 m0, %0" :: "s"(c.pfm0 + 4096u * a) : "m0", "memory");
  else if constexpr (kind == 28) o_store<a>(t);
  else if constexpr (kind == 31) real_op();
  else if constexpr (a == 200) asm volatile("s_nop 0");
  else { constexpr int d = a >> 4; asm volatile("s_waitcnt lgkmcnt(%4)" : "+v"(t.lo[d][0]), "+v"(t.lo[d][1]), "+v"(t.up[d][0]), "+v"(t.up[d][1]) : "i"(a & 15) : "memory"); }
}
template <int G, int... Is> DI void fin_gap(W4C& c, OST& t, OPS& o, iseq<Is...>) { (fin_op<FT[FS[G] + Is]>(c, t, o), ...); GAP(); }
template <int... Gs> DI void fin_gaps(W4C& c, OST& t, OPS& o, iseq<Gs...>) { (fin_gap<Gs>(c, t, o, make_iseq<FS[Gs + 1] - FS[Gs]>{}), ...); }

template <int J, int H> DI void redo_q(const __amdgpu_buffer_rsrc_t srdW, const u32x4 srdN, const unsigned vw, const unsigned vx, const unsigned sw, const unsigned sx) {
  f32x4 q[4] = {};
#pragma nounroll
  for (unsigned ks = 0; ks < DM / 32; ++ks) {
    u32x4 w;
#pragma unroll
    for (int e = 0; e < 4; ++e) {
      const unsigned so = sw + (64u * J + 8u * H) + WRS * (32u * ks + 2u * e);
      w[e] = (unsigned)__builtin_amdgcn_raw_buffer_load_b16(srdW, vw, so, 0) | ((unsigned)__builtin_amdgcn_raw_buffer_load_b16(srdW, vw, so + WRS, 0) << 16);
    }
    asm volatile("s_nop 1" : "+v"(w), "+v"(q[0]), "+v"(q[1]), "+v"(q[2]), "+v"(q[3]));
#pragma unroll
    for (int qb = 0; qb < 4; ++qb) {
      u32x4 x;
      asm volatile("buffer_load_dwordx4 %0, %1, %2, %3 offen sc1\n\ts_waitcnt vmcnt(0)" : "=v"(x) : "v"(vx), "s"(srdN), "s"(sx + 64u * ks + 16u * XRS * qb) : "memory");
      sum16<false>(q[qb], w, x);
    }
  }
  asm volatile("s_nop 7" : "+v"(q[0]), "+v"(q[1]), "+v"(q[2]), "+v"(q[3]));
  const float C = 0.12751743082f;
  unsigned r[8];
#pragma unroll
  for (int qb = 0; qb < 4; ++qb) { r[2 * qb] = cvtpk(C * q[qb][0], C * q[qb][1]); r[2 * qb + 1] = cvtpk(C * q[qb][2], C * q[qb][3]); }
  asm volatile("s_nop 1" : "+v"(r[0]), "+v"(r[1]), "+v"(r[2]), "+v"(r[3]), "+v"(r[4]), "+v"(r[5]), "+v"(r[6]), "+v"(r[7]));
  acc_write<AQ_(0, J) + 2 * H>(r[0]); acc_write<AQ_(0, J) + 2 * H + 1>(r[1]); acc_write<AQ_(1, J) + 2 * H>(r[2]); acc_write<AQ_(1, J) + 2 * H + 1>(r[3]);
  acc_write<AQ_(2, J) + 2 * H>(r[4]); acc_write<AQ_(2, J) + 2 * H + 1>(r[5]); acc_write<AQ_(3, J) + 2 * H>(r[6]); acc_write<AQ_(3, J) + 2 * H + 1>(r[7]);
}
template <int I> DI void redo_kload(const unsigned vo, const __amdgpu_buffer_rsrc_t srd, const unsigned so) {
  asm volatile("buffer_load_dwordx4 a[%c3:%c4], %0, %1, %2 offen offset:%c5" :: "v"(vo), "s"(srd), "s"(so + 4096u * (I >> 2)), "i"(192 + 4 * I), "i"(195 + 4 * I), "i"(64 * (I & 3)) : "memory");
}
template <int CM, int... Ms> DI void redo_s(W4C& c, const unsigned vk, const unsigned so, f32x4 (&T)[2][4], iseq<Ms...>) {
  redo_kload<0>(vk, c.srdK, so); redo_kload<1>(vk, c.srdK, so); redo_kload<2>(vk, c.srdK, so); redo_kload<3>(vk, c.srdK, so);
  redo_kload<4>(vk, c.srdK, so); redo_kload<5>(vk, c.srdK, so); redo_kload<6>(vk, c.srdK, so); redo_kload<7>(vk, c.srdK, so);
  asm volatile("s_waitcnt vmcnt(0)" ::: "memory");
  (qk16<(Ms >> 4), ((Ms >> 2) & 3), (Ms & 3), ((Ms & 3) ? 0 : CM)>(T[Ms >> 4][(Ms >> 2) & 3], c.NEGM[(Ms >> 2) & 3]), ...);
  asm volatile("s_nop 7" : "+v"(T[0][0]), "+v"(T[0][1]), "+v"(T[0][2]), "+v"(T[0][3]), "+v"(T[1][0]), "+v"(T[1][1]), "+v"(T[1][2]), "+v"(T[1][3]));
}
template <int DB> DI void redo_pv(const W4C& c, const unsigned vv, const unsigned so, const u32x4 (&P)[4]) {
  u32x4 v;
#pragma unroll
  for (int e = 0; e < 4; ++e) {
    const unsigned s = so + (896u + 32u * DB) + KRS * (16u * (e >> 1) + 2u * (e & 1));
    v[e] = (unsigned)__builtin_amdgcn_raw_buffer_load_b16(c.srdV, vv, s, 0) | ((unsigned)__builtin_amdgcn_raw_buffer_load_b16(c.srdV, vv, s + KRS, 0) << 16);
  }
  asm volatile("s_nop 1" : "+v"(v));
  const bf16x8 vf = __builtin_bit_cast(bf16x8, v);
  pv16<AO_(DB, 0), false>(vf, P[0]); pv16<AO_(DB, 1), false>(vf, P[1]); pv16<AO_(DB, 2), false>(vf, P[2]); pv16<AO_(DB, 3), false>(vf, P[3]);
  GAP();
}
template <int U> DI void redo_unit(OST& t) {
  t.o[0] = acc_read<8 * U>(); t.o[1] = acc_read<8 * U + 1>(); t.o[2] = acc_read<8 * U + 2>(); t.o[3] = acc_read<8 * U + 3>();
  t.o[4] = acc_read<8 * U + 4>(); t.o[5] = acc_read<8 * U + 5>(); t.o[6] = acc_read<8 * U + 6>(); t.o[7] = acc_read<8 * U + 7>();
  const float il = t.il[U >> 2];
  t.w = u32x4{cvtpk(t.o[0] * il, t.o[1] * il), cvtpk(t.o[2] * il, t.o[3] * il), cvtpk(t.o[4] * il, t.o[5] * il), cvtpk(t.o[6] * il, t.o[7] * il)};
  o_store<U>(t);
  asm volatile("s_nop 1" :: "v"(t.w));
}
template <int R0, int... Is> DI void redo_zero(const unsigned z, iseq<Is...>) { (acc_write<R0 + Is>(z), ...); }
template <int... Is> DI void redo_units(OST& t, iseq<Is...>) { (redo_unit<Is>(t), ...); }
template <int F0, int... Is> DI void redo_reqs(OPS& o, iseq<Is...>) { (op_reqB<F0 + Is>(o), ...); }
DI void redo_rows(W4C& c, OST& t, OPS& o, const u16* wq, const unsigned row0, const unsigned h, const unsigned tag) {
  unsigned l;
  asm volatile("s_waitcnt vmcnt(0)\n\tv_mbcnt_lo_u32_b32 %0, -1, 0\n\tv_mbcnt_hi_u32_b32 %0, -1, %0" : "=v"(l) :: "memory");
  const unsigned c16 = l & 15u, g = l >> 4;
  wait_flag(c.srdS, WS_KEY + 8u * (row0 + l) + 4u, tag);
  {
    const __amdgpu_buffer_rsrc_t srdW = mk_srd(wq, DM * HQ * DH * 2); const u32x4 srdN = c.srdS;
    const unsigned vw = WRS * 8u * g + 16u * (c16 >> 2) + 2u * (c16 & 3u), vx = XRS * c16 + 16u * g, sw = 256u * h, sx = WS_XN + XRS * row0;
    redo_q<0, 0>(srdW, srdN, vw, vx, sw, sx); redo_q<0, 1>(srdW, srdN, vw, vx, sw, sx); redo_q<1, 0>(srdW, srdN, vw, vx, sw, sx); redo_q<1, 1>(srdW, srdN, vw, vx, sw, sx);
    redo_q<2, 0>(srdW, srdN, vw, vx, sw, sx); redo_q<2, 1>(srdW, srdN, vw, vx, sw, sx); redo_q<3, 0>(srdW, srdN, vw, vx, sw, sx); redo_q<3, 1>(srdW, srdN, vw, vx, sw, sx);
  }
  const unsigned vk = KRS * c16 + 16u * g, vv = KRS * 4u * g + 2u * c16;
  f32x4 T[2][4];
  float m[4] = {-__builtin_inff(), -__builtin_inff(), -__builtin_inff(), -__builtin_inff()};
#pragma nounroll
  for (unsigned u = 0; u < SEQ / 32; ++u) {
    redo_s<2>(c, vk, 32u * KRS * u, T, make_iseq<32>{});
#pragma unroll
    for (int q = 0; q < 4; ++q)
#pragma unroll
      for (int e = 0; e < 8; ++e) m[q] = max2f(m[q], T[e >> 2][q][e & 3]);
  }
#pragma unroll
  for (int q = 0; q < 4; ++q) {
    asm volatile("s_nop 1" : "+v"(m[q]));
    auto p1 = __builtin_amdgcn_permlane16_swap(__float_as_uint(m[q]), __float_as_uint(m[q]), false, false);
    m[q] = max2f(__uint_as_float(p1[0]), __uint_as_float(p1[1]));
    asm volatile("s_nop 1" : "+v"(m[q]));
    auto p2 = __builtin_amdgcn_permlane32_swap(__float_as_uint(m[q]), __float_as_uint(m[q]), false, false);
    m[q] = -max2f(__uint_as_float(p2[0]), __uint_as_float(p2[1]));
    c.NEGM[q] = f32x4{m[q], m[q], m[q], m[q]};
    c.L[q] = f32x4{0.f, 0.f, 0.f, 0.f};
  }
  redo_zero<0>(0u, make_iseq<128>{});
  asm volatile("s_nop 1" : "+v"(c.NEGM[0]), "+v"(c.NEGM[1]), "+v"(c.NEGM[2]), "+v"(c.NEGM[3]), "+v"(c.L[0]), "+v"(c.L[1]), "+v"(c.L[2]), "+v"(c.L[3]));
#pragma nounroll
  for (unsigned u = 0; u < SEQ / 32; ++u) {
    redo_s<1>(c, vk, 32u * KRS * u, T, make_iseq<32>{});
    u32x4 P[4];
#pragma unroll
    for (int q = 0; q < 4; ++q)
#pragma unroll
      for (int e = 0; e < 4; ++e) P[q][e] = cvtpk(__builtin_amdgcn_exp2f(T[e >> 1][q][2 * (e & 1)]), __builtin_amdgcn_exp2f(T[e >> 1][q][2 * (e & 1) + 1]));
    asm volatile("s_nop 1" : "+v"(P[0]), "+v"(P[1]), "+v"(P[2]), "+v"(P[3]));
#pragma unroll
    for (int q = 0; q < 4; ++q) sum16<false>(c.L[q], c.ONE, P[q]);
    const unsigned so = 32u * KRS * u;
    redo_pv<0>(c, vv, so, P); redo_pv<1>(c, vv, so, P); redo_pv<2>(c, vv, so, P); redo_pv<3>(c, vv, so, P);
    redo_pv<4>(c, vv, so, P); redo_pv<5>(c, vv, so, P); redo_pv<6>(c, vv, so, P); redo_pv<7>(c, vv, so, P);
  }
  asm volatile("s_nop 7" : "+v"(c.L[0]), "+v"(c.L[1]), "+v"(c.L[2]), "+v"(c.L[3]));
#pragma unroll
  for (int q = 0; q < 4; ++q) t.il[q] = __builtin_amdgcn_rcpf(c.L[q][0]);
  redo_units(t, make_iseq<16>{});
  redo_reqs<35>(o, make_iseq<FNR>{});
  asm volatile("s_waitcnt vmcnt(0)" ::: "memory");
}

DI void oproj_phase(OPS& o, char* lds, u16* __restrict__ out, const unsigned wid  ) {
  const unsigned ldsb = 0u, tag = o.tag;
  unsigned fe;
  op_poll(o);
  o.lb5 = ldsb + SD_BASE + BP_W * wid + o.va; o.lb1 = ldsb + 4096u * wid + o.va; o.lb2 = ldsb + 0x4000u + 8192u * wid + o.va;
  asm volatile("s_waitcnt vmcnt(%6)" : "+v"(o.gv[0][0]), "+v"(o.gv[0][1]), "+v"(o.gs[0]), "+v"(o.gv[1][0]), "+v"(o.gv[1][1]), "+v"(o.gs[1]) : "i"(FVW + 1) : "memory");
  static_assert(FVS == 0, "the last request of the final phase is a store");
  op_ldsBs<0>(o, make_iseq<9>{}); op_ldsBs<13>(o, make_iseq<22>{});
  asm volatile("ds_write_b128 %0, %1 offset:4096\n\tds_write_b128 %0, %2 offset:5120\n\tds_write_b32 %0, %3 offset:6144" :: "v"(o.lb5), "v"(o.gv[1][0]), "v"(o.gv[1][1]), "v"(o.gs[1]) : "memory");
  o.idx = (o.va & 128u) ? 0xeeeeeeeeu : 0x44444444u; o.D[0][4] = f32x4{0.f, 0.f, 0.f, 0.f};
  asm volatile("" : "+v"(o.idx), "+v"(o.D[0][4]));
  redo_zero<240>(0u, make_iseq<12>{});
  PS_AT(45); ps_touch(20u + wid);
  GAP();
  {
    u32x4 sr = o.srdL; sr[2] = o.sf + 4u;
    redo_reqs<35 + FNR>(o, make_iseq<OP_NR - 35 - FNR>{});
    asm volatile("s_waitcnt vmcnt(%7)\n\tbuffer_store_dword %2, %3, %4, %5 offen sc1\n\ts_waitcnt vmcnt(%7)\n\tv_cmp_eq_u32_e64 %0, %6, %1"
                 : "=s"(o.seen), "+v"(o.fo) : "v"(tag), "v"(o.va), "s"(sr), "s"(o.sf), "s"(tag), "i"(OP_NR - 34 - FNR) : "memory");
  }
  op_there(o);
  op_reqA0(o, make_iseq<OP_NI>{});
  GAP();
  unsigned blk = blockIdx.x;
  asm volatile("" : "+s"(blk));
  const unsigned sn = o.sn;
  asm volatile("s_waitcnt lgkmcnt(0)" : "+v"(o.fb[0]), "+v"(o.fb[1]), "+v"(o.fb[2]), "+v"(o.fb[3]), "+v"(o.fb[4]), "+v"(o.fb[5]), "+v"(o.fb[6]), "+v"(o.fb[7]), "+v"(o.fb[8]), "+v"(o.fb[9]), "+v"(o.fb[10]),
               "+v"(o.fb[11]), "+v"(o.fb[12]), "+v"(o.fb[13]), "+v"(o.fb[14]), "+v"(o.fb[15]), "+v"(o.fb[16]), "+v"(o.fb[17]), "+v"(o.fb[18]), "+v"(o.fb[19]), "+v"(o.fb[20]), "+v"(o.fb[21]) :: "memory");
  if (__builtin_expect(o.bad != 0u, 0)) {
    unsigned va = o.va;
    asm volatile("" : "+v"(va));
    const unsigned lane = va >> 4, row0 = (o.sa >> 13 ^ 64u) & 2032u, ax[2] = {op_ax<0>(va), op_ax<1>(va)};
    unsigned selr, an[2] = {ax[0], ax[1]};
    wait_flag(o.srdS, 8u * (lane & 7u) + 4u, tag);
    wait_flag(o.srdS, 4u * H_X + 288u * ((blk >> 3) & 7u) + 16u * (lane < 18u ? lane : 0u), tag);
    asm volatile("buffer_load_dword %0, %1, %2, 0 offen sc1\n\ts_waitcnt vmcnt(0)" : "=v"(selr) : "v"(8u * (lane & 7u)), "s"(o.srdL) : "memory");
#pragma nounroll
    for (unsigned t = 0; t < (unsigned)NSEL; ++t) {
      const unsigned ro = (((unsigned)__builtin_amdgcn_readlane(selr, t) & 0xffffu) - row0) * XRS_;
#pragma unroll
      for (int j = 0; j < 2; ++j) { const unsigned d = ax[j] - ro; an[j] = d < XRS_ ? (SEQ + t - row0) * XRS_ + d : an[j]; }
    }
    asm volatile("buffer_load_dwordx4 %0, %3, %5, %6 offen sc1\n\tbuffer_load_dwordx4 %1, %3, %5, %6 offen offset:64 sc1\n\tbuffer_load_dword %2, %4, %5, %6 offen sc1\n\ts_waitcnt vmcnt(0)"
                 : "=&v"(o.gv[0][0]), "=&v"(o.gv[0][1]), "=&v"(o.gs[0]) : "v"(an[0]), "v"(an[1]), "s"(o.srdS), "s"(sn + WS_XN) : "memory");
  }
  __amdgpu_buffer_rsrc_t srdO; unsigned ax[2], fes, t;
#define SC(g, x, e) if constexpr (M == (g)) { x = (e); asm volatile("" : "+s"(x)); }
#define SA(g, x, e) if constexpr (M == (g)) x = (e);
#define VU(g, x, e) if constexpr (M == (g)) { x = (e); asm volatile("" :: "v"(x)); GAP(); }
#define VC(g, x, e) if constexpr (M == (g)) { x = (e); asm volatile("" : "+v"(x)); }
  auto prep = [&](auto M_) __attribute__((always_inline)) {
    constexpr int M = decltype(M_)::value;
    if constexpr (M < 6) { acc_write<op_ac(1, 1 + M / 2, 4) + 2 * (M & 1)>(0u); acc_write<op_ac(1, 1 + M / 2, 4) + 2 * (M & 1) + 1>(0u); }
    VC(6, o.ra[0], o.lb5 + BP_W * ((wid ^ 1u) - wid)) VC(7, o.ra[1], o.lb5 + BP_W * ((wid ^ 2u) - wid)) VC(8, o.ra[2], o.lb2 + 8192u * ((wid ^ 3u) - wid))
    SC(9, t, (4u * blk + wid) << 12) SC(10, o.sw, t + WS_EX) SC(11, o.si, (t ^ 65536u) + WS_EX) VC(19, o.v12, o.va - (o.va >> 2))
    SC(0, o.ks[0], 0x07060501u) SC(1, o.ks[1], 0x07060502u) SC(2, o.ks[2], 0x07060503u) SC(3, o.ks[3], 0x0c04000cu) SC(4, o.ks[4], 0x0402010cu)
    SC(12, o.sg, 128u * blk + 4u * H_E) SC(12, fes, o.sg ^ 512u) SC(13, t, (o.sg + 4u) & ((int)(wid - 1u) >> 31)) SC(14, srdO, mk_srd(out, SEQ * DM * 2))
    VC(15, ax[0], op_ax<0>(o.va)) VC(16, ax[1], op_ax<1>(o.va))
    VC(17, o.D[1][4], (f32x4{0.f, 0.f, 0.f, 0.f}))
    if constexpr (M == 29000) { unsigned& d = fe; const u32x4 sh = o.srdS; const unsigned so = fes; asm volatile("buffer_load_dword %0, off, %1, %2 sc1" : "=v"(d) : "s"(sh), "s"(so) : "memory"); }
    if constexpr (M == 28000) { unsigned& d = fe; asm volatile("s_waitcnt vmcnt(%1)" : "+v"(d) : "i"(OP_FEW) : "memory"); t = rfl(fe); asm volatile("" :: "s"(t)); }
    if constexpr (M == 28001) { if (__builtin_expect(t != tag, 0)) wait_flag(o.srdS, fes, tag); }
  };
  asm volatile("ps"); prep(IC(9)); asm volatile("ps"); prep(IC(10)); asm volatile("ps"); prep(IC(11)); asm volatile("ps"); prep(IC(12)); asm volatile("ps"); prep(IC(13)); asm volatile("ps"); prep(IC(14));
  GAP();
  op_again<0, OP_NI>(o);
  o.sl += 128u; o.srdL[2] += 128u;
  asm volatile("ps");
  auto step = [&](auto ST) __attribute__((always_inline)) {
    constexpr int st = decltype(ST)::value;
    if constexpr (st == 16) {
    }
    if constexpr (st == 24) {
    }
    if constexpr (st == 30) {
    }
    if constexpr (st == 8) { asm volatile("s_waitcnt vmcnt(%0)" :: "i"(OP_P4W) : "memory"); op_again<8, 4 * (8 - OP_DS)>(o); }
    if constexpr (st == 0 || st == 8) op_wait<st>(o);
    if constexpr (st == OP_DS) asm volatile("" : "+v"(o.fo));
    if constexpr (st == 29) asm volatile("" : "+v"(o.gv[1][0]), "+v"(o.gv[1][1]), "+v"(o.gs[1]));
    GAP();
    op_qs<st>(o, prep, make_iseq<18>{});
    GAP();
    if constexpr (st == OP_SF - 2) { o.tg = tag; o.srdL[2] = t; asm volatile("" : "+v"(o.tg), "+s"(o.srdL)); }
    GAP();
  };
  for_seq(step, make_iseq<32>{});
  op_qs<32>(o, prep, make_iseq<1>{});
  auto fin2 = [&](const float s0, const float s1, const int wd) __attribute__((always_inline)) {
    const unsigned sp = cvtpk(s0, s1), pp = cvtpk(bflo(sp) * o.gx[2 * wd], bfhi(sp) * o.gx[2 * wd + 1]);
    return cvtpk(o.gx[18 + 2 * wd] + bflo(pp), o.gx[19 + 2 * wd] + bfhi(pp));
  };
  auto fin = [&](auto J_) __attribute__((always_inline)) {
    constexpr int j = decltype(J_)::value;
    const f32x4 s = o.D[1][2 * j] + o.T[2 * j];
    const __amdgpu_buffer_rsrc_t so = srdO; const unsigned sc = sn, va = ax[j >> 1];
    if constexpr (j < 2) {
      const f32x4 r = o.D[1][2 * j + 1] + o.T[2 * j + 1];
      const u32x4 ov = {fin2(s[0], s[1], 4 * j), fin2(s[2], s[3], 4 * j + 1), fin2(r[0], r[1], 4 * j + 2), fin2(r[2], r[3], 4 * j + 3)};
      asm volatile("buffer_store_dwordx4 %0, %1, %2, %3 offen offset:%c4\n\ts_nop 1" :: "v"(ov), "v"(va), "s"(so), "s"(sc), "i"(64 * j) : "memory");
    } else {
      auto p0 = __builtin_amdgcn_permlane32_swap(__float_as_uint(s[0]), __float_as_uint(s[2]), false, false), p1 = __builtin_amdgcn_permlane32_swap(__float_as_uint(s[1]), __float_as_uint(s[3]), false, false);
      const unsigned ov = fin2((__uint_as_float(p0[0]) + __uint_as_float(p0[1])) + o.pv[2][3], (__uint_as_float(p1[0]) + __uint_as_float(p1[1])) + o.pv[3][3], 8);
      asm volatile("buffer_store_dword %0, %1, %2, %3 offen" :: "v"(ov), "v"(va), "s"(so), "s"(sc) : "memory");
    }
  };
  fin(IC(2)); fin(IC(0));
  GAP();
  fin(IC(1));
}

extern "C" __global__ __launch_bounds__(256, 1) __attribute__((aligned(4096)))
void k_attn(char* __restrict__ ws, const u16* __restrict__ wq, const u16* __restrict__ xres, const u16* __restrict__ w_r, const u16* __restrict__ Kc, const u16* __restrict__ Vc,
            const u16* __restrict__ wo, const u16* __restrict__ wg, const u16* __restrict__ wu, const u16* __restrict__ wd, u16* __restrict__ out  ) {
  asm volatile(PS_MACROS ::: "memory");
  extern __shared__ __attribute__((aligned(16))) char lds[];
  const unsigned tid = threadIdx.x, lane = tid & 63u, wid = tid >> 6, l32 = lane & 31u, hi = lane >> 5;
  constexpr int T = NT;
  const float C = 0.12751743082f;
  W4C c;
  c.pdum = c.ldsb + PF_DUM;
  asm volatile("" : "+s"(c.pdum) : "s"((unsigned)(size_t)lds));
  c.m0k = rfl(c.ldsb + KRING + 4096u * wid);
  c.sofk = rfl(KRS * 16u * wid);

  const unsigned blk = blockIdx.x, xg = blk & 7u, ii = blk >> 3;
  const unsigned kvh = 2u * xg + (ii >> 4), h = 2u * kvh + ((ii >> 3) & 1u), yb = ii & 7u;
  const unsigned row0 = 256u * yb + 64u * wid;
  const bool moe = blk < (unsigned)MOE_WG;
  const bool ret = !moe && blk < 208u;
  unsigned tag = 0u, gen;
  const unsigned lrow = 8u * blk + 2u * wid + hi;
  unsigned psc, cwo; u32x4 srdC;
  {
    QP p; LNJ lj; u32x4 cw; unsigned long long no; unsigned pb0, pb1, pb2, pb3, csw, vg, vs, kk, bad, l16, g16, ta, tb; unsigned long long pbase;
    lj.up = hi != 0u;
    const unsigned vl = 16u * l32 + XRS * hi, vl3 = 4u * l32 + XRS * hi, srow = XRS * 8u * blk + XRS * 2u * rfl(wid);
    {
      const unsigned r8 = (lane >> 2) & 7u, vws = WRS * ((r8 & 3u) + 8u * (r8 >> 2)) + 64u * hi + 16u * ((lane & 3u) ^ ((lane >> 4) & 1u));
      p.vw[0] = vws + 896u; p.vw[1] = vws; p.vw[2] = vws + (4u * WRS - 2048u + 896u); p.vw[3] = vws + (4u * WRS - 2048u);
#pragma unroll
      for (int q = 0; q < 4; ++q) p.vx[q] = XRS * (lane >> 3) + 16u * ((lane & 7u) ^ (4u * (q & 1) + (lane >> 4))) + (8u * XRS - 1024u) * q;
    }
    p.m0x = rfl(c.ldsb + 0x4000u + 8192u * wid);
    p.sw[1] = rfl(WRS * 16u * wid + 256u * h);
    p.sx[1] = rfl(XRS * row0);
    kk = (yb ^ (ii >> 3)) & 1u;
    asm volatile("s_nop 4" : "+s"(c.m0k), "+s"(c.sofk), "+s"(p.m0x), "+s"(p.sw[1]), "+s"(p.sx[1]), "+s"(kk));
    asm volatile("" : "+v"(p.vw[0]), "+v"(p.vw[1]), "+v"(p.vw[2]), "+v"(p.vw[3]), "+v"(p.vx[0]), "+v"(p.vx[1]), "+v"(p.vx[2]), "+v"(p.vx[3]));
    GAP();
    p.srdW = __builtin_amdgcn_make_buffer_rsrc((void*)((const char*)wq - 896), (short)0, DM * HQ * DH * 2 + 896, SRD_WORD3);
    p.srdX = __builtin_amdgcn_make_buffer_rsrc((void*)xres, (short)0, SEQ * DM * 2, SRD_WORD3);
    auto vm = [&](auto I_) __attribute__((always_inline)) {
      constexpr int I = decltype(I_)::value % 1000; constexpr VOp o = vop(I); constexpr int q = o.q & 3;
      auto dma = [&](const unsigned m0, auto MO_, const unsigned vo, const __amdgpu_buffer_rsrc_t srd, const unsigned so) __attribute__((always_inline)) {
        if constexpr (decltype(I_)::value >= 1000 || (o.pos < 0 && vq_newm0(I))) asm volatile("s_add_u32 m0, %0, %1" :: "s"(m0), "i"(decltype(MO_)::value) : "m0", "scc", "memory");
        if constexpr (decltype(I_)::value < 1000) {
          if constexpr (vq_newm0(I)) { GAP(); p.so = so; }
          if constexpr (o.pos < 0 && vq_newm0(I)) asm volatile("s_nop 0");
          const unsigned s_ = p.so;
          asm volatile("buffer_load_dwordx4 %0, %1, %2 offen offset:%c3 lds" :: "v"(vo), "s"(srd), "s"(s_), "i"(1024 * q) : "memory");
        }
      };
      constexpr int kd = o.st == 0 ? 1 : (o.st & 1) ? 2 : 0;
      if constexpr (o.kind == VK_S && o.q < 4) dma(c.m0k, IC(QP_STG * (o.st % 3)), p.vw[q], p.srdW, p.sw[kd] + (o.st << 19));
      else if constexpr (o.kind == VK_S) dma(p.m0x, IC(QP_STG * (o.st % 3) + (o.q >= 8 ? 4096u : 0u)), p.vx[q], p.srdX, p.sx[kd] + ((o.st << 7) + (o.q >= 8 ? 32u * XRS : 0u)));
      else if constexpr (o.kind == VK_W8) dma(c.m0k, IC(QP_W8), p.vw[q], p.srdW, p.sw[kd] + (o.st << 19));
      else if constexpr (o.kind == VK_KV && o.q < 8) dma(c.m0k, IC(0x4000u * (o.q >> 2)), (q & 2) ? c.ksx[q & 1] : c.vksrc[q & 1], c.srdK, KTILEB * (o.q >> 2) + c.sofk);
      else if constexpr (o.kind == VK_KV) dma(c.m0k, IC(VRING), (q & 1) ? c.vvsrc : c.vvsrc0, c.srdV, c.sofk);
      else if constexpr (o.kind == VK_V1) dma(c.m0k, IC(VRING + 0x4000), (q & 1) ? c.vvsrc : c.vvsrc0, c.srdV, KTILEB + c.sofk);
      else if constexpr (o.kind == VK_K2) dma(c.m0k, IC(0), (q & 2) ? c.ksx[q & 1] : c.vksrc[q & 1], c.srdK, KTILEB * 2u + c.sofk);
      else if constexpr (o.kind == VK_PF)
        asm volatile("s_mov_b32 m0, %0\n\ts_add_u32 %1, %1, %4\n\tbuffer_load_dwordx4 %2, %3, %1 offen sc1 lds\n\ts_add_u32 %0, %0, 0x400"
                     : "+s"(c.pfm0), "+s"(c.pfo) : "v"(c.pfv), "s"(c.srdP), "s"(c.pfst) : "m0", "memory", "scc");
      else if constexpr (o.kind == VK_CW) {
        unsigned long long pa; unsigned po;
        asm volatile("s_getpc_b64 s[20:21]\n\ts_add_u32 s20, s20, k_attn@rel32@lo+4\n\ts_addc_u32 s21, s21, k_attn@rel32@hi+12\n\ts_mov_b64 %0, s[20:21]" : "=s"(pa) :: "scc", "s20", "s21");
        asm volatile("s_mov_b32 %0, .Lk_end-k_attn" : "=s"(po));
        srdC = mk_srd4((const void*)pa, po); cwo = (blk >> 3) * 4096u + 1024u * rfl(wid);
        asm volatile("" : "+s"(srdC), "+s"(cwo));
        asm volatile("buffer_load_dwordx4 %0, %1, %2, %3 offen" : "=&v"(cw) : "v"(16u * lane), "s"(srdC), "s"(cwo) : "memory");
      } else if constexpr (o.kind == VK_GEN)
        asm volatile("buffer_load_dword %0, off, %1, 0 offset:%c2 sc1" : "=&v"(gen) : "s"(c.srdS), "i"(4 * H_G) : "memory");
      else if constexpr (o.kind == VK_A && o.q < 2)
        asm volatile("buffer_load_dwordx4 %0, %1, %2, %3 offen offset:%c4" : "=&v"(lj.a[o.q]) : "v"(vl), "s"(p.srdX), "s"(srow), "i"(512 * o.q) : "memory");
      else if constexpr (o.kind == VK_A) asm volatile("buffer_load_dword %0, %1, %2, %3 offen offset:1024" : "=&v"(lj.a2) : "v"(vl3), "s"(p.srdX), "s"(srow) : "memory");
      else if constexpr (o.kind == VK_WR && o.q < 2)
        asm volatile("buffer_load_dwordx4 %0, %1, %2, 0 offen offset:%c3" : "=&v"(lj.w[o.q]) : "v"(16u * l32), "s"(__builtin_amdgcn_make_buffer_rsrc((void*)w_r, (short)0, DM * 2, SRD_WORD3)), "i"(512 * o.q) : "memory");
      else if constexpr (o.kind == VK_WR)
        asm volatile("buffer_load_dword %0, %1, %2, 0 offen offset:1024" : "=&v"(lj.w2) : "v"(4u * l32), "s"(__builtin_amdgcn_make_buffer_rsrc((void*)w_r, (short)0, DM * 2, SRD_WORD3)) : "memory");
      else if constexpr (o.kind == VK_STS) {
        const u32x4 g = {__float_as_uint(C * lj.rden), tag, __float_as_uint(-lj.mb), tag};
        asm volatile("buffer_store_dwordx4 %0, %1, %2, %3 offen sc1\n\ts_nop 1" :: "v"(g), "v"(2u * vg), "s"(c.srdS), "s"(WS_ST) : "memory");
      } else if constexpr (o.kind == VK_KEY) {
        const u32x2 g = {lj.key, tag};
        asm volatile("buffer_store_dwordx2 %0, %1, %2, %3 offen sc1" :: "v"(g), "v"(vg), "s"(c.srdS), "s"(WS_KEY) : "memory");
      } else if constexpr (o.kind == VK_GAT) {
        asm volatile("buffer_load_dwordx4 %0, %1, %2, 0 offen offset:%c3 sc1" : "=&v"(p.g[o.q]) : "v"(vs), "s"(c.srdS), "i"(256 * o.q) : "memory");
      } else if constexpr (o.kind == VK_XNS) {
        if constexpr (o.q < 2) asm volatile("buffer_store_dwordx4 %0, %1, %2, %3 offen offset:%c4 sc1\n\ts_nop 1" :: "v"(lj.a[o.q & 1]), "v"(vl), "s"(c.srdS), "s"(srow + WS_XN), "i"(512 * o.q) : "memory");
        else                   asm volatile("buffer_store_dword %0, %1, %2, %3 offen offset:1024 sc1" :: "v"(lj.a2), "v"(vl3), "s"(c.srdS), "s"(srow + WS_XN) : "memory");
      }
    };
    auto hook = [&](auto P_, auto J_) __attribute__((always_inline)) {
      constexpr int pos = decltype(P_)::value, J = decltype(J_)::value;
      if constexpr (pos == 70) ps_touch(psc);
      if constexpr (pos == 71) ps_touch(psc & 3u);
      if constexpr (pos == 1) {
        asm volatile("s_waitcnt vmcnt(%5)" : "+v"(gen), "+v"(lj.a[0]), "+v"(lj.a[1]), "+v"(lj.a2), "+v"(cw) : "i"(vq_wait(VK_CW, 0, J + 1)) : "memory");
        tag = rfl(gen) * 1664525u + 1013904223u;
        asm volatile("" : "+s"(tag));
      } else if constexpr (pos == 45) asm volatile("" : "+v"(lj.w[0]), "+v"(lj.w[1]), "+v"(lj.w2));
      else if constexpr (pos >= 100 && pos < 130) {
        constexpr int M = pos - 100;
        VC(0, l16, tid & 15u) VC(0, g16, (tid >> 4) & 3u) VC(1, ta, l16 ^ g16) VC(1, tb, (tid >> 4) & 12u) VU(2, tb, ta ^ tb) VC(2, tb, 16u * l16 + tb) VU(3, c.vkb[0], tb << 4) VC(3, c.vkb[1], c.vkb[0] ^ 64u)
        VC(4, c.vkb[2], c.vkb[0] ^ 128u) VC(4, c.vkb[3], c.vkb[0] ^ 192u) VU(5, ta, 16u * g16 + ta) VC(5, c.vksrc[0], ta << 4) VC(6, c.vksrc[1], c.vksrc[0] ^ 64u) VC(6, c.ksx[0], c.vksrc[0] ^ 128u)
        VC(7, c.ksx[1], c.vksrc[0] ^ 192u) VC(7, ta, l16 & 3u) VU(8, tb, l16 & 12u) VC(8, tb, 2u * tb + ta) VU(9, l16, g16 & 1u) VC(9, tb, __umul24(l16, 36u) + tb) VU(10, g16, g16 & 2u) VC(10, tb, (g16 << 7) + tb)
        VC(11, c.vvb[0], 8u * tb + VRING) VC(12, c.vvb[1], c.vvb[0] ^ 32u) VC(12, l16, l16 << 1) VU(13, ta, ta ^ l16) VC(13, ta, 2u * g16 + ta) VU(14, tb, tid & 28u) VC(14, ta, 4u * tb + ta)
        VU(15, c.vvsrc, ta << 4) VC(15, c.vvsrc0, c.vvsrc + 896u)
      } else if constexpr (pos == -12) {
        const unsigned rs = moe ? FF * 2u : ret ? DM * 2u : 64u;
        const unsigned r16 = ret ? ((lane >> 2) & 3u) | (lane & 16u) | ((lane >> 3) & 4u) : lane >> 2;
        c.pfv = rs * r16 + 16u * ((lane & 3u) ^ ((lane >> 4) & 2u));
        asm volatile("" : "+v"(c.pfv));
      } else if constexpr (pos == -10) {
        const unsigned w = rfl(wid), fb = (blk & 7u) * 18u + (blk >> 3);
        pb0 = 64u * fb + 16u * FF * 2u * (w & 1u); pb1 = ((blk - 144u) * 4u + w) * 18432u - 9216u * (w & 1u); pb2 = ((blk - 208u) * 4u + w) * 27648u; pb3 = 18432u * w;
        const u16 *a0 = wg, *a1 = wu;
        asm volatile("s_cmp_lt_u32 %1, 2\n\ts_cselect_b64 %0, %2, %3" : "=s"(pbase) : "s"(w), "s"(a0), "s"(a1) : "scc");
        asm volatile("" : "+s"(pb0), "+s"(pb1), "+s"(pb2), "+s"(pb3));
      } else if constexpr (pos == -11) {
        const u16 *a0 = wo, *a1 = wd; const unsigned b = blk;
        unsigned long long pb; unsigned nb, st, np;
        asm volatile("s_cmp_lt_u32 %8, 208\n\ts_cselect_b64 %0, %9, %10\n\ts_cselect_b32 %5, %5, %6\n\ts_mov_b32 %1, %12\n\ts_cselect_b32 %2, 64, %13\n\ts_cselect_b32 %7, %7, 0\n\ts_cselect_b32 %3, %11, %14\n\t"
                     "s_cmp_lt_u32 %8, 144\n\ts_cselect_b64 %0, %15, %0\n\ts_cselect_b32 %5, %4, %5\n\ts_cselect_b32 %1, %16, %1\n\ts_cselect_b32 %2, %17, %2"
                     : "=&s"(pb), "=&s"(nb), "=&s"(st), "=&s"(np), "+s"(pb0), "+s"(pb1), "+s"(pb2), "+s"(pb3)
                     : "s"(b), "s"(a0), "s"(a1), "s"(18432u), "s"(27648u), "s"(1024u), "s"(20480u), "s"(pbase), "s"(559u * (FF * 2u) + 64u), "s"(FF * 2u * 32u) : "scc");
        pbase = pb; pb0 = nb; pb2 = np; c.pfst = st;
      } else if constexpr (pos == -18) {
        c.srdP = mk_srd4((const void*)(pbase + pb1), pb0);
        c.pfo = 0u - c.pfst;
        c.pfm0 = c.ldsb + SD_BASE + pb3; c.pfend = c.pfm0 + pb2;
        asm volatile("" : "+s"(c.srdP), "+s"(c.pfst), "+s"(c.pfo), "+s"(c.pfm0), "+s"(c.pfend));
      } else if constexpr (pos == -19) {
        unsigned kvo = kvh; asm volatile("" : "+s"(kvo));
        c.srdK = __builtin_amdgcn_make_buffer_rsrc((void*)((const char*)Kc + (size_t)kvo * SEQ * 256), (short)0, SEQ * 256, SRD_WORD3);
        c.srdV = __builtin_amdgcn_make_buffer_rsrc((void*)((const char*)Vc + (size_t)kvo * SEQ * 256 - 896), (short)0, SEQ * 256 + 896, SRD_WORD3);
        asm volatile("" : "+s"(c.srdK), "+s"(c.srdV));
      } else if constexpr (pos == -20) {
        unsigned kvo = kvh; asm volatile("" : "+s"(kvo));
        const unsigned id = ((blk >> 3) & 15u) * 4u + rfl(wid);
        const u16 *a0 = Kc, *a1 = Vc; unsigned long long pb;
        asm volatile("s_cmp_lt_u32 %1, 32\n\ts_cselect_b64 %0, %2, %3" : "=s"(pb) : "s"(id), "s"(a0), "s"(a1) : "scc");
        c.srdF = __builtin_amdgcn_make_buffer_rsrc((void*)(pb + (size_t)kvo * SEQ * 256), (short)0, SEQ * 256, SRD_WORD3);
        c.sfo = (c.sofk & ~(KTILEB - 1u)) + KTILEB * (LAH + ((id >> 4) & 1u) - (id >> 5)) + 1024u * (id & 15u);
        asm volatile("" : "+s"(c.srdF), "+s"(c.sfo));
      } else if constexpr (pos == 190) {
        asm volatile("s_waitcnt vmcnt(%4)" : "+v"(p.g[0]), "+v"(p.g[1]), "+v"(p.g[2]), "+v"(p.g[3]) : "i"(vq_wait(VK_GAT, 0, J + 1)) : "memory");
        bad = (p.g[0][1] ^ tag) | (p.g[0][3] ^ tag); csw = (p.g[1][1] ^ tag) | (p.g[1][3] ^ tag); PIN2(bad, csw);
      } else if constexpr (pos == 191 || pos == 192) { bad |= csw; csw = (p.g[pos - 189][1] ^ tag) | (p.g[pos - 189][3] ^ tag); PIN2(bad, csw); }
      else if constexpr (pos == 193) { no = __ballot((bad | csw) != 0u); asm volatile("" : "+s"(no)); }
      else if constexpr (pos == 194) {
        if (__builtin_expect(no != 0ull, 0)) {
          const unsigned v = vs; const u32x4 sr = c.srdS;
#pragma nounroll
          for (unsigned spin = 0; spin < (1u << 20); ++spin) {
            asm volatile("buffer_load_dwordx4 %0, %4, %5, 0 offen sc1\n\tbuffer_load_dwordx4 %1, %4, %5, 0 offen offset:256 sc1\n\tbuffer_load_dwordx4 %2, %4, %5, 0 offen offset:512 sc1\n\t"
                         "buffer_load_dwordx4 %3, %4, %5, 0 offen offset:768 sc1\n\ts_waitcnt vmcnt(0)" : "+v"(p.g[0]), "+v"(p.g[1]), "+v"(p.g[2]), "+v"(p.g[3]) : "v"(v), "s"(sr) : "memory");
            unsigned x = 0u;
#pragma unroll
            for (int q = 0; q < 4; ++q) x |= (p.g[q][1] ^ tag) | (p.g[q][3] ^ tag);
            if (__all(x == 0u)) break;
            __builtin_amdgcn_s_sleep(4);
          }
        }
      } else if constexpr (pos == 220) { lj.t0 = p.cs[0][0] + p.cs[0][1]; asm volatile("" :: "v"(lj.t0), "v"(p.cs[0])); GAP(); lj.t1 = p.cs[1][0] + p.cs[1][1]; asm volatile("" :: "v"(lj.t1), "v"(p.cs[1])); }
      else if constexpr (pos == 221) { lj.t0 = (lane & 16u) ? lj.t1 : lj.t0; asm volatile("" :: "v"(lj.t0)); }
      else if constexpr (pos == 222) lds_st32(p.ca[1], __float_as_uint(lj.t0));
      else if constexpr (pos == 223) { p.ca[2] = p.ca[0] ^ 256u; p.ca[3] = p.ca[0] ^ 384u; PIN2(p.ca[2], p.ca[3]); }
      else if constexpr (pos == 225) { p.ca[1] = p.ca[0] ^ 128u; asm volatile("" : "+v"(p.ca[1])); }
      else if constexpr (pos == -21) {
        vg = l32 == 0u ? 8u * lrow : 0x20000000u; vs = WS_ST + 16u * (row0 + (lane & 15u));
        p.ca[0] = QP_CS + 128u * wid + (lane & 48u); p.ca[1] = QP_CS + 128u * wid + 4u * l32;
        p.sw[0] = p.sw[1] - (kk << 19); p.sw[2] = p.sw[1] + (kk << 19); p.sx[0] = p.sx[1] - (kk << 7); p.sx[2] = p.sx[1] + (kk << 7);
        asm volatile("" : "+v"(vg), "+v"(vs), "+v"(p.ca[0]), "+v"(p.ca[1]), "+s"(p.sw[0]), "+s"(p.sw[2]), "+s"(p.sx[0]), "+s"(p.sx[2]));
        const unsigned a0 = ((lane & 1u) << 3) | ((lane & 2u) << 4) | (((lane >> 2) & 3u) << 6) | (((lane >> 4) & 1u) * 272u) | (hi << 12) | (wid << 9);
#pragma unroll
        for (int b = 0; b < 8; ++b) { p.aw[0][b] = a0 ^ (16u * (b & 1) + 512u * (b >> 1)); p.aw[1][b] = p.aw[0][b] + 2u * QP_STG; PIN2(p.aw[0][b], p.aw[1][b]); }
#pragma unroll
        for (int ks = 0; ks < 2; ++ks) { p.ax[0][ks] = 0x4000u + 8192u * wid + 128u * (lane & 15u) + 16u * ((4u * ks + (lane >> 4)) ^ ((lane >> 1) & 7u)); p.ax[1][ks] = p.ax[0][ks] + 2u * QP_STG; PIN2(p.ax[0][ks], p.ax[1][ks]); }
        const unsigned on = (lane & 2u) == 0u ? 0x3f803f80u : 0u; c.ONE = u32x4{on, on, on, on}; c.IDX = (lane & 1u) ? 0xeeeeeeeeu : 0x44444444u;
        p.cs[0] = p.cs[1] = f32x4{0.f, 0.f, 0.f, 0.f};
        asm volatile("" : "+v"(c.ONE), "+v"(c.IDX), "+v"(p.cs[0]), "+v"(p.cs[1]) :: W4_ACLOB, W4_ACLOB_HI);
      }
    };
    asm volatile("" :: "s"(ws), "s"(Kc), "s"(Vc), "s"(wo), "s"(wg), "s"(wu), "s"(wd), "s"(out), "s"(w_r));
    vq_issue<-3>(vm);
    asm volatile("s_branch 1f\n.Lps_ret: s_setpc_b64 vcc\n.Lps_tab:\n.irp n,0,1,2,3,4,5,6,7,8,9,10,11,12,13,14,15,16,17,18,19,20,21,22,23\ns_branch .Lpe_\\n\n.endr\n1:" ::: "memory");
    psc = rfl(wid) + 16u - 4u * (((blk - 144u) >> 31) + ((blk - 208u) >> 31) + ((blk - RED1) >> 31));
    c.srdS = mk_srd4(ws, WS_END);
    asm volatile("" : "+s"(psc), "+s"(c.srdS));
    hook(IC(-10), IC(0)); hook(IC(-11), IC(0)); hook(IC(-12), IC(0)); hook(IC(-18), IC(0)); hook(IC(-19), IC(0)); hook(IC(-20), IC(0)); hook(IC(-21), IC(0));
    vq_issue<-2>(vm);
    GAP();
    vq_issue<-1>(vm);
    GAP();
    asm volatile("s_waitcnt vmcnt(%0)\n\ts_barrier" :: "i"(vq_wait(VK_S, 0, 0)) : "memory");
    q_first(p, c, vm, hook, lj, make_iseq<NQ0>{});
    q_gaps<0>(p, c, vm, hook, lj, lrow, make_iseq<240>{});
    q_gaps<240>(p, c, vm, hook, lj, lrow, make_iseq<PJ - 240>{});
    q_gaps<PJ>(p, c, vm, hook, lj, lrow, make_iseq<150>{});
    q_gaps<PJ + 150>(p, c, vm, hook, lj, lrow, make_iseq<NJ - PJ - 150>{});
  }
  {
    c.L[0] = c.L[1] = c.L[2] = c.L[3] = f32x4{0.f, 0.f, 0.f, 0.f};
    asm volatile("" : "+v"(c.L[0]), "+v"(c.L[1]), "+v"(c.L[2]), "+v"(c.L[3]));
  }
  unsigned rstate = 3u;
  auto route = [&]() __attribute__((always_inline)) {
    unsigned lq;
    asm volatile("v_mbcnt_lo_u32_b32 %0, -1, 0\n\tv_mbcnt_hi_u32_b32 %0, -1, %0" : "=v"(lq));
    const unsigned w = rfl(wid), kb_ = c.ldsb + MO_KEY + 4096u * w;
    bool req = rstate == 0u;
    if (rstate == 1u) {
      u32x4 g[4];
      asm volatile("ds_read_b128 %0, %4\n\tds_read_b128 %1, %4 offset:1024\n\tds_read_b128 %2, %4 offset:2048\n\tds_read_b128 %3, %4 offset:3072\n\ts_waitcnt lgkmcnt(0)"
                   : "=&v"(g[0]), "=&v"(g[1]), "=&v"(g[2]), "=&v"(g[3]) : "v"(kb_ + 16u * lq) : "memory");
      bool ok = true;
#pragma unroll
      for (int j = 0; j < 4; ++j) ok = ok && g[j][1] == tag && g[j][3] == tag;
      if (__all(ok)) {
        unsigned k[8] = {g[0][0], g[0][2], g[1][0], g[1][2], g[2][0], g[2][2], g[3][0], g[3][2]};
        ce(k[0], k[1]); ce(k[2], k[3]); ce(k[4], k[5]); ce(k[6], k[7]);
        ce(k[0], k[2]); ce(k[1], k[3]); ce(k[4], k[6]); ce(k[5], k[7]);
        ce(k[1], k[2]); ce(k[5], k[6]); ce(k[0], k[4]); ce(k[3], k[7]);
        ce(k[1], k[5]); ce(k[2], k[6]);
        ce(k[1], k[4]); ce(k[3], k[6]);
        ce(k[2], k[4]); ce(k[3], k[5]);
        ce(k[3], k[4]);
        unsigned cnd = 0u;
#pragma nounroll
        for (unsigned r = 0; r < (unsigned)NSEL; ++r) {
          unsigned m = k[0], t;
          t = (unsigned)__builtin_amdgcn_update_dpp(0, (int)m, 0xB1, 0xf, 0xf, false); m = t > m ? t : m;
          t = (unsigned)__builtin_amdgcn_update_dpp(0, (int)m, 0x4E, 0xf, 0xf, false); m = t > m ? t : m;
          t = (unsigned)__builtin_amdgcn_update_dpp(0, (int)m, 0x141, 0xf, 0xf, false); m = t > m ? t : m;
          t = (unsigned)__builtin_amdgcn_update_dpp(0, (int)m, 0x140, 0xf, 0xf, false); m = t > m ? t : m;
          cnd = (lq & 15u) == (unsigned)r ? m : cnd;
          const bool pp = k[0] == m;
#pragma unroll
          for (int i = 0; i < 7; ++i) k[i] = pp ? k[i + 1] : k[i];
        }
        lds_st32(c.ldsb + MO_RS + ((lq & 8u) == 0u ? 64u + 128u * w + 2u * (lq & 48u) + 4u * (lq & 7u) : 640u + 4u * lq), cnd);
        asm volatile("s_waitcnt lgkmcnt(0)" ::: "memory");
        lds_st32(c.ldsb + MO_RS + 4u * w, 1u);
        rstate = 2u;
      } else req = true;
    } else if (rstate == 2u) {
      u32x4 fl;
      asm volatile("ds_read_b128 %0, %1\n\ts_waitcnt lgkmcnt(0)" : "=v"(fl) : "v"(c.ldsb + MO_RS) : "memory");
      if (rfl(fl[0] & fl[1] & fl[2] & fl[3]) != 0u) {
        unsigned cv, rank = 0u;
        asm volatile("ds_read_b32 %0, %1" : "=v"(cv) : "v"(c.ldsb + MO_RS + 64u + 128u * w + 4u * (lq & 31u)) : "memory");
#pragma nounroll
        for (unsigned h = 0; h < 4u; ++h) {
          u32x4 c4v[4];
          asm volatile("ds_read_b128 %0, %5\n\tds_read_b128 %1, %5 offset:16\n\tds_read_b128 %2, %5 offset:32\n\tds_read_b128 %3, %5 offset:48\n\ts_waitcnt lgkmcnt(0)"
                       : "=&v"(c4v[0]), "=&v"(c4v[1]), "=&v"(c4v[2]), "=&v"(c4v[3]), "+v"(cv) : "v"(c.ldsb + MO_RS + 64u + 256u * (lq >> 5) + 64u * h) : "memory");
#pragma unroll
          for (int j = 0; j < 4; ++j)
#pragma unroll
            for (int e = 0; e < 4; ++e) rank += c4v[j][e] > cv ? 1u : 0u;
        }
        auto pr = __builtin_amdgcn_permlane32_swap(rank, rank, false, false);
        rank = pr[0] + pr[1];
        {
          const float rs = rbf(__builtin_amdgcn_rcpf(1.f + __builtin_amdgcn_exp2f(key_val(cv) * -1.4426950408889634f)));
          const u32x2 gr = {(__float_as_uint(rs) & 0xffff0000u) | (unsigned)key_row(cv), tag};
          const unsigned vo = lq < 32u && rank < 8u ? 8u * rank : 0x40000000u, vg = (lq | w) == 0u ? 4u * H_G : 0x40000000u;
          asm volatile("s_nop 4\n\tbuffer_store_dwordx2 %0, %1, %3, 0 offen sc1\n\tbuffer_store_dword %4, %2, %3, 0 offen sc1\n\ts_nop 1" :: "v"(gr), "v"(vo), "v"(vg), "s"(c.srdS), "v"(gr[1]) : "memory");
        }
        rstate = 3u;
      }
    }
    if (req) {
#pragma unroll
      for (int j = 0; j < 4; ++j)
        asm volatile("s_mov_b32 m0, %0\n\ts_nop 4\n\tbuffer_load_dwordx4 %1, %2, %3 offen sc1 lds" :: "s"(kb_ + 1024u * j), "v"(16u * lq), "s"(c.srdS), "s"(WS_KEY + 4096u * w + 1024u * j) : "m0", "memory");
      rstate = 1u;
    }
  };
  const unsigned wids = rfl(wid);
  auto opq = [&](unsigned v) __attribute__((always_inline)) { asm volatile("" : "+s"(v)); return v; };
  auto lane_id = [&]() __attribute__((always_inline)) { unsigned z = 0u; asm volatile("" : "+s"(z)); return __builtin_amdgcn_mbcnt_hi(~0u, __builtin_amdgcn_mbcnt_lo(~0u, z)); };
  auto hand_req = [&](const unsigned m0, const unsigned word0, const unsigned nb) __attribute__((always_inline)) {
    const unsigned vo = c.vksrc[0]; u32x4 sr = c.srdS; sr[2] = 4u * word0 + nb;
    asm volatile("s_mov_b32 m0, %0\n\ts_nop 0\n\tbuffer_load_dwordx4 %1, %2, %3 offen sc1 lds" :: "s"(m0), "v"(vo), "s"(sr), "s"(4u * word0) : "m0", "memory");
  };
  auto pf_at = [&](auto M_, auto G_, auto A_) __attribute__((always_inline)) {
    constexpr int d = decltype(M_)::value - decltype(G_)::value;
    if constexpr (d == -decltype(A_)::value) asm volatile("s_add_u32 %0, %0, %1" : "+s"(c.pfo) : "s"(c.pfst) : "scc");
    if constexpr (d == -1) asm volatile("s_mov_b32 m0, %0" :: "s"(c.pfm0) : "m0", "memory");
    if constexpr (d == 0) { GAP(); c.pfm0 += 0x400u; asm volatile("" :: "s"(c.pfm0)); GAP(); asm volatile("buffer_load_dwordx4 %0, %1, %2 offen sc1 lds" :: "v"(c.pfv), "s"(c.srdP), "s"(c.pfo) : "memory"); }
  };
  auto m0_set = [&](const unsigned m0) __attribute__((always_inline)) { asm volatile("s_mov_b32 m0, %0" :: "s"(m0) : "m0", "memory"); };
  auto m0_imm = [&](auto C_) __attribute__((always_inline)) { asm volatile("s_mov_b32 m0, %0" :: "i"(decltype(C_)::value) : "m0", "memory"); };
  auto lim = [&](const unsigned n) __attribute__((always_inline)) { c.srdL[2] = n; asm volatile("" : "+s"(c.srdL)); };
  auto dma16 = [&](auto W_) __attribute__((always_inline)) { const unsigned vo = c.vksrc[0]; const u32x4 sr = c.srdL; if constexpr (decltype(W_)::value == 0) asm volatile("buffer_load_dwordx4 %0, %1, 0 offen sc1 lds" :: "v"(vo), "s"(sr) : "memory"); else asm volatile("buffer_load_dwordx4 %0, %1, %2 offen sc1 lds" :: "v"(vo), "s"(sr), "s"(4u * decltype(W_)::value) : "memory"); };
  auto flag_st = [&](const unsigned so, const unsigned tg) __attribute__((always_inline)) {
    const unsigned vo = c.vvsrc; const u32x4 sr = c.srdL; asm volatile("buffer_store_dword %0, %1, %2, %3 offen sc1" :: "v"(tg), "v"(vo), "s"(sr), "s"(so) : "memory");
  };
  auto warm_at = [&](auto M_, auto G_) __attribute__((always_inline)) {
    constexpr int d = decltype(M_)::value - decltype(G_)::value;
    if constexpr (d == -1) m0_set(c.pdum);
    if constexpr (d == 0) { const unsigned vo = c.vksrc[0], so = cwo; const u32x4 sr = srdC; asm volatile("buffer_load_dwordx4 %0, %1, %2 offen lds" :: "v"(vo), "s"(sr), "s"(so) : "memory"); }
  };
  auto is_cls = [&](const unsigned k) __attribute__((always_inline)) { asm volatile("" : "+s"(psc)); return psc == 4u * k; };
  int t1, t2;
  GAP();
  phaseA_end<2>(c);
  {
    auto hook = [&](auto M_) __attribute__((always_inline)) {
      constexpr int M = decltype(M_)::value;
      warm_at(M_, IC(40));
      SC(FB(0), c.srdL, c.srdS) SC(2, psc, psc & ~3u) SC(4, t1, 0xcc860 >> psc) SC(5, t1, t1 & 15) SC(7, t2, 0x08000 >> psc) SC(9, t2, t2 & 15) SC(10, t2, t2 + 14)
    };
    phaseB_<1, true, true, 2>(c, hook, make_iseq<NGB>{});
  }
  GAP();
  if (__builtin_expect(opq(blk) == RTB, 0)) {
    {
      const unsigned w = opq(wids), kb_ = c.ldsb + MO_KEY + 4096u * w;
      RTS r; unsigned sk;
      auto hookA2 = [&](auto M_) __attribute__((always_inline)) {
        constexpr int M = decltype(M_)::value, n = fa_of(M);
        if constexpr (n == 0) { r.lq = lane_id(); asm volatile("" : "+v"(r.lq)); }
        if constexpr (n == 1) { sk = WS_KEY + 4096u * w; asm volatile("" : "+s"(sk)); }
        if constexpr (n == 2) { r.va = 16u * r.lq; m0_set(kb_); asm volatile("" : "+v"(r.va)); }
        if constexpr (n == 4 || n == 5 || n == 7 || n == 9) {
          constexpr int j = n == 4 ? 0 : n == 5 ? 1 : n == 7 ? 2 : 3;
          const unsigned v = r.va, so = sk; const u32x4 sr = c.srdS;
          asm volatile("buffer_load_dwordx4 %0, %1, %3 offen offset:%c2 sc1 lds" :: "v"(v), "s"(sr), "i"(1024 * j), "s"(so) : "memory");
        }
      };
      phaseAh<0, 7>(c, 2, hookA2);
      phaseB<0, 2>(c);
      auto hookA3 = [&](auto M_) __attribute__((always_inline)) { if constexpr (decltype(M_)::value == FA(0)) { r.va += kb_; asm volatile("" : "+v"(r.va)); } };
      phaseAh<1, 2>(c, 3, hookA3);
      auto steps = [&](auto I0_, auto G0_, auto M_) __attribute__((always_inline)) {
        constexpr int M = decltype(M_)::value, I = decltype(I0_)::value + M - 100 - decltype(G0_)::value;
        if constexpr (M >= 100 + decltype(G0_)::value && I < RT_END) rt_step<I>(r, tag);
      };
      auto hookB3 = [&](auto M_) __attribute__((always_inline)) {
        constexpr int M = decltype(M_)::value;
        if constexpr (M == 2 || M == 4 || M == 5 || M == 7) {
          constexpr int j = M == 2 ? 0 : M == 7 ? 3 : M - 3;
          u32x4& d = r.g[j]; const unsigned v = r.va;
          asm volatile("ds_read_b128 %0, %1 offset:%2" : "=v"(d) : "v"(v), "i"(1024 * j) : "memory");
        }
        if constexpr (M == FB(0)) { u32x4 (&g)[4] = r.g; asm volatile("s_waitcnt lgkmcnt(%4)" : "+v"(g[0]), "+v"(g[1]), "+v"(g[2]), "+v"(g[3]) : "i"(kyo(7, FB(0))) : "memory"); }
        steps(IC(0), IC(FB(0)), M_);
      };
      phaseBh<1, 2>(c, hookB3);
      auto hookA4 = [&](auto M_) __attribute__((always_inline)) {
        constexpr int M = decltype(M_)::value;
        steps(IC(NGB - FB(0)), IC(0), M_);
        if constexpr (M == FA(6)) { r.va = (r.lq & 8u) == 0u ? 64u + 128u * w + 2u * (r.lq & 48u) + 4u * (r.lq & 7u) : 640u + 4u * r.lq; asm volatile("" : "+v"(r.va)); }
        if constexpr (M == FA(8)) lds_st32(c.ldsb + MO_RS + r.va, r.cnd);
        if constexpr (M == FA(9)) lds_st32(c.ldsb + MO_RS + 4u * w, __all(r.bad == 0u) ? 1u : 0u);
        if constexpr (M == FA(10)) { r.va = c.ldsb + MO_RS + 64u + 256u * (r.lq >> 5); r.m = c.ldsb + MO_RS + 64u + 128u * w + 4u * (r.lq & 31u); r.rank = 0u; asm volatile("" : "+v"(r.va), "+v"(r.m), "+v"(r.rank)); }
      };
      static_assert(NGB - FB(0) + FA(6) >= RT_END, "");
      phaseAh<0, 3>(c, 4, hookA4);
      auto hookB4 = [&](auto M_) __attribute__((always_inline)) {
        constexpr int M = decltype(M_)::value;
        if constexpr (M == 0) { unsigned& d = r.cv; u32x4& f = r.g[3]; const unsigned v = r.m, v2 = c.ldsb + MO_RS; asm volatile("ds_read_b32 %0, %2\n\tds_read_b128 %1, %3" : "=&v"(d), "=&v"(f) : "v"(v), "v"(v2) : "memory"); }
        if constexpr (M < 60 && M % 4 == 0) { u32x4& d = r.g[(M >> 2) % 3]; const unsigned v = r.va; asm volatile("ds_read_b128 %0, %1 offset:%2" : "=v"(d) : "v"(v), "i"(4 * M) : "memory"); }
        if constexpr (M >= 5 && M < 65 && M % 4 == 1) { u32x4 &d = r.g[((M - 5) >> 2) % 3], &f = r.g[3]; unsigned& cv = r.cv; asm volatile("s_waitcnt lgkmcnt(%3)" : "+v"(d), "+v"(cv), "+v"(f) : "i"(kyo(M - 5, M) + (M < 61)) : "memory"); }
        if constexpr (M >= 105 && M < 165) { unsigned long long& pm = r.pm[M & 1]; const unsigned a = r.g[((M - 105) >> 2) % 3][(M - 105) & 3], cv = r.cv; asm volatile("v_cmp_gt_u32_e64 %0, %1, %2" : "=s"(pm) : "v"(a), "v"(cv)); }
        if constexpr (M >= 106 && M < 166) cnt_s(r.rank, r.pm[(M - 1) & 1]);
        if constexpr (M == 165) { r.bad = r.g[3][0] & r.g[3][1] & r.g[3][2] & r.g[3][3]; asm volatile("" : "+v"(r.bad)); }
        if constexpr (M == 66) { u32x4& d = r.g[0]; const unsigned v = r.va; asm volatile("ds_read_b128 %0, %1 offset:240" : "=v"(d) : "v"(v) : "memory"); }
      };
      phaseBh<0, 2>(c, hookB4);
      auto hookA5 = [&](auto M_) __attribute__((always_inline)) {
        constexpr int M = decltype(M_)::value;
        if constexpr (M == 4) { u32x4& d = r.g[0]; asm volatile("s_waitcnt lgkmcnt(3)" : "+v"(d) :: "memory"); }
        if constexpr (M >= 104 && M < 108) { r.rank += r.g[0][M - 104] > r.cv ? 1u : 0u; asm volatile("" : "+v"(r.rank)); }
        if constexpr (M == FA(1)) { auto pr = __builtin_amdgcn_permlane32_swap(r.rank, r.rank, false, false); r.rank = pr[0] + pr[1]; asm volatile("" : "+v"(r.rank)); }
        if constexpr (M == FA(2)) { r.rs = key_val(r.cv) * -1.4426950408889634f; asm volatile("" : "+v"(r.rs)); }
        if constexpr (M == FA(3)) { r.rs = 1.f + __builtin_amdgcn_exp2f(r.rs); asm volatile("" : "+v"(r.rs)); }
        if constexpr (M == FA(4)) { r.rs = rbf(__builtin_amdgcn_rcpf(r.rs)); asm volatile("" : "+v"(r.rs)); }
        if constexpr (M == FA(5)) { r.m = (__float_as_uint(r.rs) & 0xffff0000u) | (unsigned)key_row(r.cv); r.bad = (r.bad - 1u) & 0x40000000u; asm volatile("" : "+v"(r.m), "+v"(r.bad)); }
        if constexpr (M == FA(7)) { r.va = (r.lq < 32u && r.rank < 8u ? 8u * r.rank : 0x40000000u) | r.bad; asm volatile("" : "+v"(r.va)); }
        if constexpr (M == 100 + FA(8)) { r.cnd = ((r.lq | w) == 0u ? 4u * H_G : 0x40000000u) | r.bad; asm volatile("" : "+v"(r.cnd)); }
        if constexpr (M == FA(9)) {
          const u32x2 gr = {r.m, tag}; const unsigned vo = r.va, vg = r.cnd; const u32x4 sr = c.srdS;
          asm volatile("buffer_store_dwordx2 %0, %1, %3, 0 offen sc1\n\tbuffer_store_dword %4, %2, %3, 0 offen sc1" :: "v"(gr), "v"(vo), "v"(vg), "s"(sr), "v"(gr[1]) : "memory");
        }
      };
      phaseAh<1, 4>(c, 5, hookA5);
      rstate = rfl(r.bad) ? 0u : 3u;
      if (rstate == 0u) lds_st32(c.ldsb + MO_RS + 4u * w, 0u);
      phaseB<1, 2>(c);
    }
#pragma nounroll
    for (int t = 6; t < 14; t += 2) {
      phaseA<0, 3>(c, t);     phaseB<0, 2>(c);
      phaseA<1, 2>(c, t + 1); phaseB<1, 2>(c);
      if (rstate != 3u) {
        route();
        if (t == 12)
#pragma nounroll
          for (unsigned spin = 0; spin < (1u << 20) && rstate != 3u; ++spin) {
            asm volatile("s_waitcnt vmcnt(0)" ::: "memory");
            route();
            __builtin_amdgcn_s_sleep(8);
          }
      }
      GAP();
    }
    t1 = 0;
  }
  GAP();
  {
#pragma nounroll
    for (int t = 0; __builtin_expect(t != t1, 1);) {
      t += 2;
      phaseA<0, 3>(c, t);     phaseB<0, 2>(c);
      phaseA<1, 2>(c, t + 1); phaseB<1, 2>(c);
    }
  }
  GAP();
  constexpr auto SG = [](int i) constexpr { constexpr int g[10] = {14, 20, 26, 31, 36, 41, 46, 52, 57, 61}; return g[i + 1]; };
  constexpr auto sg_of = [](int m) constexpr { constexpr int g[10] = {14, 20, 26, 31, 36, 41, 46, 52, 57, 61}; for (int i = 0; i < 10; ++i) if (g[i] == m) return i - 1; return -2; };
  if (__builtin_expect(is_cls(1u), 1)) {
    unsigned w, b, fbb, s0, s1, sx, mx, va, vo, vw, vx;
    {
      auto hook = [&](auto M_) __attribute__((always_inline)) {
        constexpr int M = decltype(M_)::value;
        SC(0, w, wids) SC(1, b, blk) SC(2, fbb, b & 7u) SC(3, fbb, fbb * 18u) SC(4, s0, b >> 3) SC(5, fbb, fbb + s0)
        SC(7, sx, w << 7) SC(8, sx, sx + WS_XN) SC(9, mx, w << 10) SC(10, mx, mx + MO_XR)
        SC(11, s0, w >> 1) SC(12, s0, s0 * 36864u) SC(13, s0, s0 + SD_BASE) SC(15, s1, w & 1u) SC(16, s1, s1 << 5)
        VC(FA(0), va, (c.vksrc[0] >> 1) & 0x38u) VC(FA(2), vo, (c.vksrc[0] >> 1) & 64u) VC(FA(4), vx, (c.vksrc[0] >> 4) & 0x30u) VC(FA(5), vo, vo | vx) VC(FA(5), va, va + MO_C)
        VC(FA(9), vw, c.vvb[0] & 0xffu) VC(FA(9), vx, c.vvb[0] & 32u) VC(FA(11), vw, vw + 16u * vx) VC(FA(11), vw, vw ^ s1)
      };
      phaseAh<0, 3>(c, 8, hook);
    }
    {
      auto hook = [&](auto M_) __attribute__((always_inline)) {
        constexpr int M = decltype(M_)::value;
        if constexpr (M == 38) lim(64u);
        if constexpr (M == 39) m0_imm(IC(MO_C));
        if constexpr (M == 40) dma16(IC(0));
        VC(FB(0), vx, 9u * (c.vvb[0] & 2048u)) VC(FB(1), vw, vw + vx + s0) VC(FB(1), vx, c.vksrc[0] + MO_XR)
      };
      phaseBh<0, 2>(c, hook);
    }
    {
      u32x2 sv; unsigned long long ok;
      constexpr auto yo = [](int g0, int g1) constexpr { return 2 * (pcnt(PA0, 'T', g1) - pcnt(PA0, 'T', g0)) + pcnt(PA0, 'X', g1) - pcnt(PA0, 'X', g0); };
      auto hook = [&](auto M_) __attribute__((always_inline)) {
        constexpr int M = decltype(M_)::value;
        if constexpr (M == FA(5)) asm volatile("s_waitcnt vmcnt(2)\n\tds_read_b64 %0, %1" : "=v"(sv) : "v"(va) : "memory");
        if constexpr (M == 100 + FA(6)) { asm volatile("s_waitcnt lgkmcnt(%1)" : "+v"(sv) : "i"(yo(FA(5), FA(6))) : "memory"); ok = __ballot(sv[1] != tag); asm volatile("" : "+s"(ok)); }
        if constexpr (M == 39) {
          if (__builtin_expect(ok != 0ull, 0)) {
#pragma nounroll
            for (unsigned spin = 0; spin < (1u << 20) && !__all(sv[1] == tag); ++spin) {
              __builtin_amdgcn_s_sleep(8);
              hand_req(c.ldsb + MO_C, 0u, 64u);
              asm volatile("s_waitcnt vmcnt(0)\n\tds_read_b64 %0, %1\n\ts_waitcnt lgkmcnt(0)" : "=v"(sv) : "v"(va) : "memory");
            }
          }
        }
        VC(141, va, sv[0] & 0xffffu) VC(143, vo, __umul24(va, XRS) + vo)
        if constexpr (M == 46 || M == 52 || M == 62) m0_set(mx);
        SC(49, mx, mx + 4096u) SC(50, sx, sx + 512u) SC(55, mx, mx + 4096u) SC(56, sx, sx + 512u)
        if constexpr (M == 100 + FA(8) || M == FA(9) || M == FA(11)) { const unsigned v = vo, so = sx; const u32x4 r = c.srdS; asm volatile("buffer_load_dwordx4 %0, %1, %2 offen sc1 lds" :: "v"(v), "s"(r), "s"(so) : "memory"); }
      };
      phaseAh<1, 5>(c, 9, hook);
    }
    unsigned vd, vyo, sy;
    {
      unsigned lq;
      auto hook = [&](auto M_) __attribute__((always_inline)) {
        constexpr int M = decltype(M_)::value;
        VC(FB(0), lq, lane_id()) VC(FB(1), c.pfv, 16u * (lq & 3u)) VC(FB(3), c.pfv, __umul24((lq >> 2) & 3u, XRS) + c.pfv) VC(DB(8), c.pfv, 2u * (lq & 32u) + c.pfv)
        VC(FB(4), c.pfv, __umul24(lq & 16u, XRS) + c.pfv) VC(DB(9), vyo, (lq & 48u) << 3) VC(FB(6), vyo, ((lq & 7u) << 4) | vyo) VC(FB(8), vyo, ((lq & 8u) << 27) | vyo)
        SA(2, c.pfo, sha<3>(fbb, w)) SC(4, c.pfo, c.pfo * (4u * XRS)) SC(5, c.pfo, c.pfo - 128u) SC(7, c.pfst, 128u) SC(9, c.pfm0, 18432u * w) SC(10, c.pfm0, c.pfm0 + SD_BASE)
        SC(12, sy, fbb * 36u) SC(15, sy, sy + w) SC(18, sy, sy << 9) SC(20, sy, sy + WS_YP) SC(33, c.srdP, mk_srd4(wd, DM * FF * 2))
      };
      phaseBh<1, 0>(c, hook);
    }
    f32x4 gu;
    auto gate_up = [&](auto H_) __attribute__((always_inline)) {
      constexpr int HH = decltype(H_)::value;
      bf16x4 wl[2], wh[2]; bf16x8 xf[2];
      auto hook = [&](auto M_) __attribute__((always_inline)) {
        constexpr int M = decltype(M_)::value, j = sg_of(M % 100);
        if constexpr (M < 100 && j >= 0) asm volatile("s_waitcnt lgkmcnt(%3)" : "+v"(wl[j & 1]), "+v"(wh[j & 1]), "+v"(xf[j & 1]) : "i"(kyo(SG(j - 1) + 1, M)) : "memory");
        if constexpr (M >= 100 && j >= 0) {
          const bf16x8 wfr = __builtin_shufflevector(wl[j & 1], wh[j & 1], 0, 1, 2, 3, 4, 5, 6, 7);
          if constexpr (HH == 0 && j == 0) asm volatile("mf16 %0, %1, %2, 0" : "=&v"(gu) : "v"(wfr), "v"(xf[j & 1]));
          else                             asm volatile("mf16 %0, %1, %2, %0" : "+v"(gu) : "v"(wfr), "v"(xf[j & 1]));
        }
        if constexpr (M >= 100 && j >= -1 && j < 8) {
          constexpr int n = (j + 1) & 1, i = 9 * HH + j + 1;
          asm volatile("ds_read_b64_tr_b16 %0, %1 offset:%2" : "=v"(wl[n]) : "v"(vw), "i"(1024 * i) : "memory");
          asm volatile("ds_read_b64_tr_b16 %0, %1 offset:%2" : "=v"(wh[n]) : "v"(vw), "i"(1024 * i + 256) : "memory");
          asm volatile("ds_read_b128 %0, %1 offset:%2" : "=v"(xf[n]) : "v"(vx), "i"(1024 * (i >> 1) + 128 * (i & 1)) : "memory");
        }
        if constexpr (HH == 1) {
          pf_at(M_, IC(DB(8)), IC(1)); pf_at(M_, IC(DB(9)), IC(1)); pf_at(M_, IC(FB(6)), IC(2)); pf_at(M_, IC(FB(8)), IC(2));
        }
      };
      phaseBh<HH, 0>(c, hook);
    };
    unsigned vh;
    {
      auto hook = [&](auto M_) __attribute__((always_inline)) {
        constexpr int M = decltype(M_)::value;
        SC(0, s0, w >> 1) SC(1, s0, s0 << 9) SC(2, mx, s0 + s1) SC(3, mx, mx + SD_BASE) SC(4, s1, s1 >> 2) SA(5, s0, sha<1>(s0, s1)) SC(7, s0, s0 + MO_H) SC(8, s1, w << 10) SA(9, sx, (shi<2, MO_HH>(w)))
        VC(FA(0), vd, 72u * (c.vksrc[0] & 0x300u)) VC(FA(2), vd, vd + (c.vvb[0] & 0xdfu) + mx)
      };
      phaseAh<0, 1>(c, 10, hook);
    }
    gate_up(std::integral_constant<int, 0>{});
    phaseA<1, 0>(c, 11);
    gate_up(std::integral_constant<int, 1>{});
    {
      auto hook = [&](auto M_) __attribute__((always_inline)) {
        constexpr int M = decltype(M_)::value;
        pf_at(M_, IC(FA(0)), IC(2)); pf_at(M_, IC(FA(2)), IC(2)); pf_at(M_, IC(FA(4)), IC(2)); pf_at(M_, IC(FA(5)), IC(2)); pf_at(M_, IC(FA(11)), IC(2));
        VC(100 + FA(6), vx, vx + s1) VC(100 + FA(8), vh, c.vksrc[0] + s0)
        if constexpr (M == FA(9)) { const f32x4 g4 = gu; const unsigned v = vx; asm volatile("ds_write_b128 %0, %1 offset:%2" :: "v"(v), "v"(g4), "i"(MO_H - MO_XR) : "memory"); }
      };
      phaseAh<0, 10>(c, 12, hook);
    }
    {
      f32x2 g, u, s;
      auto hook = [&](auto M_) __attribute__((always_inline)) {
        constexpr int M = decltype(M_)::value;
        if constexpr (M == FB(0)) asm volatile("ds_read_b64 %0, %2\n\tds_read_b64 %1, %2 offset:2048" : "=&v"(g), "=&v"(u) : "v"(vh) : "memory");
        if constexpr (M == 116) { vh = c.vksrc[0] + sx; asm volatile("" : "+v"(vh)); }
        if constexpr (M == FB(1)) {
          asm volatile("s_waitcnt lgkmcnt(%2)" : "+v"(g), "+v"(u) : "i"(kyo(FB(0), FB(1))) : "memory");
          const unsigned pg = cvtpk(g[0], g[1]);
          g[0] = bflo(pg); g[1] = bfhi(pg);
          asm volatile("" : "+v"(g));
        }
        if constexpr (M == FB(2)) { const unsigned pu = cvtpk(u[0], u[1]); u[0] = bflo(pu); u[1] = bfhi(pu); asm volatile("" : "+v"(u)); }
        if constexpr (M == FB(3)) { s[0] = g[0] * -1.4426950408889634f; s[1] = g[1] * -1.4426950408889634f; asm volatile("" : "+v"(s)); }
        if constexpr (M == DB(8) || M == FB(4)) { constexpr int e = M == FB(4); s[e] = __builtin_amdgcn_exp2f(s[e]); asm volatile("" : "+v"(s)); }
        if constexpr (M == DB(9)) { float s0 = s[0], s1 = s[1]; add_v(s0, 1.f); add_v(s1, 1.f); s = f32x2{s0, s1}; }
        if constexpr (M == FB(5) || M == FB(6)) { constexpr int e = M == FB(6); s[e] = __builtin_amdgcn_rcpf(s[e]); asm volatile("" : "+v"(s)); }
        if constexpr (M == FB(7)) { float g0 = g[0], g1 = g[1]; mul_v(g0, s[0]); mul_v(g1, s[1]); g = f32x2{g0, g1}; }
        if constexpr (M == FB(8)) { const unsigned ps = cvtpk(g[0], g[1]); g[0] = bflo(ps); g[1] = bfhi(ps); asm volatile("" : "+v"(g)); }
        if constexpr (M == 164 || M == 165) { float x = u[M & 1]; mul_v(x, g[M & 1]); u[M & 1] = x; }
        if constexpr (M == FB(9)) { const unsigned hv = cvtpk(u[0], u[1]); asm volatile("ds_write_b32 %0, %1" :: "v"(vh), "v"(hv) : "memory"); }
      };
      phaseBh<0, 0>(c, hook);
    }
    {
      bf16x4 dl[2], dh[2]; f32x4 y[3]; u32x4 hwv;
      { auto hookA = [&](auto M_) __attribute__((always_inline)) { constexpr int M = decltype(M_)::value; VC(FA(0), vh, c.vksrc[0] + MO_HH) }; phaseAh<1, 0>(c, 13, hookA); }
      constexpr auto st_of = [](int m) constexpr { constexpr int g[9] = {25, 33, 42, 43, 47, 50, 56, 62, 67}; for (int i = 0; i < 9; ++i) if (g[i] == m) return i; return -1; };
      auto hook = [&](auto M_) __attribute__((always_inline)) {
        constexpr int M = decltype(M_)::value, i = sg_of(M % 100), is = st_of(M), ik = st_of(M - 101);
        if constexpr (M == FB(0)) asm volatile("ds_read_b128 %0, %1" : "=v"(hwv) : "v"(vh) : "memory");
        if constexpr (M < 100 && i >= 0) asm volatile("s_waitcnt lgkmcnt(%3)" : "+v"(dl[i & 1]), "+v"(dh[i & 1]), "+v"(hwv) : "i"(kyo(SG(i - 1) + 1, M)) : "memory");
        if constexpr (M >= 100 && i >= 0) {
          const bf16x8 d8 = __builtin_shufflevector(dl[i & 1], dh[i & 1], 0, 1, 2, 3, 4, 5, 6, 7);
          asm volatile("mf16 %0, %1, %2, 0" : "=&v"(y[i % 3]) : "v"(d8), "v"(hwv));
        }
        if constexpr (M >= 100 && i >= -1 && i < 8) {
          asm volatile("ds_read_b64_tr_b16 %0, %1 offset:%2" : "=v"(dl[(i + 1) & 1]) : "v"(vd), "i"(1024 * (i + 1)) : "memory");
          asm volatile("ds_read_b64_tr_b16 %0, %1 offset:%2" : "=v"(dh[(i + 1) & 1]) : "v"(vd), "i"(1024 * (i + 1) + 256) : "memory");
        }
        if constexpr (is >= 0) {
          const unsigned vo = vyo; const u32x4 sr = c.srdS; const f32x4 yv = y[is % 3]; const unsigned so = sy + 4096u * (is >> 1);
          if constexpr (is < 8) asm volatile("buffer_store_dwordx4 %0, %1, %2, %3 offen offset:%c4 sc1" :: "v"(yv), "v"(vo), "s"(sr), "s"(so), "i"(2048 * (is & 1)) : "memory");
          else                  asm volatile("buffer_store_dwordx4 %0, %1, %2, %3 offen sc1\n\ts_nop 1" :: "v"(yv), "v"(vo), "s"(sr), "s"(so) : "memory");
        }
        if constexpr (ik >= 0) { const f32x4 yv = y[ik % 3]; asm volatile("" :: "v"(yv)); }
      };
      phaseBh<1, 0>(c, hook);
    }
  }
  GAP();
  PS_AT(41);
  if (__builtin_expect(is_cls(2u), 0)) {
    PS_AT(42);
    constexpr unsigned NTS = 128u * 4608u;
    unsigned w, b, vw[2], v0, v1, vs, vn, st0, v0s, so;
    bf16x4 wl[2], wh[2];
    auto rd = [&](auto I_) __attribute__((always_inline)) {
      constexpr int i = decltype(I_)::value;
      bf16x4 &l = wl[i & 1], &h = wh[i & 1]; const unsigned v = vw[i & 1];
      asm volatile("ds_read_b64_tr_b16 %0, %1 offset:%2" : "=v"(l) : "v"(v), "i"(1024 * (i >> 1)) : "memory");
      asm volatile("ds_read_b64_tr_b16 %0, %1 offset:%2" : "=v"(h) : "v"(v), "i"(1024 * (i >> 1) + 256) : "memory");
    };
    auto st = [&](auto I_, auto P_, auto Y_) __attribute__((always_inline)) {
      constexpr int i = decltype(I_)::value, P = decltype(P_)::value, u = 2 * i, r = u % 9;
      constexpr auto at = [](int x) constexpr { return (unsigned)(x / 9) * NTS + 2048u * ((x % 9) >> 2) + 64u * ((x % 9) & 3); };
      const u32x4 sr = c.srdS;
      if constexpr (P == 0) { so = st0 + at(r == 8 ? u + 1 : u); asm volatile("" : "+s"(so)); }
      if constexpr (P == 1) {
        asm volatile("s_waitcnt lgkmcnt(%2)" : "+v"(wl[i & 1]), "+v"(wh[i & 1]) : "i"(decltype(Y_)::value) : "memory");
        const u32x4 x = __builtin_bit_cast(u32x4, __builtin_shufflevector(wl[i & 1], wh[i & 1], 0, 1, 2, 3, 4, 5, 6, 7));
        const unsigned vo = r == 8 ? v0 | (v1 ^ 0x40000000u) : r == 7 ? v0 | v1 : (r & 3) == 3 ? vn + (v1 >> 30) * 1792u : vn, s_ = so;
        asm volatile("buffer_store_dwordx4 %0, %1, %2, %3 offen sc1" :: "v"(x), "v"(vo), "s"(sr), "s"(s_) : "memory");
      }
      if constexpr (P == 2) {
        const u32x4 x = __builtin_bit_cast(u32x4, __builtin_shufflevector(wl[i & 1], wh[i & 1], 0, 1, 2, 3, 4, 5, 6, 7));
        if constexpr (r >= 7) {
          const u32x2 e = {x[0], x[2]}, o2 = {x[1], x[3]}; const unsigned v2 = vs | (r == 8 ? v1 : v1 ^ 0x40000000u), s0 = st0;
          asm volatile("buffer_store_dwordx2 %0, %2, %3, %4 offen sc1\n\tbuffer_store_dwordx2 %1, %2, %3, %4 offen offset:128 sc1" :: "v"(e), "v"(o2), "v"(v2), "s"(sr), "s"(s0 + (unsigned)(u / 9) * NTS) : "memory");
        } else asm volatile("" :: "v"(x));
      }
    };
    auto hookB = [&](auto I0_, auto M_) __attribute__((always_inline)) {
      constexpr int M = decltype(M_)::value, i0 = decltype(I0_)::value;
      constexpr int rg[6] = {7, 16, 33, 42, 50, 53}, sg[6] = {14, 25, 43, 48, 56, 62};
      auto one = [&](auto N_) __attribute__((always_inline)) {
        constexpr int n = decltype(N_)::value;
        if constexpr (M == rg[n]) rd(IC(i0 + n));
        if constexpr (M == sg[n] - 1) st(IC(i0 + n), IC(0), IC(0));
        if constexpr (M == sg[n]) st(IC(i0 + n), IC(1), IC(kyo(rg[n], sg[n]) + (n < 5 && rg[n < 5 ? n + 1 : n] < sg[n] ? 2 : 0)));
        if constexpr (M == 101 + sg[n]) st(IC(i0 + n), IC(2), IC(0));
      };
      for_seq(one, make_iseq<6>{});
    };
    {
      auto hook = [&](auto M_) __attribute__((always_inline)) {
        constexpr int M = decltype(M_)::value;
        SC(0, b, blk) SC(1, b, b - 144u) SC(6, w, wids) SC(2, st0, b & 51u) SC(3, so, b & 4u) SA(4, st0, sha<1>(so, st0)) SC(5, so, (b >> 3) & 1u) SA(7, st0, sha<2>(so, st0)) SC(8, so, w & 1u) SC(9, v0s, so << 8)
        SC(10, st0, st0 + v0s) SC(11, st0, st0 * 9216u) SC(12, st0, st0 + WS_WOT) SC(13, v0s, w & 2u) SA(15, so, sha<1>(v0s, so)) SC(16, so, so * 9216u) SC(17, so, so + SD_BASE) SC(18, v0s, v0s << 8)
        VC(FA(0), vw[0], c.vvb[0] & 0xffu) VC(FA(0), v0, c.vvb[0] & 32u) VC(FA(2), vw[0], vw[0] + 16u * v0) VC(FA(2), v0, c.vvb[0] & 2048u) VC(120, vw[0], __umul24(v0, 9u) + vw[0])
        VC(FA(4), vw[0], vw[0] + so) VC(FA(4), vw[1], vw[0] ^ 32u) VC(FA(5), v1, lane_id())
        VC(132, vs, v1 & 7u) VC(135, v0, v1 & 32u) VC(100 + FA(6), vs, 2u * vs + v0) VC(138, v0, (v1 >> 4) & 1u) VC(141, vs, vs + v0) VC(143, vs, 8u * vs + v0s) VC(145, vs, vs + 8192u)
        SC(46, v0s, v0s << 3) VC(148, v0, v1 & 4u) VC(151, vn, v1 & 51u) VC(FA(9), v0, 16u * v0 + vn) VC(FA(9), v0, 16u * v0 + v0s) VC(157, v1, v1 & 8u) VC(FA(11), vn, 8u * v1 + v0) VC(FA(11), v1, v1 << 27)
      };
      phaseAh<0, 3>(c, 10, hook);
    }
    { auto hook = [&](auto M_) __attribute__((always_inline)) { hookB(std::integral_constant<int, 0>{}, M_); }; phaseBh<0, 0>(c, hook); }
    {
      auto hook = [&](auto M_) __attribute__((always_inline)) {
        constexpr int M = decltype(M_)::value;
        constexpr int rg[6] = {0, 8, 16, 24, 37, 55}, sg[6] = {6, 14, 22, 30, 53, 62};
        auto one = [&](auto N_) __attribute__((always_inline)) {
          constexpr int n = decltype(N_)::value, y = 2 * (pcnt(PA0, 'T', sg[n]) - pcnt(PA0, 'T', rg[n])) + pcnt(PA0, 'X', sg[n]) - pcnt(PA0, 'X', rg[n]);
          if constexpr (M == rg[n]) rd(IC(6 + n));
          if constexpr (M == sg[n] - 1) st(IC(6 + n), IC(0), IC(0));
          if constexpr (M == sg[n]) st(IC(6 + n), IC(1), IC(y));
          if constexpr (M == 101 + sg[n]) st(IC(6 + n), IC(2), IC(0));
        };
        for_seq(one, make_iseq<6>{});
      };
      phaseAh<1, 14>(c, 11, hook);
    }
    { auto hook = [&](auto M_) __attribute__((always_inline)) { hookB(std::integral_constant<int, 12>{}, M_); }; phaseBh<1, 0>(c, hook); }
    phaseA<0>(c, 12);
    {
      auto hook = [&](auto M_) __attribute__((always_inline)) {
        constexpr int M = decltype(M_)::value;
        SA(2, st0, (shi<2, 4u * H_W>(b))) SC(3, v0s, w - 1u) SC(4, v0s, (int)v0s >> 31) SC(5, so, st0 + 4u) SC(7, v0s, v0s & so) VC(109, v0, tag)
        if constexpr (M == 10) lim(v0s);
        if constexpr (M == FB(0)) flag_st(st0, v0);
      };
      phaseBh<0, 0>(c, hook);
    }
    phaseA<1>(c, 13); phaseB<1, 0>(c);
  }
  OPS o; OST ot; unsigned xo, xs, r1; unsigned long long tm;
  {
    const unsigned w = opq(wids), b = opq(blk);
    unsigned va, vs, vt, r0;
    {
      unsigned tg, m0, sf, mt, hs, t, u;
      auto hookA = [&](auto M_) __attribute__((always_inline)) {
        constexpr int M = decltype(M_)::value;
        SC(FA(0), m0, (w - 1u) & (b - 144u)) SC(FA(1), m0, (int)m0 >> 31) SA(FA(2), sf, (shi<2, 4u * H_Y>(b))) SC(FA(4), m0, m0 & (sf + 4u)) VC(FA(5), tg, tag)
        if constexpr (M == FA(5)) lim(m0);
        SC(0, mt, b & 3u) SC(1, t, b >> 6) SA(2, mt, sha<2>(mt, t)) SC(3, t, (b >> 2) & 1u) SA(4, mt, sha<1>(mt, t))
        SA(5, u, sha<2>(mt, w)) SC(7, o.sn, u * (16u * XRS)) SC(8, t, (b >> 3) & 7u) SC(9, hs, t * 144u) SC(11, o.sn, o.sn + hs)
        SA(12, hs, sha<2>(t, w)) SC(13, ot.so[0], hs << 19) SC(15, o.sf, hs >> 1) SC(16, o.sf, o.sf << 11)
        SC(17, u, u ^ 4u) SC(19, o.sa, u << 17) SC(20, u, (u >> 2) & 3u) SC(21, u, u << 10) SC(23, o.sa, o.sa | u)
        SC(24, t, ~b & 4u) SC(25, t, t + w) SC(26, u, t << 14) SC(27, o.sa, o.sa | u)
        SC(28, mt, mt >> 1) SA(29, t, sha<3>(mt, t)) SC(31, o.sl, t << 8) SC(32, o.sl, o.sl | 4u * H_O)
        SC(33, hs, b & 6u) SC(34, t, b >> 7) SC(35, hs, hs + t) SC(36, t, b & 1u) SA(38, mt, sha<1>(hs, t)) SC(39, u, (b >> 6) & 1u) SA(40, hs, sha<1>(mt, u))
        SA(42, t, sha<2>(hs, w)) SC(43, t, t << 10) SC(45, ot.so[0], ot.so[0] + t) SC(51, ot.so[0], ot.so[0] | WS_O) SC(52, o.sa, o.sa | WS_O)
        SA(44, u, sha<2>(mt, u)) SC(46, t, w & 1u) SA(48, t, sha<1>(u, t)) SA(49, t, (shi<4, 4u * H_O>(t))) SC(50, o.sf, o.sf + t)
        SC(55, t, b - RED0) SA(56, xo, sha<2>(t, w)) SA(58, xo, (shi<4, 4u * H_X>(xo))) SC(59, xs, xo + 4u)
        if constexpr (M == FA(11)) asm volatile("s_cmp_lt_u32 %1, %2\n\ts_cselect_b32 %0, %0, 0" : "+s"(xs) : "s"(t), "i"(RED1 - RED0) : "scc");
      };
      phaseAh<0, 0>(c, 14, hookA);
      auto hook = [&](auto M_) __attribute__((always_inline)) {
        constexpr int M = decltype(M_)::value;
        if constexpr (M == FB(0)) flag_st(sf, tg);
        warm_at(M_, IC(FB(6)));
        if constexpr (M == 40) lim(4u * H_W + 256u);
        if constexpr (M == 41) m0_imm(IC(WF_FL));
        if constexpr (M == FB(3)) dma16(IC(H_W));
        if constexpr (M == 45) lim(64u);
        if constexpr (M == 46) m0_imm(IC(SEL_C));
        if constexpr (M == FB(4)) dma16(IC(0));
        VC(FB(1), c.pfv, lane_id()) VC(DB(8), vt, c.pfv & 7u) VC(DB(8), c.pfv, c.pfv << 4) VC(DB(9), va, (c.vksrc[0] & 0xf0u) | WF_FL) VC(FB(8), vs, 8u * vt + SEL_C) VC(FB(8), vt, vt + SEQ)
        SC(2, t, (b >> 3) & 7u) SC(4, u, (o.sa >> 14) & 7u) SA(5, t, sha<3>(t, u)) SC(7, o.sb, t * 73728u) SC(9, o.sb, o.sb + WS_WOT) SC(10, c.pfo, o.sb - 1024u) SC(12, c.pfst, 1024u)
        SC(15, c.pfm0, BP_W * w) SC(18, c.pfm0, c.pfm0 + SD_BASE) SC(20, c.pfend, c.pfm0 + BP_W) SC(33, c.srdP, c.srdS)
        SC(23, r0, o.sa >> 13) SC(26, r0, r0 ^ 64u) SC(28, r0, r0 & 2032u) SC(30, r1, w << 7) SC(31, r1, r1 + (SEL_C + 256u))
      };
      phaseBh<0, 0>(c, hook);
    }
    {
      u32x4 f; u32x2 sv; unsigned long long no;
      phaseA<1, 1>(c, 15);
      auto hook = [&](auto M_) __attribute__((always_inline)) {
        constexpr int M = decltype(M_)::value;
        if constexpr (M == FB(0)) asm volatile("ds_read_b128 %0, %2\n\tds_read_b64 %1, %3" : "=&v"(f), "=&v"(sv) : "v"(va), "v"(vs) : "memory");
        if constexpr (M == FB(1)) { asm volatile("s_waitcnt lgkmcnt(%2)" : "+v"(f), "+v"(sv) : "i"(kyo(FB(0), FB(1))) : "memory"); f[0] = (f[0] ^ tag) | (f[1] ^ tag); asm volatile("" : "+v"(f)); }
        if constexpr (M == 126) { f[2] = (f[2] ^ tag) | (f[3] ^ tag); asm volatile("" : "+v"(f)); }
        if constexpr (M == 128) { f[0] |= f[2]; asm volatile("" : "+v"(f)); }
        if constexpr (M == 130) { no = __ballot(f[0] != 0u); asm volatile("" : "+s"(no)); }
        if constexpr (M == FB(2)) {
          if (__builtin_expect(no != 0ull, 0)) {
#pragma nounroll
            for (unsigned spin = 0; spin < (1u << 20); ++spin) {
              __builtin_amdgcn_s_sleep(8);
              hand_req(c.ldsb + WF_FL, H_W, 256u);
              asm volatile("s_waitcnt vmcnt(0)\n\tds_read_b128 %0, %1\n\ts_waitcnt lgkmcnt(0)" : "=v"(f) : "v"(va) : "memory");
              if (__all(((f[0] ^ tag) | (f[1] ^ tag) | (f[2] ^ tag) | (f[3] ^ tag)) == 0u)) break;
            }
            asm volatile("s_add_u32 m0, %0, %1" :: "s"(c.m0k), "i"(VRING) : "m0", "scc", "memory");
          }
        }
        VC(134, vs, sv[0] & 0xffffu) VC(136, va, vs - r0) VC(138, vt, vt - vs)
        if constexpr (M == FB(5)) { const unsigned x = va; asm volatile("v_cmp_gt_u32_e64 %0, 16, %1" : "=s"(tm) : "v"(x)); }
        if constexpr (M == 140) { const unsigned x = sv[1], g = tag; asm volatile("v_cmp_ne_u32_e64 %0, %1, %2" : "=s"(o.seen) : "s"(g), "v"(x)); }
        VC(158, va, va < 16u ? va : 16u) VC(FB(8), vt, __umul24(vt, XRS)) VC(FB(8), va, 4u * va + r1) SC(64, o.bad, (unsigned)o.seen)
        if constexpr (M == FB(9)) lds_st32(va, vt);
        if constexpr (M == 59) lim(0u);
        if constexpr (M == 60) flag_st(xo, vt);
      };
      phaseBh<1, 2>(c, hook);
    }
  }
  GAP();
  if (__builtin_expect(is_cls(3u), 0)) {
    PS_AT(43);
    unsigned va, vb, nv, s0, sr, sj; u32x4 f; u32x2 sv; f32x4 y[4]; float a[4]; unsigned long long no;
    auto req_fl = [&](auto W_) __attribute__((always_inline)) {
      u32x4& f_ = f; u32x2& s_ = sv; const unsigned v0 = c.pfv, v1 = vb; const u32x4 r = c.srdS;
      if constexpr (decltype(W_)::value != 1) asm volatile("buffer_load_dwordx4 %0, %1, %2, 0 offen offset:%c3 sc1" : "=v"(f_) : "v"(v0), "s"(r), "i"(4 * H_Y) : "memory");
      if constexpr (decltype(W_)::value != 0) asm volatile("buffer_load_dwordx2 %0, %1, %2, 0 offen sc1" : "=v"(s_) : "v"(v1), "s"(r) : "memory");
    };
    auto add2 = [&](auto N_, auto E_) __attribute__((always_inline)) {
      constexpr int n = decltype(N_)::value, e = decltype(E_)::value; const f32x4 v = y[n & 3];
      if constexpr (n == 0) { a[e] = v[e]; a[e + 1] = v[e + 1]; PIN2(a[e], a[e + 1]); } else if constexpr (n > 0 && n < 18) { add_v(a[e], v[e]); add_v(a[e + 1], v[e + 1]); }
    };
    auto batch = [&](auto B_, auto M_) __attribute__((always_inline)) {
      constexpr int B = decltype(B_)::value, M = decltype(M_)::value, i = M == 9 ? 0 : M == FB(0) ? 1 : M == FB(1) ? 2 : M == FB(2) ? 3 : -1, j = M == 8 ? 0 : M == 12 ? 1 : M == 23 ? 2 : M == 31 ? 3 : -1;
      if constexpr (M == 116 || M == 120) add2(IC(4 * B - 2), IC(M == 116 ? 0 : 2));
      if constexpr (M == 126 || M == 130) add2(IC(4 * B - 1), IC(M == 126 ? 0 : 2));
      if constexpr (j >= 0 && 4 * B + j < 18) { sr += 8u * NSEL * DM * 4u; asm volatile("" : "+s"(sr)); }
      if constexpr (i >= 0 && 4 * B + i < 18) {
        f32x4& d = y[i]; const unsigned v = va, so = sr; const u32x4 r = c.srdS;
        asm volatile("buffer_load_dwordx4 %0, %1, %2, %3 offen sc1" : "=v"(d) : "v"(v), "s"(r), "s"(so) : "memory");
      }
    };
    auto phase_a = [&](auto X_, auto B_) __attribute__((always_inline)) {
      constexpr int X = decltype(X_)::value, B = decltype(B_)::value, n0 = 6 + (B < 4 ? 3 : 2) + 3 - X;
      auto hook = [&](auto M_) __attribute__((always_inline)) {
        constexpr int M = decltype(M_)::value;
        if constexpr (M == FA(4)) { f32x4& d = y[0]; asm volatile("s_waitcnt vmcnt(%1)" : "+v"(d) : "i"(n0) : "memory"); }
        if constexpr (M == FA(9)) { f32x4& d = y[1]; asm volatile("s_waitcnt vmcnt(%1)" : "+v"(d) : "i"(n0 - 2) : "memory"); }
        if constexpr (M == 100 + FA(4) || M == FA(5)) add2(IC(4 * B), IC(M > 100 ? 0 : 2));
        if constexpr (M == 100 + FA(9) || M == FA(11)) add2(IC(4 * B + 1), IC(M > 100 ? 0 : 2));
      };
      phaseAh<X, 3 - X>(c, 19 + B, hook);
      if constexpr (B < 4) asm volatile("" : "+v"(y[2]), "+v"(y[3]));
    };
    phaseA<0, 3>(c, 16); phaseB<0, 2>(c);
    {
      auto hook = [&](auto M_) __attribute__((always_inline)) {
        constexpr int M = decltype(M_)::value;
        VC(FA(0), vb, (c.pfv >> 1) & 56u) VC(FA(2), va, __umul24(c.pfv >> 7, NSEL * DM * 4)) VC(FA(4), va, va + (c.pfv & 112u))
        SC(0, sj, xo - 4u * H_X) SA(1, sr, (shi<3, WS_YP - 8u * NSEL * DM * 4u>(sj))) SC(2, sj, sj >> 1) SC(3, sj, sj + WS_XN)
      };
      phaseAh<1, 2>(c, 17, hook);
    }
    { auto hook = [&](auto M_) __attribute__((always_inline)) { constexpr int M = decltype(M_)::value; if constexpr (M == FB(1)) req_fl(IC(1)); if constexpr (M == 40) req_fl(IC(0)); }; phaseBh<1, 2>(c, hook); }
    {
      auto hook = [&](auto M_) __attribute__((always_inline)) {
        constexpr int M = decltype(M_)::value;
        if constexpr (M == FA(9)) { asm volatile("s_waitcnt vmcnt(3)" : "+v"(f), "+v"(sv) :: "memory"); f[0] = (f[0] ^ tag) | (f[1] ^ tag); f[2] = (f[2] ^ tag) | (f[3] ^ tag); asm volatile("" : "+v"(f)); }
        if constexpr (M == 157) { f[0] |= f[2]; asm volatile("" : "+v"(f)); }
        if constexpr (M == FA(11)) { no = (__ballot(f[0] != 0u) << 28) | __ballot(sv[1] != tag); asm volatile("" : "+s"(no)); }
      };
      phaseAh<0, 3>(c, 18, hook);
    }
    {
      auto hook = [&](auto M_) __attribute__((always_inline)) {
        constexpr int M = decltype(M_)::value;
        if constexpr (M == 4) {
          if (__builtin_expect(no != 0ull, 0)) {
            wait_flag(c.srdS, vb + 4u, tag);
#pragma nounroll
            for (unsigned q = 0; q < 3u; ++q) { const unsigned i = 64u * q + lane_id(); wait_flag(c.srdS, 4u * H_Y + 4u * (i < (unsigned)MOE_WG ? i : 0u), tag); }
            { u32x2& s_ = sv; const unsigned v = vb; const u32x4 r = c.srdS; asm volatile("buffer_load_dwordx2 %0, %1, %2, 0 offen sc1\n\ts_waitcnt vmcnt(0)" : "+v"(s_) : "v"(v), "s"(r) : "memory"); }
          }
        }
        batch(IC(0), M_);
        if constexpr (M == 16) { unsigned& d = s0; const unsigned x = sv[0]; asm volatile("v_mov_b32_e32 %0, %1" : "=v"(d) : "v"(x)); }
      };
      phaseBh<0, 2>(c, hook);
    }
    phase_a(IC(1), IC(0));
    { auto hook = [&](auto M_) __attribute__((always_inline)) { batch(IC(1), M_); }; phaseBh<1, 2>(c, hook); }
    phase_a(IC(0), IC(1));
    { auto hook = [&](auto M_) __attribute__((always_inline)) { batch(IC(2), M_); }; phaseBh<0, 2>(c, hook); }
    phase_a(IC(1), IC(2));
    { auto hook = [&](auto M_) __attribute__((always_inline)) { batch(IC(3), M_); }; phaseBh<1, 2>(c, hook); }
    phase_a(IC(0), IC(3));
    {
      auto hook = [&](auto M_) __attribute__((always_inline)) {
        constexpr int M = decltype(M_)::value;
        batch(IC(4), M_);
        VC(123, vb, (c.pfv >> 7) & 6u) VC(FB(1), nv, s0 & 0xffffu) VC(FB(1), nv, __umul24(nv, XRS) + vb)
        if constexpr (M == FB(2)) {
          unsigned& n_ = nv; const unsigned o2 = sj; const u32x4 r = c.srdS;
          asm volatile("buffer_load_ushort %0, %0, %1, %2 offen sc1" : "+v"(n_) : "s"(r), "s"(o2) : "memory");
        }
        VC(FB(6), va, (c.pfv >> 4) & 15u) VC(FB(6), va, __umul24(va, XRS) + vb) SC(60, sj, sj + SEQ * XRS)
        if constexpr (M == 61) lim(WS_WOT);
      };
      phaseBh<0, 2>(c, hook);
    }
    phase_a(IC(1), IC(4));
    asm volatile("" : "+v"(nv));
    {
      auto hook = [&](auto M_) __attribute__((always_inline)) {
        constexpr int M = decltype(M_)::value;
        if constexpr (M == FB(0) || M == FB(1)) {
          constexpr int e = M == FB(1);
          auto pr = __builtin_amdgcn_permlane32_swap(__float_as_uint(a[e]), __float_as_uint(a[e + 2]), false, false);
          a[e] = __uint_as_float(pr[0]); a[e + 2] = __uint_as_float(pr[1]); PIN2(a[e], a[e + 2]);
        }
        if constexpr (M == 16) add_v(a[0], a[2]);
        if constexpr (M == 128) add_v(a[1], a[3]);
        if constexpr (M == FB(2)) { auto pr = __builtin_amdgcn_permlane16_swap(__float_as_uint(a[0]), __float_as_uint(a[1]), false, false); a[0] = __uint_as_float(pr[0]); a[1] = __uint_as_float(pr[1]); PIN2(a[0], a[1]); }
        if constexpr (M == 136) add_v(a[0], a[1]);
        VC(140, a[0], dpp_add<0x128>(a[0])) VC(144, a[0], rbf(a[0])) VC(FB(5), a[1], bfhi(s0)) VC(153, a[0], a[0] * a[1]) VC(158, a[0], rbf(a[0]))
        VC(FB(8), a[0], a[0] + bflo(nv)) VC(164, nv, cvtpk(a[0], 0.f))
        if constexpr (M == FB(9)) { const unsigned d = nv, vo = va, so = sj; const u32x4 r = c.srdL; asm volatile("buffer_store_short %0, %1, %2, %3 offen sc1" :: "v"(d), "v"(vo), "s"(r), "s"(so) : "memory"); }
      };
      phaseBh<1, 2>(c, hook);
    }
  }
  GAP();
  {
    int t = t2;
    unsigned tg = tag;
    asm volatile("" : "+v"(tg));
    lim(xs);
    auto hook = [&](auto M_) __attribute__((always_inline)) {
      constexpr int M = decltype(M_)::value;
      if constexpr (M == FB(8)) flag_st(xo, tg);
      if constexpr (M == FB(8) + 1) lim(0u);
    };
#pragma nounroll
    do {
      t += 2;
      phaseA<0, 4>(c, t);     phaseB<0, 2>(c);
      phaseA<1, 2>(c, t + 1); phaseBh<1, 2>(c, hook);
    } while (__builtin_expect(t != T - 4, 1));
  }
  GAP();
  {
    const unsigned w = opq(wids), b = opq(blk);
    u32x4 sh; unsigned ax[2], dl, fx, fy, t, sx;
    auto req = [&](auto X_, auto J_) __attribute__((always_inline)) {
      constexpr int X = decltype(X_)::value, J = decltype(J_)::value;
      const unsigned v = ax[J >> 1], so = X ? o.sn : sx; const u32x4 r = X ? sh : c.srdS;
      if constexpr (J < 2) asm volatile("buffer_load_dwordx4 %0, %1, %2, %3 offen offset:%c4 sc1" : "=v"(o.gv[X][J & 1]) : "v"(v), "s"(r), "s"(so), "i"(64 * J) : "memory");
      else                 asm volatile("buffer_load_dword %0, %1, %2, %3 offen offset:128 sc1" : "=v"(o.gs[X]) : "v"(v), "s"(r), "s"(so) : "memory");
    };
    auto hookA = [&](auto M_) __attribute__((always_inline)) {
      constexpr int M = decltype(M_)::value;
      SC(0, t, (b >> 3) & 7u) SC(1, t, t * 288u) SC(2, t, t + 4u * H_X)
      if constexpr (M == 3) lim(t + 288u);
      if constexpr (M == FA(0)) { unsigned& d = fx; const unsigned v = c.pfv, so = t; const u32x4 r = c.srdL; asm volatile("buffer_load_dword %0, %1, %2, %3 offen sc1" : "=v"(d) : "v"(v), "s"(r), "s"(so) : "memory"); }
      if constexpr (M == 8) lim(o.sl + 64u);
      VC(FA(2), dl, (c.pfv >> 4) & 15u) VC(FA(2), ax[1], __umul24(dl, XRS)) VC(116, fy, c.pfv >> 8) VC(FA(4), ax[0], 16u * fy + ax[1]) VC(FA(4), fy, 0x3120u >> (4u * fy))
      VC(132, fy, fy & 3u) VC(135, ax[1], 4u * fy + ax[1])
      if constexpr (M >= 9 && M < 13) { const u32x4 d = mk_srd4(xres, SEQ * DM * 2); sh[M - 9] = d[M - 9]; asm volatile("" : "+s"(sh)); }
      if constexpr (M == FA(5)) req(IC(1), IC(0));
      if constexpr (M == FA(6)) req(IC(1), IC(1));
      if constexpr (M == FA(11)) req(IC(1), IC(2));
      VC(FA(9), dl, 4u * dl + r1)
      if constexpr (M == 55) { unsigned& d = dl; asm volatile("ds_read_b32 %0, %0" : "+v"(d) :: "memory"); }
      SC(FA(10), sx, o.sn + WS_XN)
      SC(24, ot.so[1], ot.so[0] ^ 1024u) SC(25, ot.so[2], ot.so[0] ^ 2048u) SC(26, ot.so[3], ot.so[0] ^ 3072u)
    };
    phaseAh<0, 3>(c, T - 2, hookA);
    asm volatile("" : "+v"(dl), "+v"(fx));
    o.srdS = c.srdS; ot.srd = c.srdS; o.va = c.pfv;
    auto hook = [&](auto M_) __attribute__((always_inline)) {
      constexpr int M = decltype(M_)::value;
      constexpr int f = M < NGB && PB2[M < NGB ? M : 0] == 'F' ? pcnt(PB2, 'F', M) : -1;
      VC(100, ax[0], ax[0] + dl) VC(16, ax[1], ax[1] + dl)
      if constexpr (f >= 0 && f < 3) req(IC(0), IC(f));
      if constexpr (M == 33) { const unsigned x = fx, g = tag; asm volatile("v_cmp_ne_u32_e64 %0, %1, %2" : "=s"(o.seen) : "s"(g), "v"(x)); }
      SC(35, t, (unsigned)o.seen & 0x3ffffu)
      if constexpr (M == 40) { unsigned& x = t; const unsigned long long m = tm; asm volatile("s_cmp_lg_u64 %1, 0\n\ts_cselect_b32 %0, %0, 0" : "+s"(x) : "s"(m) : "scc"); }
      SC(44, o.bad, o.bad | t)
      SC(2, c.pfo, o.sb + 23u * 1024u) SC(46, c.pfo, o.sb + 27u * 1024u) SC(48, c.pfm0, 8192u * w) SC(49, c.pfm0, c.pfm0 + (c.ldsb + 0x4000u))
    };
    static_assert(pcnt(PB2, 'E', NGB) == NBL && ppos(PB2, 'W', 0) == DB(4), "");
    phaseB_<0, false, false, 0>(c, hook, make_iseq<NGB>{});
  }
  ot.vo = c.pfv;
  fin_gaps(c, ot, o, make_iseq<NFIN>{});
  asm volatile("s_nop 1" :: "v"(ot.w));
  if (__builtin_expect(!__all((ot.ls[0] + ot.ls[1]) + (ot.ls[2] + ot.ls[3]) <= LIM), 0))
    redo_rows(c, ot, o, wq, 256u * ((opq(blk) >> 3) & 7u) + 64u * opq(wids), 4u * (opq(blk) & 7u) + (opq(blk) >> 6), tag);
  o.srdA = c.srdP; o.srdL = c.srdL; o.tag = tag;
  oproj_phase(o, lds, out, opq(wids)  );
}
extern "C" __global__ void k_attn_end() { asm volatile(PS_END ".subsection 1\n.Lk_end:\n.fill 64, 4, 0xBF800000\n.subsection 0" ::: "memory"); }

extern "C" void kernel_launch(void* const* d_in, const int* in_sizes, int n_in,
                              void* d_out, int out_size, void* d_ws, size_t ws_size,
                              hipStream_t stream) {
  const u16* x   = (const u16*)d_in[0];
  const u16* kc  = (const u16*)d_in[1];
  const u16* vc  = (const u16*)d_in[2];
  const u16* wq  = (const u16*)d_in[3];
  const u16* wo  = (const u16*)d_in[4];
  const u16* w_r = (const u16*)d_in[6];
  const u16* wg  = (const u16*)d_in[7];
  const u16* wu  = (const u16*)d_in[8];
  const u16* wd  = (const u16*)d_in[9];
  char* ws = (char*)d_ws;
  (void)hipFuncSetAttribute((const void*)k_attn, hipFuncAttributeMaxDynamicSharedMemorySize, (int)SHM_ATTN);
  hipLaunchKernelGGL(k_attn, dim3(256), dim3(256), SHM_ATTN, stream, ws, wq, x, w_r, kc, vc, wo, wg, wu, wd, (u16*)d_out  );
}
```
